```python
import torch

def _softmax(x, axis=-1):
    return torch.softmax(x, dim=axis)
def _gelu(x):
    return torch.nn.functional.gelu(x, approximate="tanh")
def _silu(x):
    return torch.nn.functional.silu(x)
def _sigmoid(x):
    return torch.sigmoid(x)
def _layernorm(x, axis=-1, eps=1e-5):
    m = x.mean(dim=axis, keepdim=True)
    v = x.var(dim=axis, keepdim=True, unbiased=False)
    return (x - m) / torch.sqrt(v + eps)
def _scatter_add(tgt, idx, src, axis=0):
    return tgt.clone().index_add_(axis, idx, src)
def _segment_sum(src, seg, n):
    out = torch.zeros((n,) + src.shape[1:], dtype=src.dtype)
    return out.index_add_(0, seg, src)
def _pad(x, pw):
    flat = tuple(v for pair in reversed(pw) for v in pair)
    return torch.nn.functional.pad(x, flat)

kernel_name = 'concat_heads__linear_kernel_hedgehog__linear_kernel_hedgehog__prompt'

def setup_inputs():

    _w = (lambda a: (lambda t: t.to(torch.bfloat16) if t.is_floating_point() else t)(torch.as_tensor(a)))
    def _A_setup():
        B, S, D, H = 1, 2048, 512, 32
        Dh = D // H
        return {
            "x": _w((lambda *s: torch.randn(*s, dtype=torch.bfloat16) * 0.4)(B, S, D)),
            "wqkv": _w((lambda *s: torch.randn(*s, dtype=torch.bfloat16) * 0.4)(D, 3 * D)),
            "w_phi": _w((lambda *s: torch.randn(*s, dtype=torch.bfloat16) * 0.4)(H, Dh, Dh) * (Dh ** -0.5)),
            "wo": _w((lambda *s: torch.randn(*s, dtype=torch.bfloat16) * 0.05)(D, D)),
        }
    _A_in = _A_setup()

    def _B_setup():
        B, S, D, H = 1, 2048, 512, 32
        Dh = D // H
        return {
            "x": _w((lambda *s: torch.randn(*s, dtype=torch.bfloat16) * 0.4)(B, S, D)),
            "wqkv": _w((lambda *s: torch.randn(*s, dtype=torch.bfloat16) * 0.4)(D, 3 * D)),
            "w_phi": _w((lambda *s: torch.randn(*s, dtype=torch.bfloat16) * 0.4)(H, Dh, Dh) * (Dh ** -0.5)),
            "wo": _w((lambda *s: torch.randn(*s, dtype=torch.bfloat16) * 0.05)(D, D)),
        }
    _B_in = _B_setup()

    return {
        "A_x": _A_in["x"],
        "A_wqkv": _A_in["wqkv"],
        "A_w_phi": _A_in["w_phi"],
        "A_wo": _A_in["wo"],
        "B_x": _B_in["x"],
        "B_wqkv": _B_in["wqkv"],
        "B_w_phi": _B_in["w_phi"],
        "B_wo": _B_in["wo"],
    }

def reference(A_x, A_wqkv, A_w_phi, A_wo, B_x, B_wqkv, B_w_phi, B_wo):
    def _A_ref(x, wqkv, w_phi, wo):
        B, S, D = x.shape
        H = 32; Dh = D // H
        qkv = (lambda x, *d: x.permute(*d))((x @ wqkv).reshape(B, S, 3, H, Dh), 2, 0, 3, 1, 4)
        q, k, v = qkv[0], qkv[1], qkv[2]
        pq = torch.einsum("bhsd,hdf->bhsf", q, w_phi)
        pk = torch.einsum("bhsd,hdf->bhsf", k, w_phi)
        phi_q = torch.concatenate([_softmax(pq, axis=-1), _softmax(-pq, axis=-1)], axis=-1)
        phi_k = torch.concatenate([_softmax(pk, axis=-1), _softmax(-pk, axis=-1)], axis=-1)
        kv = torch.einsum("bhsf,bhsd->bhfd", phi_k, v)
        z  = torch.einsum("bhsf->bhf", phi_k)
        num = torch.einsum("bhqf,bhfd->bhqd", phi_q, kv)
        den = torch.einsum("bhqf,bhf->bhq", phi_q, z)[..., None] + 1e-6
        o = num / den
        return (lambda x, *d: x.permute(*d))(o, 0, 2, 1, 3).reshape(B, S, D) @ wo

    def _B_ref(x, wqkv, w_phi, wo):
        B, S, D = x.shape
        H = 32; Dh = D // H
        qkv = (lambda x, *d: x.permute(*d))((x @ wqkv).reshape(B, S, 3, H, Dh), 2, 0, 3, 1, 4)
        q, k, v = qkv[0], qkv[1], qkv[2]
        pq = torch.einsum("bhsd,hdf->bhsf", q, w_phi)
        pk = torch.einsum("bhsd,hdf->bhsf", k, w_phi)
        phi_q = torch.concatenate([_softmax(pq, axis=-1), _softmax(-pq, axis=-1)], axis=-1)
        phi_k = torch.concatenate([_softmax(pk, axis=-1), _softmax(-pk, axis=-1)], axis=-1)
        kv = torch.einsum("bhsf,bhsd->bhfd", phi_k, v)
        z  = torch.einsum("bhsf->bhf", phi_k)
        num = torch.einsum("bhqf,bhfd->bhqd", phi_q, kv)
        den = torch.einsum("bhqf,bhf->bhq", phi_q, z)[..., None] + 1e-6
        o = num / den
        return (lambda x, *d: x.permute(*d))(o, 0, 2, 1, 3).reshape(B, S, D) @ wo

    a = _A_ref(A_x, A_wqkv, A_w_phi, A_wo)
    b = _B_ref(B_x, B_wqkv, B_w_phi, B_wo)
    return torch.concatenate([a, b], axis=-1)
```

```cpp
#include <hip/hip_runtime.h>
#include <hip/hip_bf16.h>
#include <stdint.h>

typedef unsigned short u16;
__device__ __forceinline__ float bf2f(u16 b) { return __uint_as_float(((unsigned)b) << 16); }
__device__ __forceinline__ u16 f2bf(float f) { unsigned u = __float_as_uint(f); u += 0x7FFFu + ((u >> 16) & 1u); return (u16)(u >> 16); }
__device__ __forceinline__ float rbf(float f) { return bf2f(f2bf(f)); }

constexpr int S = 2048, D = 512, H = 32, DH = 16;

__global__ void n_qkv(const u16* x, const u16* w, u16* qkv) {
  int n = blockIdx.x * 256 + threadIdx.x; int s = blockIdx.y;
  float acc = 0.f;
  for (int k = 0; k < D; ++k) acc += bf2f(x[s * D + k]) * bf2f(w[k * 3 * D + n]);
  qkv[s * 3 * D + n] = f2bf(acc);
}
__global__ void n_phi(const u16* qkv, const u16* wphi, u16* phi) {
  int s = blockIdx.x * 256 + threadIdx.x; int h = blockIdx.y; int which = blockIdx.z;
  float p[16];
#pragma unroll
  for (int f = 0; f < 16; ++f) {
    float acc = 0.f;
#pragma unroll
    for (int d = 0; d < 16; ++d) acc += bf2f(qkv[s * 3 * D + which * D + h * 16 + d]) * bf2f(wphi[h * 256 + d * 16 + f]);
    p[f] = rbf(acc);
  }
  float mx = p[0], mn = p[0];
#pragma unroll
  for (int f = 1; f < 16; ++f) { mx = fmaxf(mx, p[f]); mn = fminf(mn, p[f]); }
  float s1 = 0.f, s2 = 0.f;
#pragma unroll
  for (int f = 0; f < 16; ++f) { s1 += expf(p[f] - mx); s2 += expf(mn - p[f]); }
  u16* o = phi + (((size_t)which * H + h) * S + s) * 32;
#pragma unroll
  for (int f = 0; f < 16; ++f) { o[f] = f2bf(expf(p[f] - mx) / s1); o[16 + f] = f2bf(expf(mn - p[f]) / s2); }
}
__global__ void n_kv(const u16* qkv, const u16* phi, float* kv) {
  int h = blockIdx.x; int t = threadIdx.x; if (t >= 32 * 17) return;
  int f = t / 17, d = t % 17;
  const u16* pk = phi + ((size_t)(1 * H + h) * S) * 32;
  float acc = 0.f;
  for (int s = 0; s < S; ++s) {
    float v = d < 16 ? bf2f(qkv[s * 3 * D + 2 * D + h * 16 + d]) : 1.f;
    acc += bf2f(pk[s * 32 + f]) * v;
  }
  kv[h * 32 * 17 + t] = rbf(acc);
}
__global__ void n_o(const u16* phi, const float* kv, u16* o) {
  int s = blockIdx.x * 256 + threadIdx.x; int h = blockIdx.y;
  const u16* pq = phi + ((size_t)h * S + s) * 32;
  float den = 0.f;
  for (int f = 0; f < 32; ++f) den += bf2f(pq[f]) * kv[h * 32 * 17 + f * 17 + 16];
  den = rbf(rbf(den) + 1e-6f);
  for (int d = 0; d < 16; ++d) {
    float num = 0.f;
    for (int f = 0; f < 32; ++f) num += bf2f(pq[f]) * kv[h * 32 * 17 + f * 17 + d];
    o[s * D + h * 16 + d] = f2bf(rbf(num) / den);
  }
}
__global__ void n_out(const u16* o, const u16* wo, u16* out) {
  int n = blockIdx.x * 256 + threadIdx.x; int s = blockIdx.y;
  float acc = 0.f;
  for (int k = 0; k < D; ++k) acc += bf2f(o[s * D + k]) * bf2f(wo[k * D + n]);
  out[s * 2 * D + n] = f2bf(acc);
}

static void naive_launch(void* const* d_in, void* d_out, void* d_ws, hipStream_t stream) {
  for (int b = 0; b < 2; ++b) {
    const u16* x = (const u16*)d_in[4 * b + 0];
    const u16* w = (const u16*)d_in[4 * b + 1];
    const u16* wp = (const u16*)d_in[4 * b + 2];
    const u16* wo = (const u16*)d_in[4 * b + 3];
    char* ws = (char*)d_ws + (size_t)b * (32u << 20);
    u16* qkv = (u16*)ws; u16* phi = (u16*)(ws + (8u << 20)); float* kv = (float*)(ws + (20u << 20)); u16* o = (u16*)(ws + (24u << 20));
    n_qkv<<<dim3(6, S), 256, 0, stream>>>(x, w, qkv);
    n_phi<<<dim3(8, H, 2), 256, 0, stream>>>(qkv, wp, phi);
    n_kv<<<H, 576, 0, stream>>>(qkv, phi, kv);
    n_o<<<dim3(8, H), 256, 0, stream>>>(phi, kv, o);
    n_out<<<dim3(2, S), 256, 0, stream>>>(o, wo, (u16*)d_out + b * D);
  }
}
extern "C" void kernel_launch(void* const* d_in, const int* in_sizes, int n_in,
                              void* d_out, int out_size, void* d_ws, size_t ws_size,
                              hipStream_t stream) {
  naive_launch(d_in, d_out, d_ws, stream);
}
```

```cpp
#include <hip/hip_runtime.h>
#include <stdint.h>

typedef unsigned short u16;
typedef unsigned u32;
typedef short bf16x8 __attribute__((ext_vector_type(8)));
typedef short s16x4 __attribute__((ext_vector_type(4)));
typedef float f32x16 __attribute__((ext_vector_type(16)));
typedef float f32x2 __attribute__((ext_vector_type(2)));
typedef unsigned u32x4 __attribute__((ext_vector_type(4)));
typedef unsigned u32x2 __attribute__((ext_vector_type(2)));
typedef __bf16 bf16x2 __attribute__((ext_vector_type(2)));
#define DI __device__ __forceinline__
#define GAS __attribute__((address_space(1)))
#define LAS __attribute__((address_space(3)))
#define MFMA(a, b, c) __builtin_amdgcn_mfma_f32_32x32x16_bf16((a), (b), (c), 0, 0, 0)

namespace hh {
constexpr u32 POISON = 0xFFFFFFFFu;
constexpr int AUX_SC1 = 16;
constexpr u32 SRD3 = 0x00020000u;
constexpr u32 WS_EPOCH = 0;
constexpr u32 WS_FLAG = 1024;
constexpr u32 REC = 1152;
constexpr u32 WS_PART = 4096;
constexpr u32 WS_O = WS_PART + 2 * 32 * 16 * REC;
constexpr u32 WS_END = WS_O + 2 * 64 * 32 * 1024;
constexpr u32 WS_STAMP = 8u << 20;
constexpr u32 SLOT = 40960;
constexpr u32 L_WO = 0;
constexpr u32 L_PHI = 65536;
constexpr u32 L_V = L_PHI + 32768;
constexpr u32 L_KV = L_V + 16384;
constexpr u32 L_MISC = L_KV + 6144;
constexpr u32 L_WPHI = 3 * SLOT;
constexpr u32 SPIN_CAP = 1u << 18;

DI u32 pk2(float a, float b) { return __builtin_bit_cast(u32, __builtin_convertvector((f32x2){a, b}, bf16x2)); }
DI bf16x8 pack8(const f32x16& x, int s) {
  u32x4 p = {pk2(x[8 * s], x[8 * s + 1]), pk2(x[8 * s + 2], x[8 * s + 3]), pk2(x[8 * s + 4], x[8 * s + 5]), pk2(x[8 * s + 6], x[8 * s + 7])};
  return __builtin_bit_cast(bf16x8, p);
}
DI u32x4 lds_r128(u32 a) { return *(LAS u32x4*)(size_t)a; }
DI void lds_w128(u32 a, u32x4 v) { *(LAS u32x4*)(size_t)a = v; }
DI bf16x8 lds_tr2(u32 a0, u32 a1) {
  s16x4 lo = __builtin_amdgcn_ds_read_tr16_b64_v4i16((LAS s16x4*)(size_t)a0);
  s16x4 hi = __builtin_amdgcn_ds_read_tr16_b64_v4i16((LAS s16x4*)(size_t)a1);
  return __builtin_shufflevector(lo, hi, 0, 1, 2, 3, 4, 5, 6, 7);
}
DI void dma16(u32 lds, u32 voff, __amdgpu_buffer_rsrc_t srd, u32 soff) {
  asm volatile("s_mov_b32 m0, %0\n\ts_nop 0\n\tbuffer_load_dwordx4 %1, %2, %3 offen lds" ::"s"(lds), "v"(voff), "s"(srd), "s"(soff) : "memory");
}
#define BAR() asm volatile("s_waitcnt lgkmcnt(0)\n\ts_barrier" ::: "memory")
#define PIN(x) asm volatile("" : "+v"(x))
#define WAITV(n) asm volatile("s_waitcnt vmcnt(" #n ")" ::: "memory")
DI u32 umin(u32 a, u32 b) { return a < b ? a : b; }
DI u32 umax(u32 a, u32 b) { return a > b ? a : b; }
DI u32x4 nopoison(u32x4 v) { return (u32x4){umin(v.x, POISON - 1), umin(v.y, POISON - 1), umin(v.z, POISON - 1), umin(v.w, POISON - 1)}; }

DI void softmax16(const f32x16& p, u32x4& o0, u32x4& o1) {
  float m = __builtin_fmaxf(__builtin_fmaxf(p[0], p[1]), p[2]);
#pragma unroll
  for (int i = 3; i < 15; i += 2) m = __builtin_fmaxf(__builtin_fmaxf(m, p[i]), p[i + 1]);
  m = __builtin_fmaxf(m, p[15]);
  const float L = 1.44269504088896340736f;
  const float mc = -m * L;
  f32x2 e[8];
#pragma unroll
  for (int i = 0; i < 8; ++i) {
    f32x2 t = __builtin_elementwise_fma((f32x2){p[2 * i], p[2 * i + 1]}, (f32x2){L, L}, (f32x2){mc, mc});
    e[i] = (f32x2){__builtin_amdgcn_exp2f(t.x), __builtin_amdgcn_exp2f(t.y)};
  }
  f32x2 s = ((e[0] + e[1]) + (e[2] + e[3])) + ((e[4] + e[5]) + (e[6] + e[7]));
  const float r = __builtin_amdgcn_rcpf(s.x + s.y);
  u32 w[8];
#pragma unroll
  for (int i = 0; i < 8; ++i) { f32x2 t = e[i] * (f32x2){r, r}; w[i] = pk2(t.x, t.y); }
  o0 = (u32x4){w[0], w[1], w[2], w[3]};
  o1 = (u32x4){w[4], w[5], w[6], w[7]};
}

#define STAMP(i)
#define STAMP_OUT()

__global__ __launch_bounds__(512, 2) void hedgehog(const u16* __restrict__ xA, const u16* __restrict__ wA, const u16* __restrict__ pA, const u16* __restrict__ oA,
                                                   const u16* __restrict__ xB, const u16* __restrict__ wB, const u16* __restrict__ pB, const u16* __restrict__ oB,
                                                   u16* __restrict__ out, u32* ws) {
  __shared__ __attribute__((aligned(16))) char smem[163840];
  const u32 lds0 = (u32)(size_t)(LAS char*)smem;
  const int lane = threadIdx.x & 63;
  const int wave = __builtin_amdgcn_readfirstlane(threadIdx.x >> 6);
  const int bid = blockIdx.x, xcd = bid & 7, idx = bid >> 3;
  const int br = xcd >> 2;
  const int panel = (xcd & 3) * 4 + (idx & 3), grp = idx >> 2;
  const int r = lane & 31, h = lane >> 5;
  const int blk = (lane >> 4) & 1, q = (lane & 15) >> 2, p = lane & 3;
  STAMP(0);
  const __amdgpu_buffer_rsrc_t wsr = __builtin_amdgcn_make_buffer_rsrc(ws, 0, WS_END, SRD3);

  if (wave >= 4) {
    const int lw = wave - 4;
    const __amdgpu_buffer_rsrc_t xr = __builtin_amdgcn_make_buffer_rsrc((void*)(br ? xB : xA), 0, 2048 * 512 * 2, SRD3);
    const __amdgpu_buffer_rsrc_t wr = __builtin_amdgcn_make_buffer_rsrc((void*)(br ? wB : wA), 0, 512 * 1536 * 2, SRD3);
    const __amdgpu_buffer_rsrc_t pr = __builtin_amdgcn_make_buffer_rsrc((void*)(br ? pB : pA), 0, 32 * 256 * 2, SRD3);
    const __amdgpu_buffer_rsrc_t wor = __builtin_amdgcn_make_buffer_rsrc((void*)(br ? oB : oA), 0, 512 * 512 * 2, SRD3);
    const u32 l7 = lane & 7, l3 = lane >> 3, l4 = lane >> 4;
    const u32 vx0 = l3 * 1024 + 16 * (l7 ^ l4), vx1 = l3 * 1024 + 16 * (l7 ^ (4 | l4));
    const u32 wsw = 16 * (l7 ^ ((l4 & 1) << 2));
    const u32 vw = l3 * 3072 + wsw, vo = l3 * 1024 + wsw;
    const int rot = (grp + 2 * (idx & 3)) & 7;
    auto issue = [&](int c) {
      const u32 sb = lds0 + (c & 3) * SLOT;
      const u32 kc = (c + rot) & 7;
      const u32 xs = (panel * 128 + lw * 32) * 1024 + kc * 128;
#pragma unroll
      for (int i = 0; i < 4; ++i) dma16(sb + (lw * 4 + i) * 1024, (i & 1) ? vx1 : vx0, xr, xs + i * 8192);
#pragma unroll
      for (int i = 0; i < 6; ++i) {
        const int m = i >> 1, j = lw * 2 + (i & 1);
        dma16(sb + 16384 + m * 8192 + j * 1024, vw, wr, (kc * 64 + j * 8) * 3072 + m * 1024 + grp * 128);
      }
    };
    auto issue_wo = [&](int half) {
#pragma unroll
      for (int i = 0; i < 8; ++i) {
        const u32 j = half * 32 + lw * 8 + i;
        dma16(lds0 + L_WO + j * 1024, vo, wor, ((j * 8 + grp * 64) & 511) * 1024 + grp * 128);
      }
    };
    dma16(lds0 + L_WPHI + (lw & 1) * 1024, lane * 16, pr, grp * 2048 + (lw & 1) * 1024);
    issue(0);
    issue(1);
    STAMP(1);
    for (int c = 0; c < 6; ++c) {
      WAITV(10);
      BAR();
      issue(c + 2);
    }
    WAITV(10);
    BAR();
    issue_wo(0);
    WAITV(8);
    BAR();
    STAMP(2);
    issue_wo(1);
    const int hp = lw >> 1, hh = lw & 1;
    const u32 ones_a = lds0 + L_MISC + lw * 16;
    if (lane == 0) lds_w128(ones_a, (u32x4){0x3F803F80u, 0x3F803F80u, 0x3F803F80u, 0x3F803F80u});
    const u32 va = lds0 + L_V + hp * 2048 + (h * 32 + 16 * hh + r) * 16;
    const u32 pa = lds0 + L_PHI + lw * 8192 + (4 * h + q) * 64 + 16 * ((2 * blk + (p >> 1)) ^ ((2 * h + (q >> 1)) & 3)) + 8 * (p & 1);
    f32x16 kv;
#pragma unroll
    for (int i = 0; i < 16; ++i) kv[i] = 0.f;
#pragma unroll
    for (int i = 0; i < 2; ++i) {
      BAR();
#pragma unroll
      for (int rh = 0; rh < 2; ++rh) {
        const int tile = 2 * rh + i;
#pragma unroll
        for (int ks = 0; ks < 2; ++ks) {
          const u32x4 a = lds_r128(r < 16 ? va + tile * 4096 + ks * 1024 : ones_a);
          const bf16x8 b = lds_tr2(pa + (32 * tile + 16 * ks) * 64, pa + (32 * tile + 16 * ks + 8) * 64);
          kv = MFMA(__builtin_bit_cast(bf16x8, a), b, kv);
        }
      }
    }
    STAMP(3);
    const u32 tag = *(LAS u32*)(size_t)(lds0 + L_MISC + 64);
    const u32 head = grp * 4 + lw;
    const u32 rec0 = WS_PART + ((br * 32 + head) * 16) * REC;
    {
      const u32x4 v = nopoison(__builtin_bit_cast(u32x4, pack8(kv, 0)));
      __builtin_amdgcn_raw_buffer_store_b128(v, wsr, lane * 16, rec0 + panel * REC, AUX_SC1);
      if (lane < 32) __builtin_amdgcn_raw_buffer_store_b32(umin(__float_as_uint(kv[8]), POISON - 1), wsr, 1024 + lane * 4, rec0 + panel * REC, AUX_SC1);
    }
#pragma unroll 1
    for (u32 spin = 0; spin < SPIN_CAP; ++spin) {
      const u32x2 f = __builtin_amdgcn_raw_buffer_load_b64(wsr, lane * 8, WS_FLAG + br * 512, AUX_SC1);
      if (__builtin_amdgcn_ballot_w64(f.x != tag || f.y != tag) == 0) break;
      __builtin_amdgcn_s_sleep(1);
    }
    asm volatile("" ::: "memory");
    STAMP(4);
    u32x4 g[16];
    u32 gz[16];
#pragma unroll 1
    for (u32 spin = 0; spin < SPIN_CAP; ++spin) {
#pragma unroll
      for (int i = 0; i < 16; ++i) {
        const u32 so = rec0 + ((i + panel) & 15) * REC;
        g[i] = __builtin_amdgcn_raw_buffer_load_b128(wsr, lane * 16, so, AUX_SC1);
        gz[i] = __builtin_amdgcn_raw_buffer_load_b32(wsr, 1024 + r * 4, so, AUX_SC1);
      }
      u32 pm = 0;
#pragma unroll
      for (int i = 0; i < 16; ++i) pm = umax(umax(umax(pm, g[i].x), g[i].y), umax(umax(g[i].z, g[i].w), gz[i]));
      if (__builtin_amdgcn_ballot_w64(pm == POISON) == 0) break;
      asm volatile("" ::: "memory");
    }
    STAMP(5);
    {
      f32x2 s[4];
      float z = 0.f;
#pragma unroll
      for (int j = 0; j < 4; ++j) s[j] = (f32x2){0.f, 0.f};
#pragma unroll
      for (int i = 0; i < 16; ++i) {
#pragma unroll
        for (int j = 0; j < 4; ++j) s[j] += (f32x2){__builtin_bit_cast(float, g[i][j] << 16), __builtin_bit_cast(float, g[i][j] & 0xFFFF0000u)};
        z += __builtin_bit_cast(float, gz[i]);
      }
      const u32 ka = lds0 + L_KV + lw * 1536 + r * 48;
      lds_w128(ka + h * 16, (u32x4){pk2(s[0].x, s[0].y), pk2(s[1].x, s[1].y), pk2(s[2].x, s[2].y), pk2(s[3].x, s[3].y)});
      const u32 zz = pk2(z, z);
      *(LAS u32x2*)(size_t)(ka + 32) = (u32x2){zz, zz};
    }
    WAITV(0);
    BAR();
    STAMP(6);
    const u32 of0 = WS_O + ((br * 64 + panel * 4 + lw) * 32) * 1024;
#pragma unroll 1
    for (u32 spin = 0; spin < SPIN_CAP; ++spin) {
      const u32 f = __builtin_amdgcn_raw_buffer_load_b32(wsr, r * 1024 + h * 1020, of0, AUX_SC1);
      if (__builtin_amdgcn_ballot_w64(f == POISON) == 0) break;
      asm volatile("" ::: "memory");
    }
    asm volatile("" ::: "memory");
    STAMP(7);
    if (panel == 0 && grp == 0 && lw == 0 && lane == 0) __builtin_amdgcn_raw_buffer_store_b32(tag, wsr, 0, WS_EPOCH + br * 256, AUX_SC1);
    const u32 wa = lds0 + L_WO + (8 * h + q) * 128 + 16 * ((2 * blk + (p & 1)) ^ (((q >> 1) & 1) << 2)) + 8 * (p >> 1);
    const u32 wb = wa ^ 64;
    f32x16 c0, c1;
#pragma unroll 1
    for (u32 spin = 0; spin < SPIN_CAP; ++spin) {
      u32x4 of[32];
#pragma unroll
      for (int i = 0; i < 32; ++i) of[i] = __builtin_amdgcn_raw_buffer_load_b128(wsr, lane * 16, of0 + ((i + 4 * grp) & 31) * 1024, AUX_SC1);
      __builtin_amdgcn_sched_barrier(0);
#pragma unroll
      for (int i = 0; i < 16; ++i) { c0[i] = 0.f; c1[i] = 0.f; }
      u32 pm = 0;
#pragma unroll
      for (int i = 0; i < 32; ++i) {
        const bf16x8 a0 = lds_tr2(wa + i * 2048, wa + i * 2048 + 512);
        const bf16x8 a1 = lds_tr2(wb + i * 2048, wb + i * 2048 + 512);
        const bf16x8 b = __builtin_bit_cast(bf16x8, of[i]);
        c0 = MFMA(a0, b, c0);
        c1 = MFMA(a1, b, c1);
        pm = umax(umax(umax(pm, of[i].x), of[i].y), umax(of[i].z, of[i].w));
      }
      if (__builtin_amdgcn_ballot_w64(pm == POISON) == 0) break;
      asm volatile("" ::: "memory");
    }
    STAMP(8);
    {
      u16* op = out + (size_t)(panel * 128 + lw * 32 + r) * 1024 + br * 512 + grp * 64 + 8 * h;
      *(u32x4*)(op) = __builtin_bit_cast(u32x4, pack8(c0, 0));
      *(u32x4*)(op + 16) = __builtin_bit_cast(u32x4, pack8(c0, 1));
      *(u32x4*)(op + 32) = __builtin_bit_cast(u32x4, pack8(c1, 0));
      *(u32x4*)(op + 48) = __builtin_bit_cast(u32x4, pack8(c1, 1));
    }
    STAMP(9);
    STAMP_OUT();
    return;
  }

  const int rh = wave >> 1, hp = wave & 1;
  u32 tag = 0;
  if (wave == 0) tag = __builtin_amdgcn_raw_buffer_load_b32(wsr, 0, WS_EPOCH + br * 256, AUX_SC1) + 1u;
  const u32 sw = (r >> 1) & 7;
  u32 xa[4];
#pragma unroll
  for (int s = 0; s < 4; ++s) xa[s] = lds0 + (64 * rh + r) * 128 + 16 * ((2 * s + h) ^ sw);
  const u32 wa = lds0 + 16384 + (8 * h + q) * 128 + 16 * ((4 * hp + 2 * blk + (p >> 1)) ^ (((q >> 1) & 1) << 2)) + 8 * (p & 1);
  f32x16 aq[2], ak[2], av[2];
#pragma unroll
  for (int i = 0; i < 16; ++i) { aq[0][i] = aq[1][i] = ak[0][i] = ak[1][i] = av[0][i] = av[1][i] = 0.f; }
  PIN(aq[0]); PIN(aq[1]); PIN(ak[0]); PIN(ak[1]); PIN(av[0]); PIN(av[1]);
  bf16x8 xf[2][2], wf[2][3];
  auto ld = [&](int b, u32 sb, int s) {
#pragma unroll
    for (int i = 0; i < 2; ++i) xf[b][i] = __builtin_bit_cast(bf16x8, lds_r128(xa[s] + sb + i * 4096));
#pragma unroll
    for (int m = 0; m < 3; ++m) wf[b][m] = lds_tr2(wa + sb + m * 8192 + s * 2048, wa + sb + m * 8192 + s * 2048 + 512);
  };
  auto mm = [&](int b) {
#pragma unroll
    for (int i = 0; i < 2; ++i) {
      ak[i] = MFMA(wf[b][1], xf[b][i], ak[i]);
      av[i] = MFMA(xf[b][i], wf[b][2], av[i]);
      aq[i] = MFMA(wf[b][0], xf[b][i], aq[i]);
    }
#pragma unroll
    for (int i = 0; i < 4; ++i) {
      __builtin_amdgcn_sched_group_barrier(0x8, 1, 0);
      __builtin_amdgcn_sched_group_barrier(0x100, 2, 0);
    }
    __builtin_amdgcn_sched_group_barrier(0x8, 2, 0);
    __builtin_amdgcn_sched_barrier(0);
  };
  BAR();
  STAMP(1);
  bf16x8 fa[2];
  {
    const u32 a = lds0 + L_WPHI + hp * 1024 + (4 * h + q) * 32 + (8 * blk + 4 * (p >> 1)) * 2;
    const u32 sg = (lane & 4) ? 0x80008000u : 0u;
#pragma unroll
    for (int hh = 0; hh < 2; ++hh) {
      u32x4 t = __builtin_bit_cast(u32x4, lds_tr2(a + hh * 512, a + hh * 512 + 256));
      PIN(t);
      t ^= (u32x4){sg, sg, sg, sg};
      PIN(t);
      fa[hh] = __builtin_bit_cast(bf16x8, t);
    }
  }
  const u32 o_slot = WS_O + ((br * 64 + panel * 4 + 2 * rh) * 32 + grp * 4 + 2 * hp) * 1024;
  ld(0, 0, 0);
  __builtin_amdgcn_sched_barrier(0);
  for (int c = 0; c < 8; ++c) {
    const u32 sb = (c & 3) * SLOT;
    ld(1, sb, 1); mm(0);
    ld(0, sb, 2); mm(1);
    ld(1, sb, 3); mm(0);
    if (c == 2) {
      const u32x4 pz = {POISON, POISON, POISON, POISON};
#pragma unroll
      for (int i = 0; i < 4; ++i) __builtin_amdgcn_raw_buffer_store_b128(pz, wsr, lane * 16, o_slot + (i >> 1) * 32768 + (i & 1) * 1024, AUX_SC1);
      const u32 rec = WS_PART + ((br * 32 + grp * 4 + wave) * 16 + panel) * REC;
      __builtin_amdgcn_raw_buffer_store_b128(pz, wsr, lane * 16, rec, AUX_SC1);
      if (lane < 32) __builtin_amdgcn_raw_buffer_store_b32(POISON, wsr, 1024 + lane * 4, rec, AUX_SC1);
    }
    if (c == 4) WAITV(0);
    if (c == 5 && wave == 0 && lane == 0) __builtin_amdgcn_raw_buffer_store_b32(tag, wsr, (panel * 8 + grp) * 4, WS_FLAG + br * 512, AUX_SC1);
    if (c < 7) {
      BAR();
      ld(0, ((c + 1) & 3) * SLOT, 0);
    }
    mm(1);
  }
  STAMP(2);
  if (wave == 0 && lane == 0) *(LAS u32*)(size_t)(lds0 + L_MISC + 64) = tag;
  f32x16 zero;
#pragma unroll
  for (int i = 0; i < 16; ++i) zero[i] = 0.f;
#pragma unroll
  for (int i = 0; i < 2; ++i) {
    const int tile = 2 * rh + i;
    f32x16 pk0 = MFMA(fa[0], pack8(ak[i], 0), zero);
    f32x16 pk1 = MFMA(fa[1], pack8(ak[i], 1), zero);
#pragma unroll
    for (int ks = 0; ks < 2; ++ks) lds_w128(lds0 + L_V + tile * 4096 + hp * 2048 + ks * 1024 + lane * 16, __builtin_bit_cast(u32x4, pack8(av[i], ks)));
    const u32 pa = lds0 + L_PHI + (2 * hp) * 8192 + (32 * tile + r) * 64;
    const u32 s0 = 16 * ((2 * h) ^ ((r >> 1) & 3)), s1 = s0 ^ 16;
    u32x4 o0, o1;
    softmax16(pk0, o0, o1);
    lds_w128(pa + s0, o0); lds_w128(pa + s1, o1);
    softmax16(pk1, o0, o1);
    lds_w128(pa + 8192 + s0, o0); lds_w128(pa + 8192 + s1, o1);
    BAR();
  }
  STAMP(3);
  u32x4 fq[2][2][2];
#pragma unroll
  for (int i = 0; i < 2; ++i)
#pragma unroll
    for (int hh = 0; hh < 2; ++hh) {
      f32x16 pq = MFMA(fa[hh], pack8(aq[i], hh), zero);
      softmax16(pq, fq[i][hh][0], fq[i][hh][1]);
    }
#pragma unroll
  for (int i = 0; i < 8; ++i) PIN(fq[i >> 2][(i >> 1) & 1][i & 1]);
  STAMP(4);
  BAR();
  STAMP(6);
  {
    const u32 ka = lds0 + L_KV + (2 * hp) * 1536 + (16 * h + q) * 48 + (blk ? 32 : 8 * p);
#pragma unroll
    for (int hh = 0; hh < 2; ++hh) {
      const bf16x8 a0 = lds_tr2(ka + hh * 1536, ka + hh * 1536 + 4 * 48);
      const bf16x8 a1 = lds_tr2(ka + hh * 1536 + 8 * 48, ka + hh * 1536 + 12 * 48);
#pragma unroll
      for (int i = 0; i < 2; ++i) {
        f32x16 y = MFMA(a0, __builtin_bit_cast(bf16x8, fq[i][hh][0]), zero);
        y = MFMA(a1, __builtin_bit_cast(bf16x8, fq[i][hh][1]), y);
        const float rd = __builtin_amdgcn_rcpf(y[8] + 1e-6f);
        const u32x4 o = {pk2(y[0] * rd, y[1] * rd), pk2(y[2] * rd, y[3] * rd), pk2(y[4] * rd, y[5] * rd), pk2(y[6] * rd, y[7] * rd)};
        __builtin_amdgcn_raw_buffer_store_b128(nopoison(o), wsr, lane * 16, o_slot + i * 32768 + hh * 1024, AUX_SC1);
      }
    }
  }
  STAMP(7);
  STAMP_OUT();
}
}

extern "C" void kernel_launch(void* const* d_in, const int* in_sizes, int n_in,
                              void* d_out, int out_size, void* d_ws, size_t ws_size,
                              hipStream_t stream) {
  hh::hedgehog<<<256, 512, 0, stream>>>((const u16*)d_in[0], (const u16*)d_in[1], (const u16*)d_in[2], (const u16*)d_in[3],
                                        (const u16*)d_in[4], (const u16*)d_in[5], (const u16*)d_in[6], (const u16*)d_in[7],
                                        (u16*)d_out, (u32*)d_ws);
}
```

```cpp
#define STAMPS
#define PROBE_N 0
#include <hip/hip_runtime.h>
#include <stdint.h>

typedef unsigned short u16;
typedef unsigned u32;
typedef short bf16x8 __attribute__((ext_vector_type(8)));
typedef short s16x4 __attribute__((ext_vector_type(4)));
typedef float f32x16 __attribute__((ext_vector_type(16)));
typedef float f32x2 __attribute__((ext_vector_type(2)));
typedef unsigned u32x4 __attribute__((ext_vector_type(4)));
typedef unsigned u32x2 __attribute__((ext_vector_type(2)));
typedef __bf16 bf16x2 __attribute__((ext_vector_type(2)));
#define DI __device__ __forceinline__
#define GAS __attribute__((address_space(1)))
#define LAS __attribute__((address_space(3)))
#define MFMA(a, b, c) __builtin_amdgcn_mfma_f32_32x32x16_bf16((a), (b), (c), 0, 0, 0)

namespace hh {
constexpr u32 POISON = 0xFFFFFFFFu;
constexpr int AUX_SC1 = 16;
constexpr u32 SRD3 = 0x00020000u;
constexpr u32 WS_EPOCH = 0;
constexpr u32 WS_FLAG = 1024;
constexpr u32 REC = 1152;
constexpr u32 WS_PART = 4096;
constexpr u32 WS_O = WS_PART + 2 * 32 * 16 * REC;
constexpr u32 WS_END = WS_O + 2 * 64 * 32 * 1024;
constexpr u32 WS_STAMP = 8u << 20;
constexpr u32 SLOT = 40960;
constexpr u32 L_WO = 0;
constexpr u32 L_PHI = 65536;
constexpr u32 L_V = L_PHI + 32768;
constexpr u32 L_KV = L_V + 16384;
constexpr u32 L_MISC = L_KV + 6144;
constexpr u32 L_WPHI = 3 * SLOT;
constexpr u32 SPIN_CAP = 1u << 18;

DI u32 pk2(float a, float b) { return __builtin_bit_cast(u32, __builtin_convertvector((f32x2){a, b}, bf16x2)); }
DI bf16x8 pack8(const f32x16& x, int s) {
  u32x4 p = {pk2(x[8 * s], x[8 * s + 1]), pk2(x[8 * s + 2], x[8 * s + 3]), pk2(x[8 * s + 4], x[8 * s + 5]), pk2(x[8 * s + 6], x[8 * s + 7])};
  return __builtin_bit_cast(bf16x8, p);
}
DI u32x4 lds_r128(u32 a) { return *(LAS u32x4*)(size_t)a; }
DI void lds_w128(u32 a, u32x4 v) { *(LAS u32x4*)(size_t)a = v; }
DI bf16x8 lds_tr2(u32 a0, u32 a1) {
  s16x4 lo = __builtin_amdgcn_ds_read_tr16_b64_v4i16((LAS s16x4*)(size_t)a0);
  s16x4 hi = __builtin_amdgcn_ds_read_tr16_b64_v4i16((LAS s16x4*)(size_t)a1);
  return __builtin_shufflevector(lo, hi, 0, 1, 2, 3, 4, 5, 6, 7);
}
DI void dma16(u32 lds, u32 voff, __amdgpu_buffer_rsrc_t srd, u32 soff) {
  asm volatile("s_mov_b32 m0, %0\n\ts_nop 0\n\tbuffer_load_dwordx4 %1, %2, %3 offen lds" ::"s"(lds), "v"(voff), "s"(srd), "s"(soff) : "memory");
}
#define BAR() asm volatile("s_waitcnt lgkmcnt(0)\n\ts_barrier" ::: "memory")
#define PIN(x) asm volatile("" : "+v"(x))
#define WAITV(n) asm volatile("s_waitcnt vmcnt(" #n ")" ::: "memory")
DI u32 umin(u32 a, u32 b) { return a < b ? a : b; }
DI u32 umax(u32 a, u32 b) { return a > b ? a : b; }
DI u32x4 nopoison(u32x4 v) { return (u32x4){umin(v.x, POISON - 1), umin(v.y, POISON - 1), umin(v.z, POISON - 1), umin(v.w, POISON - 1)}; }

DI void softmax16(const f32x16& p, u32x4& o0, u32x4& o1) {
  float m = __builtin_fmaxf(__builtin_fmaxf(p[0], p[1]), p[2]);
#pragma unroll
  for (int i = 3; i < 15; i += 2) m = __builtin_fmaxf(__builtin_fmaxf(m, p[i]), p[i + 1]);
  m = __builtin_fmaxf(m, p[15]);
  const float L = 1.44269504088896340736f;
  const float mc = -m * L;
  f32x2 e[8];
#pragma unroll
  for (int i = 0; i < 8; ++i) {
    f32x2 t = __builtin_elementwise_fma((f32x2){p[2 * i], p[2 * i + 1]}, (f32x2){L, L}, (f32x2){mc, mc});
    e[i] = (f32x2){__builtin_amdgcn_exp2f(t.x), __builtin_amdgcn_exp2f(t.y)};
  }
  f32x2 s = ((e[0] + e[1]) + (e[2] + e[3])) + ((e[4] + e[5]) + (e[6] + e[7]));
  const float r = __builtin_amdgcn_rcpf(s.x + s.y);
  u32 w[8];
#pragma unroll
  for (int i = 0; i < 8; ++i) { f32x2 t = e[i] * (f32x2){r, r}; w[i] = pk2(t.x, t.y); }
  o0 = (u32x4){w[0], w[1], w[2], w[3]};
  o1 = (u32x4){w[4], w[5], w[6], w[7]};
}

#define STAMP(i) do { unsigned long long t_; asm volatile("s_memrealtime %0\n\ts_waitcnt lgkmcnt(0)" : "=s"(t_) :: "memory"); st[i] = t_; } while (0)
#define STAMP_OUT() do { if (wave >= 4) { unsigned long long d_ = 0; \
  if (PROBE_N >= 1 && PROBE_N < 16) d_ = st[PROBE_N & 15] - st[0]; \
  if (PROBE_N == 16) { u32 mn_ = 0xFFFFFFFFu, mx_ = 0; _Pragma("unroll") for (int k_ = 0; k_ < 2; ++k_) { u32 v_ = __hip_atomic_load((GAS u32*)((char*)ws + WS_STAMP) + br * 128 + k_ * 64 + lane, __ATOMIC_RELAXED, __HIP_MEMORY_SCOPE_AGENT) - (u32)st[0] + 0x40000000u; mn_ = umin(mn_, v_); mx_ = umax(mx_, v_); } \
     for (int o_ = 1; o_ < 64; o_ <<= 1) { mn_ = umin(mn_, __shfl_xor(mn_, o_)); mx_ = umax(mx_, __shfl_xor(mx_, o_)); } d_ = __builtin_amdgcn_readfirstlane(mx_ - mn_); } \
  if (d_ > 100000) d_ = 100000; \
  unsigned long long e_ = st[9] + 8 * d_; for (int it_ = 0; it_ < 1000000; ++it_) { unsigned long long t_; asm volatile("s_memrealtime %0\n\ts_waitcnt lgkmcnt(0)" : "=s"(t_) :: "memory"); if (t_ >= e_) break; } } } while (0)

__global__ __launch_bounds__(512, 2) void hedgehog(const u16* __restrict__ xA, const u16* __restrict__ wA, const u16* __restrict__ pA, const u16* __restrict__ oA,
                                                   const u16* __restrict__ xB, const u16* __restrict__ wB, const u16* __restrict__ pB, const u16* __restrict__ oB,
                                                   u16* __restrict__ out, u32* ws) {
  __shared__ __attribute__((aligned(16))) char smem[163840];
  const u32 lds0 = (u32)(size_t)(LAS char*)smem;
  const int lane = threadIdx.x & 63;
  const int wave = __builtin_amdgcn_readfirstlane(threadIdx.x >> 6);
  const int bid = blockIdx.x, xcd = bid & 7, idx = bid >> 3;
  const int br = xcd >> 2;
  const int panel = (xcd & 3) * 4 + (idx & 3), grp = idx >> 2;
  const int r = lane & 31, h = lane >> 5;
  const int blk = (lane >> 4) & 1, q = (lane & 15) >> 2, p = lane & 3;
  unsigned long long st[16];
#pragma unroll
  for (int i = 0; i < 16; ++i) st[i] = 0;
  STAMP(0);
  if (PROBE_N == 16 && threadIdx.x == 0) __hip_atomic_store((GAS u32*)((char*)ws + WS_STAMP) + br * 128 + panel * 8 + grp, (u32)st[0], __ATOMIC_RELAXED, __HIP_MEMORY_SCOPE_AGENT);
  const __amdgpu_buffer_rsrc_t wsr = __builtin_amdgcn_make_buffer_rsrc(ws, 0, WS_END, SRD3);

  if (wave >= 4) {
    const int lw = wave - 4;
    const __amdgpu_buffer_rsrc_t xr = __builtin_amdgcn_make_buffer_rsrc((void*)(br ? xB : xA), 0, 2048 * 512 * 2, SRD3);
    const __amdgpu_buffer_rsrc_t wr = __builtin_amdgcn_make_buffer_rsrc((void*)(br ? wB : wA), 0, 512 * 1536 * 2, SRD3);
    const __amdgpu_buffer_rsrc_t pr = __builtin_amdgcn_make_buffer_rsrc((void*)(br ? pB : pA), 0, 32 * 256 * 2, SRD3);
    const __amdgpu_buffer_rsrc_t wor = __builtin_amdgcn_make_buffer_rsrc((void*)(br ? oB : oA), 0, 512 * 512 * 2, SRD3);
    const u32 l7 = lane & 7, l3 = lane >> 3, l4 = lane >> 4;
    const u32 vx0 = l3 * 1024 + 16 * (l7 ^ l4), vx1 = l3 * 1024 + 16 * (l7 ^ (4 | l4));
    const u32 wsw = 16 * (l7 ^ ((l4 & 1) << 2));
    const u32 vw = l3 * 3072 + wsw, vo = l3 * 1024 + wsw;
    const int rot = (grp + 2 * (idx & 3)) & 7;
    auto issue = [&](int c) {
      const u32 sb = lds0 + (c & 3) * SLOT;
      const u32 kc = (c + rot) & 7;
      const u32 xs = (panel * 128 + lw * 32) * 1024 + kc * 128;
#pragma unroll
      for (int i = 0; i < 4; ++i) dma16(sb + (lw * 4 + i) * 1024, (i & 1) ? vx1 : vx0, xr, xs + i * 8192);
#pragma unroll
      for (int i = 0; i < 6; ++i) {
        const int m = i >> 1, j = lw * 2 + (i & 1);
        dma16(sb + 16384 + m * 8192 + j * 1024, vw, wr, (kc * 64 + j * 8) * 3072 + m * 1024 + grp * 128);
      }
    };
    auto issue_wo = [&](int half) {
#pragma unroll
      for (int i = 0; i < 8; ++i) {
        const u32 j = half * 32 + lw * 8 + i;
        dma16(lds0 + L_WO + j * 1024, vo, wor, ((j * 8 + grp * 64) & 511) * 1024 + grp * 128);
      }
    };
    dma16(lds0 + L_WPHI + (lw & 1) * 1024, lane * 16, pr, grp * 2048 + (lw & 1) * 1024);
    issue(0);
    issue(1);
    STAMP(1);
    for (int c = 0; c < 6; ++c) {
      WAITV(10);
      BAR();
      issue(c + 2);
    }
    WAITV(10);
    BAR();
    issue_wo(0);
    WAITV(8);
    BAR();
    STAMP(2);
    issue_wo(1);
    const int hp = lw >> 1, hh = lw & 1;
    const u32 ones_a = lds0 + L_MISC + lw * 16;
    if (lane == 0) lds_w128(ones_a, (u32x4){0x3F803F80u, 0x3F803F80u, 0x3F803F80u, 0x3F803F80u});
    const u32 va = lds0 + L_V + hp * 2048 + (h * 32 + 16 * hh + r) * 16;
    const u32 pa = lds0 + L_PHI + lw * 8192 + (4 * h + q) * 64 + 16 * ((2 * blk + (p >> 1)) ^ ((2 * h + (q >> 1)) & 3)) + 8 * (p & 1);
    f32x16 kv;
#pragma unroll
    for (int i = 0; i < 16; ++i) kv[i] = 0.f;
#pragma unroll
    for (int i = 0; i < 2; ++i) {
      BAR();
#pragma unroll
      for (int rh = 0; rh < 2; ++rh) {
        const int tile = 2 * rh + i;
#pragma unroll
        for (int ks = 0; ks < 2; ++ks) {
          const u32x4 a = lds_r128(r < 16 ? va + tile * 4096 + ks * 1024 : ones_a);
          const bf16x8 b = lds_tr2(pa + (32 * tile + 16 * ks) * 64, pa + (32 * tile + 16 * ks + 8) * 64);
          kv = MFMA(__builtin_bit_cast(bf16x8, a), b, kv);
        }
      }
    }
    STAMP(3);
    const u32 tag = *(LAS u32*)(size_t)(lds0 + L_MISC + 64);
    const u32 head = grp * 4 + lw;
    const u32 rec0 = WS_PART + ((br * 32 + head) * 16) * REC;
    {
      const u32x4 v = nopoison(__builtin_bit_cast(u32x4, pack8(kv, 0)));
      __builtin_amdgcn_raw_buffer_store_b128(v, wsr, lane * 16, rec0 + panel * REC, AUX_SC1);
      if (lane < 32) __builtin_amdgcn_raw_buffer_store_b32(umin(__float_as_uint(kv[8]), POISON - 1), wsr, 1024 + lane * 4, rec0 + panel * REC, AUX_SC1);
    }
#pragma unroll 1
    for (u32 spin = 0; spin < SPIN_CAP; ++spin) {
      const u32x2 f = __builtin_amdgcn_raw_buffer_load_b64(wsr, lane * 8, WS_FLAG + br * 512, AUX_SC1);
      if (__builtin_amdgcn_ballot_w64(f.x != tag || f.y != tag) == 0) break;
      __builtin_amdgcn_s_sleep(1);
    }
    asm volatile("" ::: "memory");
    STAMP(4);
    u32x4 g[16];
    u32 gz[16];
#pragma unroll 1
    for (u32 spin = 0; spin < SPIN_CAP; ++spin) {
#pragma unroll
      for (int i = 0; i < 16; ++i) {
        const u32 so = rec0 + ((i + panel) & 15) * REC;
        g[i] = __builtin_amdgcn_raw_buffer_load_b128(wsr, lane * 16, so, AUX_SC1);
        gz[i] = __builtin_amdgcn_raw_buffer_load_b32(wsr, 1024 + r * 4, so, AUX_SC1);
      }
      u32 pm = 0;
#pragma unroll
      for (int i = 0; i < 16; ++i) pm = umax(umax(umax(pm, g[i].x), g[i].y), umax(umax(g[i].z, g[i].w), gz[i]));
      if (__builtin_amdgcn_ballot_w64(pm == POISON) == 0) break;
      asm volatile("" ::: "memory");
    }
    STAMP(5);
    {
      f32x2 s[4];
      float z = 0.f;
#pragma unroll
      for (int j = 0; j < 4; ++j) s[j] = (f32x2){0.f, 0.f};
#pragma unroll
      for (int i = 0; i < 16; ++i) {
#pragma unroll
        for (int j = 0; j < 4; ++j) s[j] += (f32x2){__builtin_bit_cast(float, g[i][j] << 16), __builtin_bit_cast(float, g[i][j] & 0xFFFF0000u)};
        z += __builtin_bit_cast(float, gz[i]);
      }
      const u32 ka = lds0 + L_KV + lw * 1536 + r * 48;
      lds_w128(ka + h * 16, (u32x4){pk2(s[0].x, s[0].y), pk2(s[1].x, s[1].y), pk2(s[2].x, s[2].y), pk2(s[3].x, s[3].y)});
      const u32 zz = pk2(z, z);
      *(LAS u32x2*)(size_t)(ka + 32) = (u32x2){zz, zz};
    }
    WAITV(0);
    BAR();
    STAMP(6);
    const u32 of0 = WS_O + ((br * 64 + panel * 4 + lw) * 32) * 1024;
#pragma unroll 1
    for (u32 spin = 0; spin < SPIN_CAP; ++spin) {
      const u32 f = __builtin_amdgcn_raw_buffer_load_b32(wsr, r * 1024 + h * 1020, of0, AUX_SC1);
      if (__builtin_amdgcn_ballot_w64(f == POISON) == 0) break;
      asm volatile("" ::: "memory");
    }
    asm volatile("" ::: "memory");
    STAMP(7);
    if (panel == 0 && grp == 0 && lw == 0 && lane == 0) __builtin_amdgcn_raw_buffer_store_b32(tag, wsr, 0, WS_EPOCH + br * 256, AUX_SC1);
    const u32 wa = lds0 + L_WO + (8 * h + q) * 128 + 16 * ((2 * blk + (p & 1)) ^ (((q >> 1) & 1) << 2)) + 8 * (p >> 1);
    const u32 wb = wa ^ 64;
    f32x16 c0, c1;
#pragma unroll 1
    for (u32 spin = 0; spin < SPIN_CAP; ++spin) {
      u32x4 of[32];
#pragma unroll
      for (int i = 0; i < 32; ++i) of[i] = __builtin_amdgcn_raw_buffer_load_b128(wsr, lane * 16, of0 + ((i + 4 * grp) & 31) * 1024, AUX_SC1);
      __builtin_amdgcn_sched_barrier(0);
#pragma unroll
      for (int i = 0; i < 16; ++i) { c0[i] = 0.f; c1[i] = 0.f; }
      u32 pm = 0;
#pragma unroll
      for (int i = 0; i < 32; ++i) {
        const bf16x8 a0 = lds_tr2(wa + i * 2048, wa + i * 2048 + 512);
        const bf16x8 a1 = lds_tr2(wb + i * 2048, wb + i * 2048 + 512);
        const bf16x8 b = __builtin_bit_cast(bf16x8, of[i]);
        c0 = MFMA(a0, b, c0);
        c1 = MFMA(a1, b, c1);
        pm = umax(umax(umax(pm, of[i].x), of[i].y), umax(of[i].z, of[i].w));
      }
      if (__builtin_amdgcn_ballot_w64(pm == POISON) == 0) break;
      asm volatile("" ::: "memory");
    }
    STAMP(8);
    {
      u16* op = out + (size_t)(panel * 128 + lw * 32 + r) * 1024 + br * 512 + grp * 64 + 8 * h;
      *(u32x4*)(op) = __builtin_bit_cast(u32x4, pack8(c0, 0));
      *(u32x4*)(op + 16) = __builtin_bit_cast(u32x4, pack8(c0, 1));
      *(u32x4*)(op + 32) = __builtin_bit_cast(u32x4, pack8(c1, 0));
      *(u32x4*)(op + 48) = __builtin_bit_cast(u32x4, pack8(c1, 1));
    }
    WAITV(0);
    STAMP(9);
    STAMP_OUT();
    return;
  }

  const int rh = wave >> 1, hp = wave & 1;
  u32 tag = 0;
  if (wave == 0) tag = __builtin_amdgcn_raw_buffer_load_b32(wsr, 0, WS_EPOCH + br * 256, AUX_SC1) + 1u;
  const u32 sw = (r >> 1) & 7;
  u32 xa[4];
#pragma unroll
  for (int s = 0; s < 4; ++s) xa[s] = lds0 + (64 * rh + r) * 128 + 16 * ((2 * s + h) ^ sw);
  const u32 wa = lds0 + 16384 + (8 * h + q) * 128 + 16 * ((4 * hp + 2 * blk + (p >> 1)) ^ (((q >> 1) & 1) << 2)) + 8 * (p & 1);
  f32x16 aq[2], ak[2], av[2];
#pragma unroll
  for (int i = 0; i < 16; ++i) { aq[0][i] = aq[1][i] = ak[0][i] = ak[1][i] = av[0][i] = av[1][i] = 0.f; }
  PIN(aq[0]); PIN(aq[1]); PIN(ak[0]); PIN(ak[1]); PIN(av[0]); PIN(av[1]);
  bf16x8 xf[2][2], wf[2][3];
  auto ld = [&](int b, u32 sb, int s) {
#pragma unroll
    for (int i = 0; i < 2; ++i) xf[b][i] = __builtin_bit_cast(bf16x8, lds_r128(xa[s] + sb + i * 4096));
#pragma unroll
    for (int m = 0; m < 3; ++m) wf[b][m] = lds_tr2(wa + sb + m * 8192 + s * 2048, wa + sb + m * 8192 + s * 2048 + 512);
  };
  auto mm = [&](int b) {
#pragma unroll
    for (int i = 0; i < 2; ++i) {
      ak[i] = MFMA(wf[b][1], xf[b][i], ak[i]);
      av[i] = MFMA(xf[b][i], wf[b][2], av[i]);
      aq[i] = MFMA(wf[b][0], xf[b][i], aq[i]);
    }
#pragma unroll
    for (int i = 0; i < 4; ++i) {
      __builtin_amdgcn_sched_group_barrier(0x8, 1, 0);
      __builtin_amdgcn_sched_group_barrier(0x100, 2, 0);
    }
    __builtin_amdgcn_sched_group_barrier(0x8, 2, 0);
    __builtin_amdgcn_sched_barrier(0);
  };
  BAR();
  STAMP(1);
  bf16x8 fa[2];
  {
    const u32 a = lds0 + L_WPHI + hp * 1024 + (4 * h + q) * 32 + (8 * blk + 4 * (p >> 1)) * 2;
    const u32 sg = (lane & 4) ? 0x80008000u : 0u;
#pragma unroll
    for (int hh = 0; hh < 2; ++hh) {
      u32x4 t = __builtin_bit_cast(u32x4, lds_tr2(a + hh * 512, a + hh * 512 + 256));
      PIN(t);
      t ^= (u32x4){sg, sg, sg, sg};
      PIN(t);
      fa[hh] = __builtin_bit_cast(bf16x8, t);
    }
  }
  const u32 o_slot = WS_O + ((br * 64 + panel * 4 + 2 * rh) * 32 + grp * 4 + 2 * hp) * 1024;
  ld(0, 0, 0);
  __builtin_amdgcn_sched_barrier(0);
  for (int c = 0; c < 8; ++c) {
    const u32 sb = (c & 3) * SLOT;
    ld(1, sb, 1); mm(0);
    ld(0, sb, 2); mm(1);
    ld(1, sb, 3); mm(0);
    if (c == 2) {
      const u32x4 pz = {POISON, POISON, POISON, POISON};
#pragma unroll
      for (int i = 0; i < 4; ++i) __builtin_amdgcn_raw_buffer_store_b128(pz, wsr, lane * 16, o_slot + (i >> 1) * 32768 + (i & 1) * 1024, AUX_SC1);
      const u32 rec = WS_PART + ((br * 32 + grp * 4 + wave) * 16 + panel) * REC;
      __builtin_amdgcn_raw_buffer_store_b128(pz, wsr, lane * 16, rec, AUX_SC1);
      if (lane < 32) __builtin_amdgcn_raw_buffer_store_b32(POISON, wsr, 1024 + lane * 4, rec, AUX_SC1);
    }
    if (c == 4) WAITV(0);
    if (c == 5 && wave == 0 && lane == 0) __builtin_amdgcn_raw_buffer_store_b32(tag, wsr, (panel * 8 + grp) * 4, WS_FLAG + br * 512, AUX_SC1);
    if (c < 7) {
      BAR();
      ld(0, ((c + 1) & 3) * SLOT, 0);
    }
    mm(1);
  }
  STAMP(2);
  if (wave == 0 && lane == 0) *(LAS u32*)(size_t)(lds0 + L_MISC + 64) = tag;
  f32x16 zero;
#pragma unroll
  for (int i = 0; i < 16; ++i) zero[i] = 0.f;
#pragma unroll
  for (int i = 0; i < 2; ++i) {
    const int tile = 2 * rh + i;
    f32x16 pk0 = MFMA(fa[0], pack8(ak[i], 0), zero);
    f32x16 pk1 = MFMA(fa[1], pack8(ak[i], 1), zero);
#pragma unroll
    for (int ks = 0; ks < 2; ++ks) lds_w128(lds0 + L_V + tile * 4096 + hp * 2048 + ks * 1024 + lane * 16, __builtin_bit_cast(u32x4, pack8(av[i], ks)));
    const u32 pa = lds0 + L_PHI + (2 * hp) * 8192 + (32 * tile + r) * 64;
    const u32 s0 = 16 * ((2 * h) ^ ((r >> 1) & 3)), s1 = s0 ^ 16;
    u32x4 o0, o1;
    softmax16(pk0, o0, o1);
    lds_w128(pa + s0, o0); lds_w128(pa + s1, o1);
    softmax16(pk1, o0, o1);
    lds_w128(pa + 8192 + s0, o0); lds_w128(pa + 8192 + s1, o1);
    BAR();
  }
  STAMP(3);
  u32x4 fq[2][2][2];
#pragma unroll
  for (int i = 0; i < 2; ++i)
#pragma unroll
    for (int hh = 0; hh < 2; ++hh) {
      f32x16 pq = MFMA(fa[hh], pack8(aq[i], hh), zero);
      softmax16(pq, fq[i][hh][0], fq[i][hh][1]);
    }
#pragma unroll
  for (int i = 0; i < 8; ++i) PIN(fq[i >> 2][(i >> 1) & 1][i & 1]);
  STAMP(4);
  BAR();
  STAMP(6);
  {
    const u32 ka = lds0 + L_KV + (2 * hp) * 1536 + (16 * h + q) * 48 + (blk ? 32 : 8 * p);
#pragma unroll
    for (int hh = 0; hh < 2; ++hh) {
      const bf16x8 a0 = lds_tr2(ka + hh * 1536, ka + hh * 1536 + 4 * 48);
      const bf16x8 a1 = lds_tr2(ka + hh * 1536 + 8 * 48, ka + hh * 1536 + 12 * 48);
#pragma unroll
      for (int i = 0; i < 2; ++i) {
        f32x16 y = MFMA(a0, __builtin_bit_cast(bf16x8, fq[i][hh][0]), zero);
        y = MFMA(a1, __builtin_bit_cast(bf16x8, fq[i][hh][1]), y);
        const float rd = __builtin_amdgcn_rcpf(y[8] + 1e-6f);
        const u32x4 o = {pk2(y[0] * rd, y[1] * rd), pk2(y[2] * rd, y[3] * rd), pk2(y[4] * rd, y[5] * rd), pk2(y[6] * rd, y[7] * rd)};
        __builtin_amdgcn_raw_buffer_store_b128(nopoison(o), wsr, lane * 16, o_slot + i * 32768 + hh * 1024, AUX_SC1);
      }
    }
  }
  STAMP(7);
  STAMP_OUT();
}
}

extern "C" void kernel_launch(void* const* d_in, const int* in_sizes, int n_in,
                              void* d_out, int out_size, void* d_ws, size_t ws_size,
                              hipStream_t stream) {
  hh::hedgehog<<<256, 512, 0, stream>>>((const u16*)d_in[0], (const u16*)d_in[1], (const u16*)d_in[2], (const u16*)d_in[3],
                                        (const u16*)d_in[4], (const u16*)d_in[5], (const u16*)d_in[6], (const u16*)d_in[7],
                                        (u16*)d_out, (u32*)d_ws);
}
```

```cpp
#define ROTMODE 2
#define PF_KB 0
#define PROBE_N 99
#define PROBE_ROLE 0
#include <hip/hip_runtime.h>
#include <stdint.h>

typedef unsigned short u16;
typedef unsigned u32;
typedef short bf16x8 __attribute__((ext_vector_type(8)));
typedef short s16x4 __attribute__((ext_vector_type(4)));
typedef float f32x16 __attribute__((ext_vector_type(16)));
typedef float f32x2 __attribute__((ext_vector_type(2)));
typedef unsigned u32x4 __attribute__((ext_vector_type(4)));
typedef unsigned u32x2 __attribute__((ext_vector_type(2)));
typedef __bf16 bf16x2 __attribute__((ext_vector_type(2)));
#define DI __device__ __forceinline__
#define LAS __attribute__((address_space(3)))
#define CAS __attribute__((address_space(4)))
#define MFMA(a, b, c) __builtin_amdgcn_mfma_f32_32x32x16_bf16((a), (b), (c), 0, 0, 0)
#define BC(T, x) __builtin_bit_cast(T, x)

#define DELAY1 0
#define DELAY2 16
#define OUT_AUX 16
#define OROT 1
#define QMIX 6
#define OUT_LDS 1
#define QB 30
#define WQT 0
#define DELAY3 0
#define QDEF 1
#define AEARLY 2
#define HINT 0
#define QBS 0
#define HINTQ 120
#define DELAYH 2
#define PF_W 0
#define WOT 0
#define WOPOS 2
#define BARA 0
#define AHEAD 2
#define LPOL 0
#define POL ""
#define BRMAP 0
#define OORD 0
#define FIRST1 0
#define XMAP 0

namespace hh {
constexpr int AUX_SC1 = 16;
constexpr u32 SRD3 = 0x00020000u;
constexpr u32 WS_EPOCH = 0;
constexpr u32 REC = 1280;
constexpr u32 WS_PART = 4096;
constexpr u32 OREC = 4352;
constexpr u32 WS_O = WS_PART + 2 * 32 * 16 * REC;
constexpr u32 WS_HINT = WS_O + 2 * 64 * 8 * OREC;
constexpr u32 WS_END = WS_HINT + 2 * 16 * 8 * 128;
constexpr u32 WS_STAMP = 8u << 20;
constexpr u32 SLOT = 32768;
constexpr u32 L_WQ0 = 98304;
constexpr u32 L_WQ1 = 65536;
constexpr u32 L_WPHI = 163840 - 2048;
constexpr u32 L_PHI = 0;
constexpr u32 VF = 1040;
constexpr u32 L_V = 131072;
constexpr u32 L_KV = L_V + 16 * VF;
constexpr u32 L_WA = BARA ? 32768 : 0, L_WB = BARA ? 0 : 32768;
constexpr u32 L_WO = 0;
constexpr u32 L_O = 65536;
constexpr u32 SPIN_CAP = 1u << 16;

DI u32 pk2(float a, float b) { return BC(u32, __builtin_convertvector((f32x2){a, b}, bf16x2)); }
DI u32x4 pack8(const f32x16& x, int s) {
  return (u32x4){pk2(x[8 * s], x[8 * s + 1]), pk2(x[8 * s + 2], x[8 * s + 3]), pk2(x[8 * s + 4], x[8 * s + 5]), pk2(x[8 * s + 6], x[8 * s + 7])};
}
DI f32x16 mfma(u32x4 a, u32x4 b, f32x16 c) { return MFMA(BC(bf16x8, a), BC(bf16x8, b), c); }
DI u32x4 lds_r128(u32 a) { return *(LAS u32x4*)(size_t)a; }
DI void lds_w128(u32 a, u32x4 v) { *(LAS u32x4*)(size_t)a = v; }
DI u32x4 lds_tr2(u32 a0, u32 a1) {
  s16x4 lo = __builtin_amdgcn_ds_read_tr16_b64_v4i16((LAS s16x4*)(size_t)a0);
  s16x4 hi = __builtin_amdgcn_ds_read_tr16_b64_v4i16((LAS s16x4*)(size_t)a1);
  return BC(u32x4, __builtin_shufflevector(lo, hi, 0, 1, 2, 3, 4, 5, 6, 7));
}
DI void dma1(u32 lds, u32 voff, __amdgpu_buffer_rsrc_t srd, u32 soff) {
  asm volatile("s_mov_b32 m0, %0\n\ts_nop 0\n\tbuffer_load_dwordx4 %1, %2, %3 offen lds" ::"s"(lds), "v"(voff), "s"(srd), "s"(soff) : "memory");
}
DI void dma4(u32 lds, const u32 (&v)[4], __amdgpu_buffer_rsrc_t srd, u32 soff) {
  asm volatile("s_mov_b32 m0, %0\n\ts_nop 0\n\t"
               "buffer_load_dwordx4 %1, %5, %6 offen " POL " lds\n\t"
               "buffer_load_dwordx4 %2, %5, %6 offen offset:1024 " POL " lds\n\t"
               "buffer_load_dwordx4 %3, %5, %6 offen offset:2048 " POL " lds\n\t"
               "buffer_load_dwordx4 %4, %5, %6 offen offset:3072 " POL " lds" ::"s"(lds), "v"(v[0]), "v"(v[1]), "v"(v[2]), "v"(v[3]), "s"(srd), "s"(soff) : "memory");
}
DI void dma_orec(u32 lds, u32 l4, u32 l16, __amdgpu_buffer_rsrc_t srd, u32 soff) {
  asm volatile("s_mov_b32 m0, %0\n\ts_nop 0\n\t"
               "buffer_load_dwordx4 %2, %3, %4 offen offset:256 sc1 lds\n\t"
               "buffer_load_dwordx4 %2, %3, %4 offen offset:1280 sc1 lds\n\t"
               "buffer_load_dwordx4 %2, %3, %4 offen offset:2304 sc1 lds\n\t"
               "buffer_load_dwordx4 %2, %3, %4 offen offset:3328 sc1 lds\n\t"
               "buffer_load_dword %1, %3, %4 offen sc1 lds" ::"s"(lds), "v"(l4), "v"(l16), "s"(srd), "s"(soff) : "memory");
}
DI u32 now32() { unsigned long long t; asm volatile("s_memrealtime %0\n\ts_waitcnt lgkmcnt(0)" : "=s"(t)::"memory"); return (u32)t; }
DI void st128c(u32x4 v, __amdgpu_buffer_rsrc_t srd, u32 off) { __builtin_amdgcn_raw_buffer_store_b128(v, srd, off, 0, AUX_SC1); }
DI void st32c(u32 v, __amdgpu_buffer_rsrc_t srd, u32 off) { __builtin_amdgcn_raw_buffer_store_b32(v, srd, off, 0, AUX_SC1); }
#define BAR() asm volatile("s_waitcnt lgkmcnt(0)\n\ts_barrier" ::: "memory")
#define BARE() asm volatile("s_barrier" ::: "memory")
#define OPQ(x) asm("" : "+v"(x))
#define OPQV(x) asm volatile("" : "+v"(x))
#define PIN(x) asm volatile("" : "+v"(x))
#define PINS(x) asm volatile("" : "+s"(x))
#define WAITV(n) asm volatile("s_waitcnt vmcnt(" #n ")" ::: "memory")
#define SCHED() __builtin_amdgcn_sched_barrier(0)
#define SGB(m, n) __builtin_amdgcn_sched_group_barrier(m, n, 0)
DI u32 sum4(u32 c, u32x4 v) { return (c + v.x + v.y) + v.z + v.w; }
DI u32 pair_sum(u32 s) { auto t = __builtin_amdgcn_permlane32_swap(s, s, false, false); return t[0] + t[1]; }

constexpr float LOG2E = 1.44269504088896340736f;
DI float max3f(float a, float b, float c) { return __builtin_fmaxf(__builtin_fmaxf(a, b), c); }
DI float negmax16(const f32x16& x) {
  const float a = max3f(x[0], x[1], x[2]), b = max3f(x[3], x[4], x[5]), c = max3f(x[6], x[7], x[8]);
  const float d = max3f(x[9], x[10], x[11]), e = max3f(x[12], x[13], x[14]);
  return -max3f(max3f(a, b, x[15]), max3f(c, d, e), a) * LOG2E;
}
DI void softmax16x2(const f32x16& p, const f32x16& q, const float (&mc)[2], u32x4 (&o)[2][2]) {
  const f32x16* z[2] = {&p, &q};
  f32x2 e[2][8];
#pragma unroll
  for (int i = 0; i < 8; ++i)
#pragma unroll
    for (int u = 0; u < 2; ++u) {
      const f32x16& x = *z[u];
      f32x2 t = __builtin_elementwise_fma((f32x2){x[2 * i], x[2 * i + 1]}, (f32x2){LOG2E, LOG2E}, (f32x2){mc[u], mc[u]});
      e[u][i] = (f32x2){__builtin_amdgcn_exp2f(t.x), __builtin_amdgcn_exp2f(t.y)};
    }
  SGB(0x2, 4); SGB(0x400, 8); SGB(0x2, 4); SGB(0x400, 8); SGB(0x2, 6); SGB(0x400, 8); SGB(0x2, 8); SGB(0x400, 8);
  float r[2];
#pragma unroll
  for (int u = 0; u < 2; ++u) {
    f32x2 s = ((e[u][0] + e[u][1]) + (e[u][2] + e[u][3])) + ((e[u][4] + e[u][5]) + (e[u][6] + e[u][7]));
    r[u] = __builtin_amdgcn_rcpf(s.x + s.y);
  }
#pragma unroll
  for (int u = 0; u < 2; ++u) {
    u32 w[8];
#pragma unroll
    for (int i = 0; i < 8; ++i) { f32x2 t = e[u][i] * (f32x2){r[u], r[u]}; w[i] = pk2(t.x, t.y); }
    o[u][0] = (u32x4){w[0], w[1], w[2], w[3]};
    o[u][1] = (u32x4){w[4], w[5], w[6], w[7]};
  }
}
DI void softmax16x2s(const f32x16& p, const f32x16& q, const float (&mc)[2], u32x4 (&o)[2][2]) {
  const f32x16* z[2] = {&p, &q};
  float e[2][16];
#pragma unroll
  for (int i = 0; i < 16; ++i)
#pragma unroll
    for (int u = 0; u < 2; ++u) e[u][i] = __builtin_amdgcn_exp2f(__builtin_fmaf((*z[u])[i], LOG2E, mc[u]));
  float r[2];
#pragma unroll
  for (int u = 0; u < 2; ++u) {
    float s0 = e[u][0] + e[u][1], s1 = e[u][2] + e[u][3];
#pragma unroll
    for (int i = 4; i < 16; i += 2) { s0 += e[u][i]; OPQ(s0); s1 += e[u][i + 1]; }
    r[u] = __builtin_amdgcn_rcpf(s0 + s1);
  }
#pragma unroll
  for (int u = 0; u < 2; ++u) {
    u32 w[8];
#pragma unroll
    for (int i = 0; i < 8; ++i) { float t0 = e[u][2 * i] * r[u], t1 = e[u][2 * i + 1] * r[u]; OPQ(t0); w[i] = pk2(t0, t1); }
    o[u][0] = (u32x4){w[0], w[1], w[2], w[3]};
    o[u][1] = (u32x4){w[4], w[5], w[6], w[7]};
  }
}

#define STAMP(i) do { if ((i) == PROBE_N) { SCHED(); asm volatile("s_memrealtime %0\n\ts_waitcnt lgkmcnt(0)" : "=s"(pt1) :: "memory"); SCHED(); } } while (0)
#define STAMP_OUT() do { if (PROBE_N && (wave >> 2) == PROBE_ROLE) { unsigned long long d_ = pt1 - pt0; if (d_ > 100000) d_ = 100000; unsigned long long e_ = pt0 + 3000 + 8 * d_; \
  for (int it_ = 0; it_ < 1000000; ++it_) { unsigned long long t_; asm volatile("s_memrealtime %0\n\ts_waitcnt lgkmcnt(0)" : "=s"(t_) :: "memory"); if (t_ >= e_) break; } } } while (0)

__global__ __launch_bounds__(512, 2) void hedgehog(const u16* __restrict__ xA, const u16* __restrict__ wA, const u16* __restrict__ pA, const u16* __restrict__ oA,
                                                   const u16* __restrict__ xB, const u16* __restrict__ wB, const u16* __restrict__ pB, const u16* __restrict__ oB,
                                                   u16* __restrict__ out_, u32* ws_) {
  __shared__ __attribute__((aligned(16))) char smem[163840];
  const u32 lds0 = (u32)(size_t)(LAS char*)smem;
  unsigned long long pt0 = __builtin_amdgcn_s_memrealtime(), pt1 = pt0;
  const int bid = blockIdx.x, xcd = bid & 7, idx = bid >> 3;
  const int br = xcd >> 2, xq4 = xcd & 3;
  typedef u32 u32x8 __attribute__((ext_vector_type(8)));
#define KLOAD(tail)                                                                                                            \
  u32x8 ka;                                                                                                                    \
  u32x4 kb;                                                                                                                    \
  asm volatile("s_load_dwordx8 %0, %2, %3\n\ts_load_dwordx4 %1, %2, 0x40" tail : "=&s"(ka), "=&s"(kb)                          \
               : "s"(__builtin_amdgcn_kernarg_segment_ptr()), "s"(br * 32))
#define KWAIT() asm volatile("s_waitcnt lgkmcnt(0)" : "+s"(ka), "+s"(kb))
#define KPTR(v, i) ((void*)(((unsigned long long)(v)[(i) + 1] << 32) | (v)[i]))
  const int lane = threadIdx.x & 63;
  const int wave = __builtin_amdgcn_readfirstlane(threadIdx.x >> 6);
  const int panel = xq4 * 4 + (idx & 3), grp = idx >> 2;
  const int rot = (grp + 2 * (idx & 3)) & 7;
  const int grot = idx & 3;
  const int panel2 = panel, cg = grp;
  auto ochunk = [&](u32 j) -> u32 {
    return (j + grp) & 7;
  };
  f32x16 zero;
#pragma unroll
  for (int i = 0; i < 16; ++i) zero[i] = 0.f;
#define LANE_IDS()                                                              \
  u32 lane_ = lane;                                                             \
  OPQV(lane_);                                                                  \
  const u32 r = lane_ & 31, h = lane_ >> 5;                                     \
  const u32 blk = (lane_ >> 4) & 1, q = (lane_ & 15) >> 2, p = lane_ & 3;       \
  u32* ws = (u32*)KPTR(kb, 2);                                                  \
  const __amdgpu_buffer_rsrc_t wsr = __builtin_amdgcn_make_buffer_rsrc(ws, 0, WS_END, SRD3);  \
  const __amdgpu_buffer_rsrc_t wor = __builtin_amdgcn_make_buffer_rsrc(KPTR(ka, 6), 0, 512 * 512 * 2, SRD3)

  if (wave >= 4) {
    const int cw = wave - 4;
    const int rh = cw >> 1, hp = cw & 1;
    KLOAD("\n\ts_waitcnt lgkmcnt(0)");
    LANE_IDS();
    u32 key = *(const CAS u32*)((const char*)ws + WS_EPOCH + br * 256) + 1u;
    const u32 sw = (r >> 1) & 7;
    u32 xa[4];
#pragma unroll
    for (int s = 0; s < 4; ++s) xa[s] = lds0 + (64 * rh + r) * 128 + 16 * ((2 * s + h) ^ sw);
    u32 wa = lds0 + 16384 + (8 * h + q) * 128 + 16 * ((4 * hp + 2 * blk + (p >> 1)) ^ (((q >> 1) & 1) << 2)) + 8 * (p & 1);
    u32 fpa = lds0 + L_WPHI + hp * 1024 + (4 * h + q) * 32 + (8 * blk + 4 * (p >> 1)) * 2;
    PIN(fpa);
    f32x16 ak[2], av[2];
    ak[0] = ak[1] = av[0] = av[1] = zero;
    u32 xb[4], wb2 = wa + 2 * SLOT;
#pragma unroll
    for (int s = 0; s < 4; ++s) { xb[s] = xa[s] + 2 * SLOT; PIN(xa[s]); PIN(xb[s]); }
    PIN(wa); PIN(wb2);
    PINS(key);
    u32 vra, pra, vwa, pw0, pw1, pub16, pub4;
    const u32 rec0 = WS_PART + ((br * 32 + grp * 4 + cw) * 16) * REC;
    u32x4 xf[4][2], wf[4][2];
    auto ld = [&](int c, int s) {
      const int sl = c % 3;
      const u32 x = sl == 2 ? xb[s] : xa[s] + sl * SLOT, w = (sl == 2 ? wb2 : wa + sl * SLOT) + s * 2048;
      xf[s][0] = lds_r128(x);
      wf[s][0] = lds_tr2(w, w + 512);
      xf[s][1] = lds_r128(x + 4096);
      wf[s][1] = lds_tr2(w + 8192, w + 8192 + 512);
    };
    auto mm = [&](int b, int nrd) {
      ak[0] = mfma(wf[b][0], xf[b][0], ak[0]);
      ak[1] = mfma(wf[b][0], xf[b][1], ak[1]);
      av[0] = mfma(xf[b][0], wf[b][1], av[0]);
      av[1] = mfma(xf[b][1], wf[b][1], av[1]);
      if (nrd == 3) { SGB(0x8, 1); SGB(0x100, 4); SGB(0x8, 1); SGB(0x100, 3); SGB(0x8, 1); SGB(0x100, 3); SGB(0x8, 1); SGB(0x100, 2); }
      if (nrd == 2) { SGB(0x8, 1); SGB(0x100, 2); SGB(0x8, 1); SGB(0x100, 2); SGB(0x8, 1); SGB(0x100, 2); SGB(0x8, 1); }
      if (nrd == 1) { SGB(0x8, 1); SGB(0x6, 4); SGB(0x8, 1); SGB(0x6, 4); SGB(0x8, 1); SGB(0x6, 4); SGB(0x8, 1); }
      SCHED();
    };
    BAR();
    STAMP(1);
    ld(0, 0); ld(0, 1);
    u32x4 fa[2];
#pragma unroll
    for (int hh = 0; hh < 2; ++hh) fa[hh] = lds_tr2(fpa + hh * 512, fpa + hh * 512 + 256);
    SCHED();
#pragma unroll
    for (int c = 0; c < 7; ++c) {
      ld(c, 2); mm(0, 2);
      ld(c, 3); mm(1, 2);
      u32 ln = lane;
      OPQV(ln);
      const u32 r_ = ln & 31, h_ = ln >> 5, q_ = (ln & 15) >> 2;
      if (c == 1) {
        const u32 sg = (ln & 4) ? 0x80008000u : 0u;
#pragma unroll
        for (int hh = 0; hh < 2; ++hh) { OPQ(fa[hh]); fa[hh] ^= (u32x4){sg, sg, sg, sg}; }
      }
      if (c == 2) vra = lds0 + L_V + (cw >> 1) * (2 * VF) + (r_ < 16 ? (ln + 16 * (cw & 1)) * 16 : 1024);
      if (c == 3) pra = lds0 + L_PHI + cw * 8192 + (4 * h_ + q_) * 64 + 16 * ((((ln >> 3) & 2) + ((ln >> 1) & 1)) ^ ((2 * h_ + (q_ >> 1)) & 3)) + 8 * (ln & 1);
      if (c == 4) { vwa = lds0 + L_V + (8 * rh + 2 * hp) * VF + ln * 16; pub16 = rec0 + panel * REC + ln * 16; pub4 = rec0 + panel * REC + 1024 + ln * 4; }
      if (c == 5) { pw0 = lds0 + L_PHI + (2 * hp) * 8192 + (64 * rh + r_) * 64 + 16 * ((2 * h_) ^ ((r_ >> 1) & 3)); pw1 = pw0 ^ 16; }
      mm(2, (c >= 1 && c <= 5) ? 1 : 0);
      if (c == 1) { PIN(fa[0]); PIN(fa[1]); }
      if (c == 2) PIN(vra);
      if (c == 3) PIN(pra);
      if (c == 4) { PIN(vwa); PIN(pub16); PIN(pub4); }
      if (c == 5) { PIN(pw0); PIN(pw1); }
      BAR();
      ld(c + 1, 0); ld(c + 1, 1);
      mm(3, 3);
    }
    ld(7, 2); ld(7, 3);
#pragma unroll
    for (int s = 0; s < 4; ++s) { ak[0] = mfma(wf[s][0], xf[s][0], ak[0]); ak[1] = mfma(wf[s][0], xf[s][1], ak[1]); }
    SGB(0x8, 1); SGB(0x100, 4); SGB(0x8, 1); SGB(0x100, 4); SGB(0x8, 1); SGB(0x100, 4); SGB(0x8, 5);
    SCHED();
    __builtin_amdgcn_s_setprio(2);
    STAMP(2);
    f32x16 pk[2][2];
    {
#pragma unroll
      for (int s = 0; s < 2; ++s) { av[0] = mfma(xf[s][0], wf[s][1], av[0]); av[1] = mfma(xf[s][1], wf[s][1], av[1]); }
      u32x4 kf[2][2];
#pragma unroll
      for (int i = 0; i < 2; ++i)
#pragma unroll
        for (int hh = 0; hh < 2; ++hh) kf[i][hh] = pack8(ak[i], hh);
      SGB(0x8, 2); SGB(0x2, 4); SGB(0x8, 1); SGB(0x2, 6); SGB(0x8, 1); SGB(0x2, 6);
      SCHED();
      pk[0][0] = mfma(fa[0], kf[0][0], zero);
      pk[0][1] = mfma(fa[1], kf[0][1], zero);
      PIN(pk[0][0]); PIN(pk[0][1]);
      SCHED();
#pragma unroll
      for (int s = 2; s < 4; ++s) { av[0] = mfma(xf[s][0], wf[s][1], av[0]); av[1] = mfma(xf[s][1], wf[s][1], av[1]); }
      pk[1][0] = mfma(fa[0], kf[1][0], zero);
      pk[1][1] = mfma(fa[1], kf[1][1], zero);
    }
    float m0[2], m1[2];
    m0[0] = negmax16(pk[0][0]); m0[1] = negmax16(pk[0][1]);
    SGB(0x8, 2); SGB(0x2, 5); SGB(0x8, 1); SGB(0x2, 5); SGB(0x8, 1); SGB(0x2, 4); SGB(0x8, 1); SGB(0x2, 4); SGB(0x8, 1);
    PIN(av[0]); PIN(av[1]); PIN(pk[1][0]); PIN(pk[1][1]); PIN(m0[0]); PIN(m0[1]);
    SCHED();
#pragma unroll
    for (int i = 0; i < 2; ++i)
#pragma unroll
      for (int ks = 0; ks < 2; ++ks) lds_w128(vwa + (4 * i + ks) * VF, pack8(av[i], ks));
    SCHED();
    {
      u32x4 o[2][2];
      softmax16x2(pk[0][0], pk[0][1], m0, o);
      lds_w128(pw0, o[0][0]); lds_w128(pw1, o[0][1]);
      lds_w128(pw0 + 8192, o[1][0]); lds_w128(pw1 + 8192, o[1][1]);
      SCHED();
      m1[0] = negmax16(pk[1][0]); m1[1] = negmax16(pk[1][1]);
      PIN(m1[0]); PIN(m1[1]);
      SCHED();
      if (BARA) { BARE(); SCHED(); }
      softmax16x2(pk[1][0], pk[1][1], m1, o);
      lds_w128(pw0 + 2048, o[0][0]); lds_w128(pw1 + 2048, o[0][1]);
      lds_w128(pw0 + 2048 + 8192, o[1][0]); lds_w128(pw1 + 2048 + 8192, o[1][1]);
      SGB(0x402, 26); SGB(0x200, 1); SGB(0x402, 8); SGB(0x200, 1); SGB(0x402, 8); SGB(0x200, 1); SGB(0x402, 8); SGB(0x200, 1);
      SCHED();
      BAR();
      SCHED();
    }
    STAMP(3);
    f32x16 kv = zero;
    {
      u32x4 a[8], b[8];
#pragma unroll
      for (int u = 0; u < 8; ++u) {
        a[u] = lds_r128(vra + ((u >> 1) * 4 + (u & 1)) * VF);
        b[u] = lds_tr2(pra + 16 * u * 64, pra + (16 * u + 8) * 64);
      }
#pragma unroll
      for (int u = 0; u < 8; ++u) kv = mfma(a[u], b[u], kv);
      SGB(0x100, 12); SGB(0x8, 1); SGB(0x100, 6); SGB(0x8, 1); SGB(0x100, 6); SGB(0x8, 6);
    }
    {
      const u32x4 w = pack8(kv, 0);
      st128c(w, wsr, pub16);
      const u32 zb = __float_as_uint(kv[8]);
      const u32 chk = key - pair_sum(sum4(h ? 0u : zb, w));
      st32c(h ? chk : zb, wsr, pub4);
    }
    SCHED();
    STAMP(4);
    u32 l16, l4, kwa, kza, gso[4], vo[4], wso, hv, hint0 = WS_HINT + (br * 16 + panel) * 1024;
    u32x4 sel;
    {
      u32 ln = lane;
      OPQV(ln);
#pragma unroll
      for (int i = 0; i < 4; ++i) vo[i] = (ln >> 3) * 1024 + 16 * ((ln & 7) ^ (((ln >> 4) & 1) << 2)) + i * 7168;
      if (!BARA) {
#pragma unroll
        for (int i = 0; i < 2; ++i) dma4(lds0 + L_WA + cw * 8192 + i * 4096, vo, wor, ochunk(cw) * 65536 + i * 32768 + cg * 128);
      }
      wso = ochunk(4 + cw) * 65536 + cg * 128;
      PINS(wso);
      if (WOPOS == 0) {
#pragma unroll
        for (int i = 0; i < 2; ++i) dma4(lds0 + L_WB + cw * 8192 + i * 4096, vo, wor, wso + i * 32768);
      }
      const u32 r_ = ln & 31, h_ = ln >> 5;
      l16 = ln * 16; l4 = ln * 4;
      kwa = lds0 + L_KV + cw * 1536 + r_ * 48 + h_ * 16; kza = lds0 + L_KV + cw * 1536 + r_ * 48 + 32 + h_ * 8;
      const bool hit = r_ < 16 && ((r_ >> 2) & 1) == h_;
      const u32 j = (r_ & 3) + 4 * (r_ >> 3);
#pragma unroll
      for (int e = 0; e < 4; ++e) sel[e] = (hit && (j >> 1) == e) ? ((j & 1) ? 0x3F800000u : 0x00003F80u) : 0u;
#pragma unroll
      for (int i = 0; i < 4; ++i) { gso[i] = rec0 + ((i + grot) & 3) * (4 * REC); PINS(gso[i]); }
      hv = (ln & 7) * 128;
      PIN(l16); PIN(l4); PIN(kwa); PIN(kza); PIN(sel); PIN(hv); PINS(hint0);
    }
    if (DELAY1) __builtin_amdgcn_s_sleep(DELAY1);
    STAMP(10);
    f32x16 gs[4];
    float z[4];
    {
      u32x4 g[16];
      u32 gz[16];
      auto gload = [&](int gi) {
#pragma unroll
        for (int i = 0; i < 4; ++i) {
          g[4 * gi + i] = __builtin_amdgcn_raw_buffer_load_b128(wsr, l16 + i * REC, gso[gi], AUX_SC1);
          gz[4 * gi + i] = __builtin_amdgcn_raw_buffer_load_b32(wsr, l4 + (1024 + i * REC - 1024 * (i == 3)), gso[gi] + 1024 * (i == 3), AUX_SC1);
          SCHED();
        }
      };
      auto gsum = [&](int gi) -> bool {
        u32 c = 0;
#pragma unroll
        for (int i = 0; i < 4; ++i) {
          c = sum4(c + gz[4 * gi + i], g[4 * gi + i]);
          gs[gi] = mfma(sel, g[4 * gi + i], i == 0 ? zero : gs[gi]);
          z[gi] = i == 0 ? __uint_as_float(gz[4 * gi]) : z[gi] + __uint_as_float(gz[4 * gi + i]);
        }
        PIN(z[gi]);
        const bool bad = __builtin_amdgcn_ballot_w64(pair_sum(c) != 4u * key) != 0;
        SCHED();
        return bad;
      };
      gload(0); gload(1); gload(2); gload(3);
      bool b0 = gsum(0), b1 = gsum(1), b2 = gsum(2), b3 = gsum(3);
      STAMP(11);
#pragma unroll 1
      for (u32 spin = 0; (b0 | b1 | b2 | b3) && spin < SPIN_CAP; ++spin) {
        asm volatile("" ::: "memory");
        if (b0) gload(0);
        if (b1) gload(1);
        if (b2) gload(2);
        if (b3) gload(3);
        if (b0) b0 = gsum(0);
        if (b1) b1 = gsum(1);
        if (b2) b2 = gsum(2);
        if (b3) b3 = gsum(3);
      }
    }
    __builtin_amdgcn_s_waitcnt(0x0F70);
    if (HINT && cw == 3) __builtin_amdgcn_raw_buffer_store_b64((u32x2){key, now32()}, wsr, grp * 128, hint0, AUX_SC1);
    STAMP(5);
    {
      float tt[8];
#pragma unroll
      for (int j = 0; j < 8; ++j) tt[j] = (gs[0][j] + gs[1][j]) + (gs[2][j] + gs[3][j]);
      const float zt = (z[0] + z[1]) + (z[2] + z[3]);
      lds_w128(kwa, (u32x4){pk2(tt[0], tt[1]), pk2(tt[2], tt[3]), pk2(tt[4], tt[5]), pk2(tt[6], tt[7])});
      const u32 zz = pk2(zt, zt);
      *(LAS u32x2*)(size_t)kza = (u32x2){zz, zz};
    }
    if (WOPOS == 1) {
#pragma unroll
      for (int i = 0; i < 2; ++i) dma4(lds0 + L_WB + cw * 8192 + i * 4096, vo, wor, wso + i * 32768);
    }
    BAR();
    STAMP(6);
    {
      u32 oso[8], ob = lds0 + L_O + cw * OREC;
      u32x4 hn;
      if (HINT) {
        if (DELAYH) __builtin_amdgcn_s_sleep(DELAYH);
        asm volatile("buffer_load_dwordx4 %0, %1, %2, %3 offen sc1" : "=v"(hn) : "v"(hv), "s"(wsr), "s"(hint0) : "memory");
      }
#pragma unroll
      for (int i = 1; i < 8; ++i) { oso[i] = WS_O + ((br * 64 + panel2 * 4 + cw) * 8 + ochunk(i)) * OREC; PINS(oso[i]); }
      PINS(ob);
      BARE();
      STAMP(7);
      if (WOPOS == 2) {
#pragma unroll
        for (int i = 0; i < 2; ++i) dma4(lds0 + L_WB + cw * 8192 + i * 4096, vo, wor, wso + i * 32768);
      }
      if (HINT) {
        if (WOPOS == 2) asm volatile("s_waitcnt vmcnt(8)" : "+v"(hn)::"memory");
        else asm volatile("s_waitcnt vmcnt(0)" : "+v"(hn)::"memory");
        STAMP(12);
#pragma unroll 1
        for (u32 spin = 0; spin < SPIN_CAP; ++spin) {
          if (__builtin_amdgcn_ballot_w64(hn.x != key || hn.z != key) == 0) break;
          asm volatile("buffer_load_dwordx4 %0, %1, %2, %3 offen sc1\n\ts_waitcnt vmcnt(0)" : "=&v"(hn) : "v"(hv), "s"(wsr), "s"(hint0) : "memory");
        }
        STAMP(13);
#pragma unroll 1
        for (u32 spin = 0; spin < 64; ++spin) {
          const u32 now = now32();
          if (__builtin_amdgcn_ballot_w64((int)(now - hn.y) < HINT || (int)(now - hn.w) < HINTQ) == 0) break;
        }
      } else if (DELAY2) __builtin_amdgcn_s_sleep(DELAY2);
      STAMP(8);
      auto oissue = [&](int j) { dma_orec(ob + (j % 5) * (4 * OREC), l4, l16, wsr, oso[j]); };
      oissue(1); oissue(2); oissue(3);
      if (FIRST1) { WAITV(10); BARE(); }
      WAITV(5); STAMP(15); BARE(); oissue(4);
      WAITV(5); oissue(5); BARE(); oissue(6);
      WAITV(10); BARE(); oissue(7);
      WAITV(10); BARE();
      WAITV(5); BARE();
      WAITV(0); BARE();
    }
    STAMP(9);
    STAMP_OUT();
  } else {
    KLOAD("");
    const int t = wave;
    u32 pf;
    if (PF_KB) {
      unsigned long long pc0;
      asm volatile("s_getpc_b64 %0" : "=s"(pc0));
      pc0 &= ~1023ull;
      const u32 line = idx * 8 + t * 2 + (lane & 1);
      asm volatile("global_load_dword %0, %1, %2" : "=v"(pf) : "v"((line < PF_KB * 8 ? line : 0) * 128), "s"(pc0) : "memory");
    }
    const u32 l7 = lane & 7, l3 = lane >> 3, lq = lane >> 4;
    const u32 wsw = 16 * (l7 ^ ((lq & 1) << 2));
    u32 vx[4], vw[4];
#pragma unroll
    for (int i = 0; i < 4; ++i) {
      vx[i] = l3 * 1024 + 16 * (l7 ^ (((i & 1) << 2) | lq)) + i * 7168;
      vw[i] = l3 * 3072 + wsw + i * (8 * 3072 - 1024);
    }
    auto chunk = [&](u32 c) -> u32 {
      u32 k = c - 1 + rot; k = k >= 7 ? k - 7 : k; k = k >= 7 ? k - 7 : k;
      return c == 0 ? 0 : k + 1;
    };
    const u32 xs0 = (panel * 128 + t * 32) * 1024, ws0 = (t & 1) * (32 * 3072) + (1 + (t >> 1)) * 1024 + grp * 128;
    const u32 ldst = lds0 + t * 4096;
    u32 e0 = ldst, e1 = ldst + 16384, e2 = xs0 + chunk(0) * 128, e3 = ws0 + chunk(0) * (64 * 3072);
    PINS(e0); PINS(e1); PINS(e2); PINS(e3);
#pragma unroll
    for (int i = 0; i < 4; ++i) { PIN(vx[i]); PIN(vw[i]); }
    KWAIT();
    const __amdgpu_buffer_rsrc_t xr = __builtin_amdgcn_make_buffer_rsrc(KPTR(ka, 0), 0, 2048 * 512 * 2, SRD3);
    const __amdgpu_buffer_rsrc_t wr = __builtin_amdgcn_make_buffer_rsrc(KPTR(ka, 2), 0, 512 * 1536 * 2, SRD3);
    STAMP(10);
    dma4(e0, vx, xr, e2);
    dma4(e1, vw, wr, e3);
    {
      const __amdgpu_buffer_rsrc_t pr = __builtin_amdgcn_make_buffer_rsrc(KPTR(ka, 4), 0, 32 * 256 * 2, SRD3);
      dma1(lds0 + L_WPHI + (t & 1) * 1024, lane * 16, pr, grp * 2048 + (t & 1) * 1024);
    }
    STAMP(11);
    dma4(ldst + SLOT, vx, xr, xs0 + chunk(1) * 128);
    dma4(ldst + SLOT + 16384, vw, wr, ws0 + chunk(1) * (64 * 3072));
    STAMP(1);
    LANE_IDS();
    u32 key = *(const CAS u32*)((const char*)ws + WS_EPOCH + br * 256) + 1u;
    const u32 sw = (r >> 1) & 7;
    u32 xa[4];
#pragma unroll
    for (int s = 0; s < 4; ++s) { xa[s] = lds0 + (32 * t + r) * 128 + 16 * ((2 * s + h) ^ sw); PIN(xa[s]); }
    u32 fpa = lds0 + L_WPHI + (4 * h + q) * 32 + (8 * blk + 4 * (p >> 1)) * 2;
    PIN(fpa);
    PINS(key);
    u32x4 xq[32], fa[4];
    u32 touchq = 0, touch = 0;
#pragma unroll
    for (int c = 0; c < 8; ++c) {
      u32 d0, d1, s0, s1;
      if (c < 6) {
        d0 = ldst + ((c + 2) % 3) * SLOT; d1 = d0 + 16384;
        s0 = xs0 + chunk(c + 2) * 128; s1 = ws0 + chunk(c + 2) * (64 * 3072);
      } else {
        d0 = lds0 + (c == 6 ? L_WQ0 : L_WQ1) + t * 8192; d1 = d0 + 4096;
        s0 = chunk((c - 6) * 4 + t) * (64 * 3072) + grp * 128; s1 = s0 + 32 * 3072;
      }
      PINS(d0); PINS(d1); PINS(s0); PINS(s1);
      {
        const int extra = (WQT && (c + 1 == WQT || c == WQT)) + (WOT && (c + 1 == WOT || c == WOT));
        if (extra == 0) WAITV(8); else if (extra == 1) WAITV(9); else WAITV(10);
      }
      if (c == 0 && PF_KB) PIN(pf);
      BAR();
      if (c < 6) { dma4(d0, vx, xr, s0); dma4(d1, vw, wr, s1); }
      else { dma4(d0, vw, wr, s0); dma4(d1, vw, wr, s1); }
      if (c + 2 == WQT) asm volatile("buffer_load_dword %0, %1, %2, %3 offen" : "=v"(touchq) : "v"((r + 32 * t) * 3072), "s"(wr), "s"((idx & 3) * (128 * 3072) + grp * 128) : "memory");
      if (c + 2 == WOT) asm volatile("buffer_load_dword %0, %1, %2, %3 offen" : "=v"(touch) : "v"((r + 32 * t) * 1024), "s"(wor), "s"((idx & 3) * 131072 + cg * 128) : "memory");
#pragma unroll
      for (int s = 0; s < 4; ++s) xq[4 * c + s] = lds_r128(xa[s] + (c % 3) * SLOT);
      if (c == 0) {
#pragma unroll
        for (int hd = 0; hd < 4; ++hd) fa[hd] = lds_tr2(fpa + hd * 512, fpa + hd * 512 + 256);
      }
    }
    if (!WOT) asm volatile("buffer_load_dword %0, %1, %2, %3 offen" : "=v"(touch) : "v"((r + 32 * t) * 1024), "s"(wor), "s"((idx & 3) * 131072 + cg * 128) : "memory");
    if (t == 0) lds_w128(lds0 + L_V + (lane & 15) * VF + 1024, (u32x4){0x3F803F80u, 0x3F803F80u, 0x3F803F80u, 0x3F803F80u});
    {
      const u32 sg = (lane & 4) ? 0x80008000u : 0u;
#pragma unroll
      for (int hd = 0; hd < 4; ++hd) { PIN(fa[hd]); fa[hd] ^= (u32x4){sg, sg, sg, sg}; PIN(fa[hd]); }
    }
    u32 wa = lds0 + L_WQ1 + (8 * h + q) * 128 + 16 * ((2 * blk + (p >> 1)) ^ (((q >> 1) & 1) << 2)) + 8 * (p & 1);
    u32 wb = wa ^ 64;
    u32 kra = lds0 + L_KV + (16 * h + q) * 48 + (blk ? 32 : 8 * p);
    u32 o_off = WS_O + ((br * 64 + panel * 4 + t) * 8 + grp) * OREC + 256 + lane * 16;
    u32 c_off = WS_O + ((br * 64 + panel * 4 + t) * 8 + grp) * OREC + lane * 4;
    u32 woa = lds0 + L_WO + (8 * h + q) * 128 + 16 * ((2 * blk + (p & 1)) ^ (((q >> 1) & 1) << 2)) + 8 * (p >> 1);
    u32 wob = woa ^ 64;
    u32 oa = lds0 + L_O + t * OREC + 256 + lane * 16;
    u32 orow = ((panel2 * 128 + t * 32 + l3) * 1024 + br * 512 + cg * 64 + 8 * l7) * 2;
    PIN(wa); PIN(wb); PIN(kra); PIN(o_off); PIN(c_off); PIN(woa); PIN(wob); PIN(oa); PIN(orow);
    f32x16 aq[2];
    aq[0] = aq[1] = zero;
    STAMP(2);
    u32x4 fq[2][2][2];
    f32x16 pq[2];
    float mq[2];
    {
      u32x4 wf[4];
      auto ldw = [&](int n) {
        const int k = n & 31;
        const u32 o = (n < 32 ? wa : wb) + (k < 16 ? (L_WQ0 - L_WQ1) : 0) + (k & 15) * 2048;
        wf[n & 3] = lds_tr2(o, o + 512);
      };
      if (BARA) {
        u32 vo[4], wsa = ochunk(t) * 65536 + cg * 128;
#pragma unroll
        for (int i = 0; i < 4; ++i) { vo[i] = l3 * 1024 + wsw + i * 7168; PIN(vo[i]); }
        PINS(wsa);
        BAR();
#pragma unroll
        for (int i = 0; i < 2; ++i) dma4(lds0 + L_WA + t * 8192 + i * 4096, vo, wor, wsa + i * 32768);
        asm volatile("s_waitcnt vmcnt(8)" : "+v"(touch), "+v"(touchq)::"memory");
      } else asm volatile("s_waitcnt vmcnt(0)" : "+v"(touch), "+v"(touchq)::"memory");
      BAR();
      SCHED();
      STAMP(12);
      ldw(0); ldw(1); ldw(2);
      SCHED();
#pragma unroll
      for (int n = 0; n < 34; ++n) {
        ldw(n + 3);
        aq[n >> 5] = mfma(wf[n & 3], xq[n & 31], aq[n >> 5]);
        SGB(0x8, 1); SGB(0x100, 2);
        SCHED();
      }
#pragma unroll
      for (int hh = 0; hh < 2; ++hh) pq[hh] = mfma(fa[hh], pack8(aq[0], hh), zero);
      SCHED();
      STAMP(4);
#pragma unroll
      for (int n = 34; n < 64; ++n) {
        if (n + 3 < 64) ldw(n + 3);
        aq[1] = mfma(wf[n & 3], xq[n & 31], aq[1]);
      }
      mq[0] = negmax16(pq[0]); mq[1] = negmax16(pq[1]);
      softmax16x2s(pq[0], pq[1], mq, fq[0]);
      SGB(0x8, 1); SGB(0x100, 2); SGB(0x8, 1); SGB(0x100, 2);
#pragma unroll
      for (int n = 36; n < 64; ++n) { SGB(0x8, 1); SGB(0x100, 2); SGB(0x402, QMIX); }
      SCHED();
      STAMP(3);
#pragma unroll
      for (int hh = 0; hh < 2; ++hh) { pq[hh] = mfma(fa[2 + hh], pack8(aq[1], hh), zero); PIN(pq[hh]); }
      SCHED();
      if (BARA) WAITV(0);
      if (HINT && t == 0) __builtin_amdgcn_raw_buffer_store_b64((u32x2){key, now32()}, wsr, grp * 128 + 8, WS_HINT + (br * 16 + panel) * 1024, AUX_SC1);
      SCHED();
      mq[0] = negmax16(pq[0]); mq[1] = negmax16(pq[1]);
      PIN(mq[0]); PIN(mq[1]);
      SCHED();
    }
#pragma unroll
    for (int i = 0; i < 4; ++i) PIN(fq[0][i >> 1][i & 1]);
    if (QBS) softmax16x2s(pq[0], pq[1], mq, fq[1]); else softmax16x2(pq[0], pq[1], mq, fq[1]);
#pragma unroll
    for (int i = 0; i < 4; ++i) PIN(fq[1][i >> 1][i & 1]);
    STAMP(5);
    BAR();
    STAMP(6);
    u32x4 ob[2][4];
    {
      u32x4 a0[4], a1[4];
#pragma unroll
      for (int hd = 0; hd < 4; ++hd) {
        a0[hd] = lds_tr2(kra + hd * 1536, kra + hd * 1536 + 4 * 48);
        a1[hd] = lds_tr2(kra + hd * 1536 + 8 * 48, kra + hd * 1536 + 12 * 48);
      }
      SCHED();
      f32x16 y[4];
      u32 cs = 0;
      auto num = [&](int hd) {
        y[hd] = mfma(a0[hd], fq[hd >> 1][hd & 1][0], zero);
        y[hd] = mfma(a1[hd], fq[hd >> 1][hd & 1][1], y[hd]);
      };
      auto fin = [&](int hd) {
        const f32x16& yy = y[hd];
        const float rd = __builtin_amdgcn_rcpf(yy[8] + 1e-6f);
        u32 w[4];
#pragma unroll
        for (int e = 0; e < 4; ++e) { float t0 = yy[2 * e] * rd, t1 = yy[2 * e + 1] * rd; if (hd < 2) OPQ(t0); w[e] = pk2(t0, t1); }
        ob[0][hd] = (u32x4){w[0], w[1], w[2], w[3]};
        st128c(ob[0][hd], wsr, o_off + hd * 1024);
        cs = sum4(cs, ob[0][hd]);
      };
      num(0); num(1); PIN(y[0]); PIN(y[1]); SCHED();
      num(2); fin(0); SGB(0x8, 1); SGB(0x402, 6); SGB(0x8, 1); PIN(y[2]); SCHED();
      num(3); fin(1); SGB(0x8, 1); SGB(0x402, 6); SGB(0x8, 1); PIN(y[3]); SCHED();
      fin(2); fin(3);
      st32c(key - cs, wsr, c_off);
    }
    SCHED();
    BARE();
    STAMP(7);
    u32x4 wo[16][2];
    auto ldwo = [&](int n) {
      const u32 o = (n < 16 ? L_WA : L_WB) + (n & 15) * 2048;
      wo[n & 15][0] = lds_tr2(woa + o, woa + o + 512);
      wo[n & 15][1] = lds_tr2(wob + o, wob + o + 512);
    };
#pragma unroll
    for (int n = 0; n < 16; ++n) ldwo(n);
    f32x16 c0 = zero, c1 = zero;
#pragma unroll
    for (int s = 0; s < 4; ++s) { c0 = mfma(wo[s][0], ob[0][s], c0); c1 = mfma(wo[s][1], ob[0][s], c1); }
#pragma unroll
    for (int k = 4; k < 16; ++k) { PIN(wo[k][0]); PIN(wo[k][1]); }
    PIN(c0); PIN(c1);
    const __amdgpu_buffer_rsrc_t outr = __builtin_amdgcn_make_buffer_rsrc(KPTR(kb, 0), 0, 2048 * 1024 * 2, SRD3);
    u32 oc = oa - 256 - lane * 12, l4 = lane * 4, l16 = lane * 16, oa4 = oa + 4 * (4 * OREC), oc4 = oc + 4 * (4 * OREC);
    u32 sw1 = lds0 + L_WA + t * 4096 + r * 128 + 16 * (h ^ (r & 7)), sr1 = lds0 + L_WA + t * 4096 + l3 * 128 + 16 * (l7 ^ l3);
    u32 sw2 = sw1 ^ 32, sw3 = sw1 ^ 64, sw4 = sw1 ^ 96, or1 = orow + 16384, or2 = orow + 32768, or3 = orow + 49152;
    PIN(oc); PIN(l4); PIN(l16); PIN(sw1); PIN(sr1); PIN(oa4); PIN(oc4); PIN(sw2); PIN(sw3); PIN(sw4); PIN(or1); PIN(or2); PIN(or3);
    STAMP(8);
    u32 nretry = 0;
    u32 chk[2];
    auto oread = [&](int j) {
      const u32 da = j % 5 == 4 ? oa4 : oa + (j % 5) * (4 * OREC), dc = j % 5 == 4 ? oc4 : oc + (j % 5) * (4 * OREC);
#pragma unroll
      for (int s = 0; s < 4; ++s) ob[j & 1][s] = lds_r128(da + s * 1024);
      chk[j & 1] = *(LAS u32*)(size_t)dc;
    };
    auto obad = [&](int j) -> bool {
      const u32 c = sum4(sum4(chk[j & 1], ob[j & 1][0]), ob[j & 1][1]) + sum4(sum4(0u, ob[j & 1][2]), ob[j & 1][3]);
      return __builtin_amdgcn_ballot_w64(c != key) != 0;
    };
#pragma unroll
    for (int j = 1; j < 8; ++j) {
      if (j != 2 || FIRST1) { BARE(); SCHED(); }
      oread(j);
      if (j >= 2) {
#pragma unroll
        for (int s = 0; s < 4; ++s) {
          const int k = 4 * (j - 1) + s;
          c0 = mfma(wo[k & 15][0], ob[(j - 1) & 1][s], c0);
          c1 = mfma(wo[k & 15][1], ob[(j - 1) & 1][s], c1);
        }
        if (j <= 5) {
#pragma unroll
          for (int s = 0; s < 4; ++s) ldwo(16 + 4 * (j - 2) + s);
        }
      }
      const bool bad = obad(j);
      if (j >= 2 && j <= 5) {
        SGB(0x8, 1); SGB(0x100, 3); SGB(0x8, 1); SGB(0x100, 4); SGB(0x8, 1); SGB(0x100, 4); SGB(0x8, 1); SGB(0x100, 4);
        SGB(0x8, 1); SGB(0x2, 4); SGB(0x8, 1); SGB(0x2, 5); SGB(0x8, 1); SGB(0x100, 4); SGB(0x8, 1); SGB(0x100, 2);
      } else if (j > 5) {
        SGB(0x8, 1); SGB(0x100, 3); SGB(0x8, 1); SGB(0x100, 2); SGB(0x8, 3); SGB(0x2, 4); SGB(0x8, 1); SGB(0x2, 5); SGB(0x8, 2);
      }
      SCHED();
      if (__builtin_expect(bad, 0)) {
#pragma unroll 1
        for (u32 spin = 0; spin < SPIN_CAP; ++spin) {
          dma_orec(lds0 + L_O + t * OREC + (j % 5) * (4 * OREC), l4, l16, wsr, WS_O + ((br * 64 + panel2 * 4 + t) * 8 + ochunk(j)) * OREC);
#pragma unroll 1
          for (u32 jj = j + 1; jj <= (j + AHEAD < 7 ? j + AHEAD : 7); ++jj) {
            const u32 sl = (jj >= 5 ? jj - 5 : jj) * (4 * OREC);
            u32 c = *(LAS u32*)(size_t)(oc + sl);
#pragma unroll
            for (int s = 0; s < 4; ++s) c = sum4(c, lds_r128(oa + sl + s * 1024));
            if (__builtin_amdgcn_ballot_w64(c != key) != 0)
              dma_orec(lds0 + L_O + t * OREC + sl, l4, l16, wsr, WS_O + ((br * 64 + panel2 * 4 + t) * 8 + ochunk(jj)) * OREC);
          }
          WAITV(0);
          oread(j);
          ++nretry;
          if (!obad(j)) break;
        }
      }
    }
#pragma unroll
    for (int s = 0; s < 4; ++s) c0 = mfma(wo[12 + s][0], ob[1][s], c0);
    PIN(c0);
    SCHED();
#pragma unroll
    for (int s = 0; s < 4; ++s) c1 = mfma(wo[12 + s][1], ob[1][s], c1);
    lds_w128(sw1, pack8(c0, 0));
    lds_w128(sw2, pack8(c0, 1));
    SGB(0x8, 2); SGB(0x2, 4); SGB(0x200, 1); SGB(0x8, 1); SGB(0x2, 4); SGB(0x200, 1); SGB(0x8, 1);
    SCHED();
    STAMP(9);
    lds_w128(sw3, pack8(c1, 0));
    lds_w128(sw4, pack8(c1, 1));
    asm volatile("" ::: "memory");
    {
      u32x4 v[4];
#pragma unroll
      for (int i = 0; i < 4; ++i) v[i] = lds_r128(sr1 + i * 1024);
      SCHED();
      __builtin_amdgcn_raw_buffer_store_b128(v[0], outr, orow, 0, OUT_AUX);
      __builtin_amdgcn_raw_buffer_store_b128(v[1], outr, or1, 0, OUT_AUX);
      __builtin_amdgcn_raw_buffer_store_b128(v[2], outr, or2, 0, OUT_AUX);
      __builtin_amdgcn_raw_buffer_store_b128(v[3], outr, or3, 0, OUT_AUX);
    }
    if (t == 0 && panel2 == 0 && cg == 0 && lane == 0) st32c(key, wsr, WS_EPOCH + br * 256);
    if (PROBE_N == 13) WAITV(0);
    STAMP(13);
    STAMP_OUT();
  }
}
}

extern "C" void kernel_launch(void* const* d_in, const int* in_sizes, int n_in,
                              void* d_out, int out_size, void* d_ws, size_t ws_size,
                              hipStream_t stream) {
  hh::hedgehog<<<256, 512, 0, stream>>>((const u16*)d_in[0], (const u16*)d_in[1], (const u16*)d_in[2], (const u16*)d_in[3],
                                        (const u16*)d_in[4], (const u16*)d_in[5], (const u16*)d_in[6], (const u16*)d_in[7],
                                        (u16*)d_out, (u32*)d_ws);
}
```

```cpp
#define PROBE_N 10
#define PROBE_ROLE 0
#include <hip/hip_runtime.h>
#include <stdint.h>

typedef unsigned short u16;
typedef unsigned u32;
typedef short bf16x8 __attribute__((ext_vector_type(8)));
typedef short s16x4 __attribute__((ext_vector_type(4)));
typedef float f32x16 __attribute__((ext_vector_type(16)));
typedef float f32x4 __attribute__((ext_vector_type(4)));
typedef float f32x2 __attribute__((ext_vector_type(2)));
typedef unsigned u32x8 __attribute__((ext_vector_type(8)));
typedef unsigned u32x4 __attribute__((ext_vector_type(4)));
typedef unsigned u32x2 __attribute__((ext_vector_type(2)));
typedef __bf16 bf16x2 __attribute__((ext_vector_type(2)));
#define DI __device__ __forceinline__
#define LAS __attribute__((address_space(3)))
#define CAS __attribute__((address_space(4)))
#define BC(T, x) __builtin_bit_cast(T, x)

#define DELAY1 0
#define DELAY3 3
#define ROTMODE 2
#define CODE_PF 27648
#define AHEAD 2
#define WSOFF 0
#define GQ 0
#define GC 0
#define WFIRST 0
#define FOLLOW 0
#define TIMER 2
#define LTO 1
#define NTX 0
#define NTW 0
#define H3 0
#define LTQ 0

namespace hh {
constexpr int AUX_SC1 = 16;
constexpr u32 SRD3 = 0x00020000u;
constexpr u32 WS_EPOCH = 0;
constexpr u32 REC = 1280;
constexpr u32 WS_PART = 4096;
constexpr u32 OREC = 4352;
constexpr u32 WS_O = WS_PART + 2 * 32 * 16 * REC;
constexpr u32 WS_END = WS_O + 2 * 64 * 8 * OREC;
constexpr u32 WS_STAMP = 8u << 20;
constexpr u32 SLOT = 32768;
constexpr u32 L_WQ0 = 98304;
constexpr u32 L_WQ1 = 65536;
constexpr u32 L_WPHI = 163840 - 4096;
constexpr u32 L_PHI = 0;
constexpr u32 VF = 1040;
constexpr u32 L_V = 131072;
constexpr u32 L_KV = L_V + 16 * VF;
constexpr u32 L_WO = 0;
constexpr u32 L_O = 65536;
constexpr u32 SPIN_CAP = 1u << 16;

DI u32 pk2(float a, float b) { return BC(u32, __builtin_convertvector((f32x2){a, b}, bf16x2)); }
DI u32x4 pack8(const f32x16& x, int s) {
  return (u32x4){pk2(x[8 * s], x[8 * s + 1]), pk2(x[8 * s + 2], x[8 * s + 3]), pk2(x[8 * s + 4], x[8 * s + 5]), pk2(x[8 * s + 6], x[8 * s + 7])};
}
DI f32x16 mfma(u32x4 a, u32x4 b, f32x16 c) { return __builtin_amdgcn_mfma_f32_32x32x16_bf16(BC(bf16x8, a), BC(bf16x8, b), c, 0, 0, 0); }
DI f32x4 mfma16(u32x4 a, u32x4 b, f32x4 c) { return __builtin_amdgcn_mfma_f32_16x16x32_bf16(BC(bf16x8, a), BC(bf16x8, b), c, 0, 0, 0); }
DI u32x4 lds_r128(u32 a) { return *(LAS u32x4*)(size_t)a; }
DI void lds_w128(u32 a, u32x4 v) { *(LAS u32x4*)(size_t)a = v; }
DI u32x4 lds_tr2(u32 a0, u32 a1) {
  s16x4 lo = __builtin_amdgcn_ds_read_tr16_b64_v4i16((LAS s16x4*)(size_t)a0);
  s16x4 hi = __builtin_amdgcn_ds_read_tr16_b64_v4i16((LAS s16x4*)(size_t)a1);
  return BC(u32x4, __builtin_shufflevector(lo, hi, 0, 1, 2, 3, 4, 5, 6, 7));
}
DI void dma1(u32 lds, u32 voff, __amdgpu_buffer_rsrc_t srd, u32 soff) {
  asm volatile("s_mov_b32 m0, %0\n\ts_nop 0\n\tbuffer_load_dwordx4 %1, %2, %3 offen lds" ::"s"(lds), "v"(voff), "s"(srd), "s"(soff) : "memory");
}
DI void dma4(u32 lds, const u32 (&v)[4], __amdgpu_buffer_rsrc_t srd, u32 soff) {
  asm volatile("s_mov_b32 m0, %0\n\ts_nop 0\n\t"
               "buffer_load_dwordx4 %1, %5, %6 offen lds\n\t"
               "buffer_load_dwordx4 %2, %5, %6 offen offset:1024 lds\n\t"
               "buffer_load_dwordx4 %3, %5, %6 offen offset:2048 lds\n\t"
               "buffer_load_dwordx4 %4, %5, %6 offen offset:3072 lds" ::"s"(lds), "v"(v[0]), "v"(v[1]), "v"(v[2]), "v"(v[3]), "s"(srd), "s"(soff) : "memory");
}
#define DMA4P(name, POL)                                                                       \
  DI void name(u32 lds, const u32 (&v)[4], __amdgpu_buffer_rsrc_t srd, u32 soff) {              \
    asm volatile("s_mov_b32 m0, %0\n\ts_nop 0\n\t"                                             \
                 "buffer_load_dwordx4 %1, %5, %6 offen " POL " lds\n\t"                        \
                 "buffer_load_dwordx4 %2, %5, %6 offen offset:1024 " POL " lds\n\t"            \
                 "buffer_load_dwordx4 %3, %5, %6 offen offset:2048 " POL " lds\n\t"            \
                 "buffer_load_dwordx4 %4, %5, %6 offen offset:3072 " POL " lds" ::"s"(lds), "v"(v[0]), "v"(v[1]), "v"(v[2]), "v"(v[3]), "s"(srd), "s"(soff) : "memory"); \
  }
DMA4P(dma4nt, "nt")
DI void dma_orec(u32 lds, u32 l4, u32 l16, __amdgpu_buffer_rsrc_t srd, u32 soff) {
  asm volatile("s_mov_b32 m0, %0\n\ts_nop 0\n\t"
               "buffer_load_dwordx4 %2, %3, %4 offen offset:256 sc1 lds\n\t"
               "buffer_load_dwordx4 %2, %3, %4 offen offset:1280 sc1 lds\n\t"
               "buffer_load_dwordx4 %2, %3, %4 offen offset:2304 sc1 lds\n\t"
               "buffer_load_dwordx4 %2, %3, %4 offen offset:3328 sc1 lds\n\t"
               "buffer_load_dword %1, %3, %4 offen sc1 lds" ::"s"(lds), "v"(l4), "v"(l16), "s"(srd), "s"(soff) : "memory");
}
#define TOUCH(d, voff, srd, soff) asm volatile("buffer_load_dword %0, %1, %2, %3 offen" : "=v"(d) : "v"(voff), "s"(srd), "s"(soff) : "memory")
DI void st128c(u32x4 v, __amdgpu_buffer_rsrc_t srd, u32 off) { __builtin_amdgcn_raw_buffer_store_b128(v, srd, off, 0, AUX_SC1); }
DI void st32c(u32 v, __amdgpu_buffer_rsrc_t srd, u32 off) { __builtin_amdgcn_raw_buffer_store_b32(v, srd, off, 0, AUX_SC1); }
#define BAR() asm volatile("s_waitcnt lgkmcnt(0)\n\ts_barrier" ::: "memory")
#define BARE() asm volatile("s_barrier" ::: "memory")
#define OPQ(x) asm("" : "+v"(x))
#define PIN(x) asm volatile("" : "+v"(x))
#define PINS(x) asm volatile("" : "+s"(x))
#define WAITV(n) asm volatile("s_waitcnt vmcnt(" #n ")" ::: "memory")
#define SCHED() __builtin_amdgcn_sched_barrier(0)
#define SGB(m, n) __builtin_amdgcn_sched_group_barrier(m, n, 0)
DI u32 sum4(u32 c, u32x4 v) { return (c + v.x + v.y) + v.z + v.w; }
DI u32 pair_sum(u32 s) { auto t = __builtin_amdgcn_permlane32_swap(s, s, false, false); return t[0] + t[1]; }

constexpr float LOG2E = 1.44269504088896340736f;
DI float max3f(float a, float b, float c) { return __builtin_fmaxf(__builtin_fmaxf(a, b), c); }
DI float negmax16(const f32x16& x) {
  const float a = max3f(x[0], x[1], x[2]), b = max3f(x[3], x[4], x[5]), c = max3f(x[6], x[7], x[8]);
  const float d = max3f(x[9], x[10], x[11]), e = max3f(x[12], x[13], x[14]);
  return -max3f(max3f(a, b, x[15]), max3f(c, d, e), a) * LOG2E;
}
DI void sm_exp(const f32x16& p, const f32x16& q, const float (&mc)[2], f32x2 (&e)[2][8]) {
#pragma unroll
  for (int i = 0; i < 8; ++i) {
    const f32x2 t = __builtin_elementwise_fma((f32x2){p[2 * i], p[2 * i + 1]}, (f32x2){LOG2E, LOG2E}, (f32x2){mc[0], mc[0]});
    const f32x2 u = __builtin_elementwise_fma((f32x2){q[2 * i], q[2 * i + 1]}, (f32x2){LOG2E, LOG2E}, (f32x2){mc[1], mc[1]});
    e[0][i] = (f32x2){__builtin_amdgcn_exp2f(t.x), __builtin_amdgcn_exp2f(t.y)};
    e[1][i] = (f32x2){__builtin_amdgcn_exp2f(u.x), __builtin_amdgcn_exp2f(u.y)};
  }
  SGB(0x2, 4); SGB(0x400, 8); SGB(0x2, 4); SGB(0x400, 8); SGB(0x2, 6); SGB(0x400, 8); SGB(0x2, 8); SGB(0x400, 8);
}
DI void sm_exp_s(const f32x16& p, const f32x16& q, const float (&mc)[2], f32x2 (&e)[2][8]) {
#pragma unroll
  for (int i = 0; i < 16; ++i) {
    e[0][i >> 1][i & 1] = __builtin_amdgcn_exp2f(__builtin_fmaf(p[i], LOG2E, mc[0]));
    e[1][i >> 1][i & 1] = __builtin_amdgcn_exp2f(__builtin_fmaf(q[i], LOG2E, mc[1]));
  }
}
DI void sm_finish(const f32x2 (&e)[2][8], u32x4 (&o)[2][2]) {
  float r[2];
#pragma unroll
  for (int u = 0; u < 2; ++u) {
    const f32x2 s = ((e[u][0] + e[u][1]) + (e[u][2] + e[u][3])) + ((e[u][4] + e[u][5]) + (e[u][6] + e[u][7]));
    r[u] = __builtin_amdgcn_rcpf(s.x + s.y);
  }
#pragma unroll
  for (int u = 0; u < 2; ++u) {
    u32 w[8];
#pragma unroll
    for (int i = 0; i < 8; ++i) { const f32x2 t = e[u][i] * (f32x2){r[u], r[u]}; w[i] = pk2(t.x, t.y); }
    o[u][0] = (u32x4){w[0], w[1], w[2], w[3]};
    o[u][1] = (u32x4){w[4], w[5], w[6], w[7]};
  }
}
DI void softmax16x2(const f32x16& p, const f32x16& q, const float (&mc)[2], u32x4 (&o)[2][2]) {
  f32x2 e[2][8];
  sm_exp(p, q, mc, e);
  sm_finish(e, o);
}

#define STAMP(i) do { if ((i) == PROBE_N) { SCHED(); asm volatile("s_memrealtime %0\n\ts_waitcnt lgkmcnt(0)" : "=s"(pt1) :: "memory"); SCHED(); } } while (0)
#define STAMP_OUT() do { if (PROBE_N && (wave >> 2) == PROBE_ROLE) { unsigned long long d_ = pt1 - pt0; if (d_ > 100000) d_ = 100000; unsigned long long e_ = pt0 + 3000 + 8 * d_; \
  for (int it_ = 0; it_ < 1000000; ++it_) { unsigned long long t_; asm volatile("s_memrealtime %0\n\ts_waitcnt lgkmcnt(0)" : "=s"(t_) :: "memory"); if (t_ >= e_) break; } } } while (0)

__global__ __launch_bounds__(512, 2) void hedgehog(const u16* xA, const u16* wA, const u16* pA, const u16* oA,
                                                   const u16* xB, const u16* wB, const u16* pB, const u16* oB,
                                                   u16* out_, u32* ws_) {
  __shared__ __attribute__((aligned(16))) char smem[163840];
  const u32 lds0 = (u32)(size_t)(LAS char*)smem;
  unsigned long long pt0 = __builtin_amdgcn_s_memrealtime(), pt1 = pt0;
  const int bid = blockIdx.x, xcd = bid & 7, idx = bid >> 3;
  const int br = xcd >> 2;
#define KLOAD(tail)                                                                                       \
  u32x8 ka;                                                                                               \
  u32x4 kb;                                                                                               \
  asm volatile("s_load_dwordx8 %0, %2, %3\n\ts_load_dwordx4 %1, %2, 0x40" tail : "=&s"(ka), "=&s"(kb)     \
               : "s"(__builtin_amdgcn_kernarg_segment_ptr()), "s"(br * 32))
#define KWAIT() asm volatile("s_waitcnt lgkmcnt(0)" : "+s"(ka), "+s"(kb))
#define KPTR(v, i) ((void*)(((unsigned long long)(v)[(i) + 1] << 32) | (v)[i]))
  unsigned long long pcs;
  asm volatile("s_getpc_b64 %0" : "=s"(pcs));
  pcs &= ~255ull;
  const int lane = threadIdx.x & 63;
  const int wave = __builtin_amdgcn_readfirstlane(threadIdx.x >> 6);
  const int panel = (xcd & 3) * 4 + (idx & 3), grp = idx >> 2, gq = (grp + GQ) & 7, cg = (grp + GC) & 7;
  auto ochunk = [&](u32 j) -> u32 {
    const int v = (int)j + (((int)j - 1 - gq) >> 31), z = ((int)j - 1) >> 31;
    return (u32)__builtin_amdgcn_readfirstlane((v & ~z) | (gq & z));
  };
  f32x16 zero;
#pragma unroll
  for (int i = 0; i < 16; ++i) zero[i] = 0.f;
#define LANE_IDS()                                                                            \
  u32 lane_ = lane;                                                                           \
  PIN(lane_);                                                                                 \
  const u32 r = lane_ & 31, h = lane_ >> 5;                                                   \
  const u32 blk = (lane_ >> 4) & 1, q = (lane_ & 15) >> 2, p = lane_ & 3;                     \
  u32* ws = (u32*)KPTR(kb, 2);                                                                \
  const __amdgpu_buffer_rsrc_t wsr = __builtin_amdgcn_make_buffer_rsrc(ws, 0, WS_END, SRD3);  \
  const __amdgpu_buffer_rsrc_t wor = __builtin_amdgcn_make_buffer_rsrc(KPTR(ka, 6), 0, 512 * 512 * 2, SRD3)

  if (wave >= 4) {
    const int cw = wave - 4;
    const int rh = cw >> 1, hp = cw & 1;
    KLOAD("\n\ts_waitcnt lgkmcnt(0)");
    u32 sp0, sp1, sp2, sp3;
    {
      u32 off = idx * 1024 + cw * 256;
      off = off < CODE_PF ? off : 0;
      asm volatile("s_load_dword %0, %4, %5 offset:0\n\ts_load_dword %1, %4, %5 offset:64\n\ts_load_dword %2, %4, %5 offset:128\n\ts_load_dword %3, %4, %5 offset:192"
                   : "=&s"(sp0), "=&s"(sp1), "=&s"(sp2), "=&s"(sp3) : "s"(pcs), "s"(off));
    }
    LANE_IDS();
    u32 key = *(const CAS u32*)((const char*)ws + WS_EPOCH + br * 256) + 1u;
    u32 xa[2], xb[2], wa[2], wb2[2];
    {
      const u32 r16 = lane_ & 15, g = lane_ >> 4;
#pragma unroll
      for (int s = 0; s < 2; ++s) {
        xa[s] = lds0 + (64 * rh + r16) * 128 + 16 * ((4 * s + g) ^ (r16 >> 1));
        wa[s] = lds0 + 16384 + (8 * g + q) * 128 + 16 * ((4 * hp + 2 * s + (p >> 1)) ^ ((((q >> 1) & 1) << 2) | (blk << 1))) + 8 * (p & 1);
        xb[s] = xa[s] + 2 * SLOT; wb2[s] = wa[s] + 2 * SLOT;
        PIN(xa[s]); PIN(xb[s]); PIN(wa[s]); PIN(wb2[s]);
      }
    }
    u32 fpa = lds0 + L_WPHI + hp * 1024 + (8 * h + q) * 32 + (8 * blk + 4 * (p >> 1)) * 2;
    PIN(fpa);
    PINS(key);
    f32x4 ak[4][2], av[4][2];
#pragma unroll
    for (int i = 0; i < 8; ++i) ak[i >> 1][i & 1] = av[i >> 1][i & 1] = (f32x4){0.f, 0.f, 0.f, 0.f};
    u32 vra, pra, vwa, pw0, pw1, pub16, pub4;
    u32x4 xf[2][4], wk[2][2], wv[2][2];
    auto ldx = [&](int c, int s) {
      const u32 x = c % 3 == 2 ? xb[s] : xa[s] + (c % 3) * SLOT;
#pragma unroll
      for (int i = 0; i < 4; ++i) xf[s][i] = lds_r128(x + i * 2048);
    };
    auto ldw = [&](int c, int s, int m) {
#pragma unroll
      for (int n = 0; n < 2; ++n) {
        const u32 w = (c % 3 == 2 ? wb2[n] : wa[n] + (c % 3) * SLOT) + s * 4096 + m * 8192;
        (m ? wv : wk)[s][n] = lds_tr2(w, w + 512);
      }
    };
    auto mk = [&](int s) {
#pragma unroll
      for (int i = 0; i < 8; ++i) ak[i >> 1][i & 1] = mfma16(wk[s][i & 1], xf[s][i >> 1], ak[i >> 1][i & 1]);
    };
    auto mv = [&](int s) {
#pragma unroll
      for (int i = 0; i < 8; ++i) av[i >> 1][i & 1] = mfma16(xf[s][i >> 1], wv[s][i & 1], av[i >> 1][i & 1]);
    };
    auto mix = [&](int n) {
#pragma unroll
      for (int i = 0; i < 8; ++i) {
        SGB(0x8, 1);
        if (n == 12) { if (i < 4) SGB(0x100, 2); else SGB(0x100, 1); }
        else if (n == 8) SGB(0x100, 1);
        else if (n == 4) { if (i < 4) SGB(0x100, 1); }
        else if (n < 0) SGB(0x6, 2);
      }
      SCHED();
    };
    BAR();
    asm volatile("" ::"s"(sp0), "s"(sp1), "s"(sp2), "s"(sp3));
    STAMP(1);
    ldw(0, 0, 0); ldx(0, 0); ldw(0, 0, 1);
    u32x4 fa[2];
#pragma unroll
    for (int hh = 0; hh < 2; ++hh) fa[hh] = lds_tr2(fpa + hh * 512, fpa + hh * 512 + 128);
    SCHED();
#pragma unroll
    for (int c = 0; c < 7; ++c) {
      ldw(c, 1, 0); ldx(c, 1); mk(0); mix(8);
      ldw(c, 1, 1); mv(0); mix(4);
      u32 ln = lane;
      PIN(ln);
      const u32 r_ = ln & 31, h_ = ln >> 5, q_ = (ln & 15) >> 2;
      if (c == 1) {
        const u32 sg = (ln & 4) ? 0x80008000u : 0u;
#pragma unroll
        for (int hh = 0; hh < 2; ++hh) { OPQ(fa[hh]); fa[hh] ^= (u32x4){sg, sg, sg, sg}; }
      }
      if (c == 2) vra = lds0 + L_V + (cw >> 1) * (2 * VF) + (r_ < 16 ? (ln + 16 * (cw & 1)) * 16 : 1024);
      if (c == 3) pra = lds0 + L_PHI + cw * 8192 + (4 * h_ + q_) * 64 + 16 * ((((ln >> 3) & 2) + ((ln >> 1) & 1)) ^ ((2 * h_ + (q_ >> 1)) & 3)) + 8 * (ln & 1);
      if (c == 4) {
        const u32 rec = WS_PART + ((br * 32 + grp * 4 + cw) * 16 + panel) * REC;
        pub16 = rec + ln * 16; pub4 = rec + 1024 + ln * 4;
      }
      if (c == 5) { pw0 = lds0 + L_PHI + (2 * hp) * 8192 + (64 * rh + r_) * 64 + 16 * ((2 * h_) ^ ((r_ >> 1) & 3)); pw1 = pw0 ^ 16; }
      if (c == 6) vwa = lds0 + L_V + (8 * rh + 2 * hp) * VF + ((ln & 16) * 2 + (ln & 15)) * 16 + h_ * 8;
      mk(1); mix(c >= 1 ? -1 : 0);
      if (c == 1) { PIN(fa[0]); PIN(fa[1]); }
      if (c == 2) PIN(vra);
      if (c == 3) PIN(pra);
      if (c == 4) { PIN(pub16); PIN(pub4); }
      if (c == 5) { PIN(pw0); PIN(pw1); }
      if (c == 6) PIN(vwa);
      BAR();
      ldw(c + 1, 0, 0); ldx(c + 1, 0); ldw(c + 1, 0, 1);
      mv(1); mix(12);
    }
    ldw(7, 1, 0); ldx(7, 1); ldw(7, 1, 1); mk(0); mix(12);
    mk(1); mix(0);
    __builtin_amdgcn_s_setprio(2);
    STAMP(2);
    f32x16 pk[2][2];
    {
      u32x4 kf[2][2];
      auto kpack = [&](int i) {
#pragma unroll
        for (int hh = 0; hh < 2; ++hh) {
          const f32x4 &t0 = ak[2 * i][hh], &t1 = ak[2 * i + 1][hh];
          const auto lo = __builtin_amdgcn_permlane16_swap(pk2(t0[0], t0[1]), pk2(t1[0], t1[1]), false, false);
          const auto hi = __builtin_amdgcn_permlane16_swap(pk2(t0[2], t0[3]), pk2(t1[2], t1[3]), false, false);
          kf[i][hh] = (u32x4){lo[0], hi[0], lo[1], hi[1]};
        }
      };
      mv(0); kpack(0);
      SGB(0x8, 2);
#pragma unroll
      for (int i = 0; i < 6; ++i) { SGB(0x2, 2); SGB(0x8, 1); }
      SCHED();
      pk[0][0] = mfma(fa[0], kf[0][0], zero);
      pk[0][1] = mfma(fa[1], kf[0][1], zero);
      kpack(1);
      SGB(0x8, 1); SGB(0x2, 6); SGB(0x8, 1); SGB(0x2, 6);
      PIN(pk[0][0]); PIN(pk[0][1]); PIN(kf[1][0]); PIN(kf[1][1]);
      SCHED();
      mv(1);
      pk[1][0] = mfma(fa[0], kf[1][0], zero);
      pk[1][1] = mfma(fa[1], kf[1][1], zero);
    }
    float m0[2], m1[2];
    m0[0] = negmax16(pk[0][0]); m0[1] = negmax16(pk[0][1]);
    SGB(0x8, 2);
#pragma unroll
    for (int i = 0; i < 6; ++i) { SGB(0x2, 2); SGB(0x8, 1); }
    SGB(0x8, 1); SGB(0x2, 6); SGB(0x8, 1);
#pragma unroll
    for (int i = 0; i < 8; ++i) PIN(av[i >> 1][i & 1]);
    PIN(pk[1][0]); PIN(pk[1][1]); PIN(m0[0]); PIN(m0[1]);
    SCHED();
#pragma unroll
    for (int i = 0; i < 8; ++i) {
      const f32x4& t = av[i >> 1][i & 1];
      *(LAS u32x2*)(size_t)(vwa + (4 * (i >> 2) + ((i >> 1) & 1)) * VF + (i & 1) * 256) = (u32x2){pk2(t[0], t[1]), pk2(t[2], t[3])};
    }
    SCHED();
    {
      u32x4 o[2][2];
      softmax16x2(pk[0][0], pk[0][1], m0, o);
      lds_w128(pw0, o[0][0]); lds_w128(pw1, o[0][1]);
      lds_w128(pw0 + 8192, o[1][0]); lds_w128(pw1 + 8192, o[1][1]);
      SCHED();
      m1[0] = negmax16(pk[1][0]); m1[1] = negmax16(pk[1][1]);
      PIN(m1[0]); PIN(m1[1]);
      SCHED();
      softmax16x2(pk[1][0], pk[1][1], m1, o);
      lds_w128(pw0 + 2048, o[0][0]); lds_w128(pw1 + 2048, o[0][1]);
      lds_w128(pw0 + 2048 + 8192, o[1][0]); lds_w128(pw1 + 2048 + 8192, o[1][1]);
      SGB(0x402, 26); SGB(0x200, 1); SGB(0x402, 8); SGB(0x200, 1); SGB(0x402, 8); SGB(0x200, 1); SGB(0x402, 8); SGB(0x200, 1);
      SCHED();
      BARE();
      BAR();
      SCHED();
    }
    STAMP(3);
    f32x16 kv = zero;
    {
      u32x4 a[8], b[8];
#pragma unroll
      for (int u = 0; u < 8; ++u) {
        a[u] = lds_r128(vra + ((u >> 1) * 4 + (u & 1)) * VF);
        b[u] = lds_tr2(pra + 16 * u * 64, pra + (16 * u + 8) * 64);
      }
#pragma unroll
      for (int u = 0; u < 8; ++u) kv = mfma(a[u], b[u], kv);
      SGB(0x100, 12); SGB(0x8, 1); SGB(0x100, 6); SGB(0x8, 1); SGB(0x100, 6); SGB(0x8, 6);
    }
    {
      const u32x4 w = pack8(kv, 0);
      st128c(w, wsr, pub16);
      const u32 zb = __float_as_uint(kv[8]);
      const u32 chk = key - pair_sum(sum4(h ? 0u : zb, w));
      st32c(h ? chk : zb, wsr, pub4);
    }
    SCHED();
    STAMP(4);
    u32 l16, l4, kwa, kza, gso[4], vo[4], wso;
    u32x4 sel;
    {
      u32 ln = lane;
      PIN(ln);
#pragma unroll
      for (int i = 0; i < 4; ++i) vo[i] = (ln >> 3) * 1024 + 16 * ((ln & 7) ^ (((ln >> 4) & 1) << 2)) + i * 7168;
#pragma unroll
      for (int i = 0; i < 2; ++i) dma4(lds0 + L_WO + cw * 8192 + i * 4096, vo, wor, ochunk(cw) * 65536 + i * 32768 + cg * 128);
      wso = ochunk(4 + cw) * 65536 + cg * 128;
      if (TIMER == 1) {
#pragma unroll
        for (int i = 0; i < 2; ++i) dma4(lds0 + L_WO + 32768 + cw * 8192 + i * 4096, vo, wor, wso + i * 32768);
      }
      const u32 r_ = ln & 31, h_ = ln >> 5;
      l16 = ln * 16; l4 = ln * 4;
      kwa = lds0 + L_KV + cw * 1536 + r_ * 48 + h_ * 16; kza = lds0 + L_KV + cw * 1536 + r_ * 48 + 32 + h_ * 8;
      const bool hit = r_ < 16 && ((r_ >> 2) & 1) == h_;
      const u32 j = (r_ & 3) + 4 * (r_ >> 3);
#pragma unroll
      for (int e = 0; e < 4; ++e) sel[e] = (hit && (j >> 1) == e) ? ((j & 1) ? 0x3F800000u : 0x00003F80u) : 0u;
#pragma unroll
      for (int i = 0; i < 4; ++i) { gso[i] = WS_PART + ((br * 32 + gq * 4 + cw) * 16 + ((i + idx) & 3) * 4) * REC; PINS(gso[i]); }
      PIN(l16); PIN(l4); PIN(kwa); PIN(kza); PIN(sel); PINS(wso);
    }
    if (DELAY1) __builtin_amdgcn_s_sleep(DELAY1);
    STAMP(10);
    f32x16 gs[4];
    float z[4];
    {
      u32x4 g[16];
      u32 gz[16];
      auto gload = [&](int gi) {
#pragma unroll
        for (int i = 0; i < 4; ++i) {
          g[4 * gi + i] = __builtin_amdgcn_raw_buffer_load_b128(wsr, l16 + i * REC, gso[gi], AUX_SC1);
          gz[4 * gi + i] = __builtin_amdgcn_raw_buffer_load_b32(wsr, l4 + (1024 + i * REC - 1024 * (i == 3)), gso[gi] + 1024 * (i == 3), AUX_SC1);
          SCHED();
        }
      };
      auto gsum = [&](int gi) -> bool {
        u32 c = 0;
#pragma unroll
        for (int i = 0; i < 4; ++i) {
          c = sum4(c + gz[4 * gi + i], g[4 * gi + i]);
          gs[gi] = mfma(sel, g[4 * gi + i], i == 0 ? zero : gs[gi]);
          z[gi] = i == 0 ? __uint_as_float(gz[4 * gi]) : z[gi] + __uint_as_float(gz[4 * gi + i]);
        }
        PIN(z[gi]);
        const bool bad = __builtin_amdgcn_ballot_w64(pair_sum(c) != 4u * key) != 0;
        SCHED();
        return bad;
      };
      gload(0); gload(1); gload(2); gload(3);
      bool b0 = gsum(0), b1 = gsum(1), b2 = gsum(2), b3 = gsum(3);
      STAMP(11);
#pragma unroll 1
      for (u32 spin = 0; (b0 | b1 | b2 | b3) && spin < SPIN_CAP; ++spin) {
        asm volatile("" ::: "memory");
        if (b0) gload(0);
        if (b1) gload(1);
        if (b2) gload(2);
        if (b3) gload(3);
        if (b0) b0 = gsum(0);
        if (b1) b1 = gsum(1);
        if (b2) b2 = gsum(2);
        if (b3) b3 = gsum(3);
      }
    }
    __builtin_amdgcn_s_waitcnt(0x0F70);
    STAMP(5);
    {
      float tt[8];
#pragma unroll
      for (int j = 0; j < 8; ++j) tt[j] = (gs[0][j] + gs[1][j]) + (gs[2][j] + gs[3][j]);
      const float zt = (z[0] + z[1]) + (z[2] + z[3]);
      lds_w128(kwa, (u32x4){pk2(tt[0], tt[1]), pk2(tt[2], tt[3]), pk2(tt[4], tt[5]), pk2(tt[6], tt[7])});
      const u32 zz = pk2(zt, zt);
      *(LAS u32x2*)(size_t)kza = (u32x2){zz, zz};
    }
    if (TIMER == 2) {
#pragma unroll
      for (int i = 0; i < 2; ++i) dma4(lds0 + L_WO + 32768 + cw * 8192 + i * 4096, vo, wor, wso + i * 32768);
    }
    BAR();
    STAMP(6);
    if (TIMER == 3) {
#pragma unroll
      for (int i = 0; i < 2; ++i) dma4(lds0 + L_WO + 32768 + cw * 8192 + i * 4096, vo, wor, wso + i * 32768);
    }
    {
      u32 oso[8], ob = lds0 + L_O + cw * OREC;
#pragma unroll
      for (int i = 1; i < 8; ++i) { oso[i] = WS_O + ((br * 64 + panel * 4 + cw) * 8 + ochunk(i)) * OREC; PINS(oso[i]); }
      PINS(ob);
      if (TIMER == 0) {
        BARE();
        STAMP(7);
#pragma unroll
        for (int i = 0; i < 2; ++i) dma4(lds0 + L_WO + 32768 + cw * 8192 + i * 4096, vo, wor, wso + i * 32768);
      }
      WAITV(0);
      BARE();
      if (DELAY3) __builtin_amdgcn_s_sleep(DELAY3);
      STAMP(8);
      auto oissue = [&](int j) { dma_orec(ob + (j % 5) * (4 * OREC), l4, l16, wsr, oso[j]); };
      oissue(1); oissue(2);
      WAITV(5); BARE(); oissue(3);
      WAITV(5); STAMP(15); BARE(); oissue(4); oissue(5);
      WAITV(10); BARE(); oissue(6);
      WAITV(10); BARE(); oissue(7);
      WAITV(10); BARE();
      WAITV(5); BARE();
      WAITV(0); BARE();
    }
    STAMP(9);
    STAMP_OUT();
  } else {
    KLOAD("");
    const int t = wave;
    const u32 l7 = lane & 7, l3 = lane >> 3, lq = lane >> 4;
    const u32 wsw = 16 * (l7 ^ ((lq & 1) << 2));
    u32 vx[4], vw[4];
#pragma unroll
    for (int i = 0; i < 4; ++i) {
      vx[i] = l3 * 1024 + 16 * (l7 ^ (((i & 1) << 2) | lq)) + i * 7168;
      vw[i] = l3 * 3072 + (wsw ^ ((i & 1) << 5)) + i * (8 * 3072 - 1024);
    }
    int rot = (grp + 2 * (idx & 3)) & 7;
    auto chunk = [&](u32 c) -> u32 {
      u32 k = c - 1 + rot; k = k >= 7 ? k - 7 : k; k = k >= 7 ? k - 7 : k;
      return c == 0 ? 0 : k + 1;
    };
    const u32 xs0 = (panel * 128 + t * 32) * 1024, ws0 = (t & 1) * (32 * 3072) + (1 + (t >> 1)) * 1024 + grp * 128;
    const u32 ldst = lds0 + t * 4096;
    u32 e0 = ldst, e1 = ldst + 16384, e2 = xs0 + chunk(0) * 128, e3 = ws0 + chunk(0) * (64 * 3072);
    PINS(e0); PINS(e1); PINS(e2); PINS(e3);
#pragma unroll
    for (int i = 0; i < 4; ++i) { PIN(vx[i]); PIN(vw[i]); }
    KWAIT();
    const __amdgpu_buffer_rsrc_t xr = __builtin_amdgcn_make_buffer_rsrc(KPTR(ka, 0), 0, 2048 * 512 * 2, SRD3);
    const __amdgpu_buffer_rsrc_t wr = __builtin_amdgcn_make_buffer_rsrc(KPTR(ka, 2), 0, 512 * 1536 * 2, SRD3);
    STAMP(10);
    auto dx = [&](int c, u32 d, u32 so) { if (c < NTX) dma4nt(d, vx, xr, so); else dma4(d, vx, xr, so); };
    auto dw = [&](int c, u32 d, u32 so) { if (c < NTW) dma4nt(d, vw, wr, so); else dma4(d, vw, wr, so); };
    if (WFIRST & 1) { dw(0, e1, e3); dx(0, e0, e2); } else { dx(0, e0, e2); dw(0, e1, e3); }
    if (FOLLOW) {
      const int gi = (grp + (ka[2] >> 7) - 4) & 7;
      rot = (gi == 7 ? 0 : gi) + 2 * (idx & 3);
      rot = rot >= 7 ? rot - 7 : rot;
    }
    {
      const __amdgpu_buffer_rsrc_t pr = __builtin_amdgcn_make_buffer_rsrc(KPTR(ka, 4), 0, 32 * 256 * 2, SRD3);
      dma1(lds0 + L_WPHI + t * 1024, lane * 16, pr, (t < 2 ? grp : gq) * 2048 + (t & 1) * 1024);
    }
    STAMP(11);
    u32 th3 = 0, thq = 0, tho;
    if (H3) TOUCH(th3, (l7 >> 1) * 3072 + (l7 & 1) * 1024, wr, (idx * 16 + t * 4) * 3072 + 1024 + ((3 - (ka[2] >> 7)) & 7) * 128);
    if (WFIRST & 2) dw(1, ldst + SLOT + 16384, ws0 + chunk(1) * (64 * 3072));
    dx(1, ldst + SLOT, xs0 + chunk(1) * 128);
    if (!(WFIRST & 2)) dw(1, ldst + SLOT + 16384, ws0 + chunk(1) * (64 * 3072));
    STAMP(1);
    LANE_IDS();
    u32 key = *(const CAS u32*)((const char*)ws + WS_EPOCH + br * 256) + 1u;
    const u32 sw = (r >> 1) & 7;
    u32 xa[4];
#pragma unroll
    for (int s = 0; s < 4; ++s) { xa[s] = lds0 + (32 * t + r) * 128 + 16 * ((2 * s + h) ^ sw); PIN(xa[s]); }
    u32 fpa = lds0 + L_WPHI + 2048 + (4 * h + q) * 32 + (8 * blk + 4 * (p >> 1)) * 2;
    u32 vq = (r >> 3) * 3072 + (r & 7) * 128;
    PIN(fpa); PIN(vq);
    PINS(key);
    u32x4 xq[32], fa[4];
#pragma unroll
    for (int c = 0; c < 8; ++c) {
      u32 d0, d1, s0, s1;
      if (c < 6) {
        d0 = ldst + ((c + 2) % 3) * SLOT; d1 = d0 + 16384;
        s0 = xs0 + chunk(c + 2) * 128; s1 = ws0 + chunk(c + 2) * (64 * 3072);
      } else {
        d0 = lds0 + (c == 6 ? L_WQ0 : L_WQ1) + t * 8192; d1 = d0 + 4096;
        s0 = chunk((c - 6) * 4 + t) * (64 * 3072) + gq * 128; s1 = s0 + 32 * 3072;
      }
      PINS(d0); PINS(d1); PINS(s0); PINS(s1);
      if ((H3 && c == 0) || (LTQ && c == 7)) WAITV(9); else WAITV(8);
      if (H3 && c == 1) PIN(th3);
      STAMP(20 + c);
      BAR();
      if (LTQ && c == 6) TOUCH(thq, vq, wr, (idx * 16 + t * 4) * 3072);
      if (WFIRST & 4) dw(c + 2, d1, s1); else if (c < 6) dx(c + 2, d0, s0); else dw(c + 2, d0, s0);
#pragma unroll
      for (int s = 0; s < 4; ++s) xq[4 * c + s] = lds_r128(xa[s] + (c % 3) * SLOT);
      if (!(WFIRST & 4)) dw(c + 2, d1, s1); else if (c < 6) dx(c + 2, d0, s0); else dw(c + 2, d0, s0);
      if (c == 0) {
#pragma unroll
        for (int hd = 0; hd < 4; ++hd) fa[hd] = lds_tr2(fpa + hd * 512, fpa + hd * 512 + 256);
      }
    }
    if (LTO) TOUCH(tho, r * 128, wor, (idx * 16 + t * 4) * 1024);
    else TOUCH(tho, (r + 32 * t) * 1024, wor, (idx & 3) * 131072 + cg * 128);
    if (t == 0) lds_w128(lds0 + L_V + (lane & 15) * VF + 1024, (u32x4){0x3F803F80u, 0x3F803F80u, 0x3F803F80u, 0x3F803F80u});
    {
      const u32 sg = (lane & 4) ? 0x80008000u : 0u;
#pragma unroll
      for (int hd = 0; hd < 4; ++hd) { PIN(fa[hd]); fa[hd] ^= (u32x4){sg, sg, sg, sg}; PIN(fa[hd]); }
    }
    u32 wa = lds0 + L_WQ1 + (8 * h + q) * 128 + 16 * ((2 * blk + (p >> 1)) ^ ((((q >> 1) & 1) << 2) | (h << 1))) + 8 * (p & 1);
    u32 wb = wa ^ 64;
    u32 kra = lds0 + L_KV + (16 * h + q) * 48 + (blk ? 32 : 8 * p);
    u32 o_off = WS_O + ((br * 64 + panel * 4 + t) * 8 + gq) * OREC + 256 + lane * 16;
    u32 c_off = WS_O + ((br * 64 + panel * 4 + t) * 8 + gq) * OREC + lane * 4;
    u32 woa = lds0 + L_WO + (8 * h + q) * 128 + 16 * ((2 * blk + (p & 1)) ^ (((q >> 1) & 1) << 2)) + 8 * (p >> 1);
    u32 wob = woa ^ 64;
    u32 oa = lds0 + L_O + t * OREC + 256 + lane * 16;
    u32 orow = ((panel * 128 + t * 32 + l3) * 1024 + br * 512 + cg * 64 + 8 * l7) * 2;
    PIN(wa); PIN(wb); PIN(kra); PIN(o_off); PIN(c_off); PIN(woa); PIN(wob); PIN(oa); PIN(orow);
    STAMP(2);
    u32x4 fq[2][2][2];
    {
      f32x16 aq[2], pq[2];
      float mq[2];
      aq[0] = aq[1] = zero;
      u32x4 wf[4];
      auto ldw = [&](int n) {
        const int k = n & 31;
        const u32 o = (n < 32 ? wa : wb) + (k < 16 ? (L_WQ0 - L_WQ1) : 0) + (k & 15) * 2048;
        wf[n & 3] = lds_tr2(o, o + 512);
      };
      WAITV(1);
      BAR();
      SCHED();
      STAMP(12);
      ldw(0); ldw(1); ldw(2);
      SCHED();
      BARE();
      SCHED();
#pragma unroll
      for (int n = 0; n < 34; ++n) {
        ldw(n + 3);
        aq[n >> 5] = mfma(wf[n & 3], xq[n & 31], aq[n >> 5]);
        SGB(0x8, 1); SGB(0x100, 2);
        SCHED();
      }
#pragma unroll
      for (int hh = 0; hh < 2; ++hh) pq[hh] = mfma(fa[hh], pack8(aq[0], hh), zero);
      SCHED();
      STAMP(4);
#pragma unroll
      for (int n = 34; n < 64; ++n) {
        if (n + 3 < 64) ldw(n + 3);
        aq[1] = mfma(wf[n & 3], xq[n & 31], aq[1]);
      }
      mq[0] = negmax16(pq[0]); mq[1] = negmax16(pq[1]);
      f32x2 eq[2][8];
      sm_exp_s(pq[0], pq[1], mq, eq);
      SGB(0x8, 1); SGB(0x100, 2); SGB(0x8, 1); SGB(0x100, 2);
#pragma unroll
      for (int n = 36; n < 41; ++n) { SGB(0x8, 1); SGB(0x100, 2); SGB(0x402, 4); }
#pragma unroll
      for (int n = 41; n < 64; ++n) { SGB(0x8, 1); SGB(0x100, 2); if (n & 1) SGB(0x402, 3); else SGB(0x402, 2); }
#pragma unroll
      for (int i = 0; i < 16; ++i) PIN(eq[i >> 3][i & 7]);
      SCHED();
      STAMP(3);
#pragma unroll
      for (int hh = 0; hh < 2; ++hh) { pq[hh] = mfma(fa[2 + hh], pack8(aq[1], hh), zero); PIN(pq[hh]); }
      SCHED();
      sm_finish(eq, fq[0]);
      SCHED();
      asm volatile("s_waitcnt vmcnt(0)" : "+v"(tho), "+v"(thq)::"memory");
      mq[0] = negmax16(pq[0]); mq[1] = negmax16(pq[1]);
      PIN(mq[0]); PIN(mq[1]);
      SCHED();
#pragma unroll
      for (int i = 0; i < 4; ++i) PIN(fq[0][i >> 1][i & 1]);
      softmax16x2(pq[0], pq[1], mq, fq[1]);
#pragma unroll
      for (int i = 0; i < 4; ++i) PIN(fq[1][i >> 1][i & 1]);
    }
    STAMP(5);
    BAR();
    STAMP(6);
    u32x4 ob[2][4];
    {
      u32x4 a0[4], a1[4];
#pragma unroll
      for (int hd = 0; hd < 4; ++hd) {
        a0[hd] = lds_tr2(kra + hd * 1536, kra + hd * 1536 + 4 * 48);
        a1[hd] = lds_tr2(kra + hd * 1536 + 8 * 48, kra + hd * 1536 + 12 * 48);
      }
      SCHED();
      f32x16 y[4];
      u32 cs = 0;
      auto num = [&](int hd) {
        y[hd] = mfma(a0[hd], fq[hd >> 1][hd & 1][0], zero);
        y[hd] = mfma(a1[hd], fq[hd >> 1][hd & 1][1], y[hd]);
      };
      auto fin = [&](int hd) {
        const f32x16& yy = y[hd];
        const float rd = __builtin_amdgcn_rcpf(yy[8] + 1e-6f);
        u32 w[4];
#pragma unroll
        for (int e = 0; e < 4; ++e) { float t0 = yy[2 * e] * rd, t1 = yy[2 * e + 1] * rd; if (hd < 2) OPQ(t0); w[e] = pk2(t0, t1); }
        ob[0][hd] = (u32x4){w[0], w[1], w[2], w[3]};
        st128c(ob[0][hd], wsr, o_off + hd * 1024);
        cs = sum4(cs, ob[0][hd]);
      };
      num(0); num(1); PIN(y[0]); PIN(y[1]); SCHED();
      num(2); fin(0); SGB(0x8, 1); SGB(0x402, 6); SGB(0x8, 1); PIN(y[2]); SCHED();
      num(3); fin(1); SGB(0x8, 1); SGB(0x402, 6); SGB(0x8, 1); PIN(y[3]); SCHED();
      fin(2); fin(3);
      st32c(key - cs, wsr, c_off);
    }
    SCHED();
    if (TIMER == 0) BARE();
    STAMP(7);
    u32x4 wo[32][2];
    auto ldwo = [&](int n) {
      wo[n][0] = lds_tr2(woa + n * 2048, woa + n * 2048 + 512);
      wo[n][1] = lds_tr2(wob + n * 2048, wob + n * 2048 + 512);
    };
#pragma unroll
    for (int n = 0; n < 16; ++n) ldwo(n);
    f32x16 c0 = zero, c1 = zero;
#pragma unroll
    for (int s = 0; s < 4; ++s) { c0 = mfma(wo[s][0], ob[0][s], c0); c1 = mfma(wo[s][1], ob[0][s], c1); }
#pragma unroll
    for (int k = 4; k < 16; ++k) { PIN(wo[k][0]); PIN(wo[k][1]); }
    PIN(c0); PIN(c1);
    if (TIMER) WAITV(0);
    BARE();
    SCHED();
#pragma unroll
    for (int n = 16; n < 24; ++n) ldwo(n);
#pragma unroll
    for (int k = 16; k < 24; ++k) { PIN(wo[k][0]); PIN(wo[k][1]); }
    const __amdgpu_buffer_rsrc_t outr = __builtin_amdgcn_make_buffer_rsrc(KPTR(kb, 0), 0, 2048 * 1024 * 2, SRD3);
    u32 oc = oa - 256 - lane * 12, l4 = lane * 4, l16 = lane * 16, oa4 = oa + 4 * (4 * OREC), oc4 = oc + 4 * (4 * OREC);
    u32 sw1 = lds0 + t * 4096 + r * 128 + 16 * (h ^ (r & 7)), sr1 = lds0 + t * 4096 + l3 * 128 + 16 * (l7 ^ l3);
    u32 sw2 = sw1 ^ 32, sw3 = sw1 ^ 64, sw4 = sw1 ^ 96, or1 = orow + 16384, or2 = orow + 32768, or3 = orow + 49152;
    PIN(oc); PIN(l4); PIN(l16); PIN(sw1); PIN(sr1); PIN(oa4); PIN(oc4); PIN(sw2); PIN(sw3); PIN(sw4); PIN(or1); PIN(or2); PIN(or3);
    STAMP(8);
    u32 nretry = 0;
    u32 chk[2];
    auto oread = [&](int j) {
      const u32 da = j % 5 == 4 ? oa4 : oa + (j % 5) * (4 * OREC), dc = j % 5 == 4 ? oc4 : oc + (j % 5) * (4 * OREC);
#pragma unroll
      for (int s = 0; s < 4; ++s) ob[j & 1][s] = lds_r128(da + s * 1024);
      chk[j & 1] = *(LAS u32*)(size_t)dc;
    };
    auto obad = [&](int j) -> bool {
      const u32 c = sum4(sum4(chk[j & 1], ob[j & 1][0]), ob[j & 1][1]) + sum4(sum4(0u, ob[j & 1][2]), ob[j & 1][3]);
      return __builtin_amdgcn_ballot_w64(c != key) != 0;
    };
#pragma unroll
    for (int j = 1; j < 8; ++j) {
      BARE();
      SCHED();
      oread(j);
      if (j >= 2) {
#pragma unroll
        for (int s = 0; s < 4; ++s) {
          const int k = 4 * (j - 1) + s;
          c0 = mfma(wo[k][0], ob[(j - 1) & 1][s], c0);
          c1 = mfma(wo[k][1], ob[(j - 1) & 1][s], c1);
        }
        if (j >= 3 && j <= 6) { ldwo(24 + 2 * (j - 3)); ldwo(25 + 2 * (j - 3)); }
      }
      const bool bad = obad(j);
      if (j >= 3 && j <= 6) {
        SGB(0x8, 1); SGB(0x100, 3); SGB(0x8, 1); SGB(0x100, 2); SGB(0x8, 1); SGB(0x100, 3); SGB(0x8, 1); SGB(0x100, 3);
        SGB(0x8, 1); SGB(0x2, 4); SGB(0x8, 1); SGB(0x2, 5); SGB(0x8, 1); SGB(0x100, 2); SGB(0x8, 1);
      } else if (j >= 2) {
        SGB(0x8, 1); SGB(0x100, 3); SGB(0x8, 1); SGB(0x100, 2); SGB(0x8, 3); SGB(0x2, 4); SGB(0x8, 1); SGB(0x2, 5); SGB(0x8, 2);
      }
      SCHED();
      if (__builtin_expect(bad, 0)) {
#pragma unroll 1
        for (u32 spin = 0; spin < SPIN_CAP; ++spin) {
          dma_orec(lds0 + L_O + t * OREC + (j % 5) * (4 * OREC), l4, l16, wsr, WS_O + ((br * 64 + panel * 4 + t) * 8 + ochunk(j)) * OREC);
#pragma unroll 1
          for (u32 jj = j + 1; jj <= (j + AHEAD < 7 ? j + AHEAD : 7); ++jj) {
            const u32 sl = (jj >= 5 ? jj - 5 : jj) * (4 * OREC);
            u32 c = *(LAS u32*)(size_t)(oc + sl);
#pragma unroll
            for (int s = 0; s < 4; ++s) c = sum4(c, lds_r128(oa + sl + s * 1024));
            if (__builtin_amdgcn_ballot_w64(c != key) != 0)
              dma_orec(lds0 + L_O + t * OREC + sl, l4, l16, wsr, WS_O + ((br * 64 + panel * 4 + t) * 8 + ochunk(jj)) * OREC);
          }
          WAITV(0);
          oread(j);
          ++nretry;
          if (!obad(j)) break;
        }
      }
    }
#pragma unroll
    for (int s = 0; s < 4; ++s) c0 = mfma(wo[28 + s][0], ob[1][s], c0);
    PIN(c0);
    SCHED();
#pragma unroll
    for (int s = 0; s < 4; ++s) c1 = mfma(wo[28 + s][1], ob[1][s], c1);
    lds_w128(sw1, pack8(c0, 0));
    lds_w128(sw2, pack8(c0, 1));
    SGB(0x8, 2); SGB(0x2, 4); SGB(0x200, 1); SGB(0x8, 1); SGB(0x2, 4); SGB(0x200, 1); SGB(0x8, 1);
    SCHED();
    STAMP(9);
    lds_w128(sw3, pack8(c1, 0));
    lds_w128(sw4, pack8(c1, 1));
    asm volatile("" ::: "memory");
    {
      u32x4 v[4];
#pragma unroll
      for (int i = 0; i < 4; ++i) v[i] = lds_r128(sr1 + i * 1024);
      SCHED();
      st128c(v[0], outr, orow);
      st128c(v[1], outr, or1);
      st128c(v[2], outr, or2);
      st128c(v[3], outr, or3);
    }
    if (t == 0 && panel == 0 && grp == 0 && lane == 0) st32c(key, wsr, WS_EPOCH + br * 256);
    if (PROBE_N == 13) WAITV(0);
    STAMP(13);
    STAMP_OUT();
  }
}
}

extern "C" void kernel_launch(void* const* d_in, const int* in_sizes, int n_in,
                              void* d_out, int out_size, void* d_ws, size_t ws_size,
                              hipStream_t stream) {
  hh::hedgehog<<<256, 512, 0, stream>>>((const u16*)d_in[0], (const u16*)d_in[1], (const u16*)d_in[2], (const u16*)d_in[3],
                                        (const u16*)d_in[4], (const u16*)d_in[5], (const u16*)d_in[6], (const u16*)d_in[7],
                                        (u16*)d_out, (u32*)((char*)d_ws + ((size_t)WSOFF << 20)));
}
```

```cpp
#define ROTMODE 3
#define PROBE_N 12
#define PROBE_ROLE 0
#include <hip/hip_runtime.h>
#include <stdint.h>

typedef unsigned short u16;
typedef unsigned u32;
typedef short bf16x8 __attribute__((ext_vector_type(8)));
typedef short s16x4 __attribute__((ext_vector_type(4)));
typedef float f32x16 __attribute__((ext_vector_type(16)));
typedef float f32x4 __attribute__((ext_vector_type(4)));
typedef float f32x2 __attribute__((ext_vector_type(2)));
typedef unsigned u32x8 __attribute__((ext_vector_type(8)));
typedef unsigned u32x4 __attribute__((ext_vector_type(4)));
typedef unsigned u32x2 __attribute__((ext_vector_type(2)));
typedef __bf16 bf16x2 __attribute__((ext_vector_type(2)));
#define DI __device__ __forceinline__
#define LAS __attribute__((address_space(3)))
#define CAS __attribute__((address_space(4)))
#define BC(T, x) __builtin_bit_cast(T, x)

#define DELAY1 0
#define DELAY3 3
#define CODE_PF 27648
#define AHEAD 2
#define WSOFF 0
#define GQ 0
#define GC 0
#define WFIRST 0
#define FOLLOW 0
#define TIMER 2
#define LTO 1
#define NTX 0
#define NTW 0
#define H3 0
#define LTQ 0

namespace hh {
constexpr int AUX_SC1 = 16;
constexpr u32 SRD3 = 0x00020000u;
constexpr u32 WS_EPOCH = 0;
constexpr u32 REC = 1280;
constexpr u32 WS_PART = 4096;
constexpr u32 OREC = 4352;
constexpr u32 WS_O = WS_PART + 2 * 32 * 16 * REC;
constexpr u32 WS_END = WS_O + 2 * 64 * 8 * OREC;
constexpr u32 WS_STAMP = 8u << 20;
constexpr u32 SLOT = 32768;
constexpr u32 L_WQ0 = 98304;
constexpr u32 L_WQ1 = 65536;
constexpr u32 L_WPHI = 163840 - 4096;
constexpr u32 L_PHI = 0;
constexpr u32 VF = 1040;
constexpr u32 L_V = 131072;
constexpr u32 L_KV = L_V + 16 * VF;
constexpr u32 L_WO = 0;
constexpr u32 L_O = 65536;
constexpr u32 SPIN_CAP = 1u << 16;

DI u32 pk2(float a, float b) { return BC(u32, __builtin_convertvector((f32x2){a, b}, bf16x2)); }
DI u32x4 pack8(const f32x16& x, int s) {
  return (u32x4){pk2(x[8 * s], x[8 * s + 1]), pk2(x[8 * s + 2], x[8 * s + 3]), pk2(x[8 * s + 4], x[8 * s + 5]), pk2(x[8 * s + 6], x[8 * s + 7])};
}
DI f32x16 mfma(u32x4 a, u32x4 b, f32x16 c) { return __builtin_amdgcn_mfma_f32_32x32x16_bf16(BC(bf16x8, a), BC(bf16x8, b), c, 0, 0, 0); }
DI f32x4 mfma16(u32x4 a, u32x4 b, f32x4 c) { return __builtin_amdgcn_mfma_f32_16x16x32_bf16(BC(bf16x8, a), BC(bf16x8, b), c, 0, 0, 0); }
DI u32x4 lds_r128(u32 a) { return *(LAS u32x4*)(size_t)a; }
DI void lds_w128(u32 a, u32x4 v) { *(LAS u32x4*)(size_t)a = v; }
DI u32x4 lds_tr2(u32 a0, u32 a1) {
  s16x4 lo = __builtin_amdgcn_ds_read_tr16_b64_v4i16((LAS s16x4*)(size_t)a0);
  s16x4 hi = __builtin_amdgcn_ds_read_tr16_b64_v4i16((LAS s16x4*)(size_t)a1);
  return BC(u32x4, __builtin_shufflevector(lo, hi, 0, 1, 2, 3, 4, 5, 6, 7));
}
DI void dma1(u32 lds, u32 voff, __amdgpu_buffer_rsrc_t srd, u32 soff) {
  asm volatile("s_mov_b32 m0, %0\n\ts_nop 0\n\tbuffer_load_dwordx4 %1, %2, %3 offen lds" ::"s"(lds), "v"(voff), "s"(srd), "s"(soff) : "memory");
}
DI void dma4(u32 lds, const u32 (&v)[4], __amdgpu_buffer_rsrc_t srd, u32 soff) {
  asm volatile("s_mov_b32 m0, %0\n\ts_nop 0\n\t"
               "buffer_load_dwordx4 %1, %5, %6 offen lds\n\t"
               "buffer_load_dwordx4 %2, %5, %6 offen offset:1024 lds\n\t"
               "buffer_load_dwordx4 %3, %5, %6 offen offset:2048 lds\n\t"
               "buffer_load_dwordx4 %4, %5, %6 offen offset:3072 lds" ::"s"(lds), "v"(v[0]), "v"(v[1]), "v"(v[2]), "v"(v[3]), "s"(srd), "s"(soff) : "memory");
}
#define DMA4P(name, POL)                                                                       \
  DI void name(u32 lds, const u32 (&v)[4], __amdgpu_buffer_rsrc_t srd, u32 soff) {              \
    asm volatile("s_mov_b32 m0, %0\n\ts_nop 0\n\t"                                             \
                 "buffer_load_dwordx4 %1, %5, %6 offen " POL " lds\n\t"                        \
                 "buffer_load_dwordx4 %2, %5, %6 offen offset:1024 " POL " lds\n\t"            \
                 "buffer_load_dwordx4 %3, %5, %6 offen offset:2048 " POL " lds\n\t"            \
                 "buffer_load_dwordx4 %4, %5, %6 offen offset:3072 " POL " lds" ::"s"(lds), "v"(v[0]), "v"(v[1]), "v"(v[2]), "v"(v[3]), "s"(srd), "s"(soff) : "memory"); \
  }
DMA4P(dma4nt, "nt")
DI void dma_orec(u32 lds, u32 l4, u32 l16, __amdgpu_buffer_rsrc_t srd, u32 soff) {
  asm volatile("s_mov_b32 m0, %0\n\ts_nop 0\n\t"
               "buffer_load_dwordx4 %2, %3, %4 offen offset:256 sc1 lds\n\t"
               "buffer_load_dwordx4 %2, %3, %4 offen offset:1280 sc1 lds\n\t"
               "buffer_load_dwordx4 %2, %3, %4 offen offset:2304 sc1 lds\n\t"
               "buffer_load_dwordx4 %2, %3, %4 offen offset:3328 sc1 lds\n\t"
               "buffer_load_dword %1, %3, %4 offen sc1 lds" ::"s"(lds), "v"(l4), "v"(l16), "s"(srd), "s"(soff) : "memory");
}
#define TOUCH(d, voff, srd, soff) asm volatile("buffer_load_dword %0, %1, %2, %3 offen" : "=v"(d) : "v"(voff), "s"(srd), "s"(soff) : "memory")
DI void st128c(u32x4 v, __amdgpu_buffer_rsrc_t srd, u32 off) { __builtin_amdgcn_raw_buffer_store_b128(v, srd, off, 0, AUX_SC1); }
DI void st32c(u32 v, __amdgpu_buffer_rsrc_t srd, u32 off) { __builtin_amdgcn_raw_buffer_store_b32(v, srd, off, 0, AUX_SC1); }
#define BAR() asm volatile("s_waitcnt lgkmcnt(0)\n\ts_barrier" ::: "memory")
#define BARE() asm volatile("s_barrier" ::: "memory")
#define OPQ(x) asm("" : "+v"(x))
#define PIN(x) asm volatile("" : "+v"(x))
#define PINS(x) asm volatile("" : "+s"(x))
#define WAITV(n) asm volatile("s_waitcnt vmcnt(" #n ")" ::: "memory")
#define SCHED() __builtin_amdgcn_sched_barrier(0)
#define SGB(m, n) __builtin_amdgcn_sched_group_barrier(m, n, 0)
DI u32 sum4(u32 c, u32x4 v) { return (c + v.x + v.y) + v.z + v.w; }
DI u32 pair_sum(u32 s) { auto t = __builtin_amdgcn_permlane32_swap(s, s, false, false); return t[0] + t[1]; }

constexpr float LOG2E = 1.44269504088896340736f;
DI float max3f(float a, float b, float c) { return __builtin_fmaxf(__builtin_fmaxf(a, b), c); }
DI float negmax16(const f32x16& x) {
  const float a = max3f(x[0], x[1], x[2]), b = max3f(x[3], x[4], x[5]), c = max3f(x[6], x[7], x[8]);
  const float d = max3f(x[9], x[10], x[11]), e = max3f(x[12], x[13], x[14]);
  return -max3f(max3f(a, b, x[15]), max3f(c, d, e), a) * LOG2E;
}
DI void sm_exp(const f32x16& p, const f32x16& q, const float (&mc)[2], f32x2 (&e)[2][8]) {
#pragma unroll
  for (int i = 0; i < 8; ++i) {
    const f32x2 t = __builtin_elementwise_fma((f32x2){p[2 * i], p[2 * i + 1]}, (f32x2){LOG2E, LOG2E}, (f32x2){mc[0], mc[0]});
    const f32x2 u = __builtin_elementwise_fma((f32x2){q[2 * i], q[2 * i + 1]}, (f32x2){LOG2E, LOG2E}, (f32x2){mc[1], mc[1]});
    e[0][i] = (f32x2){__builtin_amdgcn_exp2f(t.x), __builtin_amdgcn_exp2f(t.y)};
    e[1][i] = (f32x2){__builtin_amdgcn_exp2f(u.x), __builtin_amdgcn_exp2f(u.y)};
  }
  SGB(0x2, 4); SGB(0x400, 8); SGB(0x2, 4); SGB(0x400, 8); SGB(0x2, 6); SGB(0x400, 8); SGB(0x2, 8); SGB(0x400, 8);
}
DI void sm_exp_s(const f32x16& p, const f32x16& q, const float (&mc)[2], f32x2 (&e)[2][8]) {
#pragma unroll
  for (int i = 0; i < 16; ++i) {
    e[0][i >> 1][i & 1] = __builtin_amdgcn_exp2f(__builtin_fmaf(p[i], LOG2E, mc[0]));
    e[1][i >> 1][i & 1] = __builtin_amdgcn_exp2f(__builtin_fmaf(q[i], LOG2E, mc[1]));
  }
}
DI void sm_finish(const f32x2 (&e)[2][8], u32x4 (&o)[2][2]) {
  float r[2];
#pragma unroll
  for (int u = 0; u < 2; ++u) {
    const f32x2 s = ((e[u][0] + e[u][1]) + (e[u][2] + e[u][3])) + ((e[u][4] + e[u][5]) + (e[u][6] + e[u][7]));
    r[u] = __builtin_amdgcn_rcpf(s.x + s.y);
  }
#pragma unroll
  for (int u = 0; u < 2; ++u) {
    u32 w[8];
#pragma unroll
    for (int i = 0; i < 8; ++i) { const f32x2 t = e[u][i] * (f32x2){r[u], r[u]}; w[i] = pk2(t.x, t.y); }
    o[u][0] = (u32x4){w[0], w[1], w[2], w[3]};
    o[u][1] = (u32x4){w[4], w[5], w[6], w[7]};
  }
}
DI void softmax16x2(const f32x16& p, const f32x16& q, const float (&mc)[2], u32x4 (&o)[2][2]) {
  f32x2 e[2][8];
  sm_exp(p, q, mc, e);
  sm_finish(e, o);
}

#define STAMP(i) do { if ((i) == PROBE_N) { SCHED(); asm volatile("s_memrealtime %0\n\ts_waitcnt lgkmcnt(0)" : "=s"(pt1) :: "memory"); SCHED(); } } while (0)
#define STAMP_OUT() do { if (PROBE_N && (wave >> 2) == PROBE_ROLE) { unsigned long long d_ = pt1 - pt0; if (d_ > 100000) d_ = 100000; unsigned long long e_ = pt0 + 3000 + 8 * d_; \
  for (int it_ = 0; it_ < 1000000; ++it_) { unsigned long long t_; asm volatile("s_memrealtime %0\n\ts_waitcnt lgkmcnt(0)" : "=s"(t_) :: "memory"); if (t_ >= e_) break; } } } while (0)

__global__ __launch_bounds__(512, 2) void hedgehog(const u16* xA, const u16* wA, const u16* pA, const u16* oA,
                                                   const u16* xB, const u16* wB, const u16* pB, const u16* oB,
                                                   u16* out_, u32* ws_) {
  __shared__ __attribute__((aligned(16))) char smem[163840];
  const u32 lds0 = (u32)(size_t)(LAS char*)smem;
  unsigned long long pt0 = __builtin_amdgcn_s_memrealtime(), pt1 = pt0;
  const int bid = blockIdx.x, xcd = bid & 7, idx = bid >> 3;
  const int br = xcd >> 2;
#define KLOAD(tail)                                                                                       \
  u32x8 ka;                                                                                               \
  u32x4 kb;                                                                                               \
  asm volatile("s_load_dwordx8 %0, %2, %3\n\ts_load_dwordx4 %1, %2, 0x40" tail : "=&s"(ka), "=&s"(kb)     \
               : "s"(__builtin_amdgcn_kernarg_segment_ptr()), "s"(br * 32))
#define KWAIT() asm volatile("s_waitcnt lgkmcnt(0)" : "+s"(ka), "+s"(kb))
#define KPTR(v, i) ((void*)(((unsigned long long)(v)[(i) + 1] << 32) | (v)[i]))
  unsigned long long pcs;
  asm volatile("s_getpc_b64 %0" : "=s"(pcs));
  pcs &= ~255ull;
  const int lane = threadIdx.x & 63;
  const int wave = __builtin_amdgcn_readfirstlane(threadIdx.x >> 6);
  const int panel = (xcd & 3) * 4 + (idx & 3), grp = idx >> 2, gq = (grp + GQ) & 7, cg = (grp + GC) & 7;
  auto ochunk = [&](u32 j) -> u32 {
    const int v = (int)j + (((int)j - 1 - gq) >> 31), z = ((int)j - 1) >> 31;
    return (u32)__builtin_amdgcn_readfirstlane((v & ~z) | (gq & z));
  };
  f32x16 zero;
#pragma unroll
  for (int i = 0; i < 16; ++i) zero[i] = 0.f;
#define LANE_IDS()                                                                            \
  u32 lane_ = lane;                                                                           \
  PIN(lane_);                                                                                 \
  const u32 r = lane_ & 31, h = lane_ >> 5;                                                   \
  const u32 blk = (lane_ >> 4) & 1, q = (lane_ & 15) >> 2, p = lane_ & 3;                     \
  u32* ws = (u32*)KPTR(kb, 2);                                                                \
  const __amdgpu_buffer_rsrc_t wsr = __builtin_amdgcn_make_buffer_rsrc(ws, 0, WS_END, SRD3);  \
  const __amdgpu_buffer_rsrc_t wor = __builtin_amdgcn_make_buffer_rsrc(KPTR(ka, 6), 0, 512 * 512 * 2, SRD3)

  if (wave >= 4) {
    const int cw = wave - 4;
    const int rh = cw >> 1, hp = cw & 1;
    KLOAD("\n\ts_waitcnt lgkmcnt(0)");
    u32 sp0, sp1, sp2, sp3;
    {
      u32 off = idx * 1024 + cw * 256;
      off = off < CODE_PF ? off : 0;
      asm volatile("s_load_dword %0, %4, %5 offset:0\n\ts_load_dword %1, %4, %5 offset:64\n\ts_load_dword %2, %4, %5 offset:128\n\ts_load_dword %3, %4, %5 offset:192"
                   : "=&s"(sp0), "=&s"(sp1), "=&s"(sp2), "=&s"(sp3) : "s"(pcs), "s"(off));
    }
    LANE_IDS();
    u32 key = *(const CAS u32*)((const char*)ws + WS_EPOCH + br * 256) + 1u;
    u32 xa[2], xb[2], wa[2], wb2[2];
    {
      const u32 r16 = lane_ & 15, g = lane_ >> 4;
#pragma unroll
      for (int s = 0; s < 2; ++s) {
        xa[s] = lds0 + (64 * rh + r16) * 128 + 16 * ((4 * s + g) ^ (r16 >> 1));
        wa[s] = lds0 + 16384 + (8 * g + q) * 128 + 16 * ((4 * hp + 2 * s + (p >> 1)) ^ ((((q >> 1) & 1) << 2) | (blk << 1))) + 8 * (p & 1);
        xb[s] = xa[s] + 2 * SLOT; wb2[s] = wa[s] + 2 * SLOT;
        PIN(xa[s]); PIN(xb[s]); PIN(wa[s]); PIN(wb2[s]);
      }
    }
    u32 fpa = lds0 + L_WPHI + hp * 1024 + (8 * h + q) * 32 + (8 * blk + 4 * (p >> 1)) * 2;
    PIN(fpa);
    PINS(key);
    f32x4 ak[4][2], av[4][2];
#pragma unroll
    for (int i = 0; i < 8; ++i) ak[i >> 1][i & 1] = av[i >> 1][i & 1] = (f32x4){0.f, 0.f, 0.f, 0.f};
    u32 vra, pra, vwa, pw0, pw1, pub16, pub4;
    u32x4 xf[2][4], wk[2][2], wv[2][2];
    auto ldx = [&](int c, int s) {
      const u32 x = c % 3 == 2 ? xb[s] : xa[s] + (c % 3) * SLOT;
#pragma unroll
      for (int i = 0; i < 4; ++i) xf[s][i] = lds_r128(x + i * 2048);
    };
    auto ldw = [&](int c, int s, int m) {
#pragma unroll
      for (int n = 0; n < 2; ++n) {
        const u32 w = (c % 3 == 2 ? wb2[n] : wa[n] + (c % 3) * SLOT) + s * 4096 + m * 8192;
        (m ? wv : wk)[s][n] = lds_tr2(w, w + 512);
      }
    };
    auto mk = [&](int s) {
#pragma unroll
      for (int i = 0; i < 8; ++i) ak[i >> 1][i & 1] = mfma16(wk[s][i & 1], xf[s][i >> 1], ak[i >> 1][i & 1]);
    };
    auto mv = [&](int s) {
#pragma unroll
      for (int i = 0; i < 8; ++i) av[i >> 1][i & 1] = mfma16(xf[s][i >> 1], wv[s][i & 1], av[i >> 1][i & 1]);
    };
    auto mix = [&](int n) {
#pragma unroll
      for (int i = 0; i < 8; ++i) {
        SGB(0x8, 1);
        if (n == 12) { if (i < 4) SGB(0x100, 2); else SGB(0x100, 1); }
        else if (n == 8) SGB(0x100, 1);
        else if (n == 4) { if (i < 4) SGB(0x100, 1); }
        else if (n < 0) SGB(0x6, 2);
      }
      SCHED();
    };
    BAR();
    asm volatile("" ::"s"(sp0), "s"(sp1), "s"(sp2), "s"(sp3));
    STAMP(1);
    ldw(0, 0, 0); ldx(0, 0); ldw(0, 0, 1);
    u32x4 fa[2];
#pragma unroll
    for (int hh = 0; hh < 2; ++hh) fa[hh] = lds_tr2(fpa + hh * 512, fpa + hh * 512 + 128);
    SCHED();
#pragma unroll
    for (int c = 0; c < 7; ++c) {
      ldw(c, 1, 0); ldx(c, 1); mk(0); mix(8);
      ldw(c, 1, 1); mv(0); mix(4);
      u32 ln = lane;
      PIN(ln);
      const u32 r_ = ln & 31, h_ = ln >> 5, q_ = (ln & 15) >> 2;
      if (c == 1) {
        const u32 sg = (ln & 4) ? 0x80008000u : 0u;
#pragma unroll
        for (int hh = 0; hh < 2; ++hh) { OPQ(fa[hh]); fa[hh] ^= (u32x4){sg, sg, sg, sg}; }
      }
      if (c == 2) vra = lds0 + L_V + (cw >> 1) * (2 * VF) + (r_ < 16 ? (ln + 16 * (cw & 1)) * 16 : 1024);
      if (c == 3) pra = lds0 + L_PHI + cw * 8192 + (4 * h_ + q_) * 64 + 16 * ((((ln >> 3) & 2) + ((ln >> 1) & 1)) ^ ((2 * h_ + (q_ >> 1)) & 3)) + 8 * (ln & 1);
      if (c == 4) {
        const u32 rec = WS_PART + ((br * 32 + grp * 4 + cw) * 16 + panel) * REC;
        pub16 = rec + ln * 16; pub4 = rec + 1024 + ln * 4;
      }
      if (c == 5) { pw0 = lds0 + L_PHI + (2 * hp) * 8192 + (64 * rh + r_) * 64 + 16 * ((2 * h_) ^ ((r_ >> 1) & 3)); pw1 = pw0 ^ 16; }
      if (c == 6) vwa = lds0 + L_V + (8 * rh + 2 * hp) * VF + ((ln & 16) * 2 + (ln & 15)) * 16 + h_ * 8;
      mk(1); mix(c >= 1 ? -1 : 0);
      if (c == 1) { PIN(fa[0]); PIN(fa[1]); }
      if (c == 2) PIN(vra);
      if (c == 3) PIN(pra);
      if (c == 4) { PIN(pub16); PIN(pub4); }
      if (c == 5) { PIN(pw0); PIN(pw1); }
      if (c == 6) PIN(vwa);
      BAR();
      ldw(c + 1, 0, 0); ldx(c + 1, 0); ldw(c + 1, 0, 1);
      mv(1); mix(12);
    }
    ldw(7, 1, 0); ldx(7, 1); ldw(7, 1, 1); mk(0); mix(12);
    mk(1); mix(0);
    __builtin_amdgcn_s_setprio(2);
    STAMP(2);
    f32x16 pk[2][2];
    {
      u32x4 kf[2][2];
      auto kpack = [&](int i) {
#pragma unroll
        for (int hh = 0; hh < 2; ++hh) {
          const f32x4 &t0 = ak[2 * i][hh], &t1 = ak[2 * i + 1][hh];
          const auto lo = __builtin_amdgcn_permlane16_swap(pk2(t0[0], t0[1]), pk2(t1[0], t1[1]), false, false);
          const auto hi = __builtin_amdgcn_permlane16_swap(pk2(t0[2], t0[3]), pk2(t1[2], t1[3]), false, false);
          kf[i][hh] = (u32x4){lo[0], hi[0], lo[1], hi[1]};
        }
      };
      mv(0); kpack(0);
      SGB(0x8, 2);
#pragma unroll
      for (int i = 0; i < 6; ++i) { SGB(0x2, 2); SGB(0x8, 1); }
      SCHED();
      pk[0][0] = mfma(fa[0], kf[0][0], zero);
      pk[0][1] = mfma(fa[1], kf[0][1], zero);
      kpack(1);
      SGB(0x8, 1); SGB(0x2, 6); SGB(0x8, 1); SGB(0x2, 6);
      PIN(pk[0][0]); PIN(pk[0][1]); PIN(kf[1][0]); PIN(kf[1][1]);
      SCHED();
      mv(1);
      pk[1][0] = mfma(fa[0], kf[1][0], zero);
      pk[1][1] = mfma(fa[1], kf[1][1], zero);
    }
    float m0[2], m1[2];
    m0[0] = negmax16(pk[0][0]); m0[1] = negmax16(pk[0][1]);
    SGB(0x8, 2);
#pragma unroll
    for (int i = 0; i < 6; ++i) { SGB(0x2, 2); SGB(0x8, 1); }
    SGB(0x8, 1); SGB(0x2, 6); SGB(0x8, 1);
#pragma unroll
    for (int i = 0; i < 8; ++i) PIN(av[i >> 1][i & 1]);
    PIN(pk[1][0]); PIN(pk[1][1]); PIN(m0[0]); PIN(m0[1]);
    SCHED();
#pragma unroll
    for (int i = 0; i < 8; ++i) {
      const f32x4& t = av[i >> 1][i & 1];
      *(LAS u32x2*)(size_t)(vwa + (4 * (i >> 2) + ((i >> 1) & 1)) * VF + (i & 1) * 256) = (u32x2){pk2(t[0], t[1]), pk2(t[2], t[3])};
    }
    SCHED();
    {
      u32x4 o[2][2];
      softmax16x2(pk[0][0], pk[0][1], m0, o);
      lds_w128(pw0, o[0][0]); lds_w128(pw1, o[0][1]);
      lds_w128(pw0 + 8192, o[1][0]); lds_w128(pw1 + 8192, o[1][1]);
      SCHED();
      m1[0] = negmax16(pk[1][0]); m1[1] = negmax16(pk[1][1]);
      PIN(m1[0]); PIN(m1[1]);
      SCHED();
      softmax16x2(pk[1][0], pk[1][1], m1, o);
      lds_w128(pw0 + 2048, o[0][0]); lds_w128(pw1 + 2048, o[0][1]);
      lds_w128(pw0 + 2048 + 8192, o[1][0]); lds_w128(pw1 + 2048 + 8192, o[1][1]);
      SGB(0x402, 26); SGB(0x200, 1); SGB(0x402, 8); SGB(0x200, 1); SGB(0x402, 8); SGB(0x200, 1); SGB(0x402, 8); SGB(0x200, 1);
      SCHED();
      BARE();
      BAR();
      SCHED();
    }
    STAMP(3);
    f32x16 kv = zero;
    {
      u32x4 a[8], b[8];
#pragma unroll
      for (int u = 0; u < 8; ++u) {
        a[u] = lds_r128(vra + ((u >> 1) * 4 + (u & 1)) * VF);
        b[u] = lds_tr2(pra + 16 * u * 64, pra + (16 * u + 8) * 64);
      }
#pragma unroll
      for (int u = 0; u < 8; ++u) kv = mfma(a[u], b[u], kv);
      SGB(0x100, 12); SGB(0x8, 1); SGB(0x100, 6); SGB(0x8, 1); SGB(0x100, 6); SGB(0x8, 6);
    }
    {
      const u32x4 w = pack8(kv, 0);
      st128c(w, wsr, pub16);
      const u32 zb = __float_as_uint(kv[8]);
      const u32 chk = key - pair_sum(sum4(h ? 0u : zb, w));
      st32c(h ? chk : zb, wsr, pub4);
    }
    SCHED();
    STAMP(4);
    u32 l16, l4, kwa, kza, gso[4], vo[4], wso;
    u32x4 sel;
    {
      u32 ln = lane;
      PIN(ln);
#pragma unroll
      for (int i = 0; i < 4; ++i) vo[i] = (ln >> 3) * 1024 + 16 * ((ln & 7) ^ (((ln >> 4) & 1) << 2)) + i * 7168;
#pragma unroll
      for (int i = 0; i < 2; ++i) dma4(lds0 + L_WO + cw * 8192 + i * 4096, vo, wor, ochunk(cw) * 65536 + i * 32768 + cg * 128);
      wso = ochunk(4 + cw) * 65536 + cg * 128;
      if (TIMER == 1) {
#pragma unroll
        for (int i = 0; i < 2; ++i) dma4(lds0 + L_WO + 32768 + cw * 8192 + i * 4096, vo, wor, wso + i * 32768);
      }
      const u32 r_ = ln & 31, h_ = ln >> 5;
      l16 = ln * 16; l4 = ln * 4;
      kwa = lds0 + L_KV + cw * 1536 + r_ * 48 + h_ * 16; kza = lds0 + L_KV + cw * 1536 + r_ * 48 + 32 + h_ * 8;
      const bool hit = r_ < 16 && ((r_ >> 2) & 1) == h_;
      const u32 j = (r_ & 3) + 4 * (r_ >> 3);
#pragma unroll
      for (int e = 0; e < 4; ++e) sel[e] = (hit && (j >> 1) == e) ? ((j & 1) ? 0x3F800000u : 0x00003F80u) : 0u;
#pragma unroll
      for (int i = 0; i < 4; ++i) { gso[i] = WS_PART + ((br * 32 + gq * 4 + cw) * 16 + ((i + idx) & 3) * 4) * REC; PINS(gso[i]); }
      PIN(l16); PIN(l4); PIN(kwa); PIN(kza); PIN(sel); PINS(wso);
    }
    if (DELAY1) __builtin_amdgcn_s_sleep(DELAY1);
    STAMP(10);
    f32x16 gs[4];
    float z[4];
    {
      u32x4 g[16];
      u32 gz[16];
      auto gload = [&](int gi) {
#pragma unroll
        for (int i = 0; i < 4; ++i) {
          g[4 * gi + i] = __builtin_amdgcn_raw_buffer_load_b128(wsr, l16 + i * REC, gso[gi], AUX_SC1);
          gz[4 * gi + i] = __builtin_amdgcn_raw_buffer_load_b32(wsr, l4 + (1024 + i * REC - 1024 * (i == 3)), gso[gi] + 1024 * (i == 3), AUX_SC1);
          SCHED();
        }
      };
      auto gsum = [&](int gi) -> bool {
        u32 c = 0;
#pragma unroll
        for (int i = 0; i < 4; ++i) {
          c = sum4(c + gz[4 * gi + i], g[4 * gi + i]);
          gs[gi] = mfma(sel, g[4 * gi + i], i == 0 ? zero : gs[gi]);
          z[gi] = i == 0 ? __uint_as_float(gz[4 * gi]) : z[gi] + __uint_as_float(gz[4 * gi + i]);
        }
        PIN(z[gi]);
        const bool bad = __builtin_amdgcn_ballot_w64(pair_sum(c) != 4u * key) != 0;
        SCHED();
        return bad;
      };
      gload(0); gload(1); gload(2); gload(3);
      bool b0 = gsum(0), b1 = gsum(1), b2 = gsum(2), b3 = gsum(3);
      STAMP(11);
#pragma unroll 1
      for (u32 spin = 0; (b0 | b1 | b2 | b3) && spin < SPIN_CAP; ++spin) {
        asm volatile("" ::: "memory");
        if (b0) gload(0);
        if (b1) gload(1);
        if (b2) gload(2);
        if (b3) gload(3);
        if (b0) b0 = gsum(0);
        if (b1) b1 = gsum(1);
        if (b2) b2 = gsum(2);
        if (b3) b3 = gsum(3);
      }
    }
    __builtin_amdgcn_s_waitcnt(0x0F70);
    STAMP(5);
    {
      float tt[8];
#pragma unroll
      for (int j = 0; j < 8; ++j) tt[j] = (gs[0][j] + gs[1][j]) + (gs[2][j] + gs[3][j]);
      const float zt = (z[0] + z[1]) + (z[2] + z[3]);
      lds_w128(kwa, (u32x4){pk2(tt[0], tt[1]), pk2(tt[2], tt[3]), pk2(tt[4], tt[5]), pk2(tt[6], tt[7])});
      const u32 zz = pk2(zt, zt);
      *(LAS u32x2*)(size_t)kza = (u32x2){zz, zz};
    }
    if (TIMER == 2) {
#pragma unroll
      for (int i = 0; i < 2; ++i) dma4(lds0 + L_WO + 32768 + cw * 8192 + i * 4096, vo, wor, wso + i * 32768);
    }
    BAR();
    STAMP(6);
    if (TIMER == 3) {
#pragma unroll
      for (int i = 0; i < 2; ++i) dma4(lds0 + L_WO + 32768 + cw * 8192 + i * 4096, vo, wor, wso + i * 32768);
    }
    {
      u32 oso[8], ob = lds0 + L_O + cw * OREC;
#pragma unroll
      for (int i = 1; i < 8; ++i) { oso[i] = WS_O + ((br * 64 + panel * 4 + cw) * 8 + ochunk(i)) * OREC; PINS(oso[i]); }
      PINS(ob);
      if (TIMER == 0) {
        BARE();
        STAMP(7);
#pragma unroll
        for (int i = 0; i < 2; ++i) dma4(lds0 + L_WO + 32768 + cw * 8192 + i * 4096, vo, wor, wso + i * 32768);
      }
      WAITV(0);
      BARE();
      if (DELAY3) __builtin_amdgcn_s_sleep(DELAY3);
      STAMP(8);
      auto oissue = [&](int j) { dma_orec(ob + (j % 5) * (4 * OREC), l4, l16, wsr, oso[j]); };
      oissue(1); oissue(2);
      WAITV(5); BARE(); oissue(3);
      WAITV(5); STAMP(15); BARE(); oissue(4); oissue(5);
      WAITV(10); BARE(); oissue(6);
      WAITV(10); BARE(); oissue(7);
      WAITV(10); BARE();
      WAITV(5); BARE();
      WAITV(0); BARE();
    }
    STAMP(9);
    STAMP_OUT();
  } else {
    KLOAD("");
    const int t = wave;
    const u32 l7 = lane & 7, l3 = lane >> 3, lq = lane >> 4;
    const u32 wsw = 16 * (l7 ^ ((lq & 1) << 2));
    u32 vx[4], vw[4];
#pragma unroll
    for (int i = 0; i < 4; ++i) {
      vx[i] = l3 * 1024 + 16 * (l7 ^ (((i & 1) << 2) | lq)) + i * 7168;
      vw[i] = l3 * 3072 + (wsw ^ ((i & 1) << 5)) + i * (8 * 3072 - 1024);
    }
    int rot = (grp + 2 * (idx & 3)) & 7;
    auto chunk = [&](u32 c) -> u32 {
      return (c + 4 * ((grp + idx) & 1)) & 7;
    };
    const u32 xs0 = (panel * 128 + t * 32) * 1024, ws0 = (t & 1) * (32 * 3072) + (1 + (t >> 1)) * 1024 + grp * 128;
    const u32 ldst = lds0 + t * 4096;
    u32 e0 = ldst, e1 = ldst + 16384, e2 = xs0 + chunk(0) * 128, e3 = ws0 + chunk(0) * (64 * 3072);
    PINS(e0); PINS(e1); PINS(e2); PINS(e3);
#pragma unroll
    for (int i = 0; i < 4; ++i) { PIN(vx[i]); PIN(vw[i]); }
    KWAIT();
    const __amdgpu_buffer_rsrc_t xr = __builtin_amdgcn_make_buffer_rsrc(KPTR(ka, 0), 0, 2048 * 512 * 2, SRD3);
    const __amdgpu_buffer_rsrc_t wr = __builtin_amdgcn_make_buffer_rsrc(KPTR(ka, 2), 0, 512 * 1536 * 2, SRD3);
    STAMP(10);
    auto dx = [&](int c, u32 d, u32 so) { if (c < NTX) dma4nt(d, vx, xr, so); else dma4(d, vx, xr, so); };
    auto dw = [&](int c, u32 d, u32 so) { if (c < NTW) dma4nt(d, vw, wr, so); else dma4(d, vw, wr, so); };
    if (WFIRST & 1) { dw(0, e1, e3); dx(0, e0, e2); } else { dx(0, e0, e2); dw(0, e1, e3); }
    if (FOLLOW) {
      const int gi = (grp + (ka[2] >> 7) - 4) & 7;
      rot = (gi == 7 ? 0 : gi) + 2 * (idx & 3);
      rot = rot >= 7 ? rot - 7 : rot;
    }
    {
      const __amdgpu_buffer_rsrc_t pr = __builtin_amdgcn_make_buffer_rsrc(KPTR(ka, 4), 0, 32 * 256 * 2, SRD3);
      dma1(lds0 + L_WPHI + t * 1024, lane * 16, pr, (t < 2 ? grp : gq) * 2048 + (t & 1) * 1024);
    }
    STAMP(11);
    u32 th3 = 0, thq = 0, tho;
    if (H3) TOUCH(th3, (l7 >> 1) * 3072 + (l7 & 1) * 1024, wr, (idx * 16 + t * 4) * 3072 + 1024 + ((3 - (ka[2] >> 7)) & 7) * 128);
    if (WFIRST & 2) dw(1, ldst + SLOT + 16384, ws0 + chunk(1) * (64 * 3072));
    dx(1, ldst + SLOT, xs0 + chunk(1) * 128);
    if (!(WFIRST & 2)) dw(1, ldst + SLOT + 16384, ws0 + chunk(1) * (64 * 3072));
    STAMP(1);
    LANE_IDS();
    u32 key = *(const CAS u32*)((const char*)ws + WS_EPOCH + br * 256) + 1u;
    const u32 sw = (r >> 1) & 7;
    u32 xa[4];
#pragma unroll
    for (int s = 0; s < 4; ++s) { xa[s] = lds0 + (32 * t + r) * 128 + 16 * ((2 * s + h) ^ sw); PIN(xa[s]); }
    u32 fpa = lds0 + L_WPHI + 2048 + (4 * h + q) * 32 + (8 * blk + 4 * (p >> 1)) * 2;
    u32 vq = (r >> 3) * 3072 + (r & 7) * 128;
    PIN(fpa); PIN(vq);
    PINS(key);
    u32x4 xq[32], fa[4];
#pragma unroll
    for (int c = 0; c < 8; ++c) {
      u32 d0, d1, s0, s1;
      if (c < 6) {
        d0 = ldst + ((c + 2) % 3) * SLOT; d1 = d0 + 16384;
        s0 = xs0 + chunk(c + 2) * 128; s1 = ws0 + chunk(c + 2) * (64 * 3072);
      } else {
        d0 = lds0 + (c == 6 ? L_WQ0 : L_WQ1) + t * 8192; d1 = d0 + 4096;
        s0 = chunk((c - 6) * 4 + t) * (64 * 3072) + gq * 128; s1 = s0 + 32 * 3072;
      }
      PINS(d0); PINS(d1); PINS(s0); PINS(s1);
      if ((H3 && c == 0) || (LTQ && c == 7)) WAITV(9); else WAITV(8);
      if (H3 && c == 1) PIN(th3);
      STAMP(20 + c);
      BAR();
      if (LTQ && c == 6) TOUCH(thq, vq, wr, (idx * 16 + t * 4) * 3072);
      if (WFIRST & 4) dw(c + 2, d1, s1); else if (c < 6) dx(c + 2, d0, s0); else dw(c + 2, d0, s0);
#pragma unroll
      for (int s = 0; s < 4; ++s) xq[4 * c + s] = lds_r128(xa[s] + (c % 3) * SLOT);
      if (!(WFIRST & 4)) dw(c + 2, d1, s1); else if (c < 6) dx(c + 2, d0, s0); else dw(c + 2, d0, s0);
      if (c == 0) {
#pragma unroll
        for (int hd = 0; hd < 4; ++hd) fa[hd] = lds_tr2(fpa + hd * 512, fpa + hd * 512 + 256);
      }
    }
    if (LTO) TOUCH(tho, r * 128, wor, (idx * 16 + t * 4) * 1024);
    else TOUCH(tho, (r + 32 * t) * 1024, wor, (idx & 3) * 131072 + cg * 128);
    if (t == 0) lds_w128(lds0 + L_V + (lane & 15) * VF + 1024, (u32x4){0x3F803F80u, 0x3F803F80u, 0x3F803F80u, 0x3F803F80u});
    {
      const u32 sg = (lane & 4) ? 0x80008000u : 0u;
#pragma unroll
      for (int hd = 0; hd < 4; ++hd) { PIN(fa[hd]); fa[hd] ^= (u32x4){sg, sg, sg, sg}; PIN(fa[hd]); }
    }
    u32 wa = lds0 + L_WQ1 + (8 * h + q) * 128 + 16 * ((2 * blk + (p >> 1)) ^ ((((q >> 1) & 1) << 2) | (h << 1))) + 8 * (p & 1);
    u32 wb = wa ^ 64;
    u32 kra = lds0 + L_KV + (16 * h + q) * 48 + (blk ? 32 : 8 * p);
    u32 o_off = WS_O + ((br * 64 + panel * 4 + t) * 8 + gq) * OREC + 256 + lane * 16;
    u32 c_off = WS_O + ((br * 64 + panel * 4 + t) * 8 + gq) * OREC + lane * 4;
    u32 woa = lds0 + L_WO + (8 * h + q) * 128 + 16 * ((2 * blk + (p & 1)) ^ (((q >> 1) & 1) << 2)) + 8 * (p >> 1);
    u32 wob = woa ^ 64;
    u32 oa = lds0 + L_O + t * OREC + 256 + lane * 16;
    u32 orow = ((panel * 128 + t * 32 + l3) * 1024 + br * 512 + cg * 64 + 8 * l7) * 2;
    PIN(wa); PIN(wb); PIN(kra); PIN(o_off); PIN(c_off); PIN(woa); PIN(wob); PIN(oa); PIN(orow);
    STAMP(2);
    u32x4 fq[2][2][2];
    {
      f32x16 aq[2], pq[2];
      float mq[2];
      aq[0] = aq[1] = zero;
      u32x4 wf[4];
      auto ldw = [&](int n) {
        const int k = n & 31;
        const u32 o = (n < 32 ? wa : wb) + (k < 16 ? (L_WQ0 - L_WQ1) : 0) + (k & 15) * 2048;
        wf[n & 3] = lds_tr2(o, o + 512);
      };
      WAITV(1);
      BAR();
      SCHED();
      STAMP(12);
      ldw(0); ldw(1); ldw(2);
      SCHED();
      BARE();
      SCHED();
#pragma unroll
      for (int n = 0; n < 34; ++n) {
        ldw(n + 3);
        aq[n >> 5] = mfma(wf[n & 3], xq[n & 31], aq[n >> 5]);
        SGB(0x8, 1); SGB(0x100, 2);
        SCHED();
      }
#pragma unroll
      for (int hh = 0; hh < 2; ++hh) pq[hh] = mfma(fa[hh], pack8(aq[0], hh), zero);
      SCHED();
      STAMP(4);
#pragma unroll
      for (int n = 34; n < 64; ++n) {
        if (n + 3 < 64) ldw(n + 3);
        aq[1] = mfma(wf[n & 3], xq[n & 31], aq[1]);
      }
      mq[0] = negmax16(pq[0]); mq[1] = negmax16(pq[1]);
      f32x2 eq[2][8];
      sm_exp_s(pq[0], pq[1], mq, eq);
      SGB(0x8, 1); SGB(0x100, 2); SGB(0x8, 1); SGB(0x100, 2);
#pragma unroll
      for (int n = 36; n < 41; ++n) { SGB(0x8, 1); SGB(0x100, 2); SGB(0x402, 4); }
#pragma unroll
      for (int n = 41; n < 64; ++n) { SGB(0x8, 1); SGB(0x100, 2); if (n & 1) SGB(0x402, 3); else SGB(0x402, 2); }
#pragma unroll
      for (int i = 0; i < 16; ++i) PIN(eq[i >> 3][i & 7]);
      SCHED();
      STAMP(3);
#pragma unroll
      for (int hh = 0; hh < 2; ++hh) { pq[hh] = mfma(fa[2 + hh], pack8(aq[1], hh), zero); PIN(pq[hh]); }
      SCHED();
      sm_finish(eq, fq[0]);
      SCHED();
      asm volatile("s_waitcnt vmcnt(0)" : "+v"(tho), "+v"(thq)::"memory");
      mq[0] = negmax16(pq[0]); mq[1] = negmax16(pq[1]);
      PIN(mq[0]); PIN(mq[1]);
      SCHED();
#pragma unroll
      for (int i = 0; i < 4; ++i) PIN(fq[0][i >> 1][i & 1]);
      softmax16x2(pq[0], pq[1], mq, fq[1]);
#pragma unroll
      for (int i = 0; i < 4; ++i) PIN(fq[1][i >> 1][i & 1]);
    }
    STAMP(5);
    BAR();
    STAMP(6);
    u32x4 ob[2][4];
    {
      u32x4 a0[4], a1[4];
#pragma unroll
      for (int hd = 0; hd < 4; ++hd) {
        a0[hd] = lds_tr2(kra + hd * 1536, kra + hd * 1536 + 4 * 48);
        a1[hd] = lds_tr2(kra + hd * 1536 + 8 * 48, kra + hd * 1536 + 12 * 48);
      }
      SCHED();
      f32x16 y[4];
      u32 cs = 0;
      auto num = [&](int hd) {
        y[hd] = mfma(a0[hd], fq[hd >> 1][hd & 1][0], zero);
        y[hd] = mfma(a1[hd], fq[hd >> 1][hd & 1][1], y[hd]);
      };
      auto fin = [&](int hd) {
        const f32x16& yy = y[hd];
        const float rd = __builtin_amdgcn_rcpf(yy[8] + 1e-6f);
        u32 w[4];
#pragma unroll
        for (int e = 0; e < 4; ++e) { float t0 = yy[2 * e] * rd, t1 = yy[2 * e + 1] * rd; if (hd < 2) OPQ(t0); w[e] = pk2(t0, t1); }
        ob[0][hd] = (u32x4){w[0], w[1], w[2], w[3]};
        st128c(ob[0][hd], wsr, o_off + hd * 1024);
        cs = sum4(cs, ob[0][hd]);
      };
      num(0); num(1); PIN(y[0]); PIN(y[1]); SCHED();
      num(2); fin(0); SGB(0x8, 1); SGB(0x402, 6); SGB(0x8, 1); PIN(y[2]); SCHED();
      num(3); fin(1); SGB(0x8, 1); SGB(0x402, 6); SGB(0x8, 1); PIN(y[3]); SCHED();
      fin(2); fin(3);
      st32c(key - cs, wsr, c_off);
    }
    SCHED();
    if (TIMER == 0) BARE();
    STAMP(7);
    u32x4 wo[32][2];
    auto ldwo = [&](int n) {
      wo[n][0] = lds_tr2(woa + n * 2048, woa + n * 2048 + 512);
      wo[n][1] = lds_tr2(wob + n * 2048, wob + n * 2048 + 512);
    };
#pragma unroll
    for (int n = 0; n < 16; ++n) ldwo(n);
    f32x16 c0 = zero, c1 = zero;
#pragma unroll
    for (int s = 0; s < 4; ++s) { c0 = mfma(wo[s][0], ob[0][s], c0); c1 = mfma(wo[s][1], ob[0][s], c1); }
#pragma unroll
    for (int k = 4; k < 16; ++k) { PIN(wo[k][0]); PIN(wo[k][1]); }
    PIN(c0); PIN(c1);
    if (TIMER) WAITV(0);
    BARE();
    SCHED();
#pragma unroll
    for (int n = 16; n < 24; ++n) ldwo(n);
#pragma unroll
    for (int k = 16; k < 24; ++k) { PIN(wo[k][0]); PIN(wo[k][1]); }
    const __amdgpu_buffer_rsrc_t outr = __builtin_amdgcn_make_buffer_rsrc(KPTR(kb, 0), 0, 2048 * 1024 * 2, SRD3);
    u32 oc = oa - 256 - lane * 12, l4 = lane * 4, l16 = lane * 16, oa4 = oa + 4 * (4 * OREC), oc4 = oc + 4 * (4 * OREC);
    u32 sw1 = lds0 + t * 4096 + r * 128 + 16 * (h ^ (r & 7)), sr1 = lds0 + t * 4096 + l3 * 128 + 16 * (l7 ^ l3);
    u32 sw2 = sw1 ^ 32, sw3 = sw1 ^ 64, sw4 = sw1 ^ 96, or1 = orow + 16384, or2 = orow + 32768, or3 = orow + 49152;
    PIN(oc); PIN(l4); PIN(l16); PIN(sw1); PIN(sr1); PIN(oa4); PIN(oc4); PIN(sw2); PIN(sw3); PIN(sw4); PIN(or1); PIN(or2); PIN(or3);
    STAMP(8);
    u32 nretry = 0;
    u32 chk[2];
    auto oread = [&](int j) {
      const u32 da = j % 5 == 4 ? oa4 : oa + (j % 5) * (4 * OREC), dc = j % 5 == 4 ? oc4 : oc + (j % 5) * (4 * OREC);
#pragma unroll
      for (int s = 0; s < 4; ++s) ob[j & 1][s] = lds_r128(da + s * 1024);
      chk[j & 1] = *(LAS u32*)(size_t)dc;
    };
    auto obad = [&](int j) -> bool {
      const u32 c = sum4(sum4(chk[j & 1], ob[j & 1][0]), ob[j & 1][1]) + sum4(sum4(0u, ob[j & 1][2]), ob[j & 1][3]);
      return __builtin_amdgcn_ballot_w64(c != key) != 0;
    };
#pragma unroll
    for (int j = 1; j < 8; ++j) {
      BARE();
      SCHED();
      oread(j);
      if (j >= 2) {
#pragma unroll
        for (int s = 0; s < 4; ++s) {
          const int k = 4 * (j - 1) + s;
          c0 = mfma(wo[k][0], ob[(j - 1) & 1][s], c0);
          c1 = mfma(wo[k][1], ob[(j - 1) & 1][s], c1);
        }
        if (j >= 3 && j <= 6) { ldwo(24 + 2 * (j - 3)); ldwo(25 + 2 * (j - 3)); }
      }
      const bool bad = obad(j);
      if (j >= 3 && j <= 6) {
        SGB(0x8, 1); SGB(0x100, 3); SGB(0x8, 1); SGB(0x100, 2); SGB(0x8, 1); SGB(0x100, 3); SGB(0x8, 1); SGB(0x100, 3);
        SGB(0x8, 1); SGB(0x2, 4); SGB(0x8, 1); SGB(0x2, 5); SGB(0x8, 1); SGB(0x100, 2); SGB(0x8, 1);
      } else if (j >= 2) {
        SGB(0x8, 1); SGB(0x100, 3); SGB(0x8, 1); SGB(0x100, 2); SGB(0x8, 3); SGB(0x2, 4); SGB(0x8, 1); SGB(0x2, 5); SGB(0x8, 2);
      }
      SCHED();
      if (__builtin_expect(bad, 0)) {
#pragma unroll 1
        for (u32 spin = 0; spin < SPIN_CAP; ++spin) {
          dma_orec(lds0 + L_O + t * OREC + (j % 5) * (4 * OREC), l4, l16, wsr, WS_O + ((br * 64 + panel * 4 + t) * 8 + ochunk(j)) * OREC);
#pragma unroll 1
          for (u32 jj = j + 1; jj <= (j + AHEAD < 7 ? j + AHEAD : 7); ++jj) {
            const u32 sl = (jj >= 5 ? jj - 5 : jj) * (4 * OREC);
            u32 c = *(LAS u32*)(size_t)(oc + sl);
#pragma unroll
            for (int s = 0; s < 4; ++s) c = sum4(c, lds_r128(oa + sl + s * 1024));
            if (__builtin_amdgcn_ballot_w64(c != key) != 0)
              dma_orec(lds0 + L_O + t * OREC + sl, l4, l16, wsr, WS_O + ((br * 64 + panel * 4 + t) * 8 + ochunk(jj)) * OREC);
          }
          WAITV(0);
          oread(j);
          ++nretry;
          if (!obad(j)) break;
        }
      }
    }
#pragma unroll
    for (int s = 0; s < 4; ++s) c0 = mfma(wo[28 + s][0], ob[1][s], c0);
    PIN(c0);
    SCHED();
#pragma unroll
    for (int s = 0; s < 4; ++s) c1 = mfma(wo[28 + s][1], ob[1][s], c1);
    lds_w128(sw1, pack8(c0, 0));
    lds_w128(sw2, pack8(c0, 1));
    SGB(0x8, 2); SGB(0x2, 4); SGB(0x200, 1); SGB(0x8, 1); SGB(0x2, 4); SGB(0x200, 1); SGB(0x8, 1);
    SCHED();
    STAMP(9);
    lds_w128(sw3, pack8(c1, 0));
    lds_w128(sw4, pack8(c1, 1));
    asm volatile("" ::: "memory");
    {
      u32x4 v[4];
#pragma unroll
      for (int i = 0; i < 4; ++i) v[i] = lds_r128(sr1 + i * 1024);
      SCHED();
      st128c(v[0], outr, orow);
      st128c(v[1], outr, or1);
      st128c(v[2], outr, or2);
      st128c(v[3], outr, or3);
    }
    if (t == 0 && panel == 0 && grp == 0 && lane == 0) st32c(key, wsr, WS_EPOCH + br * 256);
    if (PROBE_N == 13) WAITV(0);
    STAMP(13);
    STAMP_OUT();
  }
}
}

extern "C" void kernel_launch(void* const* d_in, const int* in_sizes, int n_in,
                              void* d_out, int out_size, void* d_ws, size_t ws_size,
                              hipStream_t stream) {
  hh::hedgehog<<<256, 512, 0, stream>>>((const u16*)d_in[0], (const u16*)d_in[1], (const u16*)d_in[2], (const u16*)d_in[3],
                                        (const u16*)d_in[4], (const u16*)d_in[5], (const u16*)d_in[6], (const u16*)d_in[7],
                                        (u16*)d_out, (u32*)((char*)d_ws + ((size_t)WSOFF << 20)));
}
```

```cpp
#define ROTMODE 134
#define PROBE_N 12
#define PROBE_ROLE 0
#include <hip/hip_runtime.h>
#include <stdint.h>

typedef unsigned short u16;
typedef unsigned u32;
typedef short bf16x8 __attribute__((ext_vector_type(8)));
typedef short s16x4 __attribute__((ext_vector_type(4)));
typedef float f32x16 __attribute__((ext_vector_type(16)));
typedef float f32x4 __attribute__((ext_vector_type(4)));
typedef float f32x2 __attribute__((ext_vector_type(2)));
typedef unsigned u32x8 __attribute__((ext_vector_type(8)));
typedef unsigned u32x4 __attribute__((ext_vector_type(4)));
typedef unsigned u32x2 __attribute__((ext_vector_type(2)));
typedef __bf16 bf16x2 __attribute__((ext_vector_type(2)));
#define DI __device__ __forceinline__
#define LAS __attribute__((address_space(3)))
#define CAS __attribute__((address_space(4)))
#define BC(T, x) __builtin_bit_cast(T, x)

#define DELAY1 0
#define DELAY3 3
#define CODE_PF 27648
#define AHEAD 2
#define WSOFF 0
#define GQ 4
#define GC 2
#define WFIRST 0
#define FOLLOW 0
#define TIMER 2
#define LTO 1
#define NTX 0
#define NTW 0
#define WQSWAP (ROTMODE >= 7)
#define CSH 0
#define H3 0
#define LTQ 0

namespace hh {
constexpr int AUX_SC1 = 16;
constexpr u32 SRD3 = 0x00020000u;
constexpr u32 WS_EPOCH = 0;
constexpr u32 REC = 1280;
constexpr u32 WS_PART = 4096;
constexpr u32 OREC = 4352;
constexpr u32 WS_O = WS_PART + 2 * 32 * 16 * REC;
constexpr u32 WS_END = WS_O + 2 * 64 * 8 * OREC;
constexpr u32 WS_STAMP = 8u << 20;
constexpr u32 SLOT = 32768;
constexpr u32 L_WQ0 = 98304;
constexpr u32 L_WQ1 = 65536;
constexpr u32 L_WPHI = 163840 - 4096;
constexpr u32 L_PHI = 0;
constexpr u32 VF = 1040;
constexpr u32 L_V = 131072;
constexpr u32 L_KV = L_V + 16 * VF;
constexpr u32 L_WO = 0;
constexpr u32 L_O = 65536;
constexpr u32 SPIN_CAP = 1u << 16;

DI u32 pk2(float a, float b) { return BC(u32, __builtin_convertvector((f32x2){a, b}, bf16x2)); }
DI u32x4 pack8(const f32x16& x, int s) {
  return (u32x4){pk2(x[8 * s], x[8 * s + 1]), pk2(x[8 * s + 2], x[8 * s + 3]), pk2(x[8 * s + 4], x[8 * s + 5]), pk2(x[8 * s + 6], x[8 * s + 7])};
}
DI f32x16 mfma(u32x4 a, u32x4 b, f32x16 c) { return __builtin_amdgcn_mfma_f32_32x32x16_bf16(BC(bf16x8, a), BC(bf16x8, b), c, 0, 0, 0); }
DI f32x4 mfma16(u32x4 a, u32x4 b, f32x4 c) { return __builtin_amdgcn_mfma_f32_16x16x32_bf16(BC(bf16x8, a), BC(bf16x8, b), c, 0, 0, 0); }
DI u32x4 lds_r128(u32 a) { return *(LAS u32x4*)(size_t)a; }
DI void lds_w128(u32 a, u32x4 v) { *(LAS u32x4*)(size_t)a = v; }
DI u32x4 lds_tr2(u32 a0, u32 a1) {
  s16x4 lo = __builtin_amdgcn_ds_read_tr16_b64_v4i16((LAS s16x4*)(size_t)a0);
  s16x4 hi = __builtin_amdgcn_ds_read_tr16_b64_v4i16((LAS s16x4*)(size_t)a1);
  return BC(u32x4, __builtin_shufflevector(lo, hi, 0, 1, 2, 3, 4, 5, 6, 7));
}
DI void dma1(u32 lds, u32 voff, __amdgpu_buffer_rsrc_t srd, u32 soff) {
  asm volatile("s_mov_b32 m0, %0\n\ts_nop 0\n\tbuffer_load_dwordx4 %1, %2, %3 offen lds" ::"s"(lds), "v"(voff), "s"(srd), "s"(soff) : "memory");
}
DI void dma4(u32 lds, const u32 (&v)[4], __amdgpu_buffer_rsrc_t srd, u32 soff) {
  asm volatile("s_mov_b32 m0, %0\n\ts_nop 0\n\t"
               "buffer_load_dwordx4 %1, %5, %6 offen lds\n\t"
               "buffer_load_dwordx4 %2, %5, %6 offen offset:1024 lds\n\t"
               "buffer_load_dwordx4 %3, %5, %6 offen offset:2048 lds\n\t"
               "buffer_load_dwordx4 %4, %5, %6 offen offset:3072 lds" ::"s"(lds), "v"(v[0]), "v"(v[1]), "v"(v[2]), "v"(v[3]), "s"(srd), "s"(soff) : "memory");
}
#define DMA4P(name, POL)                                                                       \
  DI void name(u32 lds, const u32 (&v)[4], __amdgpu_buffer_rsrc_t srd, u32 soff) {              \
    asm volatile("s_mov_b32 m0, %0\n\ts_nop 0\n\t"                                             \
                 "buffer_load_dwordx4 %1, %5, %6 offen " POL " lds\n\t"                        \
                 "buffer_load_dwordx4 %2, %5, %6 offen offset:1024 " POL " lds\n\t"            \
                 "buffer_load_dwordx4 %3, %5, %6 offen offset:2048 " POL " lds\n\t"            \
                 "buffer_load_dwordx4 %4, %5, %6 offen offset:3072 " POL " lds" ::"s"(lds), "v"(v[0]), "v"(v[1]), "v"(v[2]), "v"(v[3]), "s"(srd), "s"(soff) : "memory"); \
  }
DMA4P(dma4nt, "nt")
DI void dma_orec(u32 lds, u32 l4, u32 l16, __amdgpu_buffer_rsrc_t srd, u32 soff) {
  asm volatile("s_mov_b32 m0, %0\n\ts_nop 0\n\t"
               "buffer_load_dwordx4 %2, %3, %4 offen offset:256 sc1 lds\n\t"
               "buffer_load_dwordx4 %2, %3, %4 offen offset:1280 sc1 lds\n\t"
               "buffer_load_dwordx4 %2, %3, %4 offen offset:2304 sc1 lds\n\t"
               "buffer_load_dwordx4 %2, %3, %4 offen offset:3328 sc1 lds\n\t"
               "buffer_load_dword %1, %3, %4 offen sc1 lds" ::"s"(lds), "v"(l4), "v"(l16), "s"(srd), "s"(soff) : "memory");
}
#define TOUCH(d, voff, srd, soff) asm volatile("buffer_load_dword %0, %1, %2, %3 offen" : "=v"(d) : "v"(voff), "s"(srd), "s"(soff) : "memory")
DI void st128c(u32x4 v, __amdgpu_buffer_rsrc_t srd, u32 off) { __builtin_amdgcn_raw_buffer_store_b128(v, srd, off, 0, AUX_SC1); }
DI void st32c(u32 v, __amdgpu_buffer_rsrc_t srd, u32 off) { __builtin_amdgcn_raw_buffer_store_b32(v, srd, off, 0, AUX_SC1); }
#define BAR() asm volatile("s_waitcnt lgkmcnt(0)\n\ts_barrier" ::: "memory")
#define BARE() asm volatile("s_barrier" ::: "memory")
#define OPQ(x) asm("" : "+v"(x))
#define PIN(x) asm volatile("" : "+v"(x))
#define PINS(x) asm volatile("" : "+s"(x))
#define WAITV(n) asm volatile("s_waitcnt vmcnt(" #n ")" ::: "memory")
#define SCHED() __builtin_amdgcn_sched_barrier(0)
#define SGB(m, n) __builtin_amdgcn_sched_group_barrier(m, n, 0)
DI u32 sum4(u32 c, u32x4 v) { return (c + v.x + v.y) + v.z + v.w; }
DI u32 pair_sum(u32 s) { auto t = __builtin_amdgcn_permlane32_swap(s, s, false, false); return t[0] + t[1]; }

constexpr float LOG2E = 1.44269504088896340736f;
DI float max3f(float a, float b, float c) { return __builtin_fmaxf(__builtin_fmaxf(a, b), c); }
DI float negmax16(const f32x16& x) {
  const float a = max3f(x[0], x[1], x[2]), b = max3f(x[3], x[4], x[5]), c = max3f(x[6], x[7], x[8]);
  const float d = max3f(x[9], x[10], x[11]), e = max3f(x[12], x[13], x[14]);
  return -max3f(max3f(a, b, x[15]), max3f(c, d, e), a) * LOG2E;
}
DI void sm_exp(const f32x16& p, const f32x16& q, const float (&mc)[2], f32x2 (&e)[2][8]) {
#pragma unroll
  for (int i = 0; i < 8; ++i) {
    const f32x2 t = __builtin_elementwise_fma((f32x2){p[2 * i], p[2 * i + 1]}, (f32x2){LOG2E, LOG2E}, (f32x2){mc[0], mc[0]});
    const f32x2 u = __builtin_elementwise_fma((f32x2){q[2 * i], q[2 * i + 1]}, (f32x2){LOG2E, LOG2E}, (f32x2){mc[1], mc[1]});
    e[0][i] = (f32x2){__builtin_amdgcn_exp2f(t.x), __builtin_amdgcn_exp2f(t.y)};
    e[1][i] = (f32x2){__builtin_amdgcn_exp2f(u.x), __builtin_amdgcn_exp2f(u.y)};
  }
  SGB(0x2, 4); SGB(0x400, 8); SGB(0x2, 4); SGB(0x400, 8); SGB(0x2, 6); SGB(0x400, 8); SGB(0x2, 8); SGB(0x400, 8);
}
DI void sm_exp_s(const f32x16& p, const f32x16& q, const float (&mc)[2], f32x2 (&e)[2][8]) {
#pragma unroll
  for (int i = 0; i < 16; ++i) {
    e[0][i >> 1][i & 1] = __builtin_amdgcn_exp2f(__builtin_fmaf(p[i], LOG2E, mc[0]));
    e[1][i >> 1][i & 1] = __builtin_amdgcn_exp2f(__builtin_fmaf(q[i], LOG2E, mc[1]));
  }
}
DI void sm_finish(const f32x2 (&e)[2][8], u32x4 (&o)[2][2]) {
  float r[2];
#pragma unroll
  for (int u = 0; u < 2; ++u) {
    const f32x2 s = ((e[u][0] + e[u][1]) + (e[u][2] + e[u][3])) + ((e[u][4] + e[u][5]) + (e[u][6] + e[u][7]));
    r[u] = __builtin_amdgcn_rcpf(s.x + s.y);
  }
#pragma unroll
  for (int u = 0; u < 2; ++u) {
    u32 w[8];
#pragma unroll
    for (int i = 0; i < 8; ++i) { const f32x2 t = e[u][i] * (f32x2){r[u], r[u]}; w[i] = pk2(t.x, t.y); }
    o[u][0] = (u32x4){w[0], w[1], w[2], w[3]};
    o[u][1] = (u32x4){w[4], w[5], w[6], w[7]};
  }
}
DI void softmax16x2(const f32x16& p, const f32x16& q, const float (&mc)[2], u32x4 (&o)[2][2]) {
  f32x2 e[2][8];
  sm_exp(p, q, mc, e);
  sm_finish(e, o);
}

#define STAMP(i) do { if ((i) == PROBE_N) { SCHED(); asm volatile("s_memrealtime %0\n\ts_waitcnt lgkmcnt(0)" : "=s"(pt1) :: "memory"); SCHED(); } } while (0)
#define STAMP_OUT() do { if (PROBE_N && (wave >> 2) == PROBE_ROLE) { unsigned long long d_ = pt1 - pt0; if (d_ > 100000) d_ = 100000; unsigned long long e_ = pt0 + 3000 + 8 * d_; \
  for (int it_ = 0; it_ < 1000000; ++it_) { unsigned long long t_; asm volatile("s_memrealtime %0\n\ts_waitcnt lgkmcnt(0)" : "=s"(t_) :: "memory"); if (t_ >= e_) break; } } } while (0)

__global__ __launch_bounds__(512, 2) void hedgehog(const u16* xA, const u16* wA, const u16* pA, const u16* oA,
                                                   const u16* xB, const u16* wB, const u16* pB, const u16* oB,
                                                   u16* out_, u32* ws_) {
  __shared__ __attribute__((aligned(16))) char smem[163840];
  const u32 lds0 = (u32)(size_t)(LAS char*)smem;
  unsigned long long pt0 = __builtin_amdgcn_s_memrealtime(), pt1 = pt0;
  const int bid = blockIdx.x, xcd = bid & 7, idx = bid >> 3;
  const int br = xcd >> 2;
#define KLOAD(tail)                                                                                       \
  u32x8 ka;                                                                                               \
  u32x4 kb;                                                                                               \
  asm volatile("s_load_dwordx8 %0, %2, %3\n\ts_load_dwordx4 %1, %2, 0x40" tail : "=&s"(ka), "=&s"(kb)     \
               : "s"(__builtin_amdgcn_kernarg_segment_ptr()), "s"(br * 32))
#define KWAIT() asm volatile("s_waitcnt lgkmcnt(0)" : "+s"(ka), "+s"(kb))
#define KPTR(v, i) ((void*)(((unsigned long long)(v)[(i) + 1] << 32) | (v)[i]))
  unsigned long long pcs;
  asm volatile("s_getpc_b64 %0" : "=s"(pcs));
  pcs &= ~255ull;
  const int lane = threadIdx.x & 63;
  const int wave = __builtin_amdgcn_readfirstlane(threadIdx.x >> 6);
  const int panel = (xcd & 3) * 4 + (idx & 3), grp = idx >> 2, gq = (grp + GQ) & 7, cg = (grp + GC) & 7;
  auto ochunk = [&](u32 j) -> u32 {
    const int v = (int)j + (((int)j - 1 - gq) >> 31), z = ((int)j - 1) >> 31;
    return (u32)__builtin_amdgcn_readfirstlane((v & ~z) | (gq & z));
  };
  f32x16 zero;
#pragma unroll
  for (int i = 0; i < 16; ++i) zero[i] = 0.f;
#define LANE_IDS()                                                                            \
  u32 lane_ = lane;                                                                           \
  PIN(lane_);                                                                                 \
  const u32 r = lane_ & 31, h = lane_ >> 5;                                                   \
  const u32 blk = (lane_ >> 4) & 1, q = (lane_ & 15) >> 2, p = lane_ & 3;                     \
  u32* ws = (u32*)KPTR(kb, 2);                                                                \
  const __amdgpu_buffer_rsrc_t wsr = __builtin_amdgcn_make_buffer_rsrc(ws, 0, WS_END, SRD3);  \
  const __amdgpu_buffer_rsrc_t wor = __builtin_amdgcn_make_buffer_rsrc(KPTR(ka, 6), 0, 512 * 512 * 2, SRD3)

  if (wave >= 4) {
    const int cw = wave - 4;
    const int rh = cw >> 1, hp = cw & 1;
    KLOAD("\n\ts_waitcnt lgkmcnt(0)");
    u32 sp0, sp1, sp2, sp3;
    {
      u32 off = idx * 1024 + cw * 256;
      off = off < CODE_PF ? off : 0;
      asm volatile("s_load_dword %0, %4, %5 offset:0\n\ts_load_dword %1, %4, %5 offset:64\n\ts_load_dword %2, %4, %5 offset:128\n\ts_load_dword %3, %4, %5 offset:192"
                   : "=&s"(sp0), "=&s"(sp1), "=&s"(sp2), "=&s"(sp3) : "s"(pcs), "s"(off));
    }
    LANE_IDS();
    u32 key = *(const CAS u32*)((const char*)ws + WS_EPOCH + br * 256) + 1u;
    u32 xa[2], xb[2], wa[2], wb2[2];
    {
      const u32 r16 = lane_ & 15, g = lane_ >> 4;
#pragma unroll
      for (int s = 0; s < 2; ++s) {
        xa[s] = lds0 + (64 * rh + r16) * 128 + 16 * ((4 * s + g) ^ (r16 >> 1));
        wa[s] = lds0 + 16384 + (8 * g + q) * 128 + 16 * ((4 * hp + 2 * s + (p >> 1)) ^ ((((q >> 1) & 1) << 2) | (blk << 1))) + 8 * (p & 1);
        xb[s] = xa[s] + 2 * SLOT; wb2[s] = wa[s] + 2 * SLOT;
        PIN(xa[s]); PIN(xb[s]); PIN(wa[s]); PIN(wb2[s]);
      }
    }
    u32 fpa = lds0 + L_WPHI + hp * 1024 + (8 * h + q) * 32 + (8 * blk + 4 * (p >> 1)) * 2;
    PIN(fpa);
    PINS(key);
    f32x4 ak[4][2], av[4][2];
#pragma unroll
    for (int i = 0; i < 8; ++i) ak[i >> 1][i & 1] = av[i >> 1][i & 1] = (f32x4){0.f, 0.f, 0.f, 0.f};
    u32 vra, pra, vwa, pw0, pw1, pub16, pub4;
    u32x4 xf[2][4], wk[2][2], wv[2][2];
    auto ldx = [&](int c, int s) {
      const u32 x = c % 3 == 2 ? xb[s] : xa[s] + (c % 3) * SLOT;
#pragma unroll
      for (int i = 0; i < 4; ++i) xf[s][i] = lds_r128(x + i * 2048);
    };
    auto ldw = [&](int c, int s, int m) {
#pragma unroll
      for (int n = 0; n < 2; ++n) {
        const u32 w = (c % 3 == 2 ? wb2[n] : wa[n] + (c % 3) * SLOT) + s * 4096 + m * 8192;
        (m ? wv : wk)[s][n] = lds_tr2(w, w + 512);
      }
    };
    auto mk = [&](int s) {
#pragma unroll
      for (int i = 0; i < 8; ++i) ak[i >> 1][i & 1] = mfma16(wk[s][i & 1], xf[s][i >> 1], ak[i >> 1][i & 1]);
    };
    auto mv = [&](int s) {
#pragma unroll
      for (int i = 0; i < 8; ++i) av[i >> 1][i & 1] = mfma16(xf[s][i >> 1], wv[s][i & 1], av[i >> 1][i & 1]);
    };
    auto mix = [&](int n) {
#pragma unroll
      for (int i = 0; i < 8; ++i) {
        SGB(0x8, 1);
        if (n == 12) { if (i < 4) SGB(0x100, 2); else SGB(0x100, 1); }
        else if (n == 8) SGB(0x100, 1);
        else if (n == 4) { if (i < 4) SGB(0x100, 1); }
        else if (n < 0) SGB(0x6, 2);
      }
      SCHED();
    };
    BAR();
    asm volatile("" ::"s"(sp0), "s"(sp1), "s"(sp2), "s"(sp3));
    STAMP(1);
    ldw(0, 0, 0); ldx(0, 0); ldw(0, 0, 1);
    u32x4 fa[2];
#pragma unroll
    for (int hh = 0; hh < 2; ++hh) fa[hh] = lds_tr2(fpa + hh * 512, fpa + hh * 512 + 128);
    SCHED();
#pragma unroll
    for (int c = 0; c < 7; ++c) {
      ldw(c, 1, 0); ldx(c, 1); mk(0); mix(8);
      ldw(c, 1, 1); mv(0); mix(4);
      u32 ln = lane;
      PIN(ln);
      const u32 r_ = ln & 31, h_ = ln >> 5, q_ = (ln & 15) >> 2;
      if (c == 1) {
        const u32 sg = (ln & 4) ? 0x80008000u : 0u;
#pragma unroll
        for (int hh = 0; hh < 2; ++hh) { OPQ(fa[hh]); fa[hh] ^= (u32x4){sg, sg, sg, sg}; }
      }
      if (c == 2) vra = lds0 + L_V + (cw >> 1) * (2 * VF) + (r_ < 16 ? (ln + 16 * (cw & 1)) * 16 : 1024);
      if (c == 3) pra = lds0 + L_PHI + cw * 8192 + (4 * h_ + q_) * 64 + 16 * ((((ln >> 3) & 2) + ((ln >> 1) & 1)) ^ ((2 * h_ + (q_ >> 1)) & 3)) + 8 * (ln & 1);
      if (c == 4) {
        const u32 rec = WS_PART + ((br * 32 + grp * 4 + cw) * 16 + panel) * REC;
        pub16 = rec + ln * 16; pub4 = rec + 1024 + ln * 4;
      }
      if (c == 5) { pw0 = lds0 + L_PHI + (2 * hp) * 8192 + (64 * rh + r_) * 64 + 16 * ((2 * h_) ^ ((r_ >> 1) & 3)); pw1 = pw0 ^ 16; }
      if (c == 6) vwa = lds0 + L_V + (8 * rh + 2 * hp) * VF + ((ln & 16) * 2 + (ln & 15)) * 16 + h_ * 8;
      mk(1); mix(c >= 1 ? -1 : 0);
      if (c == 1) { PIN(fa[0]); PIN(fa[1]); }
      if (c == 2) PIN(vra);
      if (c == 3) PIN(pra);
      if (c == 4) { PIN(pub16); PIN(pub4); }
      if (c == 5) { PIN(pw0); PIN(pw1); }
      if (c == 6) PIN(vwa);
      BAR();
      ldw(c + 1, 0, 0); ldx(c + 1, 0); ldw(c + 1, 0, 1);
      mv(1); mix(12);
    }
    ldw(7, 1, 0); ldx(7, 1); ldw(7, 1, 1); mk(0); mix(12);
    mk(1); mix(0);
    __builtin_amdgcn_s_setprio(2);
    STAMP(2);
    f32x16 pk[2][2];
    {
      u32x4 kf[2][2];
      auto kpack = [&](int i) {
#pragma unroll
        for (int hh = 0; hh < 2; ++hh) {
          const f32x4 &t0 = ak[2 * i][hh], &t1 = ak[2 * i + 1][hh];
          const auto lo = __builtin_amdgcn_permlane16_swap(pk2(t0[0], t0[1]), pk2(t1[0], t1[1]), false, false);
          const auto hi = __builtin_amdgcn_permlane16_swap(pk2(t0[2], t0[3]), pk2(t1[2], t1[3]), false, false);
          kf[i][hh] = (u32x4){lo[0], hi[0], lo[1], hi[1]};
        }
      };
      mv(0); kpack(0);
      SGB(0x8, 2);
#pragma unroll
      for (int i = 0; i < 6; ++i) { SGB(0x2, 2); SGB(0x8, 1); }
      SCHED();
      pk[0][0] = mfma(fa[0], kf[0][0], zero);
      pk[0][1] = mfma(fa[1], kf[0][1], zero);
      kpack(1);
      SGB(0x8, 1); SGB(0x2, 6); SGB(0x8, 1); SGB(0x2, 6);
      PIN(pk[0][0]); PIN(pk[0][1]); PIN(kf[1][0]); PIN(kf[1][1]);
      SCHED();
      mv(1);
      pk[1][0] = mfma(fa[0], kf[1][0], zero);
      pk[1][1] = mfma(fa[1], kf[1][1], zero);
    }
    float m0[2], m1[2];
    m0[0] = negmax16(pk[0][0]); m0[1] = negmax16(pk[0][1]);
    SGB(0x8, 2);
#pragma unroll
    for (int i = 0; i < 6; ++i) { SGB(0x2, 2); SGB(0x8, 1); }
    SGB(0x8, 1); SGB(0x2, 6); SGB(0x8, 1);
#pragma unroll
    for (int i = 0; i < 8; ++i) PIN(av[i >> 1][i & 1]);
    PIN(pk[1][0]); PIN(pk[1][1]); PIN(m0[0]); PIN(m0[1]);
    SCHED();
#pragma unroll
    for (int i = 0; i < 8; ++i) {
      const f32x4& t = av[i >> 1][i & 1];
      *(LAS u32x2*)(size_t)(vwa + (4 * (i >> 2) + ((i >> 1) & 1)) * VF + (i & 1) * 256) = (u32x2){pk2(t[0], t[1]), pk2(t[2], t[3])};
    }
    SCHED();
    {
      u32x4 o[2][2];
      softmax16x2(pk[0][0], pk[0][1], m0, o);
      lds_w128(pw0, o[0][0]); lds_w128(pw1, o[0][1]);
      lds_w128(pw0 + 8192, o[1][0]); lds_w128(pw1 + 8192, o[1][1]);
      SCHED();
      m1[0] = negmax16(pk[1][0]); m1[1] = negmax16(pk[1][1]);
      PIN(m1[0]); PIN(m1[1]);
      SCHED();
      softmax16x2(pk[1][0], pk[1][1], m1, o);
      lds_w128(pw0 + 2048, o[0][0]); lds_w128(pw1 + 2048, o[0][1]);
      lds_w128(pw0 + 2048 + 8192, o[1][0]); lds_w128(pw1 + 2048 + 8192, o[1][1]);
      SGB(0x402, 26); SGB(0x200, 1); SGB(0x402, 8); SGB(0x200, 1); SGB(0x402, 8); SGB(0x200, 1); SGB(0x402, 8); SGB(0x200, 1);
      SCHED();
      BARE();
      BAR();
      SCHED();
    }
    STAMP(3);
    f32x16 kv = zero;
    {
      u32x4 a[8], b[8];
#pragma unroll
      for (int u = 0; u < 8; ++u) {
        a[u] = lds_r128(vra + ((u >> 1) * 4 + (u & 1)) * VF);
        b[u] = lds_tr2(pra + 16 * u * 64, pra + (16 * u + 8) * 64);
      }
#pragma unroll
      for (int u = 0; u < 8; ++u) kv = mfma(a[u], b[u], kv);
      SGB(0x100, 12); SGB(0x8, 1); SGB(0x100, 6); SGB(0x8, 1); SGB(0x100, 6); SGB(0x8, 6);
    }
    {
      const u32x4 w = pack8(kv, 0);
      st128c(w, wsr, pub16);
      const u32 zb = __float_as_uint(kv[8]);
      const u32 chk = key - pair_sum(sum4(h ? 0u : zb, w));
      st32c(h ? chk : zb, wsr, pub4);
    }
    SCHED();
    STAMP(4);
    u32 l16, l4, kwa, kza, gso[4], vo[4], wso;
    u32x4 sel;
    {
      u32 ln = lane;
      PIN(ln);
#pragma unroll
      for (int i = 0; i < 4; ++i) vo[i] = (ln >> 3) * 1024 + 16 * ((ln & 7) ^ (((ln >> 4) & 1) << 2)) + i * 7168;
#pragma unroll
      for (int i = 0; i < 2; ++i) dma4(lds0 + L_WO + cw * 8192 + i * 4096, vo, wor, ochunk(cw) * 65536 + i * 32768 + cg * 128);
      wso = ochunk(4 + cw) * 65536 + cg * 128;
      if (TIMER == 1) {
#pragma unroll
        for (int i = 0; i < 2; ++i) dma4(lds0 + L_WO + 32768 + cw * 8192 + i * 4096, vo, wor, wso + i * 32768);
      }
      const u32 r_ = ln & 31, h_ = ln >> 5;
      l16 = ln * 16; l4 = ln * 4;
      kwa = lds0 + L_KV + cw * 1536 + r_ * 48 + h_ * 16; kza = lds0 + L_KV + cw * 1536 + r_ * 48 + 32 + h_ * 8;
      const bool hit = r_ < 16 && ((r_ >> 2) & 1) == h_;
      const u32 j = (r_ & 3) + 4 * (r_ >> 3);
#pragma unroll
      for (int e = 0; e < 4; ++e) sel[e] = (hit && (j >> 1) == e) ? ((j & 1) ? 0x3F800000u : 0x00003F80u) : 0u;
#pragma unroll
      for (int i = 0; i < 4; ++i) { gso[i] = WS_PART + ((br * 32 + gq * 4 + cw) * 16 + ((i + idx) & 3) * 4) * REC; PINS(gso[i]); }
      PIN(l16); PIN(l4); PIN(kwa); PIN(kza); PIN(sel); PINS(wso);
    }
    if (DELAY1) __builtin_amdgcn_s_sleep(DELAY1);
    STAMP(10);
    f32x16 gs[4];
    float z[4];
    {
      u32x4 g[16];
      u32 gz[16];
      auto gload = [&](int gi) {
#pragma unroll
        for (int i = 0; i < 4; ++i) {
          g[4 * gi + i] = __builtin_amdgcn_raw_buffer_load_b128(wsr, l16 + i * REC, gso[gi], AUX_SC1);
          gz[4 * gi + i] = __builtin_amdgcn_raw_buffer_load_b32(wsr, l4 + (1024 + i * REC - 1024 * (i == 3)), gso[gi] + 1024 * (i == 3), AUX_SC1);
          SCHED();
        }
      };
      auto gsum = [&](int gi) -> bool {
        u32 c = 0;
#pragma unroll
        for (int i = 0; i < 4; ++i) {
          c = sum4(c + gz[4 * gi + i], g[4 * gi + i]);
          gs[gi] = mfma(sel, g[4 * gi + i], i == 0 ? zero : gs[gi]);
          z[gi] = i == 0 ? __uint_as_float(gz[4 * gi]) : z[gi] + __uint_as_float(gz[4 * gi + i]);
        }
        PIN(z[gi]);
        const bool bad = __builtin_amdgcn_ballot_w64(pair_sum(c) != 4u * key) != 0;
        SCHED();
        return bad;
      };
      gload(0); gload(1); gload(2); gload(3);
      bool b0 = gsum(0), b1 = gsum(1), b2 = gsum(2), b3 = gsum(3);
      STAMP(11);
#pragma unroll 1
      for (u32 spin = 0; (b0 | b1 | b2 | b3) && spin < SPIN_CAP; ++spin) {
        asm volatile("" ::: "memory");
        if (b0) gload(0);
        if (b1) gload(1);
        if (b2) gload(2);
        if (b3) gload(3);
        if (b0) b0 = gsum(0);
        if (b1) b1 = gsum(1);
        if (b2) b2 = gsum(2);
        if (b3) b3 = gsum(3);
      }
    }
    __builtin_amdgcn_s_waitcnt(0x0F70);
    STAMP(5);
    {
      float tt[8];
#pragma unroll
      for (int j = 0; j < 8; ++j) tt[j] = (gs[0][j] + gs[1][j]) + (gs[2][j] + gs[3][j]);
      const float zt = (z[0] + z[1]) + (z[2] + z[3]);
      lds_w128(kwa, (u32x4){pk2(tt[0], tt[1]), pk2(tt[2], tt[3]), pk2(tt[4], tt[5]), pk2(tt[6], tt[7])});
      const u32 zz = pk2(zt, zt);
      *(LAS u32x2*)(size_t)kza = (u32x2){zz, zz};
    }
    if (TIMER == 2) {
#pragma unroll
      for (int i = 0; i < 2; ++i) dma4(lds0 + L_WO + 32768 + cw * 8192 + i * 4096, vo, wor, wso + i * 32768);
    }
    BAR();
    STAMP(6);
    if (TIMER == 3) {
#pragma unroll
      for (int i = 0; i < 2; ++i) dma4(lds0 + L_WO + 32768 + cw * 8192 + i * 4096, vo, wor, wso + i * 32768);
    }
    {
      u32 oso[8], ob = lds0 + L_O + cw * OREC;
#pragma unroll
      for (int i = 1; i < 8; ++i) { oso[i] = WS_O + ((br * 64 + panel * 4 + cw) * 8 + ochunk(i)) * OREC; PINS(oso[i]); }
      PINS(ob);
      if (TIMER == 0) {
        BARE();
        STAMP(7);
#pragma unroll
        for (int i = 0; i < 2; ++i) dma4(lds0 + L_WO + 32768 + cw * 8192 + i * 4096, vo, wor, wso + i * 32768);
      }
      WAITV(0);
      BARE();
      if (DELAY3) __builtin_amdgcn_s_sleep(DELAY3);
      STAMP(8);
      auto oissue = [&](int j) { dma_orec(ob + (j % 5) * (4 * OREC), l4, l16, wsr, oso[j]); };
      oissue(1); oissue(2);
      WAITV(5); BARE(); oissue(3);
      WAITV(5); STAMP(15); BARE(); oissue(4); oissue(5);
      WAITV(10); BARE(); oissue(6);
      WAITV(10); BARE(); oissue(7);
      WAITV(10); BARE();
      WAITV(5); BARE();
      WAITV(0); BARE();
    }
    STAMP(9);
    STAMP_OUT();
  } else {
    KLOAD("");
    const int t = wave;
    const u32 l7 = lane & 7, l3 = lane >> 3, lq = lane >> 4;
    const u32 wsw = 16 * (l7 ^ ((lq & 1) << 2));
    u32 vx[4], vw[4];
#pragma unroll
    for (int i = 0; i < 4; ++i) {
      vx[i] = l3 * 1024 + 16 * (l7 ^ (((i & 1) << 2) | lq)) + i * 7168;
      vw[i] = l3 * 3072 + (wsw ^ ((i & 1) << 5)) + i * (8 * 3072 - 1024);
    }
    int rot = (grp + 2 * (idx & 3)) & 7;
    auto chunk = [&](u32 c) -> u32 {
      constexpr u32 n0 = ROTMODE / 100, n1 = ROTMODE / 10 % 10, n2 = ROTMODE % 10;
      const u32 g = grp + (idx & 3), r3 = g - 3 * ((g * 11) >> 5);
      auto run = [&](u32 k, u32 n) -> u32 {
        if (n == 3) { const u32 v = k + r3; return v >= 3 ? v - 3 : v; }
        return k ^ (g & (n - 1));
      };
      const u32 a = run(c, n0), b = n0 + run(c - n0, n1), d = n0 + n1 + run(c - n0 - n1, n2);
      return ((c < n0 ? a : c < n0 + n1 ? b : d) + CSH) & 7;
    };
    const u32 xs0 = (panel * 128 + t * 32) * 1024, ws0 = (t & 1) * (32 * 3072) + (1 + (t >> 1)) * 1024 + grp * 128;
    const u32 ldst = lds0 + t * 4096;
    u32 e0 = ldst, e1 = ldst + 16384, e2 = xs0 + chunk(0) * 128, e3 = ws0 + chunk(0) * (64 * 3072);
    PINS(e0); PINS(e1); PINS(e2); PINS(e3);
#pragma unroll
    for (int i = 0; i < 4; ++i) { PIN(vx[i]); PIN(vw[i]); }
    KWAIT();
    const __amdgpu_buffer_rsrc_t xr = __builtin_amdgcn_make_buffer_rsrc(KPTR(ka, 0), 0, 2048 * 512 * 2, SRD3);
    const __amdgpu_buffer_rsrc_t wr = __builtin_amdgcn_make_buffer_rsrc(KPTR(ka, 2), 0, 512 * 1536 * 2, SRD3);
    STAMP(10);
    auto dx = [&](int c, u32 d, u32 so) { if (c < NTX) dma4nt(d, vx, xr, so); else dma4(d, vx, xr, so); };
    auto dw = [&](int c, u32 d, u32 so) { if (c < NTW) dma4nt(d, vw, wr, so); else dma4(d, vw, wr, so); };
    if (WFIRST & 1) { dw(0, e1, e3); dx(0, e0, e2); } else { dx(0, e0, e2); dw(0, e1, e3); }
    if (FOLLOW) {
      const int gi = (grp + (ka[2] >> 7) - 4) & 7;
      rot = (gi == 7 ? 0 : gi) + 2 * (idx & 3);
      rot = rot >= 7 ? rot - 7 : rot;
    }
    {
      const __amdgpu_buffer_rsrc_t pr = __builtin_amdgcn_make_buffer_rsrc(KPTR(ka, 4), 0, 32 * 256 * 2, SRD3);
      dma1(lds0 + L_WPHI + t * 1024, lane * 16, pr, (t < 2 ? grp : gq) * 2048 + (t & 1) * 1024);
    }
    STAMP(11);
    u32 th3 = 0, thq = 0, tho;
    if (H3) TOUCH(th3, (l7 >> 1) * 3072 + (l7 & 1) * 1024, wr, (idx * 16 + t * 4) * 3072 + 1024 + ((3 - (ka[2] >> 7)) & 7) * 128);
    if (WFIRST & 2) dw(1, ldst + SLOT + 16384, ws0 + chunk(1) * (64 * 3072));
    dx(1, ldst + SLOT, xs0 + chunk(1) * 128);
    if (!(WFIRST & 2)) dw(1, ldst + SLOT + 16384, ws0 + chunk(1) * (64 * 3072));
    STAMP(1);
    LANE_IDS();
    u32 key = *(const CAS u32*)((const char*)ws + WS_EPOCH + br * 256) + 1u;
    const u32 sw = (r >> 1) & 7;
    u32 xa[4];
#pragma unroll
    for (int s = 0; s < 4; ++s) { xa[s] = lds0 + (32 * t + r) * 128 + 16 * ((2 * s + h) ^ sw); PIN(xa[s]); }
    u32 fpa = lds0 + L_WPHI + 2048 + (4 * h + q) * 32 + (8 * blk + 4 * (p >> 1)) * 2;
    u32 vq = (r >> 3) * 3072 + (r & 7) * 128;
    PIN(fpa); PIN(vq);
    PINS(key);
    u32x4 xq[32], fa[4];
#pragma unroll
    for (int c = 0; c < 8; ++c) {
      u32 d0, d1, s0, s1;
      if (c < 6) {
        d0 = ldst + ((c + 2) % 3) * SLOT; d1 = d0 + 16384;
        s0 = xs0 + chunk(c + 2) * 128; s1 = ws0 + chunk(c + 2) * (64 * 3072);
      } else {
        const u32 half = (c - 6) ^ (WQSWAP ? (idx & 1) : 0);
        d0 = lds0 + L_WQ0 - half * (L_WQ0 - L_WQ1) + t * 8192; d1 = d0 + 4096;
        s0 = chunk(half * 4 + t) * (64 * 3072) + gq * 128; s1 = s0 + 32 * 3072;
      }
      PINS(d0); PINS(d1); PINS(s0); PINS(s1);
      if ((H3 && c == 0) || (LTQ && c == 7)) WAITV(9); else WAITV(8);
      if (H3 && c == 1) PIN(th3);
      STAMP(20 + c);
      BAR();
      if (LTQ && c == 6) TOUCH(thq, vq, wr, (idx * 16 + t * 4) * 3072);
      if (WFIRST & 4) dw(c + 2, d1, s1); else if (c < 6) dx(c + 2, d0, s0); else dw(c + 2, d0, s0);
#pragma unroll
      for (int s = 0; s < 4; ++s) xq[4 * c + s] = lds_r128(xa[s] + (c % 3) * SLOT);
      if (!(WFIRST & 4)) dw(c + 2, d1, s1); else if (c < 6) dx(c + 2, d0, s0); else dw(c + 2, d0, s0);
      if (c == 0) {
#pragma unroll
        for (int hd = 0; hd < 4; ++hd) fa[hd] = lds_tr2(fpa + hd * 512, fpa + hd * 512 + 256);
      }
    }
    if (LTO) TOUCH(tho, r * 128, wor, (idx * 16 + t * 4) * 1024);
    else TOUCH(tho, (r + 32 * t) * 1024, wor, (idx & 3) * 131072 + cg * 128);
    if (t == 0) lds_w128(lds0 + L_V + (lane & 15) * VF + 1024, (u32x4){0x3F803F80u, 0x3F803F80u, 0x3F803F80u, 0x3F803F80u});
    {
      const u32 sg = (lane & 4) ? 0x80008000u : 0u;
#pragma unroll
      for (int hd = 0; hd < 4; ++hd) { PIN(fa[hd]); fa[hd] ^= (u32x4){sg, sg, sg, sg}; PIN(fa[hd]); }
    }
    u32 wa = lds0 + L_WQ1 + (8 * h + q) * 128 + 16 * ((2 * blk + (p >> 1)) ^ ((((q >> 1) & 1) << 2) | (h << 1))) + 8 * (p & 1);
    u32 wb = wa ^ 64;
    u32 kra = lds0 + L_KV + (16 * h + q) * 48 + (blk ? 32 : 8 * p);
    u32 o_off = WS_O + ((br * 64 + panel * 4 + t) * 8 + gq) * OREC + 256 + lane * 16;
    u32 c_off = WS_O + ((br * 64 + panel * 4 + t) * 8 + gq) * OREC + lane * 4;
    u32 woa = lds0 + L_WO + (8 * h + q) * 128 + 16 * ((2 * blk + (p & 1)) ^ (((q >> 1) & 1) << 2)) + 8 * (p >> 1);
    u32 wob = woa ^ 64;
    u32 oa = lds0 + L_O + t * OREC + 256 + lane * 16;
    u32 orow = ((panel * 128 + t * 32 + l3) * 1024 + br * 512 + cg * 64 + 8 * l7) * 2;
    PIN(wa); PIN(wb); PIN(kra); PIN(o_off); PIN(c_off); PIN(woa); PIN(wob); PIN(oa); PIN(orow);
    STAMP(2);
    u32x4 fq[2][2][2];
    {
      f32x16 aq[2], pq[2];
      float mq[2];
      aq[0] = aq[1] = zero;
      u32x4 wf[4];
      auto ldw = [&](int n) {
        const int k = n & 31;
        const u32 o = (n < 32 ? wa : wb) + (k < 16 ? (L_WQ0 - L_WQ1) : 0) + (k & 15) * 2048;
        wf[n & 3] = lds_tr2(o, o + 512);
      };
      WAITV(1);
      BAR();
      SCHED();
      STAMP(12);
      ldw(0); ldw(1); ldw(2);
      SCHED();
      BARE();
      SCHED();
#pragma unroll
      for (int n = 0; n < 34; ++n) {
        ldw(n + 3);
        aq[n >> 5] = mfma(wf[n & 3], xq[n & 31], aq[n >> 5]);
        SGB(0x8, 1); SGB(0x100, 2);
        SCHED();
      }
#pragma unroll
      for (int hh = 0; hh < 2; ++hh) pq[hh] = mfma(fa[hh], pack8(aq[0], hh), zero);
      SCHED();
      STAMP(4);
#pragma unroll
      for (int n = 34; n < 64; ++n) {
        if (n + 3 < 64) ldw(n + 3);
        aq[1] = mfma(wf[n & 3], xq[n & 31], aq[1]);
      }
      mq[0] = negmax16(pq[0]); mq[1] = negmax16(pq[1]);
      f32x2 eq[2][8];
      sm_exp_s(pq[0], pq[1], mq, eq);
      SGB(0x8, 1); SGB(0x100, 2); SGB(0x8, 1); SGB(0x100, 2);
#pragma unroll
      for (int n = 36; n < 41; ++n) { SGB(0x8, 1); SGB(0x100, 2); SGB(0x402, 4); }
#pragma unroll
      for (int n = 41; n < 64; ++n) { SGB(0x8, 1); SGB(0x100, 2); if (n & 1) SGB(0x402, 3); else SGB(0x402, 2); }
#pragma unroll
      for (int i = 0; i < 16; ++i) PIN(eq[i >> 3][i & 7]);
      SCHED();
      STAMP(3);
#pragma unroll
      for (int hh = 0; hh < 2; ++hh) { pq[hh] = mfma(fa[2 + hh], pack8(aq[1], hh), zero); PIN(pq[hh]); }
      SCHED();
      sm_finish(eq, fq[0]);
      SCHED();
      asm volatile("s_waitcnt vmcnt(0)" : "+v"(tho), "+v"(thq)::"memory");
      mq[0] = negmax16(pq[0]); mq[1] = negmax16(pq[1]);
      PIN(mq[0]); PIN(mq[1]);
      SCHED();
#pragma unroll
      for (int i = 0; i < 4; ++i) PIN(fq[0][i >> 1][i & 1]);
      softmax16x2(pq[0], pq[1], mq, fq[1]);
#pragma unroll
      for (int i = 0; i < 4; ++i) PIN(fq[1][i >> 1][i & 1]);
    }
    STAMP(5);
    BAR();
    STAMP(6);
    u32x4 ob[2][4];
    {
      u32x4 a0[4], a1[4];
#pragma unroll
      for (int hd = 0; hd < 4; ++hd) {
        a0[hd] = lds_tr2(kra + hd * 1536, kra + hd * 1536 + 4 * 48);
        a1[hd] = lds_tr2(kra + hd * 1536 + 8 * 48, kra + hd * 1536 + 12 * 48);
      }
      SCHED();
      f32x16 y[4];
      u32 cs = 0;
      auto num = [&](int hd) {
        y[hd] = mfma(a0[hd], fq[hd >> 1][hd & 1][0], zero);
        y[hd] = mfma(a1[hd], fq[hd >> 1][hd & 1][1], y[hd]);
      };
      auto fin = [&](int hd) {
        const f32x16& yy = y[hd];
        const float rd = __builtin_amdgcn_rcpf(yy[8] + 1e-6f);
        u32 w[4];
#pragma unroll
        for (int e = 0; e < 4; ++e) { float t0 = yy[2 * e] * rd, t1 = yy[2 * e + 1] * rd; if (hd < 2) OPQ(t0); w[e] = pk2(t0, t1); }
        ob[0][hd] = (u32x4){w[0], w[1], w[2], w[3]};
        st128c(ob[0][hd], wsr, o_off + hd * 1024);
        cs = sum4(cs, ob[0][hd]);
      };
      num(0); num(1); PIN(y[0]); PIN(y[1]); SCHED();
      num(2); fin(0); SGB(0x8, 1); SGB(0x402, 6); SGB(0x8, 1); PIN(y[2]); SCHED();
      num(3); fin(1); SGB(0x8, 1); SGB(0x402, 6); SGB(0x8, 1); PIN(y[3]); SCHED();
      fin(2); fin(3);
      st32c(key - cs, wsr, c_off);
    }
    SCHED();
    if (TIMER == 0) BARE();
    STAMP(7);
    u32x4 wo[32][2];
    auto ldwo = [&](int n) {
      wo[n][0] = lds_tr2(woa + n * 2048, woa + n * 2048 + 512);
      wo[n][1] = lds_tr2(wob + n * 2048, wob + n * 2048 + 512);
    };
#pragma unroll
    for (int n = 0; n < 16; ++n) ldwo(n);
    f32x16 c0 = zero, c1 = zero;
#pragma unroll
    for (int s = 0; s < 4; ++s) { c0 = mfma(wo[s][0], ob[0][s], c0); c1 = mfma(wo[s][1], ob[0][s], c1); }
#pragma unroll
    for (int k = 4; k < 16; ++k) { PIN(wo[k][0]); PIN(wo[k][1]); }
    PIN(c0); PIN(c1);
    if (TIMER) WAITV(0);
    BARE();
    SCHED();
#pragma unroll
    for (int n = 16; n < 24; ++n) ldwo(n);
#pragma unroll
    for (int k = 16; k < 24; ++k) { PIN(wo[k][0]); PIN(wo[k][1]); }
    const __amdgpu_buffer_rsrc_t outr = __builtin_amdgcn_make_buffer_rsrc(KPTR(kb, 0), 0, 2048 * 1024 * 2, SRD3);
    u32 oc = oa - 256 - lane * 12, l4 = lane * 4, l16 = lane * 16, oa4 = oa + 4 * (4 * OREC), oc4 = oc + 4 * (4 * OREC);
    u32 sw1 = lds0 + t * 4096 + r * 128 + 16 * (h ^ (r & 7)), sr1 = lds0 + t * 4096 + l3 * 128 + 16 * (l7 ^ l3);
    u32 sw2 = sw1 ^ 32, sw3 = sw1 ^ 64, sw4 = sw1 ^ 96, or1 = orow + 16384, or2 = orow + 32768, or3 = orow + 49152;
    PIN(oc); PIN(l4); PIN(l16); PIN(sw1); PIN(sr1); PIN(oa4); PIN(oc4); PIN(sw2); PIN(sw3); PIN(sw4); PIN(or1); PIN(or2); PIN(or3);
    STAMP(8);
    u32 nretry = 0;
    u32 chk[2];
    auto oread = [&](int j) {
      const u32 da = j % 5 == 4 ? oa4 : oa + (j % 5) * (4 * OREC), dc = j % 5 == 4 ? oc4 : oc + (j % 5) * (4 * OREC);
#pragma unroll
      for (int s = 0; s < 4; ++s) ob[j & 1][s] = lds_r128(da + s * 1024);
      chk[j & 1] = *(LAS u32*)(size_t)dc;
    };
    auto obad = [&](int j) -> bool {
      const u32 c = sum4(sum4(chk[j & 1], ob[j & 1][0]), ob[j & 1][1]) + sum4(sum4(0u, ob[j & 1][2]), ob[j & 1][3]);
      return __builtin_amdgcn_ballot_w64(c != key) != 0;
    };
#pragma unroll
    for (int j = 1; j < 8; ++j) {
      BARE();
      SCHED();
      oread(j);
      if (j >= 2) {
#pragma unroll
        for (int s = 0; s < 4; ++s) {
          const int k = 4 * (j - 1) + s;
          c0 = mfma(wo[k][0], ob[(j - 1) & 1][s], c0);
          c1 = mfma(wo[k][1], ob[(j - 1) & 1][s], c1);
        }
        if (j >= 3 && j <= 6) { ldwo(24 + 2 * (j - 3)); ldwo(25 + 2 * (j - 3)); }
      }
      const bool bad = obad(j);
      if (j >= 3 && j <= 6) {
        SGB(0x8, 1); SGB(0x100, 3); SGB(0x8, 1); SGB(0x100, 2); SGB(0x8, 1); SGB(0x100, 3); SGB(0x8, 1); SGB(0x100, 3);
        SGB(0x8, 1); SGB(0x2, 4); SGB(0x8, 1); SGB(0x2, 5); SGB(0x8, 1); SGB(0x100, 2); SGB(0x8, 1);
      } else if (j >= 2) {
        SGB(0x8, 1); SGB(0x100, 3); SGB(0x8, 1); SGB(0x100, 2); SGB(0x8, 3); SGB(0x2, 4); SGB(0x8, 1); SGB(0x2, 5); SGB(0x8, 2);
      }
      SCHED();
      if (__builtin_expect(bad, 0)) {
#pragma unroll 1
        for (u32 spin = 0; spin < SPIN_CAP; ++spin) {
          dma_orec(lds0 + L_O + t * OREC + (j % 5) * (4 * OREC), l4, l16, wsr, WS_O + ((br * 64 + panel * 4 + t) * 8 + ochunk(j)) * OREC);
#pragma unroll 1
          for (u32 jj = j + 1; jj <= (j + AHEAD < 7 ? j + AHEAD : 7); ++jj) {
            const u32 sl = (jj >= 5 ? jj - 5 : jj) * (4 * OREC);
            u32 c = *(LAS u32*)(size_t)(oc + sl);
#pragma unroll
            for (int s = 0; s < 4; ++s) c = sum4(c, lds_r128(oa + sl + s * 1024));
            if (__builtin_amdgcn_ballot_w64(c != key) != 0)
              dma_orec(lds0 + L_O + t * OREC + sl, l4, l16, wsr, WS_O + ((br * 64 + panel * 4 + t) * 8 + ochunk(jj)) * OREC);
          }
          WAITV(0);
          oread(j);
          ++nretry;
          if (!obad(j)) break;
        }
      }
    }
#pragma unroll
    for (int s = 0; s < 4; ++s) c0 = mfma(wo[28 + s][0], ob[1][s], c0);
    PIN(c0);
    SCHED();
#pragma unroll
    for (int s = 0; s < 4; ++s) c1 = mfma(wo[28 + s][1], ob[1][s], c1);
    lds_w128(sw1, pack8(c0, 0));
    lds_w128(sw2, pack8(c0, 1));
    SGB(0x8, 2); SGB(0x2, 4); SGB(0x200, 1); SGB(0x8, 1); SGB(0x2, 4); SGB(0x200, 1); SGB(0x8, 1);
    SCHED();
    STAMP(9);
    lds_w128(sw3, pack8(c1, 0));
    lds_w128(sw4, pack8(c1, 1));
    asm volatile("" ::: "memory");
    {
      u32x4 v[4];
#pragma unroll
      for (int i = 0; i < 4; ++i) v[i] = lds_r128(sr1 + i * 1024);
      SCHED();
      st128c(v[0], outr, orow);
      st128c(v[1], outr, or1);
      st128c(v[2], outr, or2);
      st128c(v[3], outr, or3);
    }
    if (t == 0 && panel == 0 && grp == 0 && lane == 0) st32c(key, wsr, WS_EPOCH + br * 256);
    if (PROBE_N == 13) WAITV(0);
    STAMP(13);
    STAMP_OUT();
  }
}
}

extern "C" void kernel_launch(void* const* d_in, const int* in_sizes, int n_in,
                              void* d_out, int out_size, void* d_ws, size_t ws_size,
                              hipStream_t stream) {
  hh::hedgehog<<<256, 512, 0, stream>>>((const u16*)d_in[0], (const u16*)d_in[1], (const u16*)d_in[2], (const u16*)d_in[3],
                                        (const u16*)d_in[4], (const u16*)d_in[5], (const u16*)d_in[6], (const u16*)d_in[7],
                                        (u16*)d_out, (u32*)((char*)d_ws + ((size_t)WSOFF << 20)));
}
```

```cpp
#define PROBE_N 2
#define PROBE_ROLE 1
#include <hip/hip_runtime.h>
#include <stdint.h>

typedef unsigned short u16;
typedef unsigned u32;
typedef short bf16x8 __attribute__((ext_vector_type(8)));
typedef short s16x4 __attribute__((ext_vector_type(4)));
typedef float f32x16 __attribute__((ext_vector_type(16)));
typedef float f32x4 __attribute__((ext_vector_type(4)));
typedef float f32x2 __attribute__((ext_vector_type(2)));
typedef unsigned u32x8 __attribute__((ext_vector_type(8)));
typedef unsigned u32x4 __attribute__((ext_vector_type(4)));
typedef unsigned u32x2 __attribute__((ext_vector_type(2)));
typedef __bf16 bf16x2 __attribute__((ext_vector_type(2)));
#define DI __device__ __forceinline__
#define LAS __attribute__((address_space(3)))
#define CAS __attribute__((address_space(4)))
#define BC(T, x) __builtin_bit_cast(T, x)

#define DELAY1 0
#define DELAY3 3
#define ROTMODE 242
#define CODE_PF 27648
#define AHEAD 2
#define WSOFF 0
#define GQ 4
#define GC 2
#define WFIRST 0
#define FOLLOW 0
#define TIMER 2
#define LTO 1
#define NTX 0
#define NTW 0
#define WQSWAP (ROTMODE >= 7)
#define CSH 0
#define WQ4 1
#define H3 0
#define LTQ 0

namespace hh {
constexpr int AUX_SC1 = 16;
constexpr u32 SRD3 = 0x00020000u;
constexpr u32 WS_EPOCH = 0;
constexpr u32 REC = 1280;
constexpr u32 WS_PART = 4096;
constexpr u32 OREC = 4352;
constexpr u32 WS_O = WS_PART + 2 * 32 * 16 * REC;
constexpr u32 WS_END = WS_O + 2 * 64 * 8 * OREC;
constexpr u32 WS_STAMP = 8u << 20;
constexpr u32 SLOT = 32768;
constexpr u32 L_WQ0 = 98304;
constexpr u32 L_WQ1 = 65536;
constexpr u32 L_WPHI = 163840 - 4096;
constexpr u32 L_PHI = 0;
constexpr u32 VF = 1040;
constexpr u32 L_V = 131072;
constexpr u32 L_KV = L_V + 16 * VF;
constexpr u32 L_WO = 0;
constexpr u32 L_O = 65536;
constexpr u32 SPIN_CAP = 1u << 16;

DI u32 pk2(float a, float b) { return BC(u32, __builtin_convertvector((f32x2){a, b}, bf16x2)); }
DI u32x4 pack8(const f32x16& x, int s) {
  return (u32x4){pk2(x[8 * s], x[8 * s + 1]), pk2(x[8 * s + 2], x[8 * s + 3]), pk2(x[8 * s + 4], x[8 * s + 5]), pk2(x[8 * s + 6], x[8 * s + 7])};
}
DI f32x16 mfma(u32x4 a, u32x4 b, f32x16 c) { return __builtin_amdgcn_mfma_f32_32x32x16_bf16(BC(bf16x8, a), BC(bf16x8, b), c, 0, 0, 0); }
DI f32x4 mfma16(u32x4 a, u32x4 b, f32x4 c) { return __builtin_amdgcn_mfma_f32_16x16x32_bf16(BC(bf16x8, a), BC(bf16x8, b), c, 0, 0, 0); }
DI u32x4 lds_r128(u32 a) { return *(LAS u32x4*)(size_t)a; }
DI void lds_w128(u32 a, u32x4 v) { *(LAS u32x4*)(size_t)a = v; }
DI u32x4 lds_tr2(u32 a0, u32 a1) {
  s16x4 lo = __builtin_amdgcn_ds_read_tr16_b64_v4i16((LAS s16x4*)(size_t)a0);
  s16x4 hi = __builtin_amdgcn_ds_read_tr16_b64_v4i16((LAS s16x4*)(size_t)a1);
  return BC(u32x4, __builtin_shufflevector(lo, hi, 0, 1, 2, 3, 4, 5, 6, 7));
}
DI void dma1(u32 lds, u32 voff, __amdgpu_buffer_rsrc_t srd, u32 soff) {
  asm volatile("s_mov_b32 m0, %0\n\ts_nop 0\n\tbuffer_load_dwordx4 %1, %2, %3 offen lds" ::"s"(lds), "v"(voff), "s"(srd), "s"(soff) : "memory");
}
DI void dma4(u32 lds, const u32 (&v)[4], __amdgpu_buffer_rsrc_t srd, u32 soff) {
  asm volatile("s_mov_b32 m0, %0\n\ts_nop 0\n\t"
               "buffer_load_dwordx4 %1, %5, %6 offen lds\n\t"
               "buffer_load_dwordx4 %2, %5, %6 offen offset:1024 lds\n\t"
               "buffer_load_dwordx4 %3, %5, %6 offen offset:2048 lds\n\t"
               "buffer_load_dwordx4 %4, %5, %6 offen offset:3072 lds" ::"s"(lds), "v"(v[0]), "v"(v[1]), "v"(v[2]), "v"(v[3]), "s"(srd), "s"(soff) : "memory");
}
#define DMA4P(name, POL)                                                                       \
  DI void name(u32 lds, const u32 (&v)[4], __amdgpu_buffer_rsrc_t srd, u32 soff) {              \
    asm volatile("s_mov_b32 m0, %0\n\ts_nop 0\n\t"                                             \
                 "buffer_load_dwordx4 %1, %5, %6 offen " POL " lds\n\t"                        \
                 "buffer_load_dwordx4 %2, %5, %6 offen offset:1024 " POL " lds\n\t"            \
                 "buffer_load_dwordx4 %3, %5, %6 offen offset:2048 " POL " lds\n\t"            \
                 "buffer_load_dwordx4 %4, %5, %6 offen offset:3072 " POL " lds" ::"s"(lds), "v"(v[0]), "v"(v[1]), "v"(v[2]), "v"(v[3]), "s"(srd), "s"(soff) : "memory"); \
  }
DMA4P(dma4nt, "nt")
DI void dma_orec(u32 lds, u32 l4, u32 l16, __amdgpu_buffer_rsrc_t srd, u32 soff) {
  asm volatile("s_mov_b32 m0, %0\n\ts_nop 0\n\t"
               "buffer_load_dwordx4 %2, %3, %4 offen offset:256 sc1 lds\n\t"
               "buffer_load_dwordx4 %2, %3, %4 offen offset:1280 sc1 lds\n\t"
               "buffer_load_dwordx4 %2, %3, %4 offen offset:2304 sc1 lds\n\t"
               "buffer_load_dwordx4 %2, %3, %4 offen offset:3328 sc1 lds\n\t"
               "buffer_load_dword %1, %3, %4 offen sc1 lds" ::"s"(lds), "v"(l4), "v"(l16), "s"(srd), "s"(soff) : "memory");
}
#define TOUCH(d, voff, srd, soff) asm volatile("buffer_load_dword %0, %1, %2, %3 offen" : "=v"(d) : "v"(voff), "s"(srd), "s"(soff) : "memory")
DI void st128c(u32x4 v, __amdgpu_buffer_rsrc_t srd, u32 off) { __builtin_amdgcn_raw_buffer_store_b128(v, srd, off, 0, AUX_SC1); }
DI void st32c(u32 v, __amdgpu_buffer_rsrc_t srd, u32 off) { __builtin_amdgcn_raw_buffer_store_b32(v, srd, off, 0, AUX_SC1); }
#define BAR() asm volatile("s_waitcnt lgkmcnt(0)\n\ts_barrier" ::: "memory")
#define BARE() asm volatile("s_barrier" ::: "memory")
#define OPQ(x) asm("" : "+v"(x))
#define PIN(x) asm volatile("" : "+v"(x))
#define PINS(x) asm volatile("" : "+s"(x))
#define WAITV(n) asm volatile("s_waitcnt vmcnt(" #n ")" ::: "memory")
#define SCHED() __builtin_amdgcn_sched_barrier(0)
#define SGB(m, n) __builtin_amdgcn_sched_group_barrier(m, n, 0)
DI u32 sum4(u32 c, u32x4 v) { return (c + v.x + v.y) + v.z + v.w; }
DI u32 pair_sum(u32 s) { auto t = __builtin_amdgcn_permlane32_swap(s, s, false, false); return t[0] + t[1]; }

constexpr float LOG2E = 1.44269504088896340736f;
DI float max3f(float a, float b, float c) { return __builtin_fmaxf(__builtin_fmaxf(a, b), c); }
DI float negmax16(const f32x16& x) {
  const float a = max3f(x[0], x[1], x[2]), b = max3f(x[3], x[4], x[5]), c = max3f(x[6], x[7], x[8]);
  const float d = max3f(x[9], x[10], x[11]), e = max3f(x[12], x[13], x[14]);
  return -max3f(max3f(a, b, x[15]), max3f(c, d, e), a) * LOG2E;
}
DI void sm_exp(const f32x16& p, const f32x16& q, const float (&mc)[2], f32x2 (&e)[2][8]) {
#pragma unroll
  for (int i = 0; i < 8; ++i) {
    const f32x2 t = __builtin_elementwise_fma((f32x2){p[2 * i], p[2 * i + 1]}, (f32x2){LOG2E, LOG2E}, (f32x2){mc[0], mc[0]});
    const f32x2 u = __builtin_elementwise_fma((f32x2){q[2 * i], q[2 * i + 1]}, (f32x2){LOG2E, LOG2E}, (f32x2){mc[1], mc[1]});
    e[0][i] = (f32x2){__builtin_amdgcn_exp2f(t.x), __builtin_amdgcn_exp2f(t.y)};
    e[1][i] = (f32x2){__builtin_amdgcn_exp2f(u.x), __builtin_amdgcn_exp2f(u.y)};
  }
  SGB(0x2, 4); SGB(0x400, 8); SGB(0x2, 4); SGB(0x400, 8); SGB(0x2, 6); SGB(0x400, 8); SGB(0x2, 8); SGB(0x400, 8);
}
DI void sm_exp_s(const f32x16& p, const f32x16& q, const float (&mc)[2], f32x2 (&e)[2][8]) {
#pragma unroll
  for (int i = 0; i < 16; ++i) {
    e[0][i >> 1][i & 1] = __builtin_amdgcn_exp2f(__builtin_fmaf(p[i], LOG2E, mc[0]));
    e[1][i >> 1][i & 1] = __builtin_amdgcn_exp2f(__builtin_fmaf(q[i], LOG2E, mc[1]));
  }
}
DI void sm_finish(const f32x2 (&e)[2][8], u32x4 (&o)[2][2]) {
  float r[2];
#pragma unroll
  for (int u = 0; u < 2; ++u) {
    const f32x2 s = ((e[u][0] + e[u][1]) + (e[u][2] + e[u][3])) + ((e[u][4] + e[u][5]) + (e[u][6] + e[u][7]));
    r[u] = __builtin_amdgcn_rcpf(s.x + s.y);
  }
#pragma unroll
  for (int u = 0; u < 2; ++u) {
    u32 w[8];
#pragma unroll
    for (int i = 0; i < 8; ++i) { const f32x2 t = e[u][i] * (f32x2){r[u], r[u]}; w[i] = pk2(t.x, t.y); }
    o[u][0] = (u32x4){w[0], w[1], w[2], w[3]};
    o[u][1] = (u32x4){w[4], w[5], w[6], w[7]};
  }
}
DI void softmax16x2(const f32x16& p, const f32x16& q, const float (&mc)[2], u32x4 (&o)[2][2]) {
  f32x2 e[2][8];
  sm_exp(p, q, mc, e);
  sm_finish(e, o);
}

#define STAMP(i) do { if ((i) == PROBE_N) { SCHED(); asm volatile("s_memrealtime %0\n\ts_waitcnt lgkmcnt(0)" : "=s"(pt1) :: "memory"); SCHED(); } } while (0)
#define STAMP_OUT() do { if (PROBE_N && (wave >> 2) == PROBE_ROLE) { unsigned long long d_ = pt1 - pt0; if (d_ > 100000) d_ = 100000; unsigned long long e_ = pt0 + 3000 + 8 * d_; \
  for (int it_ = 0; it_ < 1000000; ++it_) { unsigned long long t_; asm volatile("s_memrealtime %0\n\ts_waitcnt lgkmcnt(0)" : "=s"(t_) :: "memory"); if (t_ >= e_) break; } } } while (0)

__global__ __launch_bounds__(512, 2) void hedgehog(const u16* xA, const u16* wA, const u16* pA, const u16* oA,
                                                   const u16* xB, const u16* wB, const u16* pB, const u16* oB,
                                                   u16* out_, u32* ws_) {
  __shared__ __attribute__((aligned(16))) char smem[163840];
  const u32 lds0 = (u32)(size_t)(LAS char*)smem;
  unsigned long long pt0 = __builtin_amdgcn_s_memrealtime(), pt1 = pt0;
  const int bid = blockIdx.x, xcd = bid & 7, idx = bid >> 3;
  const int br = xcd >> 2;
#define KLOAD(tail)                                                                                       \
  u32x8 ka;                                                                                               \
  u32x4 kb;                                                                                               \
  asm volatile("s_load_dwordx8 %0, %2, %3\n\ts_load_dwordx4 %1, %2, 0x40" tail : "=&s"(ka), "=&s"(kb)     \
               : "s"(__builtin_amdgcn_kernarg_segment_ptr()), "s"(br * 32))
#define KWAIT() asm volatile("s_waitcnt lgkmcnt(0)" : "+s"(ka), "+s"(kb))
#define KPTR(v, i) ((void*)(((unsigned long long)(v)[(i) + 1] << 32) | (v)[i]))
  unsigned long long pcs;
  asm volatile("s_getpc_b64 %0" : "=s"(pcs));
  pcs &= ~255ull;
  const int lane = threadIdx.x & 63;
  const int wave = __builtin_amdgcn_readfirstlane(threadIdx.x >> 6);
  const int panel = (xcd & 3) * 4 + (idx & 3), grp = idx >> 2, gq = (grp + GQ) & 7, cg = (grp + GC) & 7;
  auto ochunk = [&](u32 j) -> u32 {
    const int v = (int)j + (((int)j - 1 - gq) >> 31), z = ((int)j - 1) >> 31;
    return (u32)__builtin_amdgcn_readfirstlane((v & ~z) | (gq & z));
  };
  f32x16 zero;
#pragma unroll
  for (int i = 0; i < 16; ++i) zero[i] = 0.f;
#define LANE_IDS()                                                                            \
  u32 lane_ = lane;                                                                           \
  PIN(lane_);                                                                                 \
  const u32 r = lane_ & 31, h = lane_ >> 5;                                                   \
  const u32 blk = (lane_ >> 4) & 1, q = (lane_ & 15) >> 2, p = lane_ & 3;                     \
  u32* ws = (u32*)KPTR(kb, 2);                                                                \
  const __amdgpu_buffer_rsrc_t wsr = __builtin_amdgcn_make_buffer_rsrc(ws, 0, WS_END, SRD3);  \
  const __amdgpu_buffer_rsrc_t wor = __builtin_amdgcn_make_buffer_rsrc(KPTR(ka, 6), 0, 512 * 512 * 2, SRD3)

  if (wave >= 4) {
    const int cw = wave - 4;
    const int rh = cw >> 1, hp = cw & 1;
    KLOAD("\n\ts_waitcnt lgkmcnt(0)");
    u32 sp0, sp1, sp2, sp3;
    {
      u32 off = idx * 1024 + cw * 256;
      off = off < CODE_PF ? off : 0;
      asm volatile("s_load_dword %0, %4, %5 offset:0\n\ts_load_dword %1, %4, %5 offset:64\n\ts_load_dword %2, %4, %5 offset:128\n\ts_load_dword %3, %4, %5 offset:192"
                   : "=&s"(sp0), "=&s"(sp1), "=&s"(sp2), "=&s"(sp3) : "s"(pcs), "s"(off));
    }
    LANE_IDS();
    u32 key = *(const CAS u32*)((const char*)ws + WS_EPOCH + br * 256) + 1u;
    u32 xa[2], xb[2], wa[2], wb2[2];
    {
      const u32 r16 = lane_ & 15, g = lane_ >> 4;
#pragma unroll
      for (int s = 0; s < 2; ++s) {
        xa[s] = lds0 + (64 * rh + r16) * 128 + 16 * ((4 * s + g) ^ (r16 >> 1));
        wa[s] = lds0 + 16384 + (8 * g + q) * 128 + 16 * ((4 * hp + 2 * s + (p >> 1)) ^ ((((q >> 1) & 1) << 2) | (blk << 1))) + 8 * (p & 1);
        xb[s] = xa[s] + 2 * SLOT; wb2[s] = wa[s] + 2 * SLOT;
        PIN(xa[s]); PIN(xb[s]); PIN(wa[s]); PIN(wb2[s]);
      }
    }
    u32 fpa = lds0 + L_WPHI + hp * 1024 + (8 * h + q) * 32 + (8 * blk + 4 * (p >> 1)) * 2;
    PIN(fpa);
    PINS(key);
    f32x4 ak[4][2], av[4][2];
#pragma unroll
    for (int i = 0; i < 8; ++i) ak[i >> 1][i & 1] = av[i >> 1][i & 1] = (f32x4){0.f, 0.f, 0.f, 0.f};
    u32 vra, pra, vwa, pw0, pw1, pub16, pub4;
    u32x4 xf[2][4], wk[2][2], wv[2][2];
    auto ldx = [&](int c, int s) {
      const u32 x = c % 3 == 2 ? xb[s] : xa[s] + (c % 3) * SLOT;
#pragma unroll
      for (int i = 0; i < 4; ++i) xf[s][i] = lds_r128(x + i * 2048);
    };
    auto ldw = [&](int c, int s, int m) {
#pragma unroll
      for (int n = 0; n < 2; ++n) {
        const u32 w = (c % 3 == 2 ? wb2[n] : wa[n] + (c % 3) * SLOT) + s * 4096 + m * 8192;
        (m ? wv : wk)[s][n] = lds_tr2(w, w + 512);
      }
    };
    auto mk = [&](int s) {
#pragma unroll
      for (int i = 0; i < 8; ++i) ak[i >> 1][i & 1] = mfma16(wk[s][i & 1], xf[s][i >> 1], ak[i >> 1][i & 1]);
    };
    auto mv = [&](int s) {
#pragma unroll
      for (int i = 0; i < 8; ++i) av[i >> 1][i & 1] = mfma16(xf[s][i >> 1], wv[s][i & 1], av[i >> 1][i & 1]);
    };
    auto mix = [&](int n) {
#pragma unroll
      for (int i = 0; i < 8; ++i) {
        SGB(0x8, 1);
        if (n == 12) { if (i < 4) SGB(0x100, 2); else SGB(0x100, 1); }
        else if (n == 8) SGB(0x100, 1);
        else if (n == 4) { if (i < 4) SGB(0x100, 1); }
        else if (n < 0) SGB(0x6, 2);
      }
      SCHED();
    };
    BAR();
    asm volatile("" ::"s"(sp0), "s"(sp1), "s"(sp2), "s"(sp3));
    STAMP(1);
    ldw(0, 0, 0); ldx(0, 0); ldw(0, 0, 1);
    u32x4 fa[2];
#pragma unroll
    for (int hh = 0; hh < 2; ++hh) fa[hh] = lds_tr2(fpa + hh * 512, fpa + hh * 512 + 128);
    SCHED();
#pragma unroll
    for (int c = 0; c < 7; ++c) {
      ldw(c, 1, 0); ldx(c, 1); mk(0); mix(8);
      ldw(c, 1, 1); mv(0); mix(4);
      u32 ln = lane;
      PIN(ln);
      const u32 r_ = ln & 31, h_ = ln >> 5, q_ = (ln & 15) >> 2;
      if (c == 1) {
        const u32 sg = (ln & 4) ? 0x80008000u : 0u;
#pragma unroll
        for (int hh = 0; hh < 2; ++hh) { OPQ(fa[hh]); fa[hh] ^= (u32x4){sg, sg, sg, sg}; }
      }
      if (c == 2) vra = lds0 + L_V + (cw >> 1) * (2 * VF) + (r_ < 16 ? (ln + 16 * (cw & 1)) * 16 : 1024);
      if (c == 3) pra = lds0 + L_PHI + cw * 8192 + (4 * h_ + q_) * 64 + 16 * ((((ln >> 3) & 2) + ((ln >> 1) & 1)) ^ ((2 * h_ + (q_ >> 1)) & 3)) + 8 * (ln & 1);
      if (c == 4) {
        const u32 rec = WS_PART + ((br * 32 + grp * 4 + cw) * 16 + panel) * REC;
        pub16 = rec + ln * 16; pub4 = rec + 1024 + ln * 4;
      }
      if (c == 5) { pw0 = lds0 + L_PHI + (2 * hp) * 8192 + (64 * rh + r_) * 64 + 16 * ((2 * h_) ^ ((r_ >> 1) & 3)); pw1 = pw0 ^ 16; }
      if (c == 6) vwa = lds0 + L_V + (8 * rh + 2 * hp) * VF + ((ln & 16) * 2 + (ln & 15)) * 16 + h_ * 8;
      mk(1); mix(c >= 1 ? -1 : 0);
      if (c == 1) { PIN(fa[0]); PIN(fa[1]); }
      if (c == 2) PIN(vra);
      if (c == 3) PIN(pra);
      if (c == 4) { PIN(pub16); PIN(pub4); }
      if (c == 5) { PIN(pw0); PIN(pw1); }
      if (c == 6) PIN(vwa);
      BAR();
      ldw(c + 1, 0, 0); ldx(c + 1, 0); ldw(c + 1, 0, 1);
      mv(1); mix(12);
    }
    ldw(7, 1, 0); ldx(7, 1); ldw(7, 1, 1); mk(0); mix(12);
    mk(1); mix(0);
    __builtin_amdgcn_s_setprio(2);
    STAMP(2);
    f32x16 pk[2][2];
    {
      u32x4 kf[2][2];
      auto kpack = [&](int i) {
#pragma unroll
        for (int hh = 0; hh < 2; ++hh) {
          const f32x4 &t0 = ak[2 * i][hh], &t1 = ak[2 * i + 1][hh];
          const auto lo = __builtin_amdgcn_permlane16_swap(pk2(t0[0], t0[1]), pk2(t1[0], t1[1]), false, false);
          const auto hi = __builtin_amdgcn_permlane16_swap(pk2(t0[2], t0[3]), pk2(t1[2], t1[3]), false, false);
          kf[i][hh] = (u32x4){lo[0], hi[0], lo[1], hi[1]};
        }
      };
      mv(0); kpack(0);
      SGB(0x8, 2);
#pragma unroll
      for (int i = 0; i < 6; ++i) { SGB(0x2, 2); SGB(0x8, 1); }
      SCHED();
      pk[0][0] = mfma(fa[0], kf[0][0], zero);
      pk[0][1] = mfma(fa[1], kf[0][1], zero);
      kpack(1);
      SGB(0x8, 1); SGB(0x2, 6); SGB(0x8, 1); SGB(0x2, 6);
      PIN(pk[0][0]); PIN(pk[0][1]); PIN(kf[1][0]); PIN(kf[1][1]);
      SCHED();
      mv(1);
      pk[1][0] = mfma(fa[0], kf[1][0], zero);
      pk[1][1] = mfma(fa[1], kf[1][1], zero);
    }
    float m0[2], m1[2];
    m0[0] = negmax16(pk[0][0]); m0[1] = negmax16(pk[0][1]);
    SGB(0x8, 2);
#pragma unroll
    for (int i = 0; i < 6; ++i) { SGB(0x2, 2); SGB(0x8, 1); }
    SGB(0x8, 1); SGB(0x2, 6); SGB(0x8, 1);
#pragma unroll
    for (int i = 0; i < 8; ++i) PIN(av[i >> 1][i & 1]);
    PIN(pk[1][0]); PIN(pk[1][1]); PIN(m0[0]); PIN(m0[1]);
    SCHED();
#pragma unroll
    for (int i = 0; i < 8; ++i) {
      const f32x4& t = av[i >> 1][i & 1];
      *(LAS u32x2*)(size_t)(vwa + (4 * (i >> 2) + ((i >> 1) & 1)) * VF + (i & 1) * 256) = (u32x2){pk2(t[0], t[1]), pk2(t[2], t[3])};
    }
    SCHED();
    {
      u32x4 o[2][2];
      softmax16x2(pk[0][0], pk[0][1], m0, o);
      lds_w128(pw0, o[0][0]); lds_w128(pw1, o[0][1]);
      lds_w128(pw0 + 8192, o[1][0]); lds_w128(pw1 + 8192, o[1][1]);
      SCHED();
      m1[0] = negmax16(pk[1][0]); m1[1] = negmax16(pk[1][1]);
      PIN(m1[0]); PIN(m1[1]);
      SCHED();
      softmax16x2(pk[1][0], pk[1][1], m1, o);
      lds_w128(pw0 + 2048, o[0][0]); lds_w128(pw1 + 2048, o[0][1]);
      lds_w128(pw0 + 2048 + 8192, o[1][0]); lds_w128(pw1 + 2048 + 8192, o[1][1]);
      SGB(0x402, 26); SGB(0x200, 1); SGB(0x402, 8); SGB(0x200, 1); SGB(0x402, 8); SGB(0x200, 1); SGB(0x402, 8); SGB(0x200, 1);
      SCHED();
      BARE();
      BAR();
      SCHED();
    }
    STAMP(3);
    f32x16 kv = zero;
    {
      u32x4 a[8], b[8];
#pragma unroll
      for (int u = 0; u < 8; ++u) {
        a[u] = lds_r128(vra + ((u >> 1) * 4 + (u & 1)) * VF);
        b[u] = lds_tr2(pra + 16 * u * 64, pra + (16 * u + 8) * 64);
      }
#pragma unroll
      for (int u = 0; u < 8; ++u) kv = mfma(a[u], b[u], kv);
      SGB(0x100, 12); SGB(0x8, 1); SGB(0x100, 6); SGB(0x8, 1); SGB(0x100, 6); SGB(0x8, 6);
    }
    {
      const u32x4 w = pack8(kv, 0);
      st128c(w, wsr, pub16);
      const u32 zb = __float_as_uint(kv[8]);
      const u32 chk = key - pair_sum(sum4(h ? 0u : zb, w));
      st32c(h ? chk : zb, wsr, pub4);
    }
    SCHED();
    STAMP(4);
    u32 l16, l4, kwa, kza, gso[4], vo[4], wso;
    u32x4 sel;
    {
      u32 ln = lane;
      PIN(ln);
#pragma unroll
      for (int i = 0; i < 4; ++i) vo[i] = (ln >> 3) * 1024 + 16 * ((ln & 7) ^ (((ln >> 4) & 1) << 2)) + i * 7168;
#pragma unroll
      for (int i = 0; i < 2; ++i) dma4(lds0 + L_WO + cw * 8192 + i * 4096, vo, wor, ochunk(cw) * 65536 + i * 32768 + cg * 128);
      wso = ochunk(4 + cw) * 65536 + cg * 128;
      if (TIMER == 1) {
#pragma unroll
        for (int i = 0; i < 2; ++i) dma4(lds0 + L_WO + 32768 + cw * 8192 + i * 4096, vo, wor, wso + i * 32768);
      }
      const u32 r_ = ln & 31, h_ = ln >> 5;
      l16 = ln * 16; l4 = ln * 4;
      kwa = lds0 + L_KV + cw * 1536 + r_ * 48 + h_ * 16; kza = lds0 + L_KV + cw * 1536 + r_ * 48 + 32 + h_ * 8;
      const bool hit = r_ < 16 && ((r_ >> 2) & 1) == h_;
      const u32 j = (r_ & 3) + 4 * (r_ >> 3);
#pragma unroll
      for (int e = 0; e < 4; ++e) sel[e] = (hit && (j >> 1) == e) ? ((j & 1) ? 0x3F800000u : 0x00003F80u) : 0u;
#pragma unroll
      for (int i = 0; i < 4; ++i) { gso[i] = WS_PART + ((br * 32 + gq * 4 + cw) * 16 + ((i + idx) & 3) * 4) * REC; PINS(gso[i]); }
      PIN(l16); PIN(l4); PIN(kwa); PIN(kza); PIN(sel); PINS(wso);
    }
    if (DELAY1) __builtin_amdgcn_s_sleep(DELAY1);
    STAMP(10);
    f32x16 gs[4];
    float z[4];
    {
      u32x4 g[16];
      u32 gz[16];
      auto gload = [&](int gi) {
#pragma unroll
        for (int i = 0; i < 4; ++i) {
          g[4 * gi + i] = __builtin_amdgcn_raw_buffer_load_b128(wsr, l16 + i * REC, gso[gi], AUX_SC1);
          gz[4 * gi + i] = __builtin_amdgcn_raw_buffer_load_b32(wsr, l4 + (1024 + i * REC - 1024 * (i == 3)), gso[gi] + 1024 * (i == 3), AUX_SC1);
          SCHED();
        }
      };
      auto gsum = [&](int gi) -> bool {
        u32 c = 0;
#pragma unroll
        for (int i = 0; i < 4; ++i) {
          c = sum4(c + gz[4 * gi + i], g[4 * gi + i]);
          gs[gi] = mfma(sel, g[4 * gi + i], i == 0 ? zero : gs[gi]);
          z[gi] = i == 0 ? __uint_as_float(gz[4 * gi]) : z[gi] + __uint_as_float(gz[4 * gi + i]);
        }
        PIN(z[gi]);
        const bool bad = __builtin_amdgcn_ballot_w64(pair_sum(c) != 4u * key) != 0;
        SCHED();
        return bad;
      };
      gload(0); gload(1); gload(2); gload(3);
      bool b0 = gsum(0), b1 = gsum(1), b2 = gsum(2), b3 = gsum(3);
      STAMP(11);
#pragma unroll 1
      for (u32 spin = 0; (b0 | b1 | b2 | b3) && spin < SPIN_CAP; ++spin) {
        asm volatile("" ::: "memory");
        if (b0) gload(0);
        if (b1) gload(1);
        if (b2) gload(2);
        if (b3) gload(3);
        if (b0) b0 = gsum(0);
        if (b1) b1 = gsum(1);
        if (b2) b2 = gsum(2);
        if (b3) b3 = gsum(3);
      }
    }
    __builtin_amdgcn_s_waitcnt(0x0F70);
    STAMP(5);
    {
      float tt[8];
#pragma unroll
      for (int j = 0; j < 8; ++j) tt[j] = (gs[0][j] + gs[1][j]) + (gs[2][j] + gs[3][j]);
      const float zt = (z[0] + z[1]) + (z[2] + z[3]);
      lds_w128(kwa, (u32x4){pk2(tt[0], tt[1]), pk2(tt[2], tt[3]), pk2(tt[4], tt[5]), pk2(tt[6], tt[7])});
      const u32 zz = pk2(zt, zt);
      *(LAS u32x2*)(size_t)kza = (u32x2){zz, zz};
    }
    if (TIMER == 2) {
#pragma unroll
      for (int i = 0; i < 2; ++i) dma4(lds0 + L_WO + 32768 + cw * 8192 + i * 4096, vo, wor, wso + i * 32768);
    }
    BAR();
    STAMP(6);
    if (TIMER == 3) {
#pragma unroll
      for (int i = 0; i < 2; ++i) dma4(lds0 + L_WO + 32768 + cw * 8192 + i * 4096, vo, wor, wso + i * 32768);
    }
    {
      u32 oso[8], ob = lds0 + L_O + cw * OREC;
#pragma unroll
      for (int i = 1; i < 8; ++i) { oso[i] = WS_O + ((br * 64 + panel * 4 + cw) * 8 + ochunk(i)) * OREC; PINS(oso[i]); }
      PINS(ob);
      if (TIMER == 0) {
        BARE();
        STAMP(7);
#pragma unroll
        for (int i = 0; i < 2; ++i) dma4(lds0 + L_WO + 32768 + cw * 8192 + i * 4096, vo, wor, wso + i * 32768);
      }
      WAITV(0);
      BARE();
      if (DELAY3) __builtin_amdgcn_s_sleep(DELAY3);
      STAMP(8);
      auto oissue = [&](int j) { dma_orec(ob + (j % 5) * (4 * OREC), l4, l16, wsr, oso[j]); };
      oissue(1); oissue(2);
      WAITV(5); BARE(); oissue(3);
      WAITV(5); STAMP(15); BARE(); oissue(4); oissue(5);
      WAITV(10); BARE(); oissue(6);
      WAITV(10); BARE(); oissue(7);
      WAITV(10); BARE();
      WAITV(5); BARE();
      WAITV(0); BARE();
    }
    STAMP(9);
    STAMP_OUT();
  } else {
    KLOAD("");
    const int t = wave;
    const u32 l7 = lane & 7, l3 = lane >> 3, lq = lane >> 4;
    const u32 wsw = 16 * (l7 ^ ((lq & 1) << 2));
    u32 vx[4], vw[4];
#pragma unroll
    for (int i = 0; i < 4; ++i) {
      vx[i] = l3 * 1024 + 16 * (l7 ^ (((i & 1) << 2) | lq)) + i * 7168;
      vw[i] = l3 * 3072 + (wsw ^ ((i & 1) << 5)) + i * (8 * 3072 - 1024);
    }
    int rot = (grp + 2 * (idx & 3)) & 7;
    auto chunk = [&](u32 c) -> u32 {
      constexpr u32 n0 = ROTMODE / 100, n1 = ROTMODE / 10 % 10, n2 = ROTMODE % 10;
      const u32 g = grp + (idx & 3), r3 = g - 3 * ((g * 11) >> 5);
      auto run = [&](u32 k, u32 n) -> u32 {
        if (n == 3) { const u32 v = k + r3; return v >= 3 ? v - 3 : v; }
        return k ^ (g & (n - 1));
      };
      const u32 a = run(c, n0), b = n0 + run(c - n0, n1), d = n0 + n1 + run(c - n0 - n1, n2);
      return ((c < n0 ? a : c < n0 + n1 ? b : d) + CSH) & 7;
    };
    const u32 xs0 = (panel * 128 + t * 32) * 1024, ws0 = (t & 1) * (32 * 3072) + (1 + (t >> 1)) * 1024 + grp * 128;
    const u32 ldst = lds0 + t * 4096;
    u32 e0 = ldst, e1 = ldst + 16384, e2 = xs0 + chunk(0) * 128, e3 = ws0 + chunk(0) * (64 * 3072);
    PINS(e0); PINS(e1); PINS(e2); PINS(e3);
#pragma unroll
    for (int i = 0; i < 4; ++i) { PIN(vx[i]); PIN(vw[i]); }
    KWAIT();
    const __amdgpu_buffer_rsrc_t xr = __builtin_amdgcn_make_buffer_rsrc(KPTR(ka, 0), 0, 2048 * 512 * 2, SRD3);
    const __amdgpu_buffer_rsrc_t wr = __builtin_amdgcn_make_buffer_rsrc(KPTR(ka, 2), 0, 512 * 1536 * 2, SRD3);
    STAMP(10);
    auto dx = [&](int c, u32 d, u32 so) { if (c < NTX) dma4nt(d, vx, xr, so); else dma4(d, vx, xr, so); };
    auto dw = [&](int c, u32 d, u32 so) { if (c < NTW) dma4nt(d, vw, wr, so); else dma4(d, vw, wr, so); };
    if (WFIRST & 1) { dw(0, e1, e3); dx(0, e0, e2); } else { dx(0, e0, e2); dw(0, e1, e3); }
    if (FOLLOW) {
      const int gi = (grp + (ka[2] >> 7) - 4) & 7;
      rot = (gi == 7 ? 0 : gi) + 2 * (idx & 3);
      rot = rot >= 7 ? rot - 7 : rot;
    }
    {
      const __amdgpu_buffer_rsrc_t pr = __builtin_amdgcn_make_buffer_rsrc(KPTR(ka, 4), 0, 32 * 256 * 2, SRD3);
      dma1(lds0 + L_WPHI + t * 1024, lane * 16, pr, (t < 2 ? grp : gq) * 2048 + (t & 1) * 1024);
    }
    STAMP(11);
    u32 th3 = 0, thq = 0, tho;
    if (H3) TOUCH(th3, (l7 >> 1) * 3072 + (l7 & 1) * 1024, wr, (idx * 16 + t * 4) * 3072 + 1024 + ((3 - (ka[2] >> 7)) & 7) * 128);
    if (WFIRST & 2) dw(1, ldst + SLOT + 16384, ws0 + chunk(1) * (64 * 3072));
    dx(1, ldst + SLOT, xs0 + chunk(1) * 128);
    if (!(WFIRST & 2)) dw(1, ldst + SLOT + 16384, ws0 + chunk(1) * (64 * 3072));
    STAMP(1);
    LANE_IDS();
    u32 key = *(const CAS u32*)((const char*)ws + WS_EPOCH + br * 256) + 1u;
    const u32 sw = (r >> 1) & 7;
    u32 xa[4];
#pragma unroll
    for (int s = 0; s < 4; ++s) { xa[s] = lds0 + (32 * t + r) * 128 + 16 * ((2 * s + h) ^ sw); PIN(xa[s]); }
    u32 fpa = lds0 + L_WPHI + 2048 + (4 * h + q) * 32 + (8 * blk + 4 * (p >> 1)) * 2;
    u32 vq = (r >> 3) * 3072 + (r & 7) * 128;
    PIN(fpa); PIN(vq);
    PINS(key);
    u32x4 xq[32], fa[4];
#pragma unroll
    for (int c = 0; c < 8; ++c) {
      u32 d0, d1, s0, s1;
      if (c < 6) {
        d0 = ldst + ((c + 2) % 3) * SLOT; d1 = d0 + 16384;
        s0 = xs0 + chunk(c + 2) * 128; s1 = ws0 + chunk(c + 2) * (64 * 3072);
      } else {
        if (WQ4) {
          const u32 k0 = 2 * ((2 * (c - 6) + idx) & 3) + (t >> 1), k1 = (k0 + 2) & 7;
          d0 = lds0 + L_WQ1 + ((chunk(k0) + 4) & 7) * 8192 + (t & 1) * 4096; d1 = lds0 + L_WQ1 + ((chunk(k1) + 4) & 7) * 8192 + (t & 1) * 4096;
          s0 = (k0 * 64 + (t & 1) * 32) * 3072 + gq * 128; s1 = (k1 * 64 + (t & 1) * 32) * 3072 + gq * 128;
        } else {
          const u32 half = (c - 6) ^ (WQSWAP ? (idx & 1) : 0);
          d0 = lds0 + L_WQ0 - half * (L_WQ0 - L_WQ1) + t * 8192; d1 = d0 + 4096;
          s0 = chunk(half * 4 + t) * (64 * 3072) + gq * 128; s1 = s0 + 32 * 3072;
        }
      }
      PINS(d0); PINS(d1); PINS(s0); PINS(s1);
      if ((H3 && c == 0) || (LTQ && c == 7)) WAITV(9); else WAITV(8);
      if (H3 && c == 1) PIN(th3);
      STAMP(20 + c);
      BAR();
      if (LTQ && c == 6) TOUCH(thq, vq, wr, (idx * 16 + t * 4) * 3072);
      if (WFIRST & 4) dw(c + 2, d1, s1); else if (c < 6) dx(c + 2, d0, s0); else dw(c + 2, d0, s0);
#pragma unroll
      for (int s = 0; s < 4; ++s) xq[4 * c + s] = lds_r128(xa[s] + (c % 3) * SLOT);
      if (!(WFIRST & 4)) dw(c + 2, d1, s1); else if (c < 6) dx(c + 2, d0, s0); else dw(c + 2, d0, s0);
      if (c == 0) {
#pragma unroll
        for (int hd = 0; hd < 4; ++hd) fa[hd] = lds_tr2(fpa + hd * 512, fpa + hd * 512 + 256);
      }
    }
    if (LTO) TOUCH(tho, r * 128, wor, (idx * 16 + t * 4) * 1024);
    else TOUCH(tho, (r + 32 * t) * 1024, wor, (idx & 3) * 131072 + cg * 128);
    if (t == 0) lds_w128(lds0 + L_V + (lane & 15) * VF + 1024, (u32x4){0x3F803F80u, 0x3F803F80u, 0x3F803F80u, 0x3F803F80u});
    {
      const u32 sg = (lane & 4) ? 0x80008000u : 0u;
#pragma unroll
      for (int hd = 0; hd < 4; ++hd) { PIN(fa[hd]); fa[hd] ^= (u32x4){sg, sg, sg, sg}; PIN(fa[hd]); }
    }
    u32 wa = lds0 + L_WQ1 + (8 * h + q) * 128 + 16 * ((2 * blk + (p >> 1)) ^ ((((q >> 1) & 1) << 2) | (h << 1))) + 8 * (p & 1);
    u32 wb = wa ^ 64;
    u32 kra = lds0 + L_KV + (16 * h + q) * 48 + (blk ? 32 : 8 * p);
    u32 o_off = WS_O + ((br * 64 + panel * 4 + t) * 8 + gq) * OREC + 256 + lane * 16;
    u32 c_off = WS_O + ((br * 64 + panel * 4 + t) * 8 + gq) * OREC + lane * 4;
    u32 woa = lds0 + L_WO + (8 * h + q) * 128 + 16 * ((2 * blk + (p & 1)) ^ (((q >> 1) & 1) << 2)) + 8 * (p >> 1);
    u32 wob = woa ^ 64;
    u32 oa = lds0 + L_O + t * OREC + 256 + lane * 16;
    u32 orow = ((panel * 128 + t * 32 + l3) * 1024 + br * 512 + cg * 64 + 8 * l7) * 2;
    PIN(wa); PIN(wb); PIN(kra); PIN(o_off); PIN(c_off); PIN(woa); PIN(wob); PIN(oa); PIN(orow);
    STAMP(2);
    u32x4 fq[2][2][2];
    {
      f32x16 aq[2], pq[2];
      float mq[2];
      aq[0] = aq[1] = zero;
      u32x4 wf[4];
      auto ldw = [&](int n) {
        const int k = n & 31;
        const u32 o = (n < 32 ? wa : wb) + (k < 16 ? (L_WQ0 - L_WQ1) : 0) + (k & 15) * 2048;
        wf[n & 3] = lds_tr2(o, o + 512);
      };
      WAITV(1);
      BAR();
      SCHED();
      STAMP(12);
      ldw(0); ldw(1); ldw(2);
      SCHED();
      BARE();
      SCHED();
#pragma unroll
      for (int n = 0; n < 34; ++n) {
        ldw(n + 3);
        aq[n >> 5] = mfma(wf[n & 3], xq[n & 31], aq[n >> 5]);
        SGB(0x8, 1); SGB(0x100, 2);
        SCHED();
      }
#pragma unroll
      for (int hh = 0; hh < 2; ++hh) pq[hh] = mfma(fa[hh], pack8(aq[0], hh), zero);
      SCHED();
      STAMP(4);
#pragma unroll
      for (int n = 34; n < 64; ++n) {
        if (n + 3 < 64) ldw(n + 3);
        aq[1] = mfma(wf[n & 3], xq[n & 31], aq[1]);
      }
      mq[0] = negmax16(pq[0]); mq[1] = negmax16(pq[1]);
      f32x2 eq[2][8];
      sm_exp_s(pq[0], pq[1], mq, eq);
      SGB(0x8, 1); SGB(0x100, 2); SGB(0x8, 1); SGB(0x100, 2);
#pragma unroll
      for (int n = 36; n < 41; ++n) { SGB(0x8, 1); SGB(0x100, 2); SGB(0x402, 4); }
#pragma unroll
      for (int n = 41; n < 64; ++n) { SGB(0x8, 1); SGB(0x100, 2); if (n & 1) SGB(0x402, 3); else SGB(0x402, 2); }
#pragma unroll
      for (int i = 0; i < 16; ++i) PIN(eq[i >> 3][i & 7]);
      SCHED();
      STAMP(3);
#pragma unroll
      for (int hh = 0; hh < 2; ++hh) { pq[hh] = mfma(fa[2 + hh], pack8(aq[1], hh), zero); PIN(pq[hh]); }
      SCHED();
      sm_finish(eq, fq[0]);
      SCHED();
      asm volatile("s_waitcnt vmcnt(0)" : "+v"(tho), "+v"(thq)::"memory");
      mq[0] = negmax16(pq[0]); mq[1] = negmax16(pq[1]);
      PIN(mq[0]); PIN(mq[1]);
      SCHED();
#pragma unroll
      for (int i = 0; i < 4; ++i) PIN(fq[0][i >> 1][i & 1]);
      softmax16x2(pq[0], pq[1], mq, fq[1]);
#pragma unroll
      for (int i = 0; i < 4; ++i) PIN(fq[1][i >> 1][i & 1]);
    }
    STAMP(5);
    BAR();
    STAMP(6);
    u32x4 ob[2][4];
    {
      u32x4 a0[4], a1[4];
#pragma unroll
      for (int hd = 0; hd < 4; ++hd) {
        a0[hd] = lds_tr2(kra + hd * 1536, kra + hd * 1536 + 4 * 48);
        a1[hd] = lds_tr2(kra + hd * 1536 + 8 * 48, kra + hd * 1536 + 12 * 48);
      }
      SCHED();
      f32x16 y[4];
      u32 cs = 0;
      auto num = [&](int hd) {
        y[hd] = mfma(a0[hd], fq[hd >> 1][hd & 1][0], zero);
        y[hd] = mfma(a1[hd], fq[hd >> 1][hd & 1][1], y[hd]);
      };
      auto fin = [&](int hd) {
        const f32x16& yy = y[hd];
        const float rd = __builtin_amdgcn_rcpf(yy[8] + 1e-6f);
        u32 w[4];
#pragma unroll
        for (int e = 0; e < 4; ++e) { float t0 = yy[2 * e] * rd, t1 = yy[2 * e + 1] * rd; if (hd < 2) OPQ(t0); w[e] = pk2(t0, t1); }
        ob[0][hd] = (u32x4){w[0], w[1], w[2], w[3]};
        st128c(ob[0][hd], wsr, o_off + hd * 1024);
        cs = sum4(cs, ob[0][hd]);
      };
      num(0); num(1); PIN(y[0]); PIN(y[1]); SCHED();
      num(2); fin(0); SGB(0x8, 1); SGB(0x402, 6); SGB(0x8, 1); PIN(y[2]); SCHED();
      num(3); fin(1); SGB(0x8, 1); SGB(0x402, 6); SGB(0x8, 1); PIN(y[3]); SCHED();
      fin(2); fin(3);
      st32c(key - cs, wsr, c_off);
    }
    SCHED();
    if (TIMER == 0) BARE();
    STAMP(7);
    u32x4 wo[32][2];
    auto ldwo = [&](int n) {
      wo[n][0] = lds_tr2(woa + n * 2048, woa + n * 2048 + 512);
      wo[n][1] = lds_tr2(wob + n * 2048, wob + n * 2048 + 512);
    };
#pragma unroll
    for (int n = 0; n < 16; ++n) ldwo(n);
    f32x16 c0 = zero, c1 = zero;
#pragma unroll
    for (int s = 0; s < 4; ++s) { c0 = mfma(wo[s][0], ob[0][s], c0); c1 = mfma(wo[s][1], ob[0][s], c1); }
#pragma unroll
    for (int k = 4; k < 16; ++k) { PIN(wo[k][0]); PIN(wo[k][1]); }
    PIN(c0); PIN(c1);
    if (TIMER) WAITV(0);
    BARE();
    SCHED();
#pragma unroll
    for (int n = 16; n < 24; ++n) ldwo(n);
#pragma unroll
    for (int k = 16; k < 24; ++k) { PIN(wo[k][0]); PIN(wo[k][1]); }
    const __amdgpu_buffer_rsrc_t outr = __builtin_amdgcn_make_buffer_rsrc(KPTR(kb, 0), 0, 2048 * 1024 * 2, SRD3);
    u32 oc = oa - 256 - lane * 12, l4 = lane * 4, l16 = lane * 16, oa4 = oa + 4 * (4 * OREC), oc4 = oc + 4 * (4 * OREC);
    u32 sw1 = lds0 + t * 4096 + r * 128 + 16 * (h ^ (r & 7)), sr1 = lds0 + t * 4096 + l3 * 128 + 16 * (l7 ^ l3);
    u32 sw2 = sw1 ^ 32, sw3 = sw1 ^ 64, sw4 = sw1 ^ 96, or1 = orow + 16384, or2 = orow + 32768, or3 = orow + 49152;
    PIN(oc); PIN(l4); PIN(l16); PIN(sw1); PIN(sr1); PIN(oa4); PIN(oc4); PIN(sw2); PIN(sw3); PIN(sw4); PIN(or1); PIN(or2); PIN(or3);
    STAMP(8);
    u32 nretry = 0;
    u32 chk[2];
    auto oread = [&](int j) {
      const u32 da = j % 5 == 4 ? oa4 : oa + (j % 5) * (4 * OREC), dc = j % 5 == 4 ? oc4 : oc + (j % 5) * (4 * OREC);
#pragma unroll
      for (int s = 0; s < 4; ++s) ob[j & 1][s] = lds_r128(da + s * 1024);
      chk[j & 1] = *(LAS u32*)(size_t)dc;
    };
    auto obad = [&](int j) -> bool {
      const u32 c = sum4(sum4(chk[j & 1], ob[j & 1][0]), ob[j & 1][1]) + sum4(sum4(0u, ob[j & 1][2]), ob[j & 1][3]);
      return __builtin_amdgcn_ballot_w64(c != key) != 0;
    };
#pragma unroll
    for (int j = 1; j < 8; ++j) {
      BARE();
      SCHED();
      oread(j);
      if (j >= 2) {
#pragma unroll
        for (int s = 0; s < 4; ++s) {
          const int k = 4 * (j - 1) + s;
          c0 = mfma(wo[k][0], ob[(j - 1) & 1][s], c0);
          c1 = mfma(wo[k][1], ob[(j - 1) & 1][s], c1);
        }
        if (j >= 3 && j <= 6) { ldwo(24 + 2 * (j - 3)); ldwo(25 + 2 * (j - 3)); }
      }
      const bool bad = obad(j);
      if (j >= 3 && j <= 6) {
        SGB(0x8, 1); SGB(0x100, 3); SGB(0x8, 1); SGB(0x100, 2); SGB(0x8, 1); SGB(0x100, 3); SGB(0x8, 1); SGB(0x100, 3);
        SGB(0x8, 1); SGB(0x2, 4); SGB(0x8, 1); SGB(0x2, 5); SGB(0x8, 1); SGB(0x100, 2); SGB(0x8, 1);
      } else if (j >= 2) {
        SGB(0x8, 1); SGB(0x100, 3); SGB(0x8, 1); SGB(0x100, 2); SGB(0x8, 3); SGB(0x2, 4); SGB(0x8, 1); SGB(0x2, 5); SGB(0x8, 2);
      }
      SCHED();
      if (__builtin_expect(bad, 0)) {
#pragma unroll 1
        for (u32 spin = 0; spin < SPIN_CAP; ++spin) {
          dma_orec(lds0 + L_O + t * OREC + (j % 5) * (4 * OREC), l4, l16, wsr, WS_O + ((br * 64 + panel * 4 + t) * 8 + ochunk(j)) * OREC);
#pragma unroll 1
          for (u32 jj = j + 1; jj <= (j + AHEAD < 7 ? j + AHEAD : 7); ++jj) {
            const u32 sl = (jj >= 5 ? jj - 5 : jj) * (4 * OREC);
            u32 c = *(LAS u32*)(size_t)(oc + sl);
#pragma unroll
            for (int s = 0; s < 4; ++s) c = sum4(c, lds_r128(oa + sl + s * 1024));
            if (__builtin_amdgcn_ballot_w64(c != key) != 0)
              dma_orec(lds0 + L_O + t * OREC + sl, l4, l16, wsr, WS_O + ((br * 64 + panel * 4 + t) * 8 + ochunk(jj)) * OREC);
          }
          WAITV(0);
          oread(j);
          ++nretry;
          if (!obad(j)) break;
        }
      }
    }
#pragma unroll
    for (int s = 0; s < 4; ++s) c0 = mfma(wo[28 + s][0], ob[1][s], c0);
    PIN(c0);
    SCHED();
#pragma unroll
    for (int s = 0; s < 4; ++s) c1 = mfma(wo[28 + s][1], ob[1][s], c1);
    lds_w128(sw1, pack8(c0, 0));
    lds_w128(sw2, pack8(c0, 1));
    SGB(0x8, 2); SGB(0x2, 4); SGB(0x200, 1); SGB(0x8, 1); SGB(0x2, 4); SGB(0x200, 1); SGB(0x8, 1);
    SCHED();
    STAMP(9);
    lds_w128(sw3, pack8(c1, 0));
    lds_w128(sw4, pack8(c1, 1));
    asm volatile("" ::: "memory");
    {
      u32x4 v[4];
#pragma unroll
      for (int i = 0; i < 4; ++i) v[i] = lds_r128(sr1 + i * 1024);
      SCHED();
      st128c(v[0], outr, orow);
      st128c(v[1], outr, or1);
      st128c(v[2], outr, or2);
      st128c(v[3], outr, or3);
    }
    if (t == 0 && panel == 0 && grp == 0 && lane == 0) st32c(key, wsr, WS_EPOCH + br * 256);
    if (PROBE_N == 13) WAITV(0);
    STAMP(13);
    STAMP_OUT();
  }
}
}

extern "C" void kernel_launch(void* const* d_in, const int* in_sizes, int n_in,
                              void* d_out, int out_size, void* d_ws, size_t ws_size,
                              hipStream_t stream) {
  hh::hedgehog<<<256, 512, 0, stream>>>((const u16*)d_in[0], (const u16*)d_in[1], (const u16*)d_in[2], (const u16*)d_in[3],
                                        (const u16*)d_in[4], (const u16*)d_in[5], (const u16*)d_in[6], (const u16*)d_in[7],
                                        (u16*)d_out, (u32*)((char*)d_ws + ((size_t)WSOFF << 20)));
}
```

```cpp
#define PROBE_N 27
#define PROBE_ROLE 0
#include <hip/hip_runtime.h>
#include <stdint.h>

typedef unsigned short u16;
typedef unsigned u32;
typedef short bf16x8 __attribute__((ext_vector_type(8)));
typedef short s16x4 __attribute__((ext_vector_type(4)));
typedef float f32x16 __attribute__((ext_vector_type(16)));
typedef float f32x4 __attribute__((ext_vector_type(4)));
typedef float f32x2 __attribute__((ext_vector_type(2)));
typedef unsigned u32x8 __attribute__((ext_vector_type(8)));
typedef unsigned u32x4 __attribute__((ext_vector_type(4)));
typedef unsigned u32x2 __attribute__((ext_vector_type(2)));
typedef __bf16 bf16x2 __attribute__((ext_vector_type(2)));
#define DI __device__ __forceinline__
#define LAS __attribute__((address_space(3)))
#define CAS __attribute__((address_space(4)))
#define BC(T, x) __builtin_bit_cast(T, x)

#define DELAY1 0
#define DELAY3 3
#define ROTMODE 242
#define CODE_PF 27648
#define AHEAD 2
#define WSOFF 0
#define GQ 4
#define GC 2
#define WFIRST 0
#define FOLLOW 0
#define TIMER 2
#define LTO 1
#define NTX 0
#define NTW 0
#define WQSWAP (ROTMODE >= 7)
#define CSH 0
#define WQ4 1
#define H3 0
#define LTQ 0

namespace hh {
constexpr int AUX_SC1 = 16;
constexpr u32 SRD3 = 0x00020000u;
constexpr u32 WS_EPOCH = 0;
constexpr u32 REC = 1280;
constexpr u32 WS_PART = 4096;
constexpr u32 OREC = 4352;
constexpr u32 WS_O = WS_PART + 2 * 32 * 16 * REC;
constexpr u32 WS_END = WS_O + 2 * 64 * 8 * OREC;
constexpr u32 WS_STAMP = 8u << 20;
constexpr u32 SLOT = 32768;
constexpr u32 L_WQ0 = 98304;
constexpr u32 L_WQ1 = 65536;
constexpr u32 L_WPHI = 163840 - 4096;
constexpr u32 L_PHI = 0;
constexpr u32 VF = 1040;
constexpr u32 L_V = 131072;
constexpr u32 L_KV = L_V + 16 * VF;
constexpr u32 L_WO = 0;
constexpr u32 L_O = 65536;
constexpr u32 SPIN_CAP = 1u << 16;

DI u32 pk2(float a, float b) { return BC(u32, __builtin_convertvector((f32x2){a, b}, bf16x2)); }
DI u32x4 pack8(const f32x16& x, int s) {
  return (u32x4){pk2(x[8 * s], x[8 * s + 1]), pk2(x[8 * s + 2], x[8 * s + 3]), pk2(x[8 * s + 4], x[8 * s + 5]), pk2(x[8 * s + 6], x[8 * s + 7])};
}
DI f32x16 mfma(u32x4 a, u32x4 b, f32x16 c) { return __builtin_amdgcn_mfma_f32_32x32x16_bf16(BC(bf16x8, a), BC(bf16x8, b), c, 0, 0, 0); }
DI f32x4 mfma16(u32x4 a, u32x4 b, f32x4 c) { return __builtin_amdgcn_mfma_f32_16x16x32_bf16(BC(bf16x8, a), BC(bf16x8, b), c, 0, 0, 0); }
DI u32x4 lds_r128(u32 a) { return *(LAS u32x4*)(size_t)a; }
DI void lds_w128(u32 a, u32x4 v) { *(LAS u32x4*)(size_t)a = v; }
DI u32x4 lds_tr2(u32 a0, u32 a1) {
  s16x4 lo = __builtin_amdgcn_ds_read_tr16_b64_v4i16((LAS s16x4*)(size_t)a0);
  s16x4 hi = __builtin_amdgcn_ds_read_tr16_b64_v4i16((LAS s16x4*)(size_t)a1);
  return BC(u32x4, __builtin_shufflevector(lo, hi, 0, 1, 2, 3, 4, 5, 6, 7));
}
DI void dma1(u32 lds, u32 voff, __amdgpu_buffer_rsrc_t srd, u32 soff) {
  asm volatile("s_mov_b32 m0, %0\n\ts_nop 0\n\tbuffer_load_dwordx4 %1, %2, %3 offen lds" ::"s"(lds), "v"(voff), "s"(srd), "s"(soff) : "memory");
}
DI void dma4(u32 lds, const u32 (&v)[4], __amdgpu_buffer_rsrc_t srd, u32 soff) {
  asm volatile("s_mov_b32 m0, %0\n\ts_nop 0\n\t"
               "buffer_load_dwordx4 %1, %5, %6 offen lds\n\t"
               "buffer_load_dwordx4 %2, %5, %6 offen offset:1024 lds\n\t"
               "buffer_load_dwordx4 %3, %5, %6 offen offset:2048 lds\n\t"
               "buffer_load_dwordx4 %4, %5, %6 offen offset:3072 lds" ::"s"(lds), "v"(v[0]), "v"(v[1]), "v"(v[2]), "v"(v[3]), "s"(srd), "s"(soff) : "memory");
}
#define DMA4P(name, POL)                                                                       \
  DI void name(u32 lds, const u32 (&v)[4], __amdgpu_buffer_rsrc_t srd, u32 soff) {              \
    asm volatile("s_mov_b32 m0, %0\n\ts_nop 0\n\t"                                             \
                 "buffer_load_dwordx4 %1, %5, %6 offen " POL " lds\n\t"                        \
                 "buffer_load_dwordx4 %2, %5, %6 offen offset:1024 " POL " lds\n\t"            \
                 "buffer_load_dwordx4 %3, %5, %6 offen offset:2048 " POL " lds\n\t"            \
                 "buffer_load_dwordx4 %4, %5, %6 offen offset:3072 " POL " lds" ::"s"(lds), "v"(v[0]), "v"(v[1]), "v"(v[2]), "v"(v[3]), "s"(srd), "s"(soff) : "memory"); \
  }
DMA4P(dma4nt, "nt")
DI void dma_orec(u32 lds, u32 l4, u32 l16, __amdgpu_buffer_rsrc_t srd, u32 soff) {
  asm volatile("s_mov_b32 m0, %0\n\ts_nop 0\n\t"
               "buffer_load_dwordx4 %2, %3, %4 offen offset:256 sc1 lds\n\t"
               "buffer_load_dwordx4 %2, %3, %4 offen offset:1280 sc1 lds\n\t"
               "buffer_load_dwordx4 %2, %3, %4 offen offset:2304 sc1 lds\n\t"
               "buffer_load_dwordx4 %2, %3, %4 offen offset:3328 sc1 lds\n\t"
               "buffer_load_dword %1, %3, %4 offen sc1 lds" ::"s"(lds), "v"(l4), "v"(l16), "s"(srd), "s"(soff) : "memory");
}
#define TOUCH(d, voff, srd, soff) asm volatile("buffer_load_dword %0, %1, %2, %3 offen" : "=v"(d) : "v"(voff), "s"(srd), "s"(soff) : "memory")
DI void st128c(u32x4 v, __amdgpu_buffer_rsrc_t srd, u32 off) { __builtin_amdgcn_raw_buffer_store_b128(v, srd, off, 0, AUX_SC1); }
DI void st32c(u32 v, __amdgpu_buffer_rsrc_t srd, u32 off) { __builtin_amdgcn_raw_buffer_store_b32(v, srd, off, 0, AUX_SC1); }
#define BAR() asm volatile("s_waitcnt lgkmcnt(0)\n\ts_barrier" ::: "memory")
#define BARE() asm volatile("s_barrier" ::: "memory")
#define OPQ(x) asm("" : "+v"(x))
#define PIN(x) asm volatile("" : "+v"(x))
#define PINS(x) asm volatile("" : "+s"(x))
#define WAITV(n) asm volatile("s_waitcnt vmcnt(" #n ")" ::: "memory")
#define SCHED() __builtin_amdgcn_sched_barrier(0)
#define SGB(m, n) __builtin_amdgcn_sched_group_barrier(m, n, 0)
DI u32 sum4(u32 c, u32x4 v) { return (c + v.x + v.y) + v.z + v.w; }
DI u32 pair_sum(u32 s) { auto t = __builtin_amdgcn_permlane32_swap(s, s, false, false); return t[0] + t[1]; }

constexpr float LOG2E = 1.44269504088896340736f;
DI float max3f(float a, float b, float c) { return __builtin_fmaxf(__builtin_fmaxf(a, b), c); }
DI float negmax16(const f32x16& x) {
  const float a = max3f(x[0], x[1], x[2]), b = max3f(x[3], x[4], x[5]), c = max3f(x[6], x[7], x[8]);
  const float d = max3f(x[9], x[10], x[11]), e = max3f(x[12], x[13], x[14]);
  return -max3f(max3f(a, b, x[15]), max3f(c, d, e), a) * LOG2E;
}
DI void sm_exp(const f32x16& p, const f32x16& q, const float (&mc)[2], f32x2 (&e)[2][8]) {
#pragma unroll
  for (int i = 0; i < 8; ++i) {
    const f32x2 t = __builtin_elementwise_fma((f32x2){p[2 * i], p[2 * i + 1]}, (f32x2){LOG2E, LOG2E}, (f32x2){mc[0], mc[0]});
    const f32x2 u = __builtin_elementwise_fma((f32x2){q[2 * i], q[2 * i + 1]}, (f32x2){LOG2E, LOG2E}, (f32x2){mc[1], mc[1]});
    e[0][i] = (f32x2){__builtin_amdgcn_exp2f(t.x), __builtin_amdgcn_exp2f(t.y)};
    e[1][i] = (f32x2){__builtin_amdgcn_exp2f(u.x), __builtin_amdgcn_exp2f(u.y)};
  }
  SGB(0x2, 4); SGB(0x400, 8); SGB(0x2, 4); SGB(0x400, 8); SGB(0x2, 6); SGB(0x400, 8); SGB(0x2, 8); SGB(0x400, 8);
}
DI void sm_exp_s(const f32x16& p, const f32x16& q, const float (&mc)[2], f32x2 (&e)[2][8]) {
#pragma unroll
  for (int i = 0; i < 16; ++i) {
    e[0][i >> 1][i & 1] = __builtin_amdgcn_exp2f(__builtin_fmaf(p[i], LOG2E, mc[0]));
    e[1][i >> 1][i & 1] = __builtin_amdgcn_exp2f(__builtin_fmaf(q[i], LOG2E, mc[1]));
  }
}
DI void sm_finish(const f32x2 (&e)[2][8], u32x4 (&o)[2][2]) {
  float r[2];
#pragma unroll
  for (int u = 0; u < 2; ++u) {
    const f32x2 s = ((e[u][0] + e[u][1]) + (e[u][2] + e[u][3])) + ((e[u][4] + e[u][5]) + (e[u][6] + e[u][7]));
    r[u] = __builtin_amdgcn_rcpf(s.x + s.y);
  }
#pragma unroll
  for (int u = 0; u < 2; ++u) {
    u32 w[8];
#pragma unroll
    for (int i = 0; i < 8; ++i) { const f32x2 t = e[u][i] * (f32x2){r[u], r[u]}; w[i] = pk2(t.x, t.y); }
    o[u][0] = (u32x4){w[0], w[1], w[2], w[3]};
    o[u][1] = (u32x4){w[4], w[5], w[6], w[7]};
  }
}
DI void softmax16x2(const f32x16& p, const f32x16& q, const float (&mc)[2], u32x4 (&o)[2][2]) {
  f32x2 e[2][8];
  sm_exp(p, q, mc, e);
  sm_finish(e, o);
}

#define STAMP(i) do { if ((i) == PROBE_N) { SCHED(); asm volatile("s_memrealtime %0\n\ts_waitcnt lgkmcnt(0)" : "=s"(pt1) :: "memory"); SCHED(); } } while (0)
#define STAMP_OUT() do { if (PROBE_N && (wave >> 2) == PROBE_ROLE) { unsigned long long d_ = pt1 - pt0; if (d_ > 100000) d_ = 100000; unsigned long long e_ = pt0 + 3000 + 8 * d_; \
  for (int it_ = 0; it_ < 1000000; ++it_) { unsigned long long t_; asm volatile("s_memrealtime %0\n\ts_waitcnt lgkmcnt(0)" : "=s"(t_) :: "memory"); if (t_ >= e_) break; } } } while (0)

__global__ __launch_bounds__(512, 2) void hedgehog(const u16* xA, const u16* wA, const u16* pA, const u16* oA,
                                                   const u16* xB, const u16* wB, const u16* pB, const u16* oB,
                                                   u16* out_, u32* ws_) {
  __shared__ __attribute__((aligned(16))) char smem[163840];
  const u32 lds0 = (u32)(size_t)(LAS char*)smem;
  unsigned long long pt0 = __builtin_amdgcn_s_memrealtime(), pt1 = pt0;
  const int bid = blockIdx.x, xcd = bid & 7, idx = bid >> 3;
  const int br = xcd >> 2;
#define KLOAD(tail)                                                                                       \
  u32x8 ka;                                                                                               \
  u32x4 kb;                                                                                               \
  asm volatile("s_load_dwordx8 %0, %2, %3\n\ts_load_dwordx4 %1, %2, 0x40" tail : "=&s"(ka), "=&s"(kb)     \
               : "s"(__builtin_amdgcn_kernarg_segment_ptr()), "s"(br * 32))
#define KWAIT() asm volatile("s_waitcnt lgkmcnt(0)" : "+s"(ka), "+s"(kb))
#define KPTR(v, i) ((void*)(((unsigned long long)(v)[(i) + 1] << 32) | (v)[i]))
  unsigned long long pcs;
  asm volatile("s_getpc_b64 %0" : "=s"(pcs));
  pcs &= ~255ull;
  const int lane = threadIdx.x & 63;
  const int wave = __builtin_amdgcn_readfirstlane(threadIdx.x >> 6);
  const int panel = (xcd & 3) * 4 + (idx & 3), grp = idx >> 2, gq = (grp + GQ) & 7, cg = (grp + GC) & 7;
  auto ochunk = [&](u32 j) -> u32 {
    const int v = (int)j + (((int)j - 1 - gq) >> 31), z = ((int)j - 1) >> 31;
    return (u32)__builtin_amdgcn_readfirstlane((v & ~z) | (gq & z));
  };
  f32x16 zero;
#pragma unroll
  for (int i = 0; i < 16; ++i) zero[i] = 0.f;
#define LANE_IDS()                                                                            \
  u32 lane_ = lane;                                                                           \
  PIN(lane_);                                                                                 \
  const u32 r = lane_ & 31, h = lane_ >> 5;                                                   \
  const u32 blk = (lane_ >> 4) & 1, q = (lane_ & 15) >> 2, p = lane_ & 3;                     \
  u32* ws = (u32*)KPTR(kb, 2);                                                                \
  const __amdgpu_buffer_rsrc_t wsr = __builtin_amdgcn_make_buffer_rsrc(ws, 0, WS_END, SRD3);  \
  const __amdgpu_buffer_rsrc_t wor = __builtin_amdgcn_make_buffer_rsrc(KPTR(ka, 6), 0, 512 * 512 * 2, SRD3)

  if (wave >= 4) {
    const int cw = wave - 4;
    const int rh = cw >> 1, hp = cw & 1;
    KLOAD("\n\ts_waitcnt lgkmcnt(0)");
    u32 sp0, sp1, sp2, sp3;
    {
      u32 off = idx * 1024 + cw * 256;
      off = off < CODE_PF ? off : 0;
      asm volatile("s_load_dword %0, %4, %5 offset:0\n\ts_load_dword %1, %4, %5 offset:64\n\ts_load_dword %2, %4, %5 offset:128\n\ts_load_dword %3, %4, %5 offset:192"
                   : "=&s"(sp0), "=&s"(sp1), "=&s"(sp2), "=&s"(sp3) : "s"(pcs), "s"(off));
    }
    LANE_IDS();
    u32 key = *(const CAS u32*)((const char*)ws + WS_EPOCH + br * 256) + 1u;
    u32 xa[2], xb[2], wa[2], wb2[2];
    {
      const u32 r16 = lane_ & 15, g = lane_ >> 4;
#pragma unroll
      for (int s = 0; s < 2; ++s) {
        xa[s] = lds0 + (64 * rh + r16) * 128 + 16 * ((4 * s + g) ^ (r16 >> 1));
        wa[s] = lds0 + 16384 + (8 * g + q) * 128 + 16 * ((4 * hp + 2 * s + (p >> 1)) ^ ((((q >> 1) & 1) << 2) | (blk << 1))) + 8 * (p & 1);
        xb[s] = xa[s] + 2 * SLOT; wb2[s] = wa[s] + 2 * SLOT;
        PIN(xa[s]); PIN(xb[s]); PIN(wa[s]); PIN(wb2[s]);
      }
    }
    u32 fpa = lds0 + L_WPHI + hp * 1024 + (8 * h + q) * 32 + (8 * blk + 4 * (p >> 1)) * 2;
    PIN(fpa);
    PINS(key);
    f32x4 ak[4][2], av[4][2];
#pragma unroll
    for (int i = 0; i < 8; ++i) ak[i >> 1][i & 1] = av[i >> 1][i & 1] = (f32x4){0.f, 0.f, 0.f, 0.f};
    u32 vra, pra, vwa, pw0, pw1, pub16, pub4;
    u32x4 xf[2][4], wk[2][2], wv[2][2];
    auto ldx = [&](int c, int s) {
      const u32 x = c % 3 == 2 ? xb[s] : xa[s] + (c % 3) * SLOT;
#pragma unroll
      for (int i = 0; i < 4; ++i) xf[s][i] = lds_r128(x + i * 2048);
    };
    auto ldw = [&](int c, int s, int m) {
#pragma unroll
      for (int n = 0; n < 2; ++n) {
        const u32 w = (c % 3 == 2 ? wb2[n] : wa[n] + (c % 3) * SLOT) + s * 4096 + m * 8192;
        (m ? wv : wk)[s][n] = lds_tr2(w, w + 512);
      }
    };
    auto mk = [&](int s) {
#pragma unroll
      for (int i = 0; i < 8; ++i) ak[i >> 1][i & 1] = mfma16(wk[s][i & 1], xf[s][i >> 1], ak[i >> 1][i & 1]);
    };
    auto mv = [&](int s) {
#pragma unroll
      for (int i = 0; i < 8; ++i) av[i >> 1][i & 1] = mfma16(xf[s][i >> 1], wv[s][i & 1], av[i >> 1][i & 1]);
    };
    auto mix = [&](int n) {
#pragma unroll
      for (int i = 0; i < 8; ++i) {
        SGB(0x8, 1);
        if (n == 12) { if (i < 4) SGB(0x100, 2); else SGB(0x100, 1); }
        else if (n == 8) SGB(0x100, 1);
        else if (n == 4) { if (i < 4) SGB(0x100, 1); }
        else if (n < 0) SGB(0x6, 2);
      }
      SCHED();
    };
    BAR();
    asm volatile("" ::"s"(sp0), "s"(sp1), "s"(sp2), "s"(sp3));
    STAMP(1);
    ldw(0, 0, 0); ldx(0, 0); ldw(0, 0, 1);
    u32x4 fa[2];
#pragma unroll
    for (int hh = 0; hh < 2; ++hh) fa[hh] = lds_tr2(fpa + hh * 512, fpa + hh * 512 + 128);
    SCHED();
#pragma unroll
    for (int c = 0; c < 7; ++c) {
      ldw(c, 1, 0); ldx(c, 1); mk(0); mix(8);
      ldw(c, 1, 1); mv(0); mix(4);
      u32 ln = lane;
      PIN(ln);
      const u32 r_ = ln & 31, h_ = ln >> 5, q_ = (ln & 15) >> 2;
      if (c == 1) {
        const u32 sg = (ln & 4) ? 0x80008000u : 0u;
#pragma unroll
        for (int hh = 0; hh < 2; ++hh) { OPQ(fa[hh]); fa[hh] ^= (u32x4){sg, sg, sg, sg}; }
      }
      if (c == 2) vra = lds0 + L_V + (cw >> 1) * (2 * VF) + (r_ < 16 ? (ln + 16 * (cw & 1)) * 16 : 1024);
      if (c == 3) pra = lds0 + L_PHI + cw * 8192 + (4 * h_ + q_) * 64 + 16 * ((((ln >> 3) & 2) + ((ln >> 1) & 1)) ^ ((2 * h_ + (q_ >> 1)) & 3)) + 8 * (ln & 1);
      if (c == 4) {
        const u32 rec = WS_PART + ((br * 32 + grp * 4 + cw) * 16 + panel) * REC;
        pub16 = rec + ln * 16; pub4 = rec + 1024 + ln * 4;
      }
      if (c == 5) { pw0 = lds0 + L_PHI + (2 * hp) * 8192 + (64 * rh + r_) * 64 + 16 * ((2 * h_) ^ ((r_ >> 1) & 3)); pw1 = pw0 ^ 16; }
      if (c == 6) vwa = lds0 + L_V + (8 * rh + 2 * hp) * VF + ((ln & 16) * 2 + (ln & 15)) * 16 + h_ * 8;
      mk(1); mix(c >= 1 ? -1 : 0);
      if (c == 1) { PIN(fa[0]); PIN(fa[1]); }
      if (c == 2) PIN(vra);
      if (c == 3) PIN(pra);
      if (c == 4) { PIN(pub16); PIN(pub4); }
      if (c == 5) { PIN(pw0); PIN(pw1); }
      if (c == 6) PIN(vwa);
      BAR();
      ldw(c + 1, 0, 0); ldx(c + 1, 0); ldw(c + 1, 0, 1);
      mv(1); mix(12);
    }
    ldw(7, 1, 0); ldx(7, 1); ldw(7, 1, 1); mk(0); mix(12);
    mk(1); mix(0);
    __builtin_amdgcn_s_setprio(2);
    STAMP(2);
    f32x16 pk[2][2];
    {
      u32x4 kf[2][2];
      auto kpack = [&](int i) {
#pragma unroll
        for (int hh = 0; hh < 2; ++hh) {
          const f32x4 &t0 = ak[2 * i][hh], &t1 = ak[2 * i + 1][hh];
          const auto lo = __builtin_amdgcn_permlane16_swap(pk2(t0[0], t0[1]), pk2(t1[0], t1[1]), false, false);
          const auto hi = __builtin_amdgcn_permlane16_swap(pk2(t0[2], t0[3]), pk2(t1[2], t1[3]), false, false);
          kf[i][hh] = (u32x4){lo[0], hi[0], lo[1], hi[1]};
        }
      };
      mv(0); kpack(0);
      SGB(0x8, 2);
#pragma unroll
      for (int i = 0; i < 6; ++i) { SGB(0x2, 2); SGB(0x8, 1); }
      SCHED();
      pk[0][0] = mfma(fa[0], kf[0][0], zero);
      pk[0][1] = mfma(fa[1], kf[0][1], zero);
      kpack(1);
      SGB(0x8, 1); SGB(0x2, 6); SGB(0x8, 1); SGB(0x2, 6);
      PIN(pk[0][0]); PIN(pk[0][1]); PIN(kf[1][0]); PIN(kf[1][1]);
      SCHED();
      mv(1);
      pk[1][0] = mfma(fa[0], kf[1][0], zero);
      pk[1][1] = mfma(fa[1], kf[1][1], zero);
    }
    float m0[2], m1[2];
    m0[0] = negmax16(pk[0][0]); m0[1] = negmax16(pk[0][1]);
    SGB(0x8, 2);
#pragma unroll
    for (int i = 0; i < 6; ++i) { SGB(0x2, 2); SGB(0x8, 1); }
    SGB(0x8, 1); SGB(0x2, 6); SGB(0x8, 1);
#pragma unroll
    for (int i = 0; i < 8; ++i) PIN(av[i >> 1][i & 1]);
    PIN(pk[1][0]); PIN(pk[1][1]); PIN(m0[0]); PIN(m0[1]);
    SCHED();
#pragma unroll
    for (int i = 0; i < 8; ++i) {
      const f32x4& t = av[i >> 1][i & 1];
      *(LAS u32x2*)(size_t)(vwa + (4 * (i >> 2) + ((i >> 1) & 1)) * VF + (i & 1) * 256) = (u32x2){pk2(t[0], t[1]), pk2(t[2], t[3])};
    }
    SCHED();
    {
      u32x4 o[2][2];
      softmax16x2(pk[0][0], pk[0][1], m0, o);
      lds_w128(pw0, o[0][0]); lds_w128(pw1, o[0][1]);
      lds_w128(pw0 + 8192, o[1][0]); lds_w128(pw1 + 8192, o[1][1]);
      SCHED();
      m1[0] = negmax16(pk[1][0]); m1[1] = negmax16(pk[1][1]);
      PIN(m1[0]); PIN(m1[1]);
      SCHED();
      softmax16x2(pk[1][0], pk[1][1], m1, o);
      lds_w128(pw0 + 2048, o[0][0]); lds_w128(pw1 + 2048, o[0][1]);
      lds_w128(pw0 + 2048 + 8192, o[1][0]); lds_w128(pw1 + 2048 + 8192, o[1][1]);
      SGB(0x402, 26); SGB(0x200, 1); SGB(0x402, 8); SGB(0x200, 1); SGB(0x402, 8); SGB(0x200, 1); SGB(0x402, 8); SGB(0x200, 1);
      SCHED();
      STAMP(30);
      BARE();
      BAR();
      SCHED();
    }
    STAMP(3);
    f32x16 kv = zero;
    {
      u32x4 a[8], b[8];
#pragma unroll
      for (int u = 0; u < 8; ++u) {
        a[u] = lds_r128(vra + ((u >> 1) * 4 + (u & 1)) * VF);
        b[u] = lds_tr2(pra + 16 * u * 64, pra + (16 * u + 8) * 64);
      }
#pragma unroll
      for (int u = 0; u < 8; ++u) kv = mfma(a[u], b[u], kv);
      SGB(0x100, 12); SGB(0x8, 1); SGB(0x100, 6); SGB(0x8, 1); SGB(0x100, 6); SGB(0x8, 6);
    }
    {
      const u32x4 w = pack8(kv, 0);
      st128c(w, wsr, pub16);
      const u32 zb = __float_as_uint(kv[8]);
      const u32 chk = key - pair_sum(sum4(h ? 0u : zb, w));
      st32c(h ? chk : zb, wsr, pub4);
    }
    SCHED();
    STAMP(4);
    u32 l16, l4, kwa, kza, gso[4], vo[4], wso;
    u32x4 sel;
    {
      u32 ln = lane;
      PIN(ln);
#pragma unroll
      for (int i = 0; i < 4; ++i) vo[i] = (ln >> 3) * 1024 + 16 * ((ln & 7) ^ (((ln >> 4) & 1) << 2)) + i * 7168;
#pragma unroll
      for (int i = 0; i < 2; ++i) dma4(lds0 + L_WO + cw * 8192 + i * 4096, vo, wor, ochunk(cw) * 65536 + i * 32768 + cg * 128);
      wso = ochunk(4 + cw) * 65536 + cg * 128;
      if (TIMER == 1) {
#pragma unroll
        for (int i = 0; i < 2; ++i) dma4(lds0 + L_WO + 32768 + cw * 8192 + i * 4096, vo, wor, wso + i * 32768);
      }
      const u32 r_ = ln & 31, h_ = ln >> 5;
      l16 = ln * 16; l4 = ln * 4;
      kwa = lds0 + L_KV + cw * 1536 + r_ * 48 + h_ * 16; kza = lds0 + L_KV + cw * 1536 + r_ * 48 + 32 + h_ * 8;
      const bool hit = r_ < 16 && ((r_ >> 2) & 1) == h_;
      const u32 j = (r_ & 3) + 4 * (r_ >> 3);
#pragma unroll
      for (int e = 0; e < 4; ++e) sel[e] = (hit && (j >> 1) == e) ? ((j & 1) ? 0x3F800000u : 0x00003F80u) : 0u;
#pragma unroll
      for (int i = 0; i < 4; ++i) { gso[i] = WS_PART + ((br * 32 + gq * 4 + cw) * 16 + ((i + idx) & 3) * 4) * REC; PINS(gso[i]); }
      PIN(l16); PIN(l4); PIN(kwa); PIN(kza); PIN(sel); PINS(wso);
    }
    if (DELAY1) __builtin_amdgcn_s_sleep(DELAY1);
    STAMP(10);
    f32x16 gs[4];
    float z[4];
    {
      u32x4 g[16];
      u32 gz[16];
      auto gload = [&](int gi) {
#pragma unroll
        for (int i = 0; i < 4; ++i) {
          g[4 * gi + i] = __builtin_amdgcn_raw_buffer_load_b128(wsr, l16 + i * REC, gso[gi], AUX_SC1);
          gz[4 * gi + i] = __builtin_amdgcn_raw_buffer_load_b32(wsr, l4 + (1024 + i * REC - 1024 * (i == 3)), gso[gi] + 1024 * (i == 3), AUX_SC1);
          SCHED();
        }
      };
      auto gsum = [&](int gi) -> bool {
        u32 c = 0;
#pragma unroll
        for (int i = 0; i < 4; ++i) {
          c = sum4(c + gz[4 * gi + i], g[4 * gi + i]);
          gs[gi] = mfma(sel, g[4 * gi + i], i == 0 ? zero : gs[gi]);
          z[gi] = i == 0 ? __uint_as_float(gz[4 * gi]) : z[gi] + __uint_as_float(gz[4 * gi + i]);
        }
        PIN(z[gi]);
        const bool bad = __builtin_amdgcn_ballot_w64(pair_sum(c) != 4u * key) != 0;
        SCHED();
        return bad;
      };
      gload(0); gload(1); gload(2); gload(3);
      bool b0 = gsum(0), b1 = gsum(1), b2 = gsum(2), b3 = gsum(3);
      STAMP(11);
#pragma unroll 1
      for (u32 spin = 0; (b0 | b1 | b2 | b3) && spin < SPIN_CAP; ++spin) {
        asm volatile("" ::: "memory");
        if (b0) gload(0);
        if (b1) gload(1);
        if (b2) gload(2);
        if (b3) gload(3);
        if (b0) b0 = gsum(0);
        if (b1) b1 = gsum(1);
        if (b2) b2 = gsum(2);
        if (b3) b3 = gsum(3);
      }
    }
    __builtin_amdgcn_s_waitcnt(0x0F70);
    STAMP(5);
    {
      float tt[8];
#pragma unroll
      for (int j = 0; j < 8; ++j) tt[j] = (gs[0][j] + gs[1][j]) + (gs[2][j] + gs[3][j]);
      const float zt = (z[0] + z[1]) + (z[2] + z[3]);
      lds_w128(kwa, (u32x4){pk2(tt[0], tt[1]), pk2(tt[2], tt[3]), pk2(tt[4], tt[5]), pk2(tt[6], tt[7])});
      const u32 zz = pk2(zt, zt);
      *(LAS u32x2*)(size_t)kza = (u32x2){zz, zz};
    }
    if (TIMER == 2) {
#pragma unroll
      for (int i = 0; i < 2; ++i) dma4(lds0 + L_WO + 32768 + cw * 8192 + i * 4096, vo, wor, wso + i * 32768);
    }
    BAR();
    STAMP(6);
    if (TIMER == 3) {
#pragma unroll
      for (int i = 0; i < 2; ++i) dma4(lds0 + L_WO + 32768 + cw * 8192 + i * 4096, vo, wor, wso + i * 32768);
    }
    {
      u32 oso[8], ob = lds0 + L_O + cw * OREC;
#pragma unroll
      for (int i = 1; i < 8; ++i) { oso[i] = WS_O + ((br * 64 + panel * 4 + cw) * 8 + ochunk(i)) * OREC; PINS(oso[i]); }
      PINS(ob);
      if (TIMER == 0) {
        BARE();
        STAMP(7);
#pragma unroll
        for (int i = 0; i < 2; ++i) dma4(lds0 + L_WO + 32768 + cw * 8192 + i * 4096, vo, wor, wso + i * 32768);
      }
      WAITV(0);
      BARE();
      if (DELAY3) __builtin_amdgcn_s_sleep(DELAY3);
      STAMP(8);
      auto oissue = [&](int j) { dma_orec(ob + (j % 5) * (4 * OREC), l4, l16, wsr, oso[j]); };
      oissue(1); oissue(2);
      WAITV(5); BARE(); oissue(3);
      WAITV(5); STAMP(15); BARE(); oissue(4); oissue(5);
      WAITV(10); BARE(); oissue(6);
      WAITV(10); BARE(); oissue(7);
      WAITV(10); BARE();
      WAITV(5); BARE();
      WAITV(0); BARE();
    }
    STAMP(9);
    STAMP_OUT();
  } else {
    KLOAD("");
    const int t = wave;
    const u32 l7 = lane & 7, l3 = lane >> 3, lq = lane >> 4;
    const u32 wsw = 16 * (l7 ^ ((lq & 1) << 2));
    u32 vx[4], vw[4];
#pragma unroll
    for (int i = 0; i < 4; ++i) {
      vx[i] = l3 * 1024 + 16 * (l7 ^ (((i & 1) << 2) | lq)) + i * 7168;
      vw[i] = l3 * 3072 + (wsw ^ ((i & 1) << 5)) + i * (8 * 3072 - 1024);
    }
    int rot = (grp + 2 * (idx & 3)) & 7;
    auto chunk = [&](u32 c) -> u32 {
      constexpr u32 n0 = ROTMODE / 100, n1 = ROTMODE / 10 % 10, n2 = ROTMODE % 10;
      const u32 g = grp + (idx & 3), r3 = g - 3 * ((g * 11) >> 5);
      auto run = [&](u32 k, u32 n) -> u32 {
        if (n == 3) { const u32 v = k + r3; return v >= 3 ? v - 3 : v; }
        return k ^ (g & (n - 1));
      };
      const u32 a = run(c, n0), b = n0 + run(c - n0, n1), d = n0 + n1 + run(c - n0 - n1, n2);
      return ((c < n0 ? a : c < n0 + n1 ? b : d) + CSH) & 7;
    };
    const u32 xs0 = (panel * 128 + t * 32) * 1024, ws0 = (t & 1) * (32 * 3072) + (1 + (t >> 1)) * 1024 + grp * 128;
    const u32 ldst = lds0 + t * 4096;
    u32 e0 = ldst, e1 = ldst + 16384, e2 = xs0 + chunk(0) * 128, e3 = ws0 + chunk(0) * (64 * 3072);
    PINS(e0); PINS(e1); PINS(e2); PINS(e3);
#pragma unroll
    for (int i = 0; i < 4; ++i) { PIN(vx[i]); PIN(vw[i]); }
    KWAIT();
    const __amdgpu_buffer_rsrc_t xr = __builtin_amdgcn_make_buffer_rsrc(KPTR(ka, 0), 0, 2048 * 512 * 2, SRD3);
    const __amdgpu_buffer_rsrc_t wr = __builtin_amdgcn_make_buffer_rsrc(KPTR(ka, 2), 0, 512 * 1536 * 2, SRD3);
    STAMP(10);
    auto dx = [&](int c, u32 d, u32 so) { if (c < NTX) dma4nt(d, vx, xr, so); else dma4(d, vx, xr, so); };
    auto dw = [&](int c, u32 d, u32 so) { if (c < NTW) dma4nt(d, vw, wr, so); else dma4(d, vw, wr, so); };
    if (WFIRST & 1) { dw(0, e1, e3); dx(0, e0, e2); } else { dx(0, e0, e2); dw(0, e1, e3); }
    if (FOLLOW) {
      const int gi = (grp + (ka[2] >> 7) - 4) & 7;
      rot = (gi == 7 ? 0 : gi) + 2 * (idx & 3);
      rot = rot >= 7 ? rot - 7 : rot;
    }
    {
      const __amdgpu_buffer_rsrc_t pr = __builtin_amdgcn_make_buffer_rsrc(KPTR(ka, 4), 0, 32 * 256 * 2, SRD3);
      dma1(lds0 + L_WPHI + t * 1024, lane * 16, pr, (t < 2 ? grp : gq) * 2048 + (t & 1) * 1024);
    }
    STAMP(11);
    u32 th3 = 0, thq = 0, tho;
    if (H3) TOUCH(th3, (l7 >> 1) * 3072 + (l7 & 1) * 1024, wr, (idx * 16 + t * 4) * 3072 + 1024 + ((3 - (ka[2] >> 7)) & 7) * 128);
    if (WFIRST & 2) dw(1, ldst + SLOT + 16384, ws0 + chunk(1) * (64 * 3072));
    dx(1, ldst + SLOT, xs0 + chunk(1) * 128);
    if (!(WFIRST & 2)) dw(1, ldst + SLOT + 16384, ws0 + chunk(1) * (64 * 3072));
    STAMP(1);
    LANE_IDS();
    u32 key = *(const CAS u32*)((const char*)ws + WS_EPOCH + br * 256) + 1u;
    const u32 sw = (r >> 1) & 7;
    u32 xa[4];
#pragma unroll
    for (int s = 0; s < 4; ++s) { xa[s] = lds0 + (32 * t + r) * 128 + 16 * ((2 * s + h) ^ sw); PIN(xa[s]); }
    u32 fpa = lds0 + L_WPHI + 2048 + (4 * h + q) * 32 + (8 * blk + 4 * (p >> 1)) * 2;
    u32 vq = (r >> 3) * 3072 + (r & 7) * 128;
    PIN(fpa); PIN(vq);
    PINS(key);
    u32x4 xq[32], fa[4];
#pragma unroll
    for (int c = 0; c < 8; ++c) {
      u32 d0, d1, s0, s1;
      if (c < 6) {
        d0 = ldst + ((c + 2) % 3) * SLOT; d1 = d0 + 16384;
        s0 = xs0 + chunk(c + 2) * 128; s1 = ws0 + chunk(c + 2) * (64 * 3072);
      } else {
        if (WQ4) {
          const u32 k0 = 2 * ((2 * (c - 6) + idx) & 3) + (t >> 1), k1 = (k0 + 2) & 7;
          d0 = lds0 + L_WQ1 + ((chunk(k0) + 4) & 7) * 8192 + (t & 1) * 4096; d1 = lds0 + L_WQ1 + ((chunk(k1) + 4) & 7) * 8192 + (t & 1) * 4096;
          s0 = (k0 * 64 + (t & 1) * 32) * 3072 + gq * 128; s1 = (k1 * 64 + (t & 1) * 32) * 3072 + gq * 128;
        } else {
          const u32 half = (c - 6) ^ (WQSWAP ? (idx & 1) : 0);
          d0 = lds0 + L_WQ0 - half * (L_WQ0 - L_WQ1) + t * 8192; d1 = d0 + 4096;
          s0 = chunk(half * 4 + t) * (64 * 3072) + gq * 128; s1 = s0 + 32 * 3072;
        }
      }
      PINS(d0); PINS(d1); PINS(s0); PINS(s1);
      if ((H3 && c == 0) || (LTQ && c == 7)) WAITV(9); else WAITV(8);
      if (H3 && c == 1) PIN(th3);
      STAMP(20 + c);
      BAR();
      if (LTQ && c == 6) TOUCH(thq, vq, wr, (idx * 16 + t * 4) * 3072);
      if (WFIRST & 4) dw(c + 2, d1, s1); else if (c < 6) dx(c + 2, d0, s0); else dw(c + 2, d0, s0);
#pragma unroll
      for (int s = 0; s < 4; ++s) xq[4 * c + s] = lds_r128(xa[s] + (c % 3) * SLOT);
      if (!(WFIRST & 4)) dw(c + 2, d1, s1); else if (c < 6) dx(c + 2, d0, s0); else dw(c + 2, d0, s0);
      if (c == 0) {
#pragma unroll
        for (int hd = 0; hd < 4; ++hd) fa[hd] = lds_tr2(fpa + hd * 512, fpa + hd * 512 + 256);
      }
    }
    STAMP(32);
    if (LTO) TOUCH(tho, r * 128, wor, (idx * 16 + t * 4) * 1024);
    else TOUCH(tho, (r + 32 * t) * 1024, wor, (idx & 3) * 131072 + cg * 128);
    if (t == 0) lds_w128(lds0 + L_V + (lane & 15) * VF + 1024, (u32x4){0x3F803F80u, 0x3F803F80u, 0x3F803F80u, 0x3F803F80u});
    {
      const u32 sg = (lane & 4) ? 0x80008000u : 0u;
#pragma unroll
      for (int hd = 0; hd < 4; ++hd) { PIN(fa[hd]); fa[hd] ^= (u32x4){sg, sg, sg, sg}; PIN(fa[hd]); }
    }
    u32 wa = lds0 + L_WQ1 + (8 * h + q) * 128 + 16 * ((2 * blk + (p >> 1)) ^ ((((q >> 1) & 1) << 2) | (h << 1))) + 8 * (p & 1);
    u32 wb = wa ^ 64;
    u32 kra = lds0 + L_KV + (16 * h + q) * 48 + (blk ? 32 : 8 * p);
    u32 o_off = WS_O + ((br * 64 + panel * 4 + t) * 8 + gq) * OREC + 256 + lane * 16;
    u32 c_off = WS_O + ((br * 64 + panel * 4 + t) * 8 + gq) * OREC + lane * 4;
    u32 woa = lds0 + L_WO + (8 * h + q) * 128 + 16 * ((2 * blk + (p & 1)) ^ (((q >> 1) & 1) << 2)) + 8 * (p >> 1);
    u32 wob = woa ^ 64;
    u32 oa = lds0 + L_O + t * OREC + 256 + lane * 16;
    u32 orow = ((panel * 128 + t * 32 + l3) * 1024 + br * 512 + cg * 64 + 8 * l7) * 2;
    PIN(wa); PIN(wb); PIN(kra); PIN(o_off); PIN(c_off); PIN(woa); PIN(wob); PIN(oa); PIN(orow);
    STAMP(2);
    u32x4 fq[2][2][2];
    {
      f32x16 aq[2], pq[2];
      float mq[2];
      aq[0] = aq[1] = zero;
      u32x4 wf[4];
      auto ldw = [&](int n) {
        const int k = n & 31;
        const u32 o = (n < 32 ? wa : wb) + (k < 16 ? (L_WQ0 - L_WQ1) : 0) + (k & 15) * 2048;
        wf[n & 3] = lds_tr2(o, o + 512);
      };
      WAITV(1);
      STAMP(31);
      BAR();
      SCHED();
      STAMP(12);
      ldw(0); ldw(1); ldw(2);
      SCHED();
      BARE();
      SCHED();
#pragma unroll
      for (int n = 0; n < 34; ++n) {
        ldw(n + 3);
        aq[n >> 5] = mfma(wf[n & 3], xq[n & 31], aq[n >> 5]);
        SGB(0x8, 1); SGB(0x100, 2);
        SCHED();
      }
#pragma unroll
      for (int hh = 0; hh < 2; ++hh) pq[hh] = mfma(fa[hh], pack8(aq[0], hh), zero);
      SCHED();
      STAMP(4);
#pragma unroll
      for (int n = 34; n < 64; ++n) {
        if (n + 3 < 64) ldw(n + 3);
        aq[1] = mfma(wf[n & 3], xq[n & 31], aq[1]);
      }
      mq[0] = negmax16(pq[0]); mq[1] = negmax16(pq[1]);
      f32x2 eq[2][8];
      sm_exp_s(pq[0], pq[1], mq, eq);
      SGB(0x8, 1); SGB(0x100, 2); SGB(0x8, 1); SGB(0x100, 2);
#pragma unroll
      for (int n = 36; n < 41; ++n) { SGB(0x8, 1); SGB(0x100, 2); SGB(0x402, 4); }
#pragma unroll
      for (int n = 41; n < 64; ++n) { SGB(0x8, 1); SGB(0x100, 2); if (n & 1) SGB(0x402, 3); else SGB(0x402, 2); }
#pragma unroll
      for (int i = 0; i < 16; ++i) PIN(eq[i >> 3][i & 7]);
      SCHED();
      STAMP(3);
#pragma unroll
      for (int hh = 0; hh < 2; ++hh) { pq[hh] = mfma(fa[2 + hh], pack8(aq[1], hh), zero); PIN(pq[hh]); }
      SCHED();
      sm_finish(eq, fq[0]);
      SCHED();
      asm volatile("s_waitcnt vmcnt(0)" : "+v"(tho), "+v"(thq)::"memory");
      mq[0] = negmax16(pq[0]); mq[1] = negmax16(pq[1]);
      PIN(mq[0]); PIN(mq[1]);
      SCHED();
#pragma unroll
      for (int i = 0; i < 4; ++i) PIN(fq[0][i >> 1][i & 1]);
      softmax16x2(pq[0], pq[1], mq, fq[1]);
#pragma unroll
      for (int i = 0; i < 4; ++i) PIN(fq[1][i >> 1][i & 1]);
    }
    STAMP(5);
    BAR();
    STAMP(6);
    u32x4 ob[2][4];
    {
      u32x4 a0[4], a1[4];
#pragma unroll
      for (int hd = 0; hd < 4; ++hd) {
        a0[hd] = lds_tr2(kra + hd * 1536, kra + hd * 1536 + 4 * 48);
        a1[hd] = lds_tr2(kra + hd * 1536 + 8 * 48, kra + hd * 1536 + 12 * 48);
      }
      SCHED();
      f32x16 y[4];
      u32 cs = 0;
      auto num = [&](int hd) {
        y[hd] = mfma(a0[hd], fq[hd >> 1][hd & 1][0], zero);
        y[hd] = mfma(a1[hd], fq[hd >> 1][hd & 1][1], y[hd]);
      };
      auto fin = [&](int hd) {
        const f32x16& yy = y[hd];
        const float rd = __builtin_amdgcn_rcpf(yy[8] + 1e-6f);
        u32 w[4];
#pragma unroll
        for (int e = 0; e < 4; ++e) { float t0 = yy[2 * e] * rd, t1 = yy[2 * e + 1] * rd; if (hd < 2) OPQ(t0); w[e] = pk2(t0, t1); }
        ob[0][hd] = (u32x4){w[0], w[1], w[2], w[3]};
        st128c(ob[0][hd], wsr, o_off + hd * 1024);
        cs = sum4(cs, ob[0][hd]);
      };
      num(0); num(1); PIN(y[0]); PIN(y[1]); SCHED();
      num(2); fin(0); SGB(0x8, 1); SGB(0x402, 6); SGB(0x8, 1); PIN(y[2]); SCHED();
      num(3); fin(1); SGB(0x8, 1); SGB(0x402, 6); SGB(0x8, 1); PIN(y[3]); SCHED();
      fin(2); fin(3);
      st32c(key - cs, wsr, c_off);
    }
    SCHED();
    if (TIMER == 0) BARE();
    STAMP(7);
    u32x4 wo[32][2];
    auto ldwo = [&](int n) {
      wo[n][0] = lds_tr2(woa + n * 2048, woa + n * 2048 + 512);
      wo[n][1] = lds_tr2(wob + n * 2048, wob + n * 2048 + 512);
    };
#pragma unroll
    for (int n = 0; n < 16; ++n) ldwo(n);
    f32x16 c0 = zero, c1 = zero;
#pragma unroll
    for (int s = 0; s < 4; ++s) { c0 = mfma(wo[s][0], ob[0][s], c0); c1 = mfma(wo[s][1], ob[0][s], c1); }
#pragma unroll
    for (int k = 4; k < 16; ++k) { PIN(wo[k][0]); PIN(wo[k][1]); }
    PIN(c0); PIN(c1);
    if (TIMER) WAITV(0);
    BARE();
    SCHED();
#pragma unroll
    for (int n = 16; n < 24; ++n) ldwo(n);
#pragma unroll
    for (int k = 16; k < 24; ++k) { PIN(wo[k][0]); PIN(wo[k][1]); }
    const __amdgpu_buffer_rsrc_t outr = __builtin_amdgcn_make_buffer_rsrc(KPTR(kb, 0), 0, 2048 * 1024 * 2, SRD3);
    u32 oc = oa - 256 - lane * 12, l4 = lane * 4, l16 = lane * 16, oa4 = oa + 4 * (4 * OREC), oc4 = oc + 4 * (4 * OREC);
    u32 sw1 = lds0 + t * 4096 + r * 128 + 16 * (h ^ (r & 7)), sr1 = lds0 + t * 4096 + l3 * 128 + 16 * (l7 ^ l3);
    u32 sw2 = sw1 ^ 32, sw3 = sw1 ^ 64, sw4 = sw1 ^ 96, or1 = orow + 16384, or2 = orow + 32768, or3 = orow + 49152;
    PIN(oc); PIN(l4); PIN(l16); PIN(sw1); PIN(sr1); PIN(oa4); PIN(oc4); PIN(sw2); PIN(sw3); PIN(sw4); PIN(or1); PIN(or2); PIN(or3);
    STAMP(8);
    u32 nretry = 0;
    u32 chk[2];
    auto oread = [&](int j) {
      const u32 da = j % 5 == 4 ? oa4 : oa + (j % 5) * (4 * OREC), dc = j % 5 == 4 ? oc4 : oc + (j % 5) * (4 * OREC);
#pragma unroll
      for (int s = 0; s < 4; ++s) ob[j & 1][s] = lds_r128(da + s * 1024);
      chk[j & 1] = *(LAS u32*)(size_t)dc;
    };
    auto obad = [&](int j) -> bool {
      const u32 c = sum4(sum4(chk[j & 1], ob[j & 1][0]), ob[j & 1][1]) + sum4(sum4(0u, ob[j & 1][2]), ob[j & 1][3]);
      return __builtin_amdgcn_ballot_w64(c != key) != 0;
    };
#pragma unroll
    for (int j = 1; j < 8; ++j) {
      BARE();
      SCHED();
      oread(j);
      if (j >= 2) {
#pragma unroll
        for (int s = 0; s < 4; ++s) {
          const int k = 4 * (j - 1) + s;
          c0 = mfma(wo[k][0], ob[(j - 1) & 1][s], c0);
          c1 = mfma(wo[k][1], ob[(j - 1) & 1][s], c1);
        }
        if (j >= 3 && j <= 6) { ldwo(24 + 2 * (j - 3)); ldwo(25 + 2 * (j - 3)); }
      }
      const bool bad = obad(j);
      if (j >= 3 && j <= 6) {
        SGB(0x8, 1); SGB(0x100, 3); SGB(0x8, 1); SGB(0x100, 2); SGB(0x8, 1); SGB(0x100, 3); SGB(0x8, 1); SGB(0x100, 3);
        SGB(0x8, 1); SGB(0x2, 4); SGB(0x8, 1); SGB(0x2, 5); SGB(0x8, 1); SGB(0x100, 2); SGB(0x8, 1);
      } else if (j >= 2) {
        SGB(0x8, 1); SGB(0x100, 3); SGB(0x8, 1); SGB(0x100, 2); SGB(0x8, 3); SGB(0x2, 4); SGB(0x8, 1); SGB(0x2, 5); SGB(0x8, 2);
      }
      SCHED();
      if (__builtin_expect(bad, 0)) {
#pragma unroll 1
        for (u32 spin = 0; spin < SPIN_CAP; ++spin) {
          dma_orec(lds0 + L_O + t * OREC + (j % 5) * (4 * OREC), l4, l16, wsr, WS_O + ((br * 64 + panel * 4 + t) * 8 + ochunk(j)) * OREC);
#pragma unroll 1
          for (u32 jj = j + 1; jj <= (j + AHEAD < 7 ? j + AHEAD : 7); ++jj) {
            const u32 sl = (jj >= 5 ? jj - 5 : jj) * (4 * OREC);
            u32 c = *(LAS u32*)(size_t)(oc + sl);
#pragma unroll
            for (int s = 0; s < 4; ++s) c = sum4(c, lds_r128(oa + sl + s * 1024));
            if (__builtin_amdgcn_ballot_w64(c != key) != 0)
              dma_orec(lds0 + L_O + t * OREC + sl, l4, l16, wsr, WS_O + ((br * 64 + panel * 4 + t) * 8 + ochunk(jj)) * OREC);
          }
          WAITV(0);
          oread(j);
          ++nretry;
          if (!obad(j)) break;
        }
      }
    }
#pragma unroll
    for (int s = 0; s < 4; ++s) c0 = mfma(wo[28 + s][0], ob[1][s], c0);
    PIN(c0);
    SCHED();
#pragma unroll
    for (int s = 0; s < 4; ++s) c1 = mfma(wo[28 + s][1], ob[1][s], c1);
    lds_w128(sw1, pack8(c0, 0));
    lds_w128(sw2, pack8(c0, 1));
    SGB(0x8, 2); SGB(0x2, 4); SGB(0x200, 1); SGB(0x8, 1); SGB(0x2, 4); SGB(0x200, 1); SGB(0x8, 1);
    SCHED();
    STAMP(9);
    lds_w128(sw3, pack8(c1, 0));
    lds_w128(sw4, pack8(c1, 1));
    asm volatile("" ::: "memory");
    {
      u32x4 v[4];
#pragma unroll
      for (int i = 0; i < 4; ++i) v[i] = lds_r128(sr1 + i * 1024);
      SCHED();
      st128c(v[0], outr, orow);
      st128c(v[1], outr, or1);
      st128c(v[2], outr, or2);
      st128c(v[3], outr, or3);
    }
    if (t == 0 && panel == 0 && grp == 0 && lane == 0) st32c(key, wsr, WS_EPOCH + br * 256);
    if (PROBE_N == 13) WAITV(0);
    STAMP(13);
    STAMP_OUT();
  }
}
}

extern "C" void kernel_launch(void* const* d_in, const int* in_sizes, int n_in,
                              void* d_out, int out_size, void* d_ws, size_t ws_size,
                              hipStream_t stream) {
  hh::hedgehog<<<256, 512, 0, stream>>>((const u16*)d_in[0], (const u16*)d_in[1], (const u16*)d_in[2], (const u16*)d_in[3],
                                        (const u16*)d_in[4], (const u16*)d_in[5], (const u16*)d_in[6], (const u16*)d_in[7],
                                        (u16*)d_out, (u32*)((char*)d_ws + ((size_t)WSOFF << 20)));
}
```

```cpp
#define PROBE_N 5
#define PROBE_ROLE 1
#include <hip/hip_runtime.h>
#include <stdint.h>

typedef unsigned short u16;
typedef unsigned u32;
typedef short bf16x8 __attribute__((ext_vector_type(8)));
typedef short s16x4 __attribute__((ext_vector_type(4)));
typedef float f32x16 __attribute__((ext_vector_type(16)));
typedef float f32x4 __attribute__((ext_vector_type(4)));
typedef float f32x2 __attribute__((ext_vector_type(2)));
typedef unsigned u32x8 __attribute__((ext_vector_type(8)));
typedef unsigned u32x4 __attribute__((ext_vector_type(4)));
typedef unsigned u32x2 __attribute__((ext_vector_type(2)));
typedef __bf16 bf16x2 __attribute__((ext_vector_type(2)));
#define DI __device__ __forceinline__
#define LAS __attribute__((address_space(3)))
#define CAS __attribute__((address_space(4)))
#define BC(T, x) __builtin_bit_cast(T, x)

#define DELAY1 0
#define DELAY3 3
#define ROTMODE 242
#define CODE_PF 27648
#define AHEAD 2
#define WSOFF 0
#define GQ 4
#define GC 2
#define WFIRST 0
#define FOLLOW 0
#define TIMER 2
#define LTO 1
#define NTX 0
#define NTW 0
#define WQSWAP (ROTMODE >= 7)
#define CSH 0
#define WQ4 1
#define QSET 1
#define H3 0
#define LTQ 0

namespace hh {
constexpr int AUX_SC1 = 16;
constexpr u32 SRD3 = 0x00020000u;
constexpr u32 WS_EPOCH = 0;
constexpr u32 REC = 1280;
constexpr u32 WS_PART = 4096;
constexpr u32 OREC = 4352;
constexpr u32 WS_O = WS_PART + 2 * 32 * 16 * REC;
constexpr u32 WS_END = WS_O + 2 * 64 * 8 * OREC;
constexpr u32 WS_STAMP = 8u << 20;
constexpr u32 SLOT = 32768;
constexpr u32 L_WQ0 = 98304;
constexpr u32 L_WQ1 = 65536;
constexpr u32 L_WPHI = 163840 - 4096;
constexpr u32 L_PHI = 0;
constexpr u32 VF = 1040;
constexpr u32 L_V = 131072;
constexpr u32 L_KV = L_V + 16 * VF;
constexpr u32 L_WO = 0;
constexpr u32 L_O = 65536;
constexpr u32 SPIN_CAP = 1u << 16;

DI u32 pk2(float a, float b) { return BC(u32, __builtin_convertvector((f32x2){a, b}, bf16x2)); }
DI u32x4 pack8(const f32x16& x, int s) {
  return (u32x4){pk2(x[8 * s], x[8 * s + 1]), pk2(x[8 * s + 2], x[8 * s + 3]), pk2(x[8 * s + 4], x[8 * s + 5]), pk2(x[8 * s + 6], x[8 * s + 7])};
}
DI f32x16 mfma(u32x4 a, u32x4 b, f32x16 c) { return __builtin_amdgcn_mfma_f32_32x32x16_bf16(BC(bf16x8, a), BC(bf16x8, b), c, 0, 0, 0); }
DI f32x4 mfma16(u32x4 a, u32x4 b, f32x4 c) { return __builtin_amdgcn_mfma_f32_16x16x32_bf16(BC(bf16x8, a), BC(bf16x8, b), c, 0, 0, 0); }
DI u32x4 lds_r128(u32 a) { return *(LAS u32x4*)(size_t)a; }
DI void lds_w128(u32 a, u32x4 v) { *(LAS u32x4*)(size_t)a = v; }
DI u32x4 lds_tr2(u32 a0, u32 a1) {
  s16x4 lo = __builtin_amdgcn_ds_read_tr16_b64_v4i16((LAS s16x4*)(size_t)a0);
  s16x4 hi = __builtin_amdgcn_ds_read_tr16_b64_v4i16((LAS s16x4*)(size_t)a1);
  return BC(u32x4, __builtin_shufflevector(lo, hi, 0, 1, 2, 3, 4, 5, 6, 7));
}
DI void dma1(u32 lds, u32 voff, __amdgpu_buffer_rsrc_t srd, u32 soff) {
  asm volatile("s_mov_b32 m0, %0\n\ts_nop 0\n\tbuffer_load_dwordx4 %1, %2, %3 offen lds" ::"s"(lds), "v"(voff), "s"(srd), "s"(soff) : "memory");
}
DI void dma4(u32 lds, const u32 (&v)[4], __amdgpu_buffer_rsrc_t srd, u32 soff) {
  asm volatile("s_mov_b32 m0, %0\n\ts_nop 0\n\t"
               "buffer_load_dwordx4 %1, %5, %6 offen lds\n\t"
               "buffer_load_dwordx4 %2, %5, %6 offen offset:1024 lds\n\t"
               "buffer_load_dwordx4 %3, %5, %6 offen offset:2048 lds\n\t"
               "buffer_load_dwordx4 %4, %5, %6 offen offset:3072 lds" ::"s"(lds), "v"(v[0]), "v"(v[1]), "v"(v[2]), "v"(v[3]), "s"(srd), "s"(soff) : "memory");
}
#define DMA4P(name, POL)                                                                       \
  DI void name(u32 lds, const u32 (&v)[4], __amdgpu_buffer_rsrc_t srd, u32 soff) {              \
    asm volatile("s_mov_b32 m0, %0\n\ts_nop 0\n\t"                                             \
                 "buffer_load_dwordx4 %1, %5, %6 offen " POL " lds\n\t"                        \
                 "buffer_load_dwordx4 %2, %5, %6 offen offset:1024 " POL " lds\n\t"            \
                 "buffer_load_dwordx4 %3, %5, %6 offen offset:2048 " POL " lds\n\t"            \
                 "buffer_load_dwordx4 %4, %5, %6 offen offset:3072 " POL " lds" ::"s"(lds), "v"(v[0]), "v"(v[1]), "v"(v[2]), "v"(v[3]), "s"(srd), "s"(soff) : "memory"); \
  }
DMA4P(dma4nt, "nt")
DI void dma_orec(u32 lds, u32 l4, u32 l16, __amdgpu_buffer_rsrc_t srd, u32 soff) {
  asm volatile("s_mov_b32 m0, %0\n\ts_nop 0\n\t"
               "buffer_load_dwordx4 %2, %3, %4 offen offset:256 sc1 lds\n\t"
               "buffer_load_dwordx4 %2, %3, %4 offen offset:1280 sc1 lds\n\t"
               "buffer_load_dwordx4 %2, %3, %4 offen offset:2304 sc1 lds\n\t"
               "buffer_load_dwordx4 %2, %3, %4 offen offset:3328 sc1 lds\n\t"
               "buffer_load_dword %1, %3, %4 offen sc1 lds" ::"s"(lds), "v"(l4), "v"(l16), "s"(srd), "s"(soff) : "memory");
}
#define TOUCH(d, voff, srd, soff) asm volatile("buffer_load_dword %0, %1, %2, %3 offen" : "=v"(d) : "v"(voff), "s"(srd), "s"(soff) : "memory")
DI void st128c(u32x4 v, __amdgpu_buffer_rsrc_t srd, u32 off) { __builtin_amdgcn_raw_buffer_store_b128(v, srd, off, 0, AUX_SC1); }
DI void st32c(u32 v, __amdgpu_buffer_rsrc_t srd, u32 off) { __builtin_amdgcn_raw_buffer_store_b32(v, srd, off, 0, AUX_SC1); }
#define BAR() asm volatile("s_waitcnt lgkmcnt(0)\n\ts_barrier" ::: "memory")
#define BARE() asm volatile("s_barrier" ::: "memory")
#define OPQ(x) asm("" : "+v"(x))
#define PIN(x) asm volatile("" : "+v"(x))
#define PINS(x) asm volatile("" : "+s"(x))
#define WAITV(n) asm volatile("s_waitcnt vmcnt(" #n ")" ::: "memory")
#define SCHED() __builtin_amdgcn_sched_barrier(0)
#define SGB(m, n) __builtin_amdgcn_sched_group_barrier(m, n, 0)
DI u32 sum4(u32 c, u32x4 v) { return (c + v.x + v.y) + v.z + v.w; }
DI u32 pair_sum(u32 s) { auto t = __builtin_amdgcn_permlane32_swap(s, s, false, false); return t[0] + t[1]; }

constexpr float LOG2E = 1.44269504088896340736f;
DI float max3f(float a, float b, float c) { return __builtin_fmaxf(__builtin_fmaxf(a, b), c); }
DI float negmax16(const f32x16& x) {
  const float a = max3f(x[0], x[1], x[2]), b = max3f(x[3], x[4], x[5]), c = max3f(x[6], x[7], x[8]);
  const float d = max3f(x[9], x[10], x[11]), e = max3f(x[12], x[13], x[14]);
  return -max3f(max3f(a, b, x[15]), max3f(c, d, e), a) * LOG2E;
}
DI void sm_exp(const f32x16& p, const f32x16& q, const float (&mc)[2], f32x2 (&e)[2][8]) {
#pragma unroll
  for (int i = 0; i < 8; ++i) {
    const f32x2 t = __builtin_elementwise_fma((f32x2){p[2 * i], p[2 * i + 1]}, (f32x2){LOG2E, LOG2E}, (f32x2){mc[0], mc[0]});
    const f32x2 u = __builtin_elementwise_fma((f32x2){q[2 * i], q[2 * i + 1]}, (f32x2){LOG2E, LOG2E}, (f32x2){mc[1], mc[1]});
    e[0][i] = (f32x2){__builtin_amdgcn_exp2f(t.x), __builtin_amdgcn_exp2f(t.y)};
    e[1][i] = (f32x2){__builtin_amdgcn_exp2f(u.x), __builtin_amdgcn_exp2f(u.y)};
  }
  SGB(0x2, 4); SGB(0x400, 8); SGB(0x2, 4); SGB(0x400, 8); SGB(0x2, 6); SGB(0x400, 8); SGB(0x2, 8); SGB(0x400, 8);
}
DI void sm_exp_s(const f32x16& p, const f32x16& q, const float (&mc)[2], f32x2 (&e)[2][8]) {
#pragma unroll
  for (int i = 0; i < 16; ++i) {
    e[0][i >> 1][i & 1] = __builtin_amdgcn_exp2f(__builtin_fmaf(p[i], LOG2E, mc[0]));
    e[1][i >> 1][i & 1] = __builtin_amdgcn_exp2f(__builtin_fmaf(q[i], LOG2E, mc[1]));
  }
}
DI void sm_finish(const f32x2 (&e)[2][8], u32x4 (&o)[2][2]) {
  float r[2];
#pragma unroll
  for (int u = 0; u < 2; ++u) {
    const f32x2 s = ((e[u][0] + e[u][1]) + (e[u][2] + e[u][3])) + ((e[u][4] + e[u][5]) + (e[u][6] + e[u][7]));
    r[u] = __builtin_amdgcn_rcpf(s.x + s.y);
  }
#pragma unroll
  for (int u = 0; u < 2; ++u) {
    u32 w[8];
#pragma unroll
    for (int i = 0; i < 8; ++i) { const f32x2 t = e[u][i] * (f32x2){r[u], r[u]}; w[i] = pk2(t.x, t.y); }
    o[u][0] = (u32x4){w[0], w[1], w[2], w[3]};
    o[u][1] = (u32x4){w[4], w[5], w[6], w[7]};
  }
}
DI void softmax16x2(const f32x16& p, const f32x16& q, const float (&mc)[2], u32x4 (&o)[2][2]) {
  f32x2 e[2][8];
  sm_exp(p, q, mc, e);
  sm_finish(e, o);
}

#define STAMP(i) do { if ((i) == PROBE_N) { SCHED(); asm volatile("s_memrealtime %0\n\ts_waitcnt lgkmcnt(0)" : "=s"(pt1) :: "memory"); SCHED(); } } while (0)
#define STAMP_OUT() do { if (PROBE_N && (wave >> 2) == PROBE_ROLE) { unsigned long long d_ = pt1 - pt0; if (d_ > 100000) d_ = 100000; unsigned long long e_ = pt0 + 3000 + 8 * d_; \
  for (int it_ = 0; it_ < 1000000; ++it_) { unsigned long long t_; asm volatile("s_memrealtime %0\n\ts_waitcnt lgkmcnt(0)" : "=s"(t_) :: "memory"); if (t_ >= e_) break; } } } while (0)

__global__ __launch_bounds__(512, 2) void hedgehog(const u16* xA, const u16* wA, const u16* pA, const u16* oA,
                                                   const u16* xB, const u16* wB, const u16* pB, const u16* oB,
                                                   u16* out_, u32* ws_) {
  __shared__ __attribute__((aligned(16))) char smem[163840];
  const u32 lds0 = (u32)(size_t)(LAS char*)smem;
  unsigned long long pt0 = __builtin_amdgcn_s_memrealtime(), pt1 = pt0;
  const int bid = blockIdx.x, xcd = bid & 7, idx = bid >> 3;
  const int br = xcd >> 2;
#define KLOAD(tail)                                                                                       \
  u32x8 ka;                                                                                               \
  u32x4 kb;                                                                                               \
  asm volatile("s_load_dwordx8 %0, %2, %3\n\ts_load_dwordx4 %1, %2, 0x40" tail : "=&s"(ka), "=&s"(kb)     \
               : "s"(__builtin_amdgcn_kernarg_segment_ptr()), "s"(br * 32))
#define KWAIT() asm volatile("s_waitcnt lgkmcnt(0)" : "+s"(ka), "+s"(kb))
#define KPTR(v, i) ((void*)(((unsigned long long)(v)[(i) + 1] << 32) | (v)[i]))
  unsigned long long pcs;
  asm volatile("s_getpc_b64 %0" : "=s"(pcs));
  pcs &= ~255ull;
  const int lane = threadIdx.x & 63;
  const int wave = __builtin_amdgcn_readfirstlane(threadIdx.x >> 6);
  const int panel = (xcd & 3) * 4 + (idx & 3), grp = idx >> 2, gq = (grp + GQ) & 7, cg = (grp + GC) & 7;
  auto ochunk = [&](u32 j) -> u32 {
    const int v = (int)j + (((int)j - 1 - gq) >> 31), z = ((int)j - 1) >> 31;
    return (u32)__builtin_amdgcn_readfirstlane((v & ~z) | (gq & z));
  };
  f32x16 zero;
#pragma unroll
  for (int i = 0; i < 16; ++i) zero[i] = 0.f;
#define LANE_IDS()                                                                            \
  u32 lane_ = lane;                                                                           \
  PIN(lane_);                                                                                 \
  const u32 r = lane_ & 31, h = lane_ >> 5;                                                   \
  const u32 blk = (lane_ >> 4) & 1, q = (lane_ & 15) >> 2, p = lane_ & 3;                     \
  u32* ws = (u32*)KPTR(kb, 2);                                                                \
  const __amdgpu_buffer_rsrc_t wsr = __builtin_amdgcn_make_buffer_rsrc(ws, 0, WS_END, SRD3);  \
  const __amdgpu_buffer_rsrc_t wor = __builtin_amdgcn_make_buffer_rsrc(KPTR(ka, 6), 0, 512 * 512 * 2, SRD3)

  if (wave >= 4) {
    const int cw = wave - 4;
    const int rh = cw >> 1, hp = cw & 1;
    KLOAD("\n\ts_waitcnt lgkmcnt(0)");
    u32 sp0, sp1, sp2, sp3;
    {
      u32 off = idx * 1024 + cw * 256;
      off = off < CODE_PF ? off : 0;
      asm volatile("s_load_dword %0, %4, %5 offset:0\n\ts_load_dword %1, %4, %5 offset:64\n\ts_load_dword %2, %4, %5 offset:128\n\ts_load_dword %3, %4, %5 offset:192"
                   : "=&s"(sp0), "=&s"(sp1), "=&s"(sp2), "=&s"(sp3) : "s"(pcs), "s"(off));
    }
    LANE_IDS();
    u32 key = *(const CAS u32*)((const char*)ws + WS_EPOCH + br * 256) + 1u;
    u32 xa[2], xb[2], wa[2], wb2[2];
    {
      const u32 r16 = lane_ & 15, g = lane_ >> 4;
#pragma unroll
      for (int s = 0; s < 2; ++s) {
        xa[s] = lds0 + (64 * rh + r16) * 128 + 16 * ((4 * s + g) ^ (r16 >> 1));
        wa[s] = lds0 + 16384 + (8 * g + q) * 128 + 16 * ((4 * hp + 2 * s + (p >> 1)) ^ ((((q >> 1) & 1) << 2) | (blk << 1))) + 8 * (p & 1);
        xb[s] = xa[s] + 2 * SLOT; wb2[s] = wa[s] + 2 * SLOT;
        PIN(xa[s]); PIN(xb[s]); PIN(wa[s]); PIN(wb2[s]);
      }
    }
    u32 fpa = lds0 + L_WPHI + hp * 1024 + (8 * h + q) * 32 + (8 * blk + 4 * (p >> 1)) * 2;
    PIN(fpa);
    PINS(key);
    f32x4 ak[4][2], av[4][2];
#pragma unroll
    for (int i = 0; i < 8; ++i) ak[i >> 1][i & 1] = av[i >> 1][i & 1] = (f32x4){0.f, 0.f, 0.f, 0.f};
    u32 vra, pra, vwa, pw0, pw1, pub16, pub4;
    u32x4 xf[2][4], wk[2][2], wv[2][2];
    auto ldx = [&](int c, int s) {
      const u32 x = c % 3 == 2 ? xb[s] : xa[s] + (c % 3) * SLOT;
#pragma unroll
      for (int i = 0; i < 4; ++i) xf[s][i] = lds_r128(x + i * 2048);
    };
    auto ldw = [&](int c, int s, int m) {
#pragma unroll
      for (int n = 0; n < 2; ++n) {
        const u32 w = (c % 3 == 2 ? wb2[n] : wa[n] + (c % 3) * SLOT) + s * 4096 + m * 8192;
        (m ? wv : wk)[s][n] = lds_tr2(w, w + 512);
      }
    };
    auto mk = [&](int s) {
#pragma unroll
      for (int i = 0; i < 8; ++i) ak[i >> 1][i & 1] = mfma16(wk[s][i & 1], xf[s][i >> 1], ak[i >> 1][i & 1]);
    };
    auto mv = [&](int s) {
#pragma unroll
      for (int i = 0; i < 8; ++i) av[i >> 1][i & 1] = mfma16(xf[s][i >> 1], wv[s][i & 1], av[i >> 1][i & 1]);
    };
    auto mix = [&](int n) {
#pragma unroll
      for (int i = 0; i < 8; ++i) {
        SGB(0x8, 1);
        if (n == 12) { if (i < 4) SGB(0x100, 2); else SGB(0x100, 1); }
        else if (n == 8) SGB(0x100, 1);
        else if (n == 4) { if (i < 4) SGB(0x100, 1); }
        else if (n < 0) SGB(0x6, 2);
      }
      SCHED();
    };
    BAR();
    asm volatile("" ::"s"(sp0), "s"(sp1), "s"(sp2), "s"(sp3));
    STAMP(1);
    ldw(0, 0, 0); ldx(0, 0); ldw(0, 0, 1);
    u32x4 fa[2];
#pragma unroll
    for (int hh = 0; hh < 2; ++hh) fa[hh] = lds_tr2(fpa + hh * 512, fpa + hh * 512 + 128);
    SCHED();
#pragma unroll
    for (int c = 0; c < 7; ++c) {
      ldw(c, 1, 0); ldx(c, 1); mk(0); mix(8);
      ldw(c, 1, 1); mv(0); mix(4);
      u32 ln = lane;
      PIN(ln);
      const u32 r_ = ln & 31, h_ = ln >> 5, q_ = (ln & 15) >> 2;
      if (c == 1) {
        const u32 sg = (ln & 4) ? 0x80008000u : 0u;
#pragma unroll
        for (int hh = 0; hh < 2; ++hh) { OPQ(fa[hh]); fa[hh] ^= (u32x4){sg, sg, sg, sg}; }
      }
      if (c == 2) vra = lds0 + L_V + (cw >> 1) * (2 * VF) + (r_ < 16 ? (ln + 16 * (cw & 1)) * 16 : 1024);
      if (c == 3) pra = lds0 + L_PHI + cw * 8192 + (4 * h_ + q_) * 64 + 16 * ((((ln >> 3) & 2) + ((ln >> 1) & 1)) ^ ((2 * h_ + (q_ >> 1)) & 3)) + 8 * (ln & 1);
      if (c == 4) {
        const u32 rec = WS_PART + ((br * 32 + grp * 4 + cw) * 16 + panel) * REC;
        pub16 = rec + ln * 16; pub4 = rec + 1024 + ln * 4;
      }
      if (c == 5) { pw0 = lds0 + L_PHI + (2 * hp) * 8192 + (64 * rh + r_) * 64 + 16 * ((2 * h_) ^ ((r_ >> 1) & 3)); pw1 = pw0 ^ 16; }
      if (c == 6) vwa = lds0 + L_V + (8 * rh + 2 * hp) * VF + ((ln & 16) * 2 + (ln & 15)) * 16 + h_ * 8;
      mk(1); mix(c >= 1 ? -1 : 0);
      if (c == 1) { PIN(fa[0]); PIN(fa[1]); }
      if (c == 2) PIN(vra);
      if (c == 3) PIN(pra);
      if (c == 4) { PIN(pub16); PIN(pub4); }
      if (c == 5) { PIN(pw0); PIN(pw1); }
      if (c == 6) PIN(vwa);
      BAR();
      ldw(c + 1, 0, 0); ldx(c + 1, 0); ldw(c + 1, 0, 1);
      mv(1); mix(12);
    }
    ldw(7, 1, 0); ldx(7, 1); ldw(7, 1, 1); mk(0); mix(12);
    mk(1); mix(0);
    __builtin_amdgcn_s_setprio(2);
    STAMP(2);
    f32x16 pk[2][2];
    {
      u32x4 kf[2][2];
      auto kpack = [&](int i) {
#pragma unroll
        for (int hh = 0; hh < 2; ++hh) {
          const f32x4 &t0 = ak[2 * i][hh], &t1 = ak[2 * i + 1][hh];
          const auto lo = __builtin_amdgcn_permlane16_swap(pk2(t0[0], t0[1]), pk2(t1[0], t1[1]), false, false);
          const auto hi = __builtin_amdgcn_permlane16_swap(pk2(t0[2], t0[3]), pk2(t1[2], t1[3]), false, false);
          kf[i][hh] = (u32x4){lo[0], hi[0], lo[1], hi[1]};
        }
      };
      mv(0); kpack(0);
      SGB(0x8, 2);
#pragma unroll
      for (int i = 0; i < 6; ++i) { SGB(0x2, 2); SGB(0x8, 1); }
      SCHED();
      pk[0][0] = mfma(fa[0], kf[0][0], zero);
      pk[0][1] = mfma(fa[1], kf[0][1], zero);
      kpack(1);
      SGB(0x8, 1); SGB(0x2, 6); SGB(0x8, 1); SGB(0x2, 6);
      PIN(pk[0][0]); PIN(pk[0][1]); PIN(kf[1][0]); PIN(kf[1][1]);
      SCHED();
      mv(1);
      pk[1][0] = mfma(fa[0], kf[1][0], zero);
      pk[1][1] = mfma(fa[1], kf[1][1], zero);
    }
    float m0[2], m1[2];
    m0[0] = negmax16(pk[0][0]); m0[1] = negmax16(pk[0][1]);
    SGB(0x8, 2);
#pragma unroll
    for (int i = 0; i < 6; ++i) { SGB(0x2, 2); SGB(0x8, 1); }
    SGB(0x8, 1); SGB(0x2, 6); SGB(0x8, 1);
#pragma unroll
    for (int i = 0; i < 8; ++i) PIN(av[i >> 1][i & 1]);
    PIN(pk[1][0]); PIN(pk[1][1]); PIN(m0[0]); PIN(m0[1]);
    SCHED();
#pragma unroll
    for (int i = 0; i < 8; ++i) {
      const f32x4& t = av[i >> 1][i & 1];
      *(LAS u32x2*)(size_t)(vwa + (4 * (i >> 2) + ((i >> 1) & 1)) * VF + (i & 1) * 256) = (u32x2){pk2(t[0], t[1]), pk2(t[2], t[3])};
    }
    SCHED();
    {
      u32x4 o[2][2];
      softmax16x2(pk[0][0], pk[0][1], m0, o);
      lds_w128(pw0, o[0][0]); lds_w128(pw1, o[0][1]);
      lds_w128(pw0 + 8192, o[1][0]); lds_w128(pw1 + 8192, o[1][1]);
      SCHED();
      m1[0] = negmax16(pk[1][0]); m1[1] = negmax16(pk[1][1]);
      PIN(m1[0]); PIN(m1[1]);
      SCHED();
      softmax16x2(pk[1][0], pk[1][1], m1, o);
      lds_w128(pw0 + 2048, o[0][0]); lds_w128(pw1 + 2048, o[0][1]);
      lds_w128(pw0 + 2048 + 8192, o[1][0]); lds_w128(pw1 + 2048 + 8192, o[1][1]);
      SGB(0x402, 26); SGB(0x200, 1); SGB(0x402, 8); SGB(0x200, 1); SGB(0x402, 8); SGB(0x200, 1); SGB(0x402, 8); SGB(0x200, 1);
      SCHED();
      STAMP(30);
      BARE();
      BAR();
      SCHED();
    }
    STAMP(3);
    f32x16 kv = zero;
    {
      u32x4 a[8], b[8];
#pragma unroll
      for (int u = 0; u < 8; ++u) {
        a[u] = lds_r128(vra + ((u >> 1) * 4 + (u & 1)) * VF);
        b[u] = lds_tr2(pra + 16 * u * 64, pra + (16 * u + 8) * 64);
      }
#pragma unroll
      for (int u = 0; u < 8; ++u) kv = mfma(a[u], b[u], kv);
      SGB(0x100, 12); SGB(0x8, 1); SGB(0x100, 6); SGB(0x8, 1); SGB(0x100, 6); SGB(0x8, 6);
    }
    {
      const u32x4 w = pack8(kv, 0);
      st128c(w, wsr, pub16);
      const u32 zb = __float_as_uint(kv[8]);
      const u32 chk = key - pair_sum(sum4(h ? 0u : zb, w));
      st32c(h ? chk : zb, wsr, pub4);
    }
    SCHED();
    STAMP(4);
    u32 l16, l4, kwa, kza, gso[4], vo[4], wso;
    u32x4 sel;
    {
      u32 ln = lane;
      PIN(ln);
#pragma unroll
      for (int i = 0; i < 4; ++i) vo[i] = (ln >> 3) * 1024 + 16 * ((ln & 7) ^ (((ln >> 4) & 1) << 2)) + i * 7168;
#pragma unroll
      for (int i = 0; i < 2; ++i) dma4(lds0 + L_WO + cw * 8192 + i * 4096, vo, wor, ochunk(cw) * 65536 + i * 32768 + cg * 128);
      wso = ochunk(4 + cw) * 65536 + cg * 128;
      if (TIMER == 1) {
#pragma unroll
        for (int i = 0; i < 2; ++i) dma4(lds0 + L_WO + 32768 + cw * 8192 + i * 4096, vo, wor, wso + i * 32768);
      }
      const u32 r_ = ln & 31, h_ = ln >> 5;
      l16 = ln * 16; l4 = ln * 4;
      kwa = lds0 + L_KV + cw * 1536 + r_ * 48 + h_ * 16; kza = lds0 + L_KV + cw * 1536 + r_ * 48 + 32 + h_ * 8;
      const bool hit = r_ < 16 && ((r_ >> 2) & 1) == h_;
      const u32 j = (r_ & 3) + 4 * (r_ >> 3);
#pragma unroll
      for (int e = 0; e < 4; ++e) sel[e] = (hit && (j >> 1) == e) ? ((j & 1) ? 0x3F800000u : 0x00003F80u) : 0u;
#pragma unroll
      for (int i = 0; i < 4; ++i) { gso[i] = WS_PART + ((br * 32 + gq * 4 + cw) * 16 + ((i + idx) & 3) * 4) * REC; PINS(gso[i]); }
      PIN(l16); PIN(l4); PIN(kwa); PIN(kza); PIN(sel); PINS(wso);
    }
    if (DELAY1) __builtin_amdgcn_s_sleep(DELAY1);
    STAMP(10);
    f32x16 gs[4];
    float z[4];
    {
      u32x4 g[16];
      u32 gz[16];
      auto gload = [&](int gi) {
#pragma unroll
        for (int i = 0; i < 4; ++i) {
          g[4 * gi + i] = __builtin_amdgcn_raw_buffer_load_b128(wsr, l16 + i * REC, gso[gi], AUX_SC1);
          gz[4 * gi + i] = __builtin_amdgcn_raw_buffer_load_b32(wsr, l4 + (1024 + i * REC - 1024 * (i == 3)), gso[gi] + 1024 * (i == 3), AUX_SC1);
          SCHED();
        }
      };
      auto gsum = [&](int gi) -> bool {
        u32 c = 0;
#pragma unroll
        for (int i = 0; i < 4; ++i) {
          c = sum4(c + gz[4 * gi + i], g[4 * gi + i]);
          gs[gi] = mfma(sel, g[4 * gi + i], i == 0 ? zero : gs[gi]);
          z[gi] = i == 0 ? __uint_as_float(gz[4 * gi]) : z[gi] + __uint_as_float(gz[4 * gi + i]);
        }
        PIN(z[gi]);
        const bool bad = __builtin_amdgcn_ballot_w64(pair_sum(c) != 4u * key) != 0;
        SCHED();
        return bad;
      };
      gload(0); gload(1); gload(2); gload(3);
      bool b0 = gsum(0), b1 = gsum(1), b2 = gsum(2), b3 = gsum(3);
      STAMP(11);
#pragma unroll 1
      for (u32 spin = 0; (b0 | b1 | b2 | b3) && spin < SPIN_CAP; ++spin) {
        asm volatile("" ::: "memory");
        if (b0) gload(0);
        if (b1) gload(1);
        if (b2) gload(2);
        if (b3) gload(3);
        if (b0) b0 = gsum(0);
        if (b1) b1 = gsum(1);
        if (b2) b2 = gsum(2);
        if (b3) b3 = gsum(3);
      }
    }
    __builtin_amdgcn_s_waitcnt(0x0F70);
    STAMP(5);
    {
      float tt[8];
#pragma unroll
      for (int j = 0; j < 8; ++j) tt[j] = (gs[0][j] + gs[1][j]) + (gs[2][j] + gs[3][j]);
      const float zt = (z[0] + z[1]) + (z[2] + z[3]);
      lds_w128(kwa, (u32x4){pk2(tt[0], tt[1]), pk2(tt[2], tt[3]), pk2(tt[4], tt[5]), pk2(tt[6], tt[7])});
      const u32 zz = pk2(zt, zt);
      *(LAS u32x2*)(size_t)kza = (u32x2){zz, zz};
    }
    if (TIMER == 2) {
#pragma unroll
      for (int i = 0; i < 2; ++i) dma4(lds0 + L_WO + 32768 + cw * 8192 + i * 4096, vo, wor, wso + i * 32768);
    }
    BAR();
    STAMP(6);
    if (TIMER == 3) {
#pragma unroll
      for (int i = 0; i < 2; ++i) dma4(lds0 + L_WO + 32768 + cw * 8192 + i * 4096, vo, wor, wso + i * 32768);
    }
    {
      u32 oso[8], ob = lds0 + L_O + cw * OREC;
#pragma unroll
      for (int i = 1; i < 8; ++i) { oso[i] = WS_O + ((br * 64 + panel * 4 + cw) * 8 + ochunk(i)) * OREC; PINS(oso[i]); }
      PINS(ob);
      if (TIMER == 0) {
        BARE();
        STAMP(7);
#pragma unroll
        for (int i = 0; i < 2; ++i) dma4(lds0 + L_WO + 32768 + cw * 8192 + i * 4096, vo, wor, wso + i * 32768);
      }
      WAITV(0);
      BARE();
      if (DELAY3) __builtin_amdgcn_s_sleep(DELAY3);
      STAMP(8);
      auto oissue = [&](int j) { dma_orec(ob + (j % 5) * (4 * OREC), l4, l16, wsr, oso[j]); };
      oissue(1); oissue(2);
      WAITV(5); BARE(); oissue(3);
      WAITV(5); STAMP(15); BARE(); oissue(4); oissue(5);
      WAITV(10); BARE(); oissue(6);
      WAITV(10); BARE(); oissue(7);
      WAITV(10); BARE();
      WAITV(5); BARE();
      WAITV(0); BARE();
    }
    STAMP(9);
    STAMP_OUT();
  } else {
    KLOAD("");
    const int t = wave;
    const u32 l7 = lane & 7, l3 = lane >> 3, lq = lane >> 4;
    const u32 wsw = 16 * (l7 ^ ((lq & 1) << 2));
    u32 vx[4], vw[4];
#pragma unroll
    for (int i = 0; i < 4; ++i) {
      vx[i] = l3 * 1024 + 16 * (l7 ^ (((i & 1) << 2) | lq)) + i * 7168;
      vw[i] = l3 * 3072 + (wsw ^ ((i & 1) << 5)) + i * (8 * 3072 - 1024);
    }
    int rot = (grp + 2 * (idx & 3)) & 7;
    auto chunk = [&](u32 c) -> u32 {
      constexpr u32 n0 = ROTMODE / 100, n1 = ROTMODE / 10 % 10, n2 = ROTMODE % 10;
      const u32 g = grp + (idx & 3), r3 = g - 3 * ((g * 11) >> 5);
      auto run = [&](u32 k, u32 n) -> u32 {
        if (n == 3) { const u32 v = k + r3; return v >= 3 ? v - 3 : v; }
        return k ^ (g & (n - 1));
      };
      const u32 a = run(c, n0), b = n0 + run(c - n0, n1), d = n0 + n1 + run(c - n0 - n1, n2);
      return ((c < n0 ? a : c < n0 + n1 ? b : d) + CSH) & 7;
    };
    const u32 xs0 = (panel * 128 + t * 32) * 1024, ws0 = (t & 1) * (32 * 3072) + (1 + (t >> 1)) * 1024 + grp * 128;
    const u32 ldst = lds0 + t * 4096;
    u32 e0 = ldst, e1 = ldst + 16384, e2 = xs0 + chunk(0) * 128, e3 = ws0 + chunk(0) * (64 * 3072);
    PINS(e0); PINS(e1); PINS(e2); PINS(e3);
#pragma unroll
    for (int i = 0; i < 4; ++i) { PIN(vx[i]); PIN(vw[i]); }
    KWAIT();
    const __amdgpu_buffer_rsrc_t xr = __builtin_amdgcn_make_buffer_rsrc(KPTR(ka, 0), 0, 2048 * 512 * 2, SRD3);
    const __amdgpu_buffer_rsrc_t wr = __builtin_amdgcn_make_buffer_rsrc(KPTR(ka, 2), 0, 512 * 1536 * 2, SRD3);
    STAMP(10);
    auto dx = [&](int c, u32 d, u32 so) { if (c < NTX) dma4nt(d, vx, xr, so); else dma4(d, vx, xr, so); };
    auto dw = [&](int c, u32 d, u32 so) { if (c < NTW) dma4nt(d, vw, wr, so); else dma4(d, vw, wr, so); };
    if (WFIRST & 1) { dw(0, e1, e3); dx(0, e0, e2); } else { dx(0, e0, e2); dw(0, e1, e3); }
    if (FOLLOW) {
      const int gi = (grp + (ka[2] >> 7) - 4) & 7;
      rot = (gi == 7 ? 0 : gi) + 2 * (idx & 3);
      rot = rot >= 7 ? rot - 7 : rot;
    }
    {
      const __amdgpu_buffer_rsrc_t pr = __builtin_amdgcn_make_buffer_rsrc(KPTR(ka, 4), 0, 32 * 256 * 2, SRD3);
      dma1(lds0 + L_WPHI + t * 1024, lane * 16, pr, (t < 2 ? grp : gq) * 2048 + (t & 1) * 1024);
    }
    STAMP(11);
    u32 th3 = 0, thq = 0, tho;
    if (H3) TOUCH(th3, (l7 >> 1) * 3072 + (l7 & 1) * 1024, wr, (idx * 16 + t * 4) * 3072 + 1024 + ((3 - (ka[2] >> 7)) & 7) * 128);
    if (WFIRST & 2) dw(1, ldst + SLOT + 16384, ws0 + chunk(1) * (64 * 3072));
    dx(1, ldst + SLOT, xs0 + chunk(1) * 128);
    if (!(WFIRST & 2)) dw(1, ldst + SLOT + 16384, ws0 + chunk(1) * (64 * 3072));
    STAMP(1);
    LANE_IDS();
    u32 key = *(const CAS u32*)((const char*)ws + WS_EPOCH + br * 256) + 1u;
    const u32 sw = (r >> 1) & 7;
    u32 xa[4];
#pragma unroll
    for (int s = 0; s < 4; ++s) { xa[s] = lds0 + (32 * t + r) * 128 + 16 * ((2 * s + h) ^ sw); PIN(xa[s]); }
    u32 fpa = lds0 + L_WPHI + 2048 + (4 * h + q) * 32 + (8 * blk + 4 * (p >> 1)) * 2;
    u32 vq = (r >> 3) * 3072 + (r & 7) * 128;
    PIN(fpa); PIN(vq);
    PINS(key);
    u32x4 xq[32], fa[4];
    u32 wa, wb, kra, o_off, c_off, woa, wob, vto, sto;
#pragma unroll
    for (int c = 0; c < 8; ++c) {
      u32 d0, d1, s0, s1;
      if (c < 6) {
        d0 = ldst + ((c + 2) % 3) * SLOT; d1 = d0 + 16384;
        s0 = xs0 + chunk(c + 2) * 128; s1 = ws0 + chunk(c + 2) * (64 * 3072);
      } else {
        if (WQ4) {
          const u32 k0 = 2 * ((2 * (c - 6) + idx) & 3) + (t >> 1), k1 = (k0 + 2) & 7;
          d0 = lds0 + L_WQ1 + ((chunk(k0) + 4) & 7) * 8192 + (t & 1) * 4096; d1 = lds0 + L_WQ1 + ((chunk(k1) + 4) & 7) * 8192 + (t & 1) * 4096;
          s0 = (k0 * 64 + (t & 1) * 32) * 3072 + gq * 128; s1 = (k1 * 64 + (t & 1) * 32) * 3072 + gq * 128;
        } else {
          const u32 half = (c - 6) ^ (WQSWAP ? (idx & 1) : 0);
          d0 = lds0 + L_WQ0 - half * (L_WQ0 - L_WQ1) + t * 8192; d1 = d0 + 4096;
          s0 = chunk(half * 4 + t) * (64 * 3072) + gq * 128; s1 = s0 + 32 * 3072;
        }
      }
      PINS(d0); PINS(d1); PINS(s0); PINS(s1);
      if ((H3 && c == 0) || (LTQ && c == 7)) WAITV(9); else WAITV(8);
      if (H3 && c == 1) PIN(th3);
      STAMP(20 + c);
      BAR();
      if (LTQ && c == 6) TOUCH(thq, vq, wr, (idx * 16 + t * 4) * 3072);
      if (WFIRST & 4) dw(c + 2, d1, s1); else if (c < 6) dx(c + 2, d0, s0); else dw(c + 2, d0, s0);
#pragma unroll
      for (int s = 0; s < 4; ++s) xq[4 * c + s] = lds_r128(xa[s] + (c % 3) * SLOT);
      if (!(WFIRST & 4)) dw(c + 2, d1, s1); else if (c < 6) dx(c + 2, d0, s0); else dw(c + 2, d0, s0);
      if (c == 0) {
#pragma unroll
        for (int hd = 0; hd < 4; ++hd) fa[hd] = lds_tr2(fpa + hd * 512, fpa + hd * 512 + 256);
      }
      if (c == QSET) {
        lds_w128(lds0 + L_V + (lane & 15) * VF + 1024, (u32x4){0x3F803F80u, 0x3F803F80u, 0x3F803F80u, 0x3F803F80u});
        const u32 sg = (lane & 4) ? 0x80008000u : 0u;
#pragma unroll
        for (int hd = 0; hd < 4; ++hd) { PIN(fa[hd]); fa[hd] ^= (u32x4){sg, sg, sg, sg}; PIN(fa[hd]); }
        wa = lds0 + L_WQ1 + (8 * h + q) * 128 + 16 * ((2 * blk + (p >> 1)) ^ ((((q >> 1) & 1) << 2) | (h << 1))) + 8 * (p & 1);
        wb = wa ^ 64;
        kra = lds0 + L_KV + (16 * h + q) * 48 + (blk ? 32 : 8 * p);
        o_off = WS_O + ((br * 64 + panel * 4 + t) * 8 + gq) * OREC + 256 + lane * 16;
        c_off = WS_O + ((br * 64 + panel * 4 + t) * 8 + gq) * OREC + lane * 4;
        woa = lds0 + L_WO + (8 * h + q) * 128 + 16 * ((2 * blk + (p & 1)) ^ (((q >> 1) & 1) << 2)) + 8 * (p >> 1);
        wob = woa ^ 64;
        vto = r * 128; sto = (idx * 16 + t * 4) * 1024;
        PIN(wa); PIN(wb); PIN(kra); PIN(o_off); PIN(c_off); PIN(woa); PIN(wob); PIN(vto); PINS(sto);
      }
    }
    STAMP(32);
    TOUCH(tho, vto, wor, sto);
    STAMP(2);
    u32x4 fq[2][2][2];
    {
      f32x16 aq[2], pq[2];
      float mq[2];
      aq[0] = aq[1] = zero;
      u32x4 wf[4];
      auto ldw = [&](int n) {
        const int k = n & 31;
        const u32 o = (n < 32 ? wa : wb) + (k < 16 ? (L_WQ0 - L_WQ1) : 0) + (k & 15) * 2048;
        wf[n & 3] = lds_tr2(o, o + 512);
      };
      WAITV(1);
      STAMP(31);
      BAR();
      SCHED();
      STAMP(12);
      ldw(0); ldw(1); ldw(2);
      SCHED();
      BARE();
      SCHED();
#pragma unroll
      for (int n = 0; n < 34; ++n) {
        ldw(n + 3);
        aq[n >> 5] = mfma(wf[n & 3], xq[n & 31], aq[n >> 5]);
        SGB(0x8, 1); SGB(0x100, 2);
        SCHED();
      }
#pragma unroll
      for (int hh = 0; hh < 2; ++hh) pq[hh] = mfma(fa[hh], pack8(aq[0], hh), zero);
      SCHED();
      STAMP(4);
#pragma unroll
      for (int n = 34; n < 64; ++n) {
        if (n + 3 < 64) ldw(n + 3);
        aq[1] = mfma(wf[n & 3], xq[n & 31], aq[1]);
      }
      mq[0] = negmax16(pq[0]); mq[1] = negmax16(pq[1]);
      f32x2 eq[2][8];
      sm_exp_s(pq[0], pq[1], mq, eq);
      SGB(0x8, 1); SGB(0x100, 2); SGB(0x8, 1); SGB(0x100, 2);
#pragma unroll
      for (int n = 36; n < 41; ++n) { SGB(0x8, 1); SGB(0x100, 2); SGB(0x402, 4); }
#pragma unroll
      for (int n = 41; n < 64; ++n) { SGB(0x8, 1); SGB(0x100, 2); if (n & 1) SGB(0x402, 3); else SGB(0x402, 2); }
#pragma unroll
      for (int i = 0; i < 16; ++i) PIN(eq[i >> 3][i & 7]);
      SCHED();
      STAMP(3);
#pragma unroll
      for (int hh = 0; hh < 2; ++hh) { pq[hh] = mfma(fa[2 + hh], pack8(aq[1], hh), zero); PIN(pq[hh]); }
      SCHED();
      sm_finish(eq, fq[0]);
      SCHED();
      asm volatile("s_waitcnt vmcnt(0)" : "+v"(tho), "+v"(thq)::"memory");
      mq[0] = negmax16(pq[0]); mq[1] = negmax16(pq[1]);
      PIN(mq[0]); PIN(mq[1]);
      SCHED();
#pragma unroll
      for (int i = 0; i < 4; ++i) PIN(fq[0][i >> 1][i & 1]);
      softmax16x2(pq[0], pq[1], mq, fq[1]);
#pragma unroll
      for (int i = 0; i < 4; ++i) PIN(fq[1][i >> 1][i & 1]);
    }
    STAMP(5);
    BAR();
    STAMP(6);
    u32x4 ob[2][4];
    {
      u32x4 a0[4], a1[4];
#pragma unroll
      for (int hd = 0; hd < 4; ++hd) {
        a0[hd] = lds_tr2(kra + hd * 1536, kra + hd * 1536 + 4 * 48);
        a1[hd] = lds_tr2(kra + hd * 1536 + 8 * 48, kra + hd * 1536 + 12 * 48);
      }
      SCHED();
      f32x16 y[4];
      u32 cs = 0;
      auto num = [&](int hd) {
        y[hd] = mfma(a0[hd], fq[hd >> 1][hd & 1][0], zero);
        y[hd] = mfma(a1[hd], fq[hd >> 1][hd & 1][1], y[hd]);
      };
      auto fin = [&](int hd) {
        const f32x16& yy = y[hd];
        const float rd = __builtin_amdgcn_rcpf(yy[8] + 1e-6f);
        u32 w[4];
#pragma unroll
        for (int e = 0; e < 4; ++e) { float t0 = yy[2 * e] * rd, t1 = yy[2 * e + 1] * rd; if (hd < 2) OPQ(t0); w[e] = pk2(t0, t1); }
        ob[0][hd] = (u32x4){w[0], w[1], w[2], w[3]};
        st128c(ob[0][hd], wsr, o_off + hd * 1024);
        cs = sum4(cs, ob[0][hd]);
      };
      num(0); num(1); PIN(y[0]); PIN(y[1]); SCHED();
      num(2); fin(0); SGB(0x8, 1); SGB(0x402, 6); SGB(0x8, 1); PIN(y[2]); SCHED();
      num(3); fin(1); SGB(0x8, 1); SGB(0x402, 6); SGB(0x8, 1); PIN(y[3]); SCHED();
      fin(2); fin(3);
      st32c(key - cs, wsr, c_off);
    }
    SCHED();
    if (TIMER == 0) BARE();
    STAMP(7);
    u32x4 wo[32][2];
    auto ldwo = [&](int n) {
      wo[n][0] = lds_tr2(woa + n * 2048, woa + n * 2048 + 512);
      wo[n][1] = lds_tr2(wob + n * 2048, wob + n * 2048 + 512);
    };
#pragma unroll
    for (int n = 0; n < 16; ++n) ldwo(n);
    f32x16 c0 = zero, c1 = zero;
#pragma unroll
    for (int s = 0; s < 4; ++s) { c0 = mfma(wo[s][0], ob[0][s], c0); c1 = mfma(wo[s][1], ob[0][s], c1); }
#pragma unroll
    for (int k = 4; k < 16; ++k) { PIN(wo[k][0]); PIN(wo[k][1]); }
    PIN(c0); PIN(c1);
    if (TIMER) WAITV(0);
    BARE();
    SCHED();
#pragma unroll
    for (int n = 16; n < 24; ++n) ldwo(n);
#pragma unroll
    for (int k = 16; k < 24; ++k) { PIN(wo[k][0]); PIN(wo[k][1]); }
    const __amdgpu_buffer_rsrc_t outr = __builtin_amdgcn_make_buffer_rsrc(KPTR(kb, 0), 0, 2048 * 1024 * 2, SRD3);
    u32 oa = lds0 + L_O + t * OREC + 256 + lane * 16;
    u32 orow = ((panel * 128 + t * 32 + l3) * 1024 + br * 512 + cg * 64 + 8 * l7) * 2;
    PIN(oa); PIN(orow);
    u32 oc = oa - 256 - lane * 12, l4 = lane * 4, l16 = lane * 16, oa4 = oa + 4 * (4 * OREC), oc4 = oc + 4 * (4 * OREC);
    u32 sw1 = lds0 + t * 4096 + r * 128 + 16 * (h ^ (r & 7)), sr1 = lds0 + t * 4096 + l3 * 128 + 16 * (l7 ^ l3);
    u32 sw2 = sw1 ^ 32, sw3 = sw1 ^ 64, sw4 = sw1 ^ 96, or1 = orow + 16384, or2 = orow + 32768, or3 = orow + 49152;
    PIN(oc); PIN(l4); PIN(l16); PIN(sw1); PIN(sr1); PIN(oa4); PIN(oc4); PIN(sw2); PIN(sw3); PIN(sw4); PIN(or1); PIN(or2); PIN(or3);
    STAMP(8);
    u32 nretry = 0;
    u32 chk[2];
    auto oread = [&](int j) {
      const u32 da = j % 5 == 4 ? oa4 : oa + (j % 5) * (4 * OREC), dc = j % 5 == 4 ? oc4 : oc + (j % 5) * (4 * OREC);
#pragma unroll
      for (int s = 0; s < 4; ++s) ob[j & 1][s] = lds_r128(da + s * 1024);
      chk[j & 1] = *(LAS u32*)(size_t)dc;
    };
    auto obad = [&](int j) -> bool {
      const u32 c = sum4(sum4(chk[j & 1], ob[j & 1][0]), ob[j & 1][1]) + sum4(sum4(0u, ob[j & 1][2]), ob[j & 1][3]);
      return __builtin_amdgcn_ballot_w64(c != key) != 0;
    };
#pragma unroll
    for (int j = 1; j < 8; ++j) {
      BARE();
      SCHED();
      oread(j);
      if (j >= 2) {
#pragma unroll
        for (int s = 0; s < 4; ++s) {
          const int k = 4 * (j - 1) + s;
          c0 = mfma(wo[k][0], ob[(j - 1) & 1][s], c0);
          c1 = mfma(wo[k][1], ob[(j - 1) & 1][s], c1);
        }
        if (j >= 3 && j <= 6) { ldwo(24 + 2 * (j - 3)); ldwo(25 + 2 * (j - 3)); }
      }
      const bool bad = obad(j);
      if (j >= 3 && j <= 6) {
        SGB(0x8, 1); SGB(0x100, 3); SGB(0x8, 1); SGB(0x100, 2); SGB(0x8, 1); SGB(0x100, 3); SGB(0x8, 1); SGB(0x100, 3);
        SGB(0x8, 1); SGB(0x2, 4); SGB(0x8, 1); SGB(0x2, 5); SGB(0x8, 1); SGB(0x100, 2); SGB(0x8, 1);
      } else if (j >= 2) {
        SGB(0x8, 1); SGB(0x100, 3); SGB(0x8, 1); SGB(0x100, 2); SGB(0x8, 3); SGB(0x2, 4); SGB(0x8, 1); SGB(0x2, 5); SGB(0x8, 2);
      }
      SCHED();
      if (__builtin_expect(bad, 0)) {
#pragma unroll 1
        for (u32 spin = 0; spin < SPIN_CAP; ++spin) {
          dma_orec(lds0 + L_O + t * OREC + (j % 5) * (4 * OREC), l4, l16, wsr, WS_O + ((br * 64 + panel * 4 + t) * 8 + ochunk(j)) * OREC);
#pragma unroll 1
          for (u32 jj = j + 1; jj <= (j + AHEAD < 7 ? j + AHEAD : 7); ++jj) {
            const u32 sl = (jj >= 5 ? jj - 5 : jj) * (4 * OREC);
            u32 c = *(LAS u32*)(size_t)(oc + sl);
#pragma unroll
            for (int s = 0; s < 4; ++s) c = sum4(c, lds_r128(oa + sl + s * 1024));
            if (__builtin_amdgcn_ballot_w64(c != key) != 0)
              dma_orec(lds0 + L_O + t * OREC + sl, l4, l16, wsr, WS_O + ((br * 64 + panel * 4 + t) * 8 + ochunk(jj)) * OREC);
          }
          WAITV(0);
          oread(j);
          ++nretry;
          if (!obad(j)) break;
        }
      }
    }
#pragma unroll
    for (int s = 0; s < 4; ++s) c0 = mfma(wo[28 + s][0], ob[1][s], c0);
    PIN(c0);
    SCHED();
#pragma unroll
    for (int s = 0; s < 4; ++s) c1 = mfma(wo[28 + s][1], ob[1][s], c1);
    lds_w128(sw1, pack8(c0, 0));
    lds_w128(sw2, pack8(c0, 1));
    SGB(0x8, 2); SGB(0x2, 4); SGB(0x200, 1); SGB(0x8, 1); SGB(0x2, 4); SGB(0x200, 1); SGB(0x8, 1);
    SCHED();
    STAMP(9);
    lds_w128(sw3, pack8(c1, 0));
    lds_w128(sw4, pack8(c1, 1));
    asm volatile("" ::: "memory");
    {
      u32x4 v[4];
#pragma unroll
      for (int i = 0; i < 4; ++i) v[i] = lds_r128(sr1 + i * 1024);
      SCHED();
      st128c(v[0], outr, orow);
      st128c(v[1], outr, or1);
      st128c(v[2], outr, or2);
      st128c(v[3], outr, or3);
    }
    if (t == 0 && panel == 0 && grp == 0 && lane == 0) st32c(key, wsr, WS_EPOCH + br * 256);
    if (PROBE_N == 13) WAITV(0);
    STAMP(13);
    STAMP_OUT();
  }
}
}

extern "C" void kernel_launch(void* const* d_in, const int* in_sizes, int n_in,
                              void* d_out, int out_size, void* d_ws, size_t ws_size,
                              hipStream_t stream) {
  hh::hedgehog<<<256, 512, 0, stream>>>((const u16*)d_in[0], (const u16*)d_in[1], (const u16*)d_in[2], (const u16*)d_in[3],
                                        (const u16*)d_in[4], (const u16*)d_in[5], (const u16*)d_in[6], (const u16*)d_in[7],
                                        (u16*)d_out, (u32*)((char*)d_ws + ((size_t)WSOFF << 20)));
}
```

```cpp
#define DELAY1 5
#define PROBE_N 5
#define PROBE_ROLE 0
#include <hip/hip_runtime.h>
#include <stdint.h>

typedef unsigned short u16;
typedef unsigned u32;
typedef short bf16x8 __attribute__((ext_vector_type(8)));
typedef short s16x4 __attribute__((ext_vector_type(4)));
typedef float f32x16 __attribute__((ext_vector_type(16)));
typedef float f32x4 __attribute__((ext_vector_type(4)));
typedef float f32x2 __attribute__((ext_vector_type(2)));
typedef unsigned u32x8 __attribute__((ext_vector_type(8)));
typedef unsigned u32x4 __attribute__((ext_vector_type(4)));
typedef unsigned u32x2 __attribute__((ext_vector_type(2)));
typedef __bf16 bf16x2 __attribute__((ext_vector_type(2)));
#define DI __device__ __forceinline__
#define LAS __attribute__((address_space(3)))
#define CAS __attribute__((address_space(4)))
#define BC(T, x) __builtin_bit_cast(T, x)

#define DELAY3 5
#define ROTMODE 242
#define CODE_PF 27648
#define AHEAD 2
#define WSOFF 0
#define GQ 4
#define GC 2
#define WFIRST 0
#define FOLLOW 0
#define TIMER 2
#define LTO 1
#define NTX 0
#define NTW 0
#define WQSWAP (ROTMODE >= 7)
#define CSH 0
#define WQ4 1
#define QSET 1
#define KSET 2
#define H3 0
#define LTQ 0

namespace hh {
constexpr int AUX_SC1 = 16;
constexpr u32 SRD3 = 0x00020000u;
constexpr u32 WS_EPOCH = 0;
constexpr u32 REC = 1280;
constexpr u32 WS_PART = 4096;
constexpr u32 OREC = 4352;
constexpr u32 WS_O = WS_PART + 2 * 32 * 16 * REC;
constexpr u32 WS_END = WS_O + 2 * 64 * 8 * OREC;
constexpr u32 WS_STAMP = 8u << 20;
constexpr u32 SLOT = 32768;
constexpr u32 L_WQ0 = 98304;
constexpr u32 L_WQ1 = 65536;
constexpr u32 L_WPHI = 163840 - 4096;
constexpr u32 L_PHI = 0;
constexpr u32 VF = 1040;
constexpr u32 L_V = 131072;
constexpr u32 L_KV = L_V + 16 * VF;
constexpr u32 L_WO = 0;
constexpr u32 L_O = 65536;
constexpr u32 SPIN_CAP = 1u << 16;

DI u32 pk2(float a, float b) { return BC(u32, __builtin_convertvector((f32x2){a, b}, bf16x2)); }
DI u32x4 pack8(const f32x16& x, int s) {
  return (u32x4){pk2(x[8 * s], x[8 * s + 1]), pk2(x[8 * s + 2], x[8 * s + 3]), pk2(x[8 * s + 4], x[8 * s + 5]), pk2(x[8 * s + 6], x[8 * s + 7])};
}
DI f32x16 mfma(u32x4 a, u32x4 b, f32x16 c) { return __builtin_amdgcn_mfma_f32_32x32x16_bf16(BC(bf16x8, a), BC(bf16x8, b), c, 0, 0, 0); }
DI f32x4 mfma16(u32x4 a, u32x4 b, f32x4 c) { return __builtin_amdgcn_mfma_f32_16x16x32_bf16(BC(bf16x8, a), BC(bf16x8, b), c, 0, 0, 0); }
DI u32x4 lds_r128(u32 a) { return *(LAS u32x4*)(size_t)a; }
DI void lds_w128(u32 a, u32x4 v) { *(LAS u32x4*)(size_t)a = v; }
DI u32x4 lds_tr2(u32 a0, u32 a1) {
  s16x4 lo = __builtin_amdgcn_ds_read_tr16_b64_v4i16((LAS s16x4*)(size_t)a0);
  s16x4 hi = __builtin_amdgcn_ds_read_tr16_b64_v4i16((LAS s16x4*)(size_t)a1);
  return BC(u32x4, __builtin_shufflevector(lo, hi, 0, 1, 2, 3, 4, 5, 6, 7));
}
DI void dma1(u32 lds, u32 voff, __amdgpu_buffer_rsrc_t srd, u32 soff) {
  asm volatile("s_mov_b32 m0, %0\n\ts_nop 0\n\tbuffer_load_dwordx4 %1, %2, %3 offen lds" ::"s"(lds), "v"(voff), "s"(srd), "s"(soff) : "memory");
}
DI void dma4(u32 lds, const u32 (&v)[4], __amdgpu_buffer_rsrc_t srd, u32 soff) {
  asm volatile("s_mov_b32 m0, %0\n\ts_nop 0\n\t"
               "buffer_load_dwordx4 %1, %5, %6 offen lds\n\t"
               "buffer_load_dwordx4 %2, %5, %6 offen offset:1024 lds\n\t"
               "buffer_load_dwordx4 %3, %5, %6 offen offset:2048 lds\n\t"
               "buffer_load_dwordx4 %4, %5, %6 offen offset:3072 lds" ::"s"(lds), "v"(v[0]), "v"(v[1]), "v"(v[2]), "v"(v[3]), "s"(srd), "s"(soff) : "memory");
}
#define DMA4P(name, POL)                                                                       \
  DI void name(u32 lds, const u32 (&v)[4], __amdgpu_buffer_rsrc_t srd, u32 soff) {              \
    asm volatile("s_mov_b32 m0, %0\n\ts_nop 0\n\t"                                             \
                 "buffer_load_dwordx4 %1, %5, %6 offen " POL " lds\n\t"                        \
                 "buffer_load_dwordx4 %2, %5, %6 offen offset:1024 " POL " lds\n\t"            \
                 "buffer_load_dwordx4 %3, %5, %6 offen offset:2048 " POL " lds\n\t"            \
                 "buffer_load_dwordx4 %4, %5, %6 offen offset:3072 " POL " lds" ::"s"(lds), "v"(v[0]), "v"(v[1]), "v"(v[2]), "v"(v[3]), "s"(srd), "s"(soff) : "memory"); \
  }
DMA4P(dma4nt, "nt")
DI void dma_orec(u32 lds, u32 l4, u32 l16, __amdgpu_buffer_rsrc_t srd, u32 soff) {
  asm volatile("s_mov_b32 m0, %0\n\ts_nop 0\n\t"
               "buffer_load_dwordx4 %2, %3, %4 offen offset:256 sc1 lds\n\t"
               "buffer_load_dwordx4 %2, %3, %4 offen offset:1280 sc1 lds\n\t"
               "buffer_load_dwordx4 %2, %3, %4 offen offset:2304 sc1 lds\n\t"
               "buffer_load_dwordx4 %2, %3, %4 offen offset:3328 sc1 lds\n\t"
               "buffer_load_dword %1, %3, %4 offen sc1 lds" ::"s"(lds), "v"(l4), "v"(l16), "s"(srd), "s"(soff) : "memory");
}
#define TOUCH(d, voff, srd, soff) asm volatile("buffer_load_dword %0, %1, %2, %3 offen" : "=v"(d) : "v"(voff), "s"(srd), "s"(soff) : "memory")
DI void st128c(u32x4 v, __amdgpu_buffer_rsrc_t srd, u32 off) { __builtin_amdgcn_raw_buffer_store_b128(v, srd, off, 0, AUX_SC1); }
DI void st32c(u32 v, __amdgpu_buffer_rsrc_t srd, u32 off) { __builtin_amdgcn_raw_buffer_store_b32(v, srd, off, 0, AUX_SC1); }
#define BAR() asm volatile("s_waitcnt lgkmcnt(0)\n\ts_barrier" ::: "memory")
#define BARE() asm volatile("s_barrier" ::: "memory")
#define OPQ(x) asm("" : "+v"(x))
#define PIN(x) asm volatile("" : "+v"(x))
#define PINS(x) asm volatile("" : "+s"(x))
#define WAITV(n) asm volatile("s_waitcnt vmcnt(" #n ")" ::: "memory")
#define SCHED() __builtin_amdgcn_sched_barrier(0)
#define SGB(m, n) __builtin_amdgcn_sched_group_barrier(m, n, 0)
DI u32 sum4(u32 c, u32x4 v) { return (c + v.x + v.y) + v.z + v.w; }
DI u32 pair_sum(u32 s) { auto t = __builtin_amdgcn_permlane32_swap(s, s, false, false); return t[0] + t[1]; }

constexpr float LOG2E = 1.44269504088896340736f;
DI float max3f(float a, float b, float c) { return __builtin_fmaxf(__builtin_fmaxf(a, b), c); }
DI float negmax16(const f32x16& x) {
  const float a = max3f(x[0], x[1], x[2]), b = max3f(x[3], x[4], x[5]), c = max3f(x[6], x[7], x[8]);
  const float d = max3f(x[9], x[10], x[11]), e = max3f(x[12], x[13], x[14]);
  return -max3f(max3f(a, b, x[15]), max3f(c, d, e), a) * LOG2E;
}
DI void sm_exp(const f32x16& p, const f32x16& q, const float (&mc)[2], f32x2 (&e)[2][8]) {
#pragma unroll
  for (int i = 0; i < 8; ++i) {
    const f32x2 t = __builtin_elementwise_fma((f32x2){p[2 * i], p[2 * i + 1]}, (f32x2){LOG2E, LOG2E}, (f32x2){mc[0], mc[0]});
    const f32x2 u = __builtin_elementwise_fma((f32x2){q[2 * i], q[2 * i + 1]}, (f32x2){LOG2E, LOG2E}, (f32x2){mc[1], mc[1]});
    e[0][i] = (f32x2){__builtin_amdgcn_exp2f(t.x), __builtin_amdgcn_exp2f(t.y)};
    e[1][i] = (f32x2){__builtin_amdgcn_exp2f(u.x), __builtin_amdgcn_exp2f(u.y)};
  }
  SGB(0x2, 4); SGB(0x400, 8); SGB(0x2, 4); SGB(0x400, 8); SGB(0x2, 6); SGB(0x400, 8); SGB(0x2, 8); SGB(0x400, 8);
}
DI void sm_exp_s(const f32x16& p, const f32x16& q, const float (&mc)[2], f32x2 (&e)[2][8]) {
#pragma unroll
  for (int i = 0; i < 16; ++i) {
    e[0][i >> 1][i & 1] = __builtin_amdgcn_exp2f(__builtin_fmaf(p[i], LOG2E, mc[0]));
    e[1][i >> 1][i & 1] = __builtin_amdgcn_exp2f(__builtin_fmaf(q[i], LOG2E, mc[1]));
  }
}
DI void sm_finish(const f32x2 (&e)[2][8], u32x4 (&o)[2][2]) {
  float r[2];
#pragma unroll
  for (int u = 0; u < 2; ++u) {
    const f32x2 s = ((e[u][0] + e[u][1]) + (e[u][2] + e[u][3])) + ((e[u][4] + e[u][5]) + (e[u][6] + e[u][7]));
    r[u] = __builtin_amdgcn_rcpf(s.x + s.y);
  }
#pragma unroll
  for (int u = 0; u < 2; ++u) {
    u32 w[8];
#pragma unroll
    for (int i = 0; i < 8; ++i) { const f32x2 t = e[u][i] * (f32x2){r[u], r[u]}; w[i] = pk2(t.x, t.y); }
    o[u][0] = (u32x4){w[0], w[1], w[2], w[3]};
    o[u][1] = (u32x4){w[4], w[5], w[6], w[7]};
  }
}
DI void softmax16x2(const f32x16& p, const f32x16& q, const float (&mc)[2], u32x4 (&o)[2][2]) {
  f32x2 e[2][8];
  sm_exp(p, q, mc, e);
  sm_finish(e, o);
}

#define STAMP(i) do { if ((i) == PROBE_N) { SCHED(); asm volatile("s_memrealtime %0\n\ts_waitcnt lgkmcnt(0)" : "=s"(pt1) :: "memory"); SCHED(); } } while (0)
#define STAMP_OUT() do { if (PROBE_N && (wave >> 2) == PROBE_ROLE) { unsigned long long d_ = pt1 - pt0; if (d_ > 100000) d_ = 100000; unsigned long long e_ = pt0 + 3000 + 8 * d_; \
  for (int it_ = 0; it_ < 1000000; ++it_) { unsigned long long t_; asm volatile("s_memrealtime %0\n\ts_waitcnt lgkmcnt(0)" : "=s"(t_) :: "memory"); if (t_ >= e_) break; } } } while (0)

__global__ __launch_bounds__(512, 2) void hedgehog(const u16* xA, const u16* wA, const u16* pA, const u16* oA,
                                                   const u16* xB, const u16* wB, const u16* pB, const u16* oB,
                                                   u16* out_, u32* ws_) {
  __shared__ __attribute__((aligned(16))) char smem[163840];
  const u32 lds0 = (u32)(size_t)(LAS char*)smem;
  unsigned long long pt0 = __builtin_amdgcn_s_memrealtime(), pt1 = pt0;
  const int bid = blockIdx.x, xcd = bid & 7, idx = bid >> 3;
  const int br = xcd >> 2;
#define KLOAD(tail)                                                                                       \
  u32x8 ka;                                                                                               \
  u32x4 kb;                                                                                               \
  asm volatile("s_load_dwordx8 %0, %2, %3\n\ts_load_dwordx4 %1, %2, 0x40" tail : "=&s"(ka), "=&s"(kb)     \
               : "s"(__builtin_amdgcn_kernarg_segment_ptr()), "s"(br * 32))
#define KWAIT() asm volatile("s_waitcnt lgkmcnt(0)" : "+s"(ka), "+s"(kb))
#define KPTR(v, i) ((void*)(((unsigned long long)(v)[(i) + 1] << 32) | (v)[i]))
  unsigned long long pcs;
  asm volatile("s_getpc_b64 %0" : "=s"(pcs));
  pcs &= ~255ull;
  const int lane = threadIdx.x & 63;
  const int wave = __builtin_amdgcn_readfirstlane(threadIdx.x >> 6);
  const int panel = (xcd & 3) * 4 + (idx & 3), grp = idx >> 2, gq = (grp + GQ) & 7, cg = (grp + GC) & 7;
  auto ochunk = [&](u32 j) -> u32 {
    const int v = (int)j + (((int)j - 1 - gq) >> 31), z = ((int)j - 1) >> 31;
    return (u32)__builtin_amdgcn_readfirstlane((v & ~z) | (gq & z));
  };
  f32x16 zero;
#pragma unroll
  for (int i = 0; i < 16; ++i) zero[i] = 0.f;
#define LANE_IDS()                                                                            \
  u32 lane_ = lane;                                                                           \
  PIN(lane_);                                                                                 \
  const u32 r = lane_ & 31, h = lane_ >> 5;                                                   \
  const u32 blk = (lane_ >> 4) & 1, q = (lane_ & 15) >> 2, p = lane_ & 3;                     \
  u32* ws = (u32*)KPTR(kb, 2);                                                                \
  const __amdgpu_buffer_rsrc_t wsr = __builtin_amdgcn_make_buffer_rsrc(ws, 0, WS_END, SRD3);  \
  const __amdgpu_buffer_rsrc_t wor = __builtin_amdgcn_make_buffer_rsrc(KPTR(ka, 6), 0, 512 * 512 * 2, SRD3)

  if (wave >= 4) {
    const int cw = wave - 4;
    const int rh = cw >> 1, hp = cw & 1;
    KLOAD("\n\ts_waitcnt lgkmcnt(0)");
    u32 sp0, sp1, sp2, sp3;
    {
      u32 off = idx * 1024 + cw * 256;
      off = off < CODE_PF ? off : 0;
      asm volatile("s_load_dword %0, %4, %5 offset:0\n\ts_load_dword %1, %4, %5 offset:64\n\ts_load_dword %2, %4, %5 offset:128\n\ts_load_dword %3, %4, %5 offset:192"
                   : "=&s"(sp0), "=&s"(sp1), "=&s"(sp2), "=&s"(sp3) : "s"(pcs), "s"(off));
    }
    LANE_IDS();
    u32 key = *(const CAS u32*)((const char*)ws + WS_EPOCH + br * 256) + 1u;
    u32 xa[2], xb[2], wa[2], wb2[2];
    {
      const u32 r16 = lane_ & 15, g = lane_ >> 4;
#pragma unroll
      for (int s = 0; s < 2; ++s) {
        xa[s] = lds0 + (64 * rh + r16) * 128 + 16 * ((4 * s + g) ^ (r16 >> 1));
        wa[s] = lds0 + 16384 + (8 * g + q) * 128 + 16 * ((4 * hp + 2 * s + (p >> 1)) ^ ((((q >> 1) & 1) << 2) | (blk << 1))) + 8 * (p & 1);
        xb[s] = xa[s] + 2 * SLOT; wb2[s] = wa[s] + 2 * SLOT;
        PIN(xa[s]); PIN(xb[s]); PIN(wa[s]); PIN(wb2[s]);
      }
    }
    u32 fpa = lds0 + L_WPHI + hp * 1024 + (8 * h + q) * 32 + (8 * blk + 4 * (p >> 1)) * 2;
    PIN(fpa);
    PINS(key);
    f32x4 ak[4][2], av[4][2];
#pragma unroll
    for (int i = 0; i < 8; ++i) ak[i >> 1][i & 1] = av[i >> 1][i & 1] = (f32x4){0.f, 0.f, 0.f, 0.f};
    u32 vra, pra, vwa, pw0, pw1, pub16, pub4;
    u32 l16, l4, kwa, kza, gso[4], vo[4], wsa, wso;
    u32x4 sel;
    auto kset = [&]() {
      u32 ln = lane;
      PIN(ln);
#pragma unroll
      for (int i = 0; i < 4; ++i) { vo[i] = (ln >> 3) * 1024 + 16 * ((ln & 7) ^ (((ln >> 4) & 1) << 2)) + i * 7168; PIN(vo[i]); }
      wsa = ochunk(cw) * 65536 + cg * 128; wso = ochunk(4 + cw) * 65536 + cg * 128;
      const u32 r_ = ln & 31, h_ = ln >> 5;
      l16 = ln * 16; l4 = ln * 4;
      kwa = lds0 + L_KV + cw * 1536 + r_ * 48 + h_ * 16; kza = kwa + 32 - h_ * 8;
      const u32 j = (r_ & 3) + 4 * (r_ >> 3);
      const u32 one = (r_ < 16 && ((r_ >> 2) & 1) == h_) ? ((j & 1) ? 0x3F800000u : 0x00003F80u) : 0u;
#pragma unroll
      for (int e = 0; e < 4; ++e) sel[e] = (j >> 1) == e ? one : 0u;
#pragma unroll
      for (int i = 0; i < 4; ++i) { gso[i] = WS_PART + ((br * 32 + gq * 4 + cw) * 16 + ((i + idx) & 3) * 4) * REC; PINS(gso[i]); }
      PIN(l16); PIN(l4); PIN(kwa); PIN(kza); PIN(sel); PINS(wsa); PINS(wso);
    };
    if (KSET == 0) kset();
    u32x4 xf[2][4], wk[2][2], wv[2][2];
    auto ldx = [&](int c, int s) {
      const u32 x = c % 3 == 2 ? xb[s] : xa[s] + (c % 3) * SLOT;
#pragma unroll
      for (int i = 0; i < 4; ++i) xf[s][i] = lds_r128(x + i * 2048);
    };
    auto ldw = [&](int c, int s, int m) {
#pragma unroll
      for (int n = 0; n < 2; ++n) {
        const u32 w = (c % 3 == 2 ? wb2[n] : wa[n] + (c % 3) * SLOT) + s * 4096 + m * 8192;
        (m ? wv : wk)[s][n] = lds_tr2(w, w + 512);
      }
    };
    auto mk = [&](int s) {
#pragma unroll
      for (int i = 0; i < 8; ++i) ak[i >> 1][i & 1] = mfma16(wk[s][i & 1], xf[s][i >> 1], ak[i >> 1][i & 1]);
    };
    auto mv = [&](int s) {
#pragma unroll
      for (int i = 0; i < 8; ++i) av[i >> 1][i & 1] = mfma16(xf[s][i >> 1], wv[s][i & 1], av[i >> 1][i & 1]);
    };
    auto mix = [&](int n) {
#pragma unroll
      for (int i = 0; i < 8; ++i) {
        SGB(0x8, 1);
        if (n == 12) { if (i < 4) SGB(0x100, 2); else SGB(0x100, 1); }
        else if (n == 8) SGB(0x100, 1);
        else if (n == 4) { if (i < 4) SGB(0x100, 1); }
        else if (n < 0) SGB(0x6, 2);
      }
      SCHED();
    };
    BAR();
    asm volatile("" ::"s"(sp0), "s"(sp1), "s"(sp2), "s"(sp3));
    STAMP(1);
    ldw(0, 0, 0); ldx(0, 0); ldw(0, 0, 1);
    u32x4 fa[2];
#pragma unroll
    for (int hh = 0; hh < 2; ++hh) fa[hh] = lds_tr2(fpa + hh * 512, fpa + hh * 512 + 128);
    SCHED();
#pragma unroll
    for (int c = 0; c < 7; ++c) {
      ldw(c, 1, 0); ldx(c, 1); mk(0); mix(8);
      ldw(c, 1, 1); mv(0); mix(4);
      u32 ln = lane;
      PIN(ln);
      const u32 r_ = ln & 31, h_ = ln >> 5, q_ = (ln & 15) >> 2;
      if (c == 1) {
        const u32 sg = (ln & 4) ? 0x80008000u : 0u;
#pragma unroll
        for (int hh = 0; hh < 2; ++hh) { OPQ(fa[hh]); fa[hh] ^= (u32x4){sg, sg, sg, sg}; }
      }
      if (c == 2) vra = lds0 + L_V + (cw >> 1) * (2 * VF) + (r_ < 16 ? (ln + 16 * (cw & 1)) * 16 : 1024);
      if (c == 3) pra = lds0 + L_PHI + cw * 8192 + (4 * h_ + q_) * 64 + 16 * ((((ln >> 3) & 2) + ((ln >> 1) & 1)) ^ ((2 * h_ + (q_ >> 1)) & 3)) + 8 * (ln & 1);
      if (c == 4) {
        const u32 rec = WS_PART + ((br * 32 + grp * 4 + cw) * 16 + panel) * REC;
        pub16 = rec + ln * 16; pub4 = rec + 1024 + ln * 4;
      }
      if (c == 5) { pw0 = lds0 + L_PHI + (2 * hp) * 8192 + (64 * rh + r_) * 64 + 16 * ((2 * h_) ^ ((r_ >> 1) & 3)); pw1 = pw0 ^ 16; }
      if (c == 6) vwa = lds0 + L_V + (8 * rh + 2 * hp) * VF + ((ln & 16) * 2 + (ln & 15)) * 16 + h_ * 8;
      mk(1); mix(c >= 1 ? -1 : 0);
      if (c == 1) { PIN(fa[0]); PIN(fa[1]); }
      if (c == 2) PIN(vra);
      if (c == 3) PIN(pra);
      if (c == 4) { PIN(pub16); PIN(pub4); }
      if (c == 5) { PIN(pw0); PIN(pw1); }
      if (c == 6) PIN(vwa);
      if (c + 1 == KSET) { kset(); SCHED(); }
      BAR();
      ldw(c + 1, 0, 0); ldx(c + 1, 0); ldw(c + 1, 0, 1);
      mv(1); mix(12);
    }
    ldw(7, 1, 0); ldx(7, 1); ldw(7, 1, 1); mk(0); mix(12);
    mk(1); mix(0);
    __builtin_amdgcn_s_setprio(2);
    STAMP(2);
    f32x16 pk[2][2];
    {
      u32x4 kf[2][2];
      auto kpack = [&](int i) {
#pragma unroll
        for (int hh = 0; hh < 2; ++hh) {
          const f32x4 &t0 = ak[2 * i][hh], &t1 = ak[2 * i + 1][hh];
          const auto lo = __builtin_amdgcn_permlane16_swap(pk2(t0[0], t0[1]), pk2(t1[0], t1[1]), false, false);
          const auto hi = __builtin_amdgcn_permlane16_swap(pk2(t0[2], t0[3]), pk2(t1[2], t1[3]), false, false);
          kf[i][hh] = (u32x4){lo[0], hi[0], lo[1], hi[1]};
        }
      };
      mv(0); kpack(0);
      SGB(0x8, 2);
#pragma unroll
      for (int i = 0; i < 6; ++i) { SGB(0x2, 2); SGB(0x8, 1); }
      SCHED();
      pk[0][0] = mfma(fa[0], kf[0][0], zero);
      pk[0][1] = mfma(fa[1], kf[0][1], zero);
      kpack(1);
      SGB(0x8, 1); SGB(0x2, 6); SGB(0x8, 1); SGB(0x2, 6);
      PIN(pk[0][0]); PIN(pk[0][1]); PIN(kf[1][0]); PIN(kf[1][1]);
      SCHED();
      mv(1);
      pk[1][0] = mfma(fa[0], kf[1][0], zero);
      pk[1][1] = mfma(fa[1], kf[1][1], zero);
    }
    float m0[2], m1[2];
    m0[0] = negmax16(pk[0][0]); m0[1] = negmax16(pk[0][1]);
    SGB(0x8, 2);
#pragma unroll
    for (int i = 0; i < 6; ++i) { SGB(0x2, 2); SGB(0x8, 1); }
    SGB(0x8, 1); SGB(0x2, 6); SGB(0x8, 1);
#pragma unroll
    for (int i = 0; i < 8; ++i) PIN(av[i >> 1][i & 1]);
    PIN(pk[1][0]); PIN(pk[1][1]); PIN(m0[0]); PIN(m0[1]);
    SCHED();
#pragma unroll
    for (int i = 0; i < 8; ++i) {
      const f32x4& t = av[i >> 1][i & 1];
      *(LAS u32x2*)(size_t)(vwa + (4 * (i >> 2) + ((i >> 1) & 1)) * VF + (i & 1) * 256) = (u32x2){pk2(t[0], t[1]), pk2(t[2], t[3])};
    }
    SCHED();
    {
      u32x4 o[2][2];
      softmax16x2(pk[0][0], pk[0][1], m0, o);
      lds_w128(pw0, o[0][0]); lds_w128(pw1, o[0][1]);
      lds_w128(pw0 + 8192, o[1][0]); lds_w128(pw1 + 8192, o[1][1]);
      SCHED();
      m1[0] = negmax16(pk[1][0]); m1[1] = negmax16(pk[1][1]);
      PIN(m1[0]); PIN(m1[1]);
      SCHED();
      softmax16x2(pk[1][0], pk[1][1], m1, o);
      lds_w128(pw0 + 2048, o[0][0]); lds_w128(pw1 + 2048, o[0][1]);
      lds_w128(pw0 + 2048 + 8192, o[1][0]); lds_w128(pw1 + 2048 + 8192, o[1][1]);
      SGB(0x402, 26); SGB(0x200, 1); SGB(0x402, 8); SGB(0x200, 1); SGB(0x402, 8); SGB(0x200, 1); SGB(0x402, 8); SGB(0x200, 1);
      SCHED();
      STAMP(30);
      BARE();
      BAR();
      SCHED();
    }
    STAMP(3);
    f32x16 kv = zero;
    {
      u32x4 a[8], b[8];
#pragma unroll
      for (int u = 0; u < 8; ++u) {
        a[u] = lds_r128(vra + ((u >> 1) * 4 + (u & 1)) * VF);
        b[u] = lds_tr2(pra + 16 * u * 64, pra + (16 * u + 8) * 64);
      }
#pragma unroll
      for (int u = 0; u < 8; ++u) kv = mfma(a[u], b[u], kv);
      SGB(0x100, 12); SGB(0x8, 1); SGB(0x100, 6); SGB(0x8, 1); SGB(0x100, 6); SGB(0x8, 6);
    }
    {
      const u32x4 w = pack8(kv, 0);
      st128c(w, wsr, pub16);
      const u32 zb = __float_as_uint(kv[8]);
      const u32 chk = key - pair_sum(sum4(h ? 0u : zb, w));
      st32c(h ? chk : zb, wsr, pub4);
    }
    SCHED();
    STAMP(4);
    if (KSET < 0) kset();
#pragma unroll
    for (int i = 0; i < 2; ++i) dma4(lds0 + L_WO + cw * 8192 + i * 4096, vo, wor, wsa + i * 32768);
    if (DELAY1) __builtin_amdgcn_s_sleep(DELAY1);
    STAMP(10);
    f32x16 gs[4];
    float z[4];
    {
      u32x4 g[16];
      u32 gz[16];
      auto gload = [&](int gi) {
#pragma unroll
        for (int i = 0; i < 4; ++i) {
          g[4 * gi + i] = __builtin_amdgcn_raw_buffer_load_b128(wsr, l16 + i * REC, gso[gi], AUX_SC1);
          gz[4 * gi + i] = __builtin_amdgcn_raw_buffer_load_b32(wsr, l4 + (1024 + i * REC - 1024 * (i == 3)), gso[gi] + 1024 * (i == 3), AUX_SC1);
          SCHED();
        }
      };
      auto gsum = [&](int gi) -> bool {
        u32 c = 0;
#pragma unroll
        for (int i = 0; i < 4; ++i) {
          c = sum4(c + gz[4 * gi + i], g[4 * gi + i]);
          gs[gi] = mfma(sel, g[4 * gi + i], i == 0 ? zero : gs[gi]);
          z[gi] = i == 0 ? __uint_as_float(gz[4 * gi]) : z[gi] + __uint_as_float(gz[4 * gi + i]);
        }
        PIN(z[gi]);
        const bool bad = __builtin_amdgcn_ballot_w64(pair_sum(c) != 4u * key) != 0;
        SCHED();
        return bad;
      };
      gload(0); gload(1); gload(2); gload(3);
      bool b0 = gsum(0), b1 = gsum(1), b2 = gsum(2), b3 = gsum(3);
      STAMP(11);
#pragma unroll 1
      for (u32 spin = 0; (b0 | b1 | b2 | b3) && spin < SPIN_CAP; ++spin) {
        asm volatile("" ::: "memory");
        if (b0) gload(0);
        if (b1) gload(1);
        if (b2) gload(2);
        if (b3) gload(3);
        if (b0) b0 = gsum(0);
        if (b1) b1 = gsum(1);
        if (b2) b2 = gsum(2);
        if (b3) b3 = gsum(3);
      }
    }
    __builtin_amdgcn_s_waitcnt(0x0F70);
    STAMP(5);
    {
      float tt[8];
#pragma unroll
      for (int j = 0; j < 8; ++j) tt[j] = (gs[0][j] + gs[1][j]) + (gs[2][j] + gs[3][j]);
      const float zt = (z[0] + z[1]) + (z[2] + z[3]);
      lds_w128(kwa, (u32x4){pk2(tt[0], tt[1]), pk2(tt[2], tt[3]), pk2(tt[4], tt[5]), pk2(tt[6], tt[7])});
      const u32 zz = pk2(zt, zt);
      *(LAS u32x2*)(size_t)kza = (u32x2){zz, zz};
    }
    if (TIMER == 2) {
#pragma unroll
      for (int i = 0; i < 2; ++i) dma4(lds0 + L_WO + 32768 + cw * 8192 + i * 4096, vo, wor, wso + i * 32768);
    }
    BAR();
    STAMP(6);
    if (TIMER == 3) {
#pragma unroll
      for (int i = 0; i < 2; ++i) dma4(lds0 + L_WO + 32768 + cw * 8192 + i * 4096, vo, wor, wso + i * 32768);
    }
    {
      u32 oso[8], ob = lds0 + L_O + cw * OREC;
#pragma unroll
      for (int i = 1; i < 8; ++i) { oso[i] = WS_O + ((br * 64 + panel * 4 + cw) * 8 + ochunk(i)) * OREC; PINS(oso[i]); }
      PINS(ob);
      if (TIMER == 0) {
        BARE();
        STAMP(7);
#pragma unroll
        for (int i = 0; i < 2; ++i) dma4(lds0 + L_WO + 32768 + cw * 8192 + i * 4096, vo, wor, wso + i * 32768);
      }
      WAITV(0);
      BARE();
      if (DELAY3) __builtin_amdgcn_s_sleep(DELAY3);
      STAMP(8);
      auto oissue = [&](int j) { dma_orec(ob + (j % 5) * (4 * OREC), l4, l16, wsr, oso[j]); };
      oissue(1); oissue(2);
      WAITV(5); BARE(); oissue(3);
      WAITV(5); STAMP(15); BARE(); oissue(4); oissue(5);
      WAITV(10); BARE(); oissue(6);
      WAITV(10); BARE(); oissue(7);
      WAITV(10); BARE();
      WAITV(5); BARE();
      WAITV(0); BARE();
    }
    STAMP(9);
    STAMP_OUT();
  } else {
    KLOAD("");
    const int t = wave;
    const u32 l7 = lane & 7, l3 = lane >> 3, lq = lane >> 4;
    const u32 wsw = 16 * (l7 ^ ((lq & 1) << 2));
    u32 vx[4], vw[4];
#pragma unroll
    for (int i = 0; i < 4; ++i) {
      vx[i] = l3 * 1024 + 16 * (l7 ^ (((i & 1) << 2) | lq)) + i * 7168;
      vw[i] = l3 * 3072 + (wsw ^ ((i & 1) << 5)) + i * (8 * 3072 - 1024);
    }
    int rot = (grp + 2 * (idx & 3)) & 7;
    auto chunk = [&](u32 c) -> u32 {
      constexpr u32 n0 = ROTMODE / 100, n1 = ROTMODE / 10 % 10, n2 = ROTMODE % 10;
      const u32 g = grp + (idx & 3), r3 = g - 3 * ((g * 11) >> 5);
      auto run = [&](u32 k, u32 n) -> u32 {
        if (n == 3) { const u32 v = k + r3; return v >= 3 ? v - 3 : v; }
        return k ^ (g & (n - 1));
      };
      const u32 a = run(c, n0), b = n0 + run(c - n0, n1), d = n0 + n1 + run(c - n0 - n1, n2);
      return ((c < n0 ? a : c < n0 + n1 ? b : d) + CSH) & 7;
    };
    const u32 xs0 = (panel * 128 + t * 32) * 1024, ws0 = (t & 1) * (32 * 3072) + (1 + (t >> 1)) * 1024 + grp * 128;
    const u32 ldst = lds0 + t * 4096;
    u32 e0 = ldst, e1 = ldst + 16384, e2 = xs0 + chunk(0) * 128, e3 = ws0 + chunk(0) * (64 * 3072);
    PINS(e0); PINS(e1); PINS(e2); PINS(e3);
#pragma unroll
    for (int i = 0; i < 4; ++i) { PIN(vx[i]); PIN(vw[i]); }
    KWAIT();
    const __amdgpu_buffer_rsrc_t xr = __builtin_amdgcn_make_buffer_rsrc(KPTR(ka, 0), 0, 2048 * 512 * 2, SRD3);
    const __amdgpu_buffer_rsrc_t wr = __builtin_amdgcn_make_buffer_rsrc(KPTR(ka, 2), 0, 512 * 1536 * 2, SRD3);
    STAMP(10);
    auto dx = [&](int c, u32 d, u32 so) { if (c < NTX) dma4nt(d, vx, xr, so); else dma4(d, vx, xr, so); };
    auto dw = [&](int c, u32 d, u32 so) { if (c < NTW) dma4nt(d, vw, wr, so); else dma4(d, vw, wr, so); };
    if (WFIRST & 1) { dw(0, e1, e3); dx(0, e0, e2); } else { dx(0, e0, e2); dw(0, e1, e3); }
    if (FOLLOW) {
      const int gi = (grp + (ka[2] >> 7) - 4) & 7;
      rot = (gi == 7 ? 0 : gi) + 2 * (idx & 3);
      rot = rot >= 7 ? rot - 7 : rot;
    }
    {
      const __amdgpu_buffer_rsrc_t pr = __builtin_amdgcn_make_buffer_rsrc(KPTR(ka, 4), 0, 32 * 256 * 2, SRD3);
      dma1(lds0 + L_WPHI + t * 1024, lane * 16, pr, (t < 2 ? grp : gq) * 2048 + (t & 1) * 1024);
    }
    STAMP(11);
    u32 th3 = 0, thq = 0, tho;
    if (H3) TOUCH(th3, (l7 >> 1) * 3072 + (l7 & 1) * 1024, wr, (idx * 16 + t * 4) * 3072 + 1024 + ((3 - (ka[2] >> 7)) & 7) * 128);
    if (WFIRST & 2) dw(1, ldst + SLOT + 16384, ws0 + chunk(1) * (64 * 3072));
    dx(1, ldst + SLOT, xs0 + chunk(1) * 128);
    if (!(WFIRST & 2)) dw(1, ldst + SLOT + 16384, ws0 + chunk(1) * (64 * 3072));
    STAMP(1);
    LANE_IDS();
    u32 key = *(const CAS u32*)((const char*)ws + WS_EPOCH + br * 256) + 1u;
    const u32 sw = (r >> 1) & 7;
    u32 xa[4];
#pragma unroll
    for (int s = 0; s < 4; ++s) { xa[s] = lds0 + (32 * t + r) * 128 + 16 * ((2 * s + h) ^ sw); PIN(xa[s]); }
    u32 fpa = lds0 + L_WPHI + 2048 + (4 * h + q) * 32 + (8 * blk + 4 * (p >> 1)) * 2;
    u32 vq = (r >> 3) * 3072 + (r & 7) * 128;
    PIN(fpa); PIN(vq);
    PINS(key);
    u32x4 xq[32], fa[4];
    u32 wa, wb, kra, o_off, c_off, woa, wob, vto, sto;
#pragma unroll
    for (int c = 0; c < 8; ++c) {
      u32 d0, d1, s0, s1;
      if (c < 6) {
        d0 = ldst + ((c + 2) % 3) * SLOT; d1 = d0 + 16384;
        s0 = xs0 + chunk(c + 2) * 128; s1 = ws0 + chunk(c + 2) * (64 * 3072);
      } else {
        if (WQ4) {
          const u32 k0 = 2 * ((2 * (c - 6) + idx) & 3) + (t >> 1), k1 = (k0 + 2) & 7;
          d0 = lds0 + L_WQ1 + ((chunk(k0) + 4) & 7) * 8192 + (t & 1) * 4096; d1 = lds0 + L_WQ1 + ((chunk(k1) + 4) & 7) * 8192 + (t & 1) * 4096;
          s0 = (k0 * 64 + (t & 1) * 32) * 3072 + gq * 128; s1 = (k1 * 64 + (t & 1) * 32) * 3072 + gq * 128;
        } else {
          const u32 half = (c - 6) ^ (WQSWAP ? (idx & 1) : 0);
          d0 = lds0 + L_WQ0 - half * (L_WQ0 - L_WQ1) + t * 8192; d1 = d0 + 4096;
          s0 = chunk(half * 4 + t) * (64 * 3072) + gq * 128; s1 = s0 + 32 * 3072;
        }
      }
      PINS(d0); PINS(d1); PINS(s0); PINS(s1);
      if ((H3 && c == 0) || (LTQ && c == 7)) WAITV(9); else WAITV(8);
      if (H3 && c == 1) PIN(th3);
      STAMP(20 + c);
      BAR();
      if (LTQ && c == 6) TOUCH(thq, vq, wr, (idx * 16 + t * 4) * 3072);
      if (WFIRST & 4) dw(c + 2, d1, s1); else if (c < 6) dx(c + 2, d0, s0); else dw(c + 2, d0, s0);
#pragma unroll
      for (int s = 0; s < 4; ++s) xq[4 * c + s] = lds_r128(xa[s] + (c % 3) * SLOT);
      if (!(WFIRST & 4)) dw(c + 2, d1, s1); else if (c < 6) dx(c + 2, d0, s0); else dw(c + 2, d0, s0);
      if (c == 0) {
#pragma unroll
        for (int hd = 0; hd < 4; ++hd) fa[hd] = lds_tr2(fpa + hd * 512, fpa + hd * 512 + 256);
      }
      if (c == QSET) {
        lds_w128(lds0 + L_V + (lane & 15) * VF + 1024, (u32x4){0x3F803F80u, 0x3F803F80u, 0x3F803F80u, 0x3F803F80u});
        const u32 sg = (lane & 4) ? 0x80008000u : 0u;
#pragma unroll
        for (int hd = 0; hd < 4; ++hd) { PIN(fa[hd]); fa[hd] ^= (u32x4){sg, sg, sg, sg}; PIN(fa[hd]); }
        wa = lds0 + L_WQ1 + (8 * h + q) * 128 + 16 * ((2 * blk + (p >> 1)) ^ ((((q >> 1) & 1) << 2) | (h << 1))) + 8 * (p & 1);
        wb = wa ^ 64;
        kra = lds0 + L_KV + (16 * h + q) * 48 + (blk ? 32 : 8 * p);
        o_off = WS_O + ((br * 64 + panel * 4 + t) * 8 + gq) * OREC + 256 + lane * 16;
        c_off = WS_O + ((br * 64 + panel * 4 + t) * 8 + gq) * OREC + lane * 4;
        woa = lds0 + L_WO + (8 * h + q) * 128 + 16 * ((2 * blk + (p & 1)) ^ (((q >> 1) & 1) << 2)) + 8 * (p >> 1);
        wob = woa ^ 64;
        vto = r * 128; sto = (idx * 16 + t * 4) * 1024;
        PIN(wa); PIN(wb); PIN(kra); PIN(o_off); PIN(c_off); PIN(woa); PIN(wob); PIN(vto); PINS(sto);
      }
    }
    STAMP(32);
    TOUCH(tho, vto, wor, sto);
    STAMP(2);
    u32x4 fq[2][2][2];
    {
      f32x16 aq[2], pq[2];
      float mq[2];
      aq[0] = aq[1] = zero;
      u32x4 wf[4];
      auto ldw = [&](int n) {
        const int k = n & 31;
        const u32 o = (n < 32 ? wa : wb) + (k < 16 ? (L_WQ0 - L_WQ1) : 0) + (k & 15) * 2048;
        wf[n & 3] = lds_tr2(o, o + 512);
      };
      WAITV(1);
      STAMP(31);
      BAR();
      SCHED();
      STAMP(12);
      ldw(0); ldw(1); ldw(2);
      SCHED();
      BARE();
      SCHED();
#pragma unroll
      for (int n = 0; n < 34; ++n) {
        ldw(n + 3);
        aq[n >> 5] = mfma(wf[n & 3], xq[n & 31], aq[n >> 5]);
        SGB(0x8, 1); SGB(0x100, 2);
        SCHED();
      }
#pragma unroll
      for (int hh = 0; hh < 2; ++hh) pq[hh] = mfma(fa[hh], pack8(aq[0], hh), zero);
      SCHED();
      STAMP(4);
#pragma unroll
      for (int n = 34; n < 64; ++n) {
        if (n + 3 < 64) ldw(n + 3);
        aq[1] = mfma(wf[n & 3], xq[n & 31], aq[1]);
      }
      mq[0] = negmax16(pq[0]); mq[1] = negmax16(pq[1]);
      f32x2 eq[2][8];
      sm_exp_s(pq[0], pq[1], mq, eq);
      SGB(0x8, 1); SGB(0x100, 2); SGB(0x8, 1); SGB(0x100, 2);
#pragma unroll
      for (int n = 36; n < 41; ++n) { SGB(0x8, 1); SGB(0x100, 2); SGB(0x402, 4); }
#pragma unroll
      for (int n = 41; n < 64; ++n) { SGB(0x8, 1); SGB(0x100, 2); if (n & 1) SGB(0x402, 3); else SGB(0x402, 2); }
#pragma unroll
      for (int i = 0; i < 16; ++i) PIN(eq[i >> 3][i & 7]);
      SCHED();
      STAMP(3);
#pragma unroll
      for (int hh = 0; hh < 2; ++hh) { pq[hh] = mfma(fa[2 + hh], pack8(aq[1], hh), zero); PIN(pq[hh]); }
      SCHED();
      sm_finish(eq, fq[0]);
      SCHED();
      asm volatile("s_waitcnt vmcnt(0)" : "+v"(tho), "+v"(thq)::"memory");
      mq[0] = negmax16(pq[0]); mq[1] = negmax16(pq[1]);
      PIN(mq[0]); PIN(mq[1]);
      SCHED();
#pragma unroll
      for (int i = 0; i < 4; ++i) PIN(fq[0][i >> 1][i & 1]);
      softmax16x2(pq[0], pq[1], mq, fq[1]);
#pragma unroll
      for (int i = 0; i < 4; ++i) PIN(fq[1][i >> 1][i & 1]);
    }
    STAMP(5);
    BAR();
    STAMP(6);
    u32x4 ob[2][4];
    {
      u32x4 a0[4], a1[4];
#pragma unroll
      for (int hd = 0; hd < 4; ++hd) {
        a0[hd] = lds_tr2(kra + hd * 1536, kra + hd * 1536 + 4 * 48);
        a1[hd] = lds_tr2(kra + hd * 1536 + 8 * 48, kra + hd * 1536 + 12 * 48);
      }
      SCHED();
      f32x16 y[4];
      u32 cs = 0;
      auto num = [&](int hd) {
        y[hd] = mfma(a0[hd], fq[hd >> 1][hd & 1][0], zero);
        y[hd] = mfma(a1[hd], fq[hd >> 1][hd & 1][1], y[hd]);
      };
      auto fin = [&](int hd) {
        const f32x16& yy = y[hd];
        const float rd = __builtin_amdgcn_rcpf(yy[8] + 1e-6f);
        u32 w[4];
#pragma unroll
        for (int e = 0; e < 4; ++e) { float t0 = yy[2 * e] * rd, t1 = yy[2 * e + 1] * rd; if (hd < 2) OPQ(t0); w[e] = pk2(t0, t1); }
        ob[0][hd] = (u32x4){w[0], w[1], w[2], w[3]};
        st128c(ob[0][hd], wsr, o_off + hd * 1024);
        cs = sum4(cs, ob[0][hd]);
      };
      num(0); num(1); PIN(y[0]); PIN(y[1]); SCHED();
      num(2); fin(0); SGB(0x8, 1); SGB(0x402, 6); SGB(0x8, 1); PIN(y[2]); SCHED();
      num(3); fin(1); SGB(0x8, 1); SGB(0x402, 6); SGB(0x8, 1); PIN(y[3]); SCHED();
      fin(2); fin(3);
      st32c(key - cs, wsr, c_off);
    }
    SCHED();
    if (TIMER == 0) BARE();
    STAMP(7);
    u32x4 wo[32][2];
    auto ldwo = [&](int n) {
      wo[n][0] = lds_tr2(woa + n * 2048, woa + n * 2048 + 512);
      wo[n][1] = lds_tr2(wob + n * 2048, wob + n * 2048 + 512);
    };
#pragma unroll
    for (int n = 0; n < 16; ++n) ldwo(n);
    f32x16 c0 = zero, c1 = zero;
#pragma unroll
    for (int s = 0; s < 4; ++s) { c0 = mfma(wo[s][0], ob[0][s], c0); c1 = mfma(wo[s][1], ob[0][s], c1); }
#pragma unroll
    for (int k = 4; k < 16; ++k) { PIN(wo[k][0]); PIN(wo[k][1]); }
    PIN(c0); PIN(c1);
    if (TIMER) WAITV(0);
    BARE();
    SCHED();
#pragma unroll
    for (int n = 16; n < 24; ++n) ldwo(n);
#pragma unroll
    for (int k = 16; k < 24; ++k) { PIN(wo[k][0]); PIN(wo[k][1]); }
    const __amdgpu_buffer_rsrc_t outr = __builtin_amdgcn_make_buffer_rsrc(KPTR(kb, 0), 0, 2048 * 1024 * 2, SRD3);
    u32 oa = lds0 + L_O + t * OREC + 256 + lane * 16;
    u32 orow = ((panel * 128 + t * 32 + l3) * 1024 + br * 512 + cg * 64 + 8 * l7) * 2;
    PIN(oa); PIN(orow);
    u32 oc = oa - 256 - lane * 12, l4 = lane * 4, l16 = lane * 16, oa4 = oa + 4 * (4 * OREC), oc4 = oc + 4 * (4 * OREC);
    u32 sw1 = lds0 + t * 4096 + r * 128 + 16 * (h ^ (r & 7)), sr1 = lds0 + t * 4096 + l3 * 128 + 16 * (l7 ^ l3);
    u32 sw2 = sw1 ^ 32, sw3 = sw1 ^ 64, sw4 = sw1 ^ 96, or1 = orow + 16384, or2 = orow + 32768, or3 = orow + 49152;
    PIN(oc); PIN(l4); PIN(l16); PIN(sw1); PIN(sr1); PIN(oa4); PIN(oc4); PIN(sw2); PIN(sw3); PIN(sw4); PIN(or1); PIN(or2); PIN(or3);
    STAMP(8);
    u32 nretry = 0;
    u32 chk[2];
    auto oread = [&](int j) {
      const u32 da = j % 5 == 4 ? oa4 : oa + (j % 5) * (4 * OREC), dc = j % 5 == 4 ? oc4 : oc + (j % 5) * (4 * OREC);
#pragma unroll
      for (int s = 0; s < 4; ++s) ob[j & 1][s] = lds_r128(da + s * 1024);
      chk[j & 1] = *(LAS u32*)(size_t)dc;
    };
    auto obad = [&](int j) -> bool {
      const u32 c = sum4(sum4(chk[j & 1], ob[j & 1][0]), ob[j & 1][1]) + sum4(sum4(0u, ob[j & 1][2]), ob[j & 1][3]);
      return __builtin_amdgcn_ballot_w64(c != key) != 0;
    };
#pragma unroll
    for (int j = 1; j < 8; ++j) {
      BARE();
      SCHED();
      oread(j);
      if (j >= 2) {
#pragma unroll
        for (int s = 0; s < 4; ++s) {
          const int k = 4 * (j - 1) + s;
          c0 = mfma(wo[k][0], ob[(j - 1) & 1][s], c0);
          c1 = mfma(wo[k][1], ob[(j - 1) & 1][s], c1);
        }
        if (j >= 3 && j <= 6) { ldwo(24 + 2 * (j - 3)); ldwo(25 + 2 * (j - 3)); }
      }
      const bool bad = obad(j);
      if (j >= 3 && j <= 6) {
        SGB(0x8, 1); SGB(0x100, 3); SGB(0x8, 1); SGB(0x100, 2); SGB(0x8, 1); SGB(0x100, 3); SGB(0x8, 1); SGB(0x100, 3);
        SGB(0x8, 1); SGB(0x2, 4); SGB(0x8, 1); SGB(0x2, 5); SGB(0x8, 1); SGB(0x100, 2); SGB(0x8, 1);
      } else if (j >= 2) {
        SGB(0x8, 1); SGB(0x100, 3); SGB(0x8, 1); SGB(0x100, 2); SGB(0x8, 3); SGB(0x2, 4); SGB(0x8, 1); SGB(0x2, 5); SGB(0x8, 2);
      }
      SCHED();
      if (__builtin_expect(bad, 0)) {
#pragma unroll 1
        for (u32 spin = 0; spin < SPIN_CAP; ++spin) {
          dma_orec(lds0 + L_O + t * OREC + (j % 5) * (4 * OREC), l4, l16, wsr, WS_O + ((br * 64 + panel * 4 + t) * 8 + ochunk(j)) * OREC);
#pragma unroll 1
          for (u32 jj = j + 1; jj <= (j + AHEAD < 7 ? j + AHEAD : 7); ++jj) {
            const u32 sl = (jj >= 5 ? jj - 5 : jj) * (4 * OREC);
            u32 c = *(LAS u32*)(size_t)(oc + sl);
#pragma unroll
            for (int s = 0; s < 4; ++s) c = sum4(c, lds_r128(oa + sl + s * 1024));
            if (__builtin_amdgcn_ballot_w64(c != key) != 0)
              dma_orec(lds0 + L_O + t * OREC + sl, l4, l16, wsr, WS_O + ((br * 64 + panel * 4 + t) * 8 + ochunk(jj)) * OREC);
          }
          WAITV(0);
          oread(j);
          ++nretry;
          if (!obad(j)) break;
        }
      }
    }
#pragma unroll
    for (int s = 0; s < 4; ++s) c0 = mfma(wo[28 + s][0], ob[1][s], c0);
    PIN(c0);
    SCHED();
#pragma unroll
    for (int s = 0; s < 4; ++s) c1 = mfma(wo[28 + s][1], ob[1][s], c1);
    lds_w128(sw1, pack8(c0, 0));
    lds_w128(sw2, pack8(c0, 1));
    SGB(0x8, 2); SGB(0x2, 4); SGB(0x200, 1); SGB(0x8, 1); SGB(0x2, 4); SGB(0x200, 1); SGB(0x8, 1);
    SCHED();
    STAMP(9);
    lds_w128(sw3, pack8(c1, 0));
    lds_w128(sw4, pack8(c1, 1));
    asm volatile("" ::: "memory");
    {
      u32x4 v[4];
#pragma unroll
      for (int i = 0; i < 4; ++i) v[i] = lds_r128(sr1 + i * 1024);
      SCHED();
      st128c(v[0], outr, orow);
      st128c(v[1], outr, or1);
      st128c(v[2], outr, or2);
      st128c(v[3], outr, or3);
    }
    if (t == 0 && panel == 0 && grp == 0 && lane == 0) st32c(key, wsr, WS_EPOCH + br * 256);
    if (PROBE_N == 13) WAITV(0);
    STAMP(13);
    STAMP_OUT();
  }
}
}

extern "C" void kernel_launch(void* const* d_in, const int* in_sizes, int n_in,
                              void* d_out, int out_size, void* d_ws, size_t ws_size,
                              hipStream_t stream) {
  hh::hedgehog<<<256, 512, 0, stream>>>((const u16*)d_in[0], (const u16*)d_in[1], (const u16*)d_in[2], (const u16*)d_in[3],
                                        (const u16*)d_in[4], (const u16*)d_in[5], (const u16*)d_in[6], (const u16*)d_in[7],
                                        (u16*)d_out, (u32*)((char*)d_ws + ((size_t)WSOFF << 20)));
}
```

```cpp
#define CODE_PF 28672
#define PROBE_N 13
#define PROBE_ROLE 0
#include <hip/hip_runtime.h>
#include <stdint.h>

typedef unsigned short u16;
typedef unsigned u32;
typedef short bf16x8 __attribute__((ext_vector_type(8)));
typedef short s16x4 __attribute__((ext_vector_type(4)));
typedef float f32x16 __attribute__((ext_vector_type(16)));
typedef float f32x4 __attribute__((ext_vector_type(4)));
typedef float f32x2 __attribute__((ext_vector_type(2)));
typedef unsigned u32x8 __attribute__((ext_vector_type(8)));
typedef unsigned u32x4 __attribute__((ext_vector_type(4)));
typedef unsigned u32x2 __attribute__((ext_vector_type(2)));
typedef __bf16 bf16x2 __attribute__((ext_vector_type(2)));
#define DI __device__ __forceinline__
#define LAS __attribute__((address_space(3)))
#define CAS __attribute__((address_space(4)))
#define BC(T, x) __builtin_bit_cast(T, x)

#define DELAY1 3
#define DELAY3 5
#define AHEAD 2
#define OORD 1

namespace hh {
constexpr int AUX_SC1 = 16;
constexpr u32 SRD3 = 0x00020000u;
constexpr u32 WS_EPOCH = 0;
constexpr u32 REC = 1280;
constexpr u32 WS_PART = 4096;
constexpr u32 OREC = 4352;
constexpr u32 WS_O = WS_PART + 2 * 32 * 16 * REC;
constexpr u32 WS_END = WS_O + 2 * 64 * 8 * OREC;
constexpr u32 WS_STAMP = 8u << 20;
constexpr u32 SLOT = 32768;
constexpr u32 L_WQ0 = 98304;
constexpr u32 L_WQ1 = 65536;
constexpr u32 L_WPHI = 163840 - 4096;
constexpr u32 L_PHI = 0;
constexpr u32 VF = 1040;
constexpr u32 L_V = 131072;
constexpr u32 L_KV = L_V + 16 * VF;
constexpr u32 L_WO = 0;
constexpr u32 L_O = 65536;
constexpr u32 SPIN_CAP = 1u << 16;

DI u32 pk2(float a, float b) { return BC(u32, __builtin_convertvector((f32x2){a, b}, bf16x2)); }
DI u32x4 pack8(const f32x16& x, int s) {
  return (u32x4){pk2(x[8 * s], x[8 * s + 1]), pk2(x[8 * s + 2], x[8 * s + 3]), pk2(x[8 * s + 4], x[8 * s + 5]), pk2(x[8 * s + 6], x[8 * s + 7])};
}
DI f32x16 mfma(u32x4 a, u32x4 b, f32x16 c) { return __builtin_amdgcn_mfma_f32_32x32x16_bf16(BC(bf16x8, a), BC(bf16x8, b), c, 0, 0, 0); }
DI f32x4 mfma16(u32x4 a, u32x4 b, f32x4 c) { return __builtin_amdgcn_mfma_f32_16x16x32_bf16(BC(bf16x8, a), BC(bf16x8, b), c, 0, 0, 0); }
DI u32x4 lds_r128(u32 a) { return *(LAS u32x4*)(size_t)a; }
DI void lds_w128(u32 a, u32x4 v) { *(LAS u32x4*)(size_t)a = v; }
DI u32x4 lds_tr2(u32 a0, u32 a1) {
  s16x4 lo = __builtin_amdgcn_ds_read_tr16_b64_v4i16((LAS s16x4*)(size_t)a0);
  s16x4 hi = __builtin_amdgcn_ds_read_tr16_b64_v4i16((LAS s16x4*)(size_t)a1);
  return BC(u32x4, __builtin_shufflevector(lo, hi, 0, 1, 2, 3, 4, 5, 6, 7));
}
DI void dma1(u32 lds, u32 voff, __amdgpu_buffer_rsrc_t srd, u32 soff) {
  asm volatile("s_mov_b32 m0, %0\n\ts_nop 0\n\tbuffer_load_dwordx4 %1, %2, %3 offen lds" ::"s"(lds), "v"(voff), "s"(srd), "s"(soff) : "memory");
}
DI void dma4(u32 lds, const u32 (&v)[4], __amdgpu_buffer_rsrc_t srd, u32 soff) {
  asm volatile("s_mov_b32 m0, %0\n\ts_nop 0\n\t"
               "buffer_load_dwordx4 %1, %5, %6 offen lds\n\t"
               "buffer_load_dwordx4 %2, %5, %6 offen offset:1024 lds\n\t"
               "buffer_load_dwordx4 %3, %5, %6 offen offset:2048 lds\n\t"
               "buffer_load_dwordx4 %4, %5, %6 offen offset:3072 lds" ::"s"(lds), "v"(v[0]), "v"(v[1]), "v"(v[2]), "v"(v[3]), "s"(srd), "s"(soff) : "memory");
}
DI void dma_orec(u32 lds, u32 l4, u32 l16, __amdgpu_buffer_rsrc_t srd, u32 soff) {
  asm volatile("s_mov_b32 m0, %0\n\ts_nop 0\n\t"
               "buffer_load_dwordx4 %2, %3, %4 offen offset:256 sc1 lds\n\t"
               "buffer_load_dwordx4 %2, %3, %4 offen offset:1280 sc1 lds\n\t"
               "buffer_load_dwordx4 %2, %3, %4 offen offset:2304 sc1 lds\n\t"
               "buffer_load_dwordx4 %2, %3, %4 offen offset:3328 sc1 lds\n\t"
               "buffer_load_dword %1, %3, %4 offen sc1 lds" ::"s"(lds), "v"(l4), "v"(l16), "s"(srd), "s"(soff) : "memory");
}
#define TOUCH(d, voff, srd, soff) asm volatile("buffer_load_dword %0, %1, %2, %3 offen" : "=v"(d) : "v"(voff), "s"(srd), "s"(soff) : "memory")
DI void st128c(u32x4 v, __amdgpu_buffer_rsrc_t srd, u32 off) { __builtin_amdgcn_raw_buffer_store_b128(v, srd, off, 0, AUX_SC1); }
DI void st32c(u32 v, __amdgpu_buffer_rsrc_t srd, u32 off) { __builtin_amdgcn_raw_buffer_store_b32(v, srd, off, 0, AUX_SC1); }
#define BAR() asm volatile("s_waitcnt lgkmcnt(0)\n\ts_barrier" ::: "memory")
#define BARE() asm volatile("s_barrier" ::: "memory")
#define OPQ(x) asm("" : "+v"(x))
#define PIN(x) asm volatile("" : "+v"(x))
#define PINS(x) asm volatile("" : "+s"(x))
#define WAITV(n) asm volatile("s_waitcnt vmcnt(" #n ")" ::: "memory")
#define SCHED() __builtin_amdgcn_sched_barrier(0)
#define SGB(m, n) __builtin_amdgcn_sched_group_barrier(m, n, 0)
DI u32 sum4(u32 c, u32x4 v) { return (c + v.x + v.y) + v.z + v.w; }
DI u32 pair_sum(u32 s) { auto t = __builtin_amdgcn_permlane32_swap(s, s, false, false); return t[0] + t[1]; }

constexpr float LOG2E = 1.44269504088896340736f;
DI float max3f(float a, float b, float c) { return __builtin_fmaxf(__builtin_fmaxf(a, b), c); }
DI float negmax16(const f32x16& x) {
  const float a = max3f(x[0], x[1], x[2]), b = max3f(x[3], x[4], x[5]), c = max3f(x[6], x[7], x[8]);
  const float d = max3f(x[9], x[10], x[11]), e = max3f(x[12], x[13], x[14]);
  return -max3f(max3f(a, b, x[15]), max3f(c, d, e), a) * LOG2E;
}
DI void sm_exp(const f32x16& p, const f32x16& q, const float (&mc)[2], f32x2 (&e)[2][8]) {
#pragma unroll
  for (int i = 0; i < 8; ++i) {
    const f32x2 t = __builtin_elementwise_fma((f32x2){p[2 * i], p[2 * i + 1]}, (f32x2){LOG2E, LOG2E}, (f32x2){mc[0], mc[0]});
    const f32x2 u = __builtin_elementwise_fma((f32x2){q[2 * i], q[2 * i + 1]}, (f32x2){LOG2E, LOG2E}, (f32x2){mc[1], mc[1]});
    e[0][i] = (f32x2){__builtin_amdgcn_exp2f(t.x), __builtin_amdgcn_exp2f(t.y)};
    e[1][i] = (f32x2){__builtin_amdgcn_exp2f(u.x), __builtin_amdgcn_exp2f(u.y)};
  }
  SGB(0x2, 4); SGB(0x400, 8); SGB(0x2, 4); SGB(0x400, 8); SGB(0x2, 6); SGB(0x400, 8); SGB(0x2, 8); SGB(0x400, 8);
}
DI void sm_exp_s(const f32x16& p, const f32x16& q, const float (&mc)[2], f32x2 (&e)[2][8]) {
#pragma unroll
  for (int i = 0; i < 16; ++i) {
    e[0][i >> 1][i & 1] = __builtin_amdgcn_exp2f(__builtin_fmaf(p[i], LOG2E, mc[0]));
    e[1][i >> 1][i & 1] = __builtin_amdgcn_exp2f(__builtin_fmaf(q[i], LOG2E, mc[1]));
  }
}
DI void sm_finish(const f32x2 (&e)[2][8], u32x4 (&o)[2][2]) {
  float r[2];
#pragma unroll
  for (int u = 0; u < 2; ++u) {
    const f32x2 s = ((e[u][0] + e[u][1]) + (e[u][2] + e[u][3])) + ((e[u][4] + e[u][5]) + (e[u][6] + e[u][7]));
    r[u] = __builtin_amdgcn_rcpf(s.x + s.y);
  }
#pragma unroll
  for (int u = 0; u < 2; ++u) {
    u32 w[8];
#pragma unroll
    for (int i = 0; i < 8; ++i) { const f32x2 t = e[u][i] * (f32x2){r[u], r[u]}; w[i] = pk2(t.x, t.y); }
    o[u][0] = (u32x4){w[0], w[1], w[2], w[3]};
    o[u][1] = (u32x4){w[4], w[5], w[6], w[7]};
  }
}
DI void softmax16x2(const f32x16& p, const f32x16& q, const float (&mc)[2], u32x4 (&o)[2][2]) {
  f32x2 e[2][8];
  sm_exp(p, q, mc, e);
  sm_finish(e, o);
}

#define STAMP(i) do { if ((i) == PROBE_N) { SCHED(); asm volatile("s_memrealtime %0\n\ts_waitcnt lgkmcnt(0)" : "=s"(pt1) :: "memory"); SCHED(); } } while (0)
#define STAMP_OUT() do { if (PROBE_N && (wave >> 2) == PROBE_ROLE) { unsigned long long d_ = pt1 - pt0; if (d_ > 100000) d_ = 100000; unsigned long long e_ = pt0 + 3000 + 8 * d_; \
  for (int it_ = 0; it_ < 1000000; ++it_) { unsigned long long t_; asm volatile("s_memrealtime %0\n\ts_waitcnt lgkmcnt(0)" : "=s"(t_) :: "memory"); if (t_ >= e_) break; } } } while (0)

__global__ __launch_bounds__(512, 2) void hedgehog(const u16* xA, const u16* wA, const u16* pA, const u16* oA,
                                                   const u16* xB, const u16* wB, const u16* pB, const u16* oB,
                                                   u16* out_, u32* ws_) {
  __shared__ __attribute__((aligned(16))) char smem[163840];
  const u32 lds0 = (u32)(size_t)(LAS char*)smem;
  unsigned long long pt0 = __builtin_amdgcn_s_memrealtime(), pt1 = pt0;
  const int bid = blockIdx.x, xcd = bid & 7, idx = bid >> 3;
  const int br = xcd >> 2;
#define KLOAD(tail)                                                                                       \
  u32x8 ka;                                                                                               \
  u32x4 kb;                                                                                               \
  asm volatile("s_load_dwordx8 %0, %2, %3\n\ts_load_dwordx4 %1, %2, 0x40" tail : "=&s"(ka), "=&s"(kb)     \
               : "s"(__builtin_amdgcn_kernarg_segment_ptr()), "s"(br * 32))
#define KWAIT() asm volatile("s_waitcnt lgkmcnt(0)" : "+s"(ka), "+s"(kb))
#define KPTR(v, i) ((void*)(((unsigned long long)(v)[(i) + 1] << 32) | (v)[i]))
  unsigned long long pcs;
  asm volatile("s_getpc_b64 %0" : "=s"(pcs));
  pcs &= ~255ull;
  const int lane = threadIdx.x & 63;
  const int wave = __builtin_amdgcn_readfirstlane(threadIdx.x >> 6);
  const int panel = (xcd & 3) * 4 + (idx & 3), grp = idx >> 2, gq = grp ^ 4, cg = (grp + 2) & 7;
  auto ochunk = [&](u32 j) -> u32 {
    if (OORD == 0) return (j + gq) & 7;
    const int v = (int)j + (((int)j - 1 - gq) >> 31), z = ((int)j - 1) >> 31;
    return (u32)__builtin_amdgcn_readfirstlane((v & ~z) | (gq & z));
  };
  f32x16 zero;
#pragma unroll
  for (int i = 0; i < 16; ++i) zero[i] = 0.f;
#define LANE_IDS()                                                                            \
  u32 lane_ = lane;                                                                           \
  PIN(lane_);                                                                                 \
  const u32 r = lane_ & 31, h = lane_ >> 5;                                                   \
  const u32 blk = (lane_ >> 4) & 1, q = (lane_ & 15) >> 2, p = lane_ & 3;                     \
  u32* ws = (u32*)KPTR(kb, 2);                                                                \
  const __amdgpu_buffer_rsrc_t wsr = __builtin_amdgcn_make_buffer_rsrc(ws, 0, WS_END, SRD3);  \
  const __amdgpu_buffer_rsrc_t wor = __builtin_amdgcn_make_buffer_rsrc(KPTR(ka, 6), 0, 512 * 512 * 2, SRD3)

  if (wave >= 4) {
    const int cw = wave - 4;
    const int rh = cw >> 1, hp = cw & 1;
    KLOAD("\n\ts_waitcnt lgkmcnt(0)");
    u32 sp0, sp1, sp2, sp3;
    {
      u32 off = idx * 1024 + cw * 256;
      off = off < CODE_PF ? off : 0;
      asm volatile("s_load_dword %0, %4, %5 offset:0\n\ts_load_dword %1, %4, %5 offset:64\n\ts_load_dword %2, %4, %5 offset:128\n\ts_load_dword %3, %4, %5 offset:192"
                   : "=&s"(sp0), "=&s"(sp1), "=&s"(sp2), "=&s"(sp3) : "s"(pcs), "s"(off));
    }
    LANE_IDS();
    u32 key = *(const CAS u32*)((const char*)ws + WS_EPOCH + br * 256) + 1u;
    u32 xa[2], xb[2], wa[2], wb2[2];
    {
      const u32 r16 = lane_ & 15, g = lane_ >> 4;
#pragma unroll
      for (int s = 0; s < 2; ++s) {
        xa[s] = lds0 + (64 * rh + r16) * 128 + 16 * ((4 * s + g) ^ (r16 >> 1));
        wa[s] = lds0 + 16384 + (8 * g + q) * 128 + 16 * ((4 * hp + 2 * s + (p >> 1)) ^ ((((q >> 1) & 1) << 2) | (blk << 1))) + 8 * (p & 1);
        xb[s] = xa[s] + 2 * SLOT; wb2[s] = wa[s] + 2 * SLOT;
        PIN(xa[s]); PIN(xb[s]); PIN(wa[s]); PIN(wb2[s]);
      }
    }
    u32 fpa = lds0 + L_WPHI + hp * 1024 + (8 * h + q) * 32 + (8 * blk + 4 * (p >> 1)) * 2;
    PIN(fpa);
    PINS(key);
    f32x4 ak[4][2], av[4][2];
#pragma unroll
    for (int i = 0; i < 8; ++i) ak[i >> 1][i & 1] = av[i >> 1][i & 1] = (f32x4){0.f, 0.f, 0.f, 0.f};
    u32 vra, pra, vwa, pw0, pw1, pub16, pub4;
    u32x4 xf[2][4], wk[2][2], wv[2][2];
    auto ldx = [&](int c, int s) {
      const u32 x = c % 3 == 2 ? xb[s] : xa[s] + (c % 3) * SLOT;
#pragma unroll
      for (int i = 0; i < 4; ++i) xf[s][i] = lds_r128(x + i * 2048);
    };
    auto ldw = [&](int c, int s, int m) {
#pragma unroll
      for (int n = 0; n < 2; ++n) {
        const u32 w = (c % 3 == 2 ? wb2[n] : wa[n] + (c % 3) * SLOT) + s * 4096 + m * 8192;
        (m ? wv : wk)[s][n] = lds_tr2(w, w + 512);
      }
    };
    auto mk = [&](int s) {
#pragma unroll
      for (int i = 0; i < 8; ++i) ak[i >> 1][i & 1] = mfma16(wk[s][i & 1], xf[s][i >> 1], ak[i >> 1][i & 1]);
    };
    auto mv = [&](int s) {
#pragma unroll
      for (int i = 0; i < 8; ++i) av[i >> 1][i & 1] = mfma16(xf[s][i >> 1], wv[s][i & 1], av[i >> 1][i & 1]);
    };
    auto mix = [&](int n) {
#pragma unroll
      for (int i = 0; i < 8; ++i) {
        SGB(0x8, 1);
        if (n == 12) { if (i < 4) SGB(0x100, 2); else SGB(0x100, 1); }
        else if (n == 8) SGB(0x100, 1);
        else if (n == 4) { if (i < 4) SGB(0x100, 1); }
        else if (n < 0) SGB(0x6, 2);
      }
      SCHED();
    };
    BAR();
    asm volatile("" ::"s"(sp0), "s"(sp1), "s"(sp2), "s"(sp3));
    STAMP(1);
    ldw(0, 0, 0); ldx(0, 0); ldw(0, 0, 1);
    u32x4 fa[2];
#pragma unroll
    for (int hh = 0; hh < 2; ++hh) fa[hh] = lds_tr2(fpa + hh * 512, fpa + hh * 512 + 128);
    SCHED();
#pragma unroll
    for (int c = 0; c < 7; ++c) {
      ldw(c, 1, 0); ldx(c, 1); mk(0); mix(8);
      ldw(c, 1, 1); mv(0); mix(4);
      u32 ln = lane;
      PIN(ln);
      const u32 r_ = ln & 31, h_ = ln >> 5, q_ = (ln & 15) >> 2;
      if (c == 1) {
        const u32 sg = (ln & 4) ? 0x80008000u : 0u;
#pragma unroll
        for (int hh = 0; hh < 2; ++hh) { OPQ(fa[hh]); fa[hh] ^= (u32x4){sg, sg, sg, sg}; }
      }
      if (c == 2) vra = lds0 + L_V + (cw >> 1) * (2 * VF) + (r_ < 16 ? (ln + 16 * (cw & 1)) * 16 : 1024);
      if (c == 3) pra = lds0 + L_PHI + cw * 8192 + (4 * h_ + q_) * 64 + 16 * ((((ln >> 3) & 2) + ((ln >> 1) & 1)) ^ ((2 * h_ + (q_ >> 1)) & 3)) + 8 * (ln & 1);
      if (c == 4) {
        const u32 rec = WS_PART + ((br * 32 + grp * 4 + cw) * 16 + panel) * REC;
        pub16 = rec + ln * 16; pub4 = rec + 1024 + ln * 4;
      }
      if (c == 5) { pw0 = lds0 + L_PHI + (2 * hp) * 8192 + (64 * rh + r_) * 64 + 16 * ((2 * h_) ^ ((r_ >> 1) & 3)); pw1 = pw0 ^ 16; }
      if (c == 6) vwa = lds0 + L_V + (8 * rh + 2 * hp) * VF + ((ln & 16) * 2 + (ln & 15)) * 16 + h_ * 8;
      mk(1); mix(c >= 1 ? -1 : 0);
      if (c == 1) { PIN(fa[0]); PIN(fa[1]); }
      if (c == 2) PIN(vra);
      if (c == 3) PIN(pra);
      if (c == 4) { PIN(pub16); PIN(pub4); }
      if (c == 5) { PIN(pw0); PIN(pw1); }
      if (c == 6) PIN(vwa);
      BAR();
      ldw(c + 1, 0, 0); ldx(c + 1, 0); ldw(c + 1, 0, 1);
      mv(1); mix(12);
    }
    ldw(7, 1, 0); ldx(7, 1); ldw(7, 1, 1); mk(0); mix(12);
    mk(1); mix(0);
    __builtin_amdgcn_s_setprio(2);
    STAMP(2);
    f32x16 pk[2][2];
    {
      u32x4 kf[2][2];
      auto kpack = [&](int i) {
#pragma unroll
        for (int hh = 0; hh < 2; ++hh) {
          const f32x4 &t0 = ak[2 * i][hh], &t1 = ak[2 * i + 1][hh];
          const auto lo = __builtin_amdgcn_permlane16_swap(pk2(t0[0], t0[1]), pk2(t1[0], t1[1]), false, false);
          const auto hi = __builtin_amdgcn_permlane16_swap(pk2(t0[2], t0[3]), pk2(t1[2], t1[3]), false, false);
          kf[i][hh] = (u32x4){lo[0], hi[0], lo[1], hi[1]};
        }
      };
      mv(0); kpack(0);
      SGB(0x8, 2);
#pragma unroll
      for (int i = 0; i < 6; ++i) { SGB(0x2, 2); SGB(0x8, 1); }
      SCHED();
      pk[0][0] = mfma(fa[0], kf[0][0], zero);
      pk[0][1] = mfma(fa[1], kf[0][1], zero);
      kpack(1);
      SGB(0x8, 1); SGB(0x2, 6); SGB(0x8, 1); SGB(0x2, 6);
      PIN(pk[0][0]); PIN(pk[0][1]); PIN(kf[1][0]); PIN(kf[1][1]);
      SCHED();
      mv(1);
      pk[1][0] = mfma(fa[0], kf[1][0], zero);
      pk[1][1] = mfma(fa[1], kf[1][1], zero);
    }
    float m0[2], m1[2];
    m0[0] = negmax16(pk[0][0]); m0[1] = negmax16(pk[0][1]);
    SGB(0x8, 2);
#pragma unroll
    for (int i = 0; i < 6; ++i) { SGB(0x2, 2); SGB(0x8, 1); }
    SGB(0x8, 1); SGB(0x2, 6); SGB(0x8, 1);
#pragma unroll
    for (int i = 0; i < 8; ++i) PIN(av[i >> 1][i & 1]);
    PIN(pk[1][0]); PIN(pk[1][1]); PIN(m0[0]); PIN(m0[1]);
    SCHED();
#pragma unroll
    for (int i = 0; i < 8; ++i) {
      const f32x4& t = av[i >> 1][i & 1];
      *(LAS u32x2*)(size_t)(vwa + (4 * (i >> 2) + ((i >> 1) & 1)) * VF + (i & 1) * 256) = (u32x2){pk2(t[0], t[1]), pk2(t[2], t[3])};
    }
    SCHED();
    {
      u32x4 o[2][2];
      softmax16x2(pk[0][0], pk[0][1], m0, o);
      lds_w128(pw0, o[0][0]); lds_w128(pw1, o[0][1]);
      lds_w128(pw0 + 8192, o[1][0]); lds_w128(pw1 + 8192, o[1][1]);
      SCHED();
      m1[0] = negmax16(pk[1][0]); m1[1] = negmax16(pk[1][1]);
      PIN(m1[0]); PIN(m1[1]);
      SCHED();
      softmax16x2(pk[1][0], pk[1][1], m1, o);
      lds_w128(pw0 + 2048, o[0][0]); lds_w128(pw1 + 2048, o[0][1]);
      lds_w128(pw0 + 2048 + 8192, o[1][0]); lds_w128(pw1 + 2048 + 8192, o[1][1]);
      SGB(0x402, 26); SGB(0x200, 1); SGB(0x402, 8); SGB(0x200, 1); SGB(0x402, 8); SGB(0x200, 1); SGB(0x402, 8); SGB(0x200, 1);
      SCHED();
      BARE();
      BAR();
      SCHED();
    }
    STAMP(3);
    f32x16 kv = zero;
    {
      u32x4 a[8], b[8];
#pragma unroll
      for (int u = 0; u < 8; ++u) {
        a[u] = lds_r128(vra + ((u >> 1) * 4 + (u & 1)) * VF);
        b[u] = lds_tr2(pra + 16 * u * 64, pra + (16 * u + 8) * 64);
      }
#pragma unroll
      for (int u = 0; u < 8; ++u) kv = mfma(a[u], b[u], kv);
      SGB(0x100, 12); SGB(0x8, 1); SGB(0x100, 6); SGB(0x8, 1); SGB(0x100, 6); SGB(0x8, 6);
    }
    {
      const u32x4 w = pack8(kv, 0);
      st128c(w, wsr, pub16);
      const u32 zb = __float_as_uint(kv[8]);
      const u32 chk = key - pair_sum(sum4(h ? 0u : zb, w));
      st32c(h ? chk : zb, wsr, pub4);
    }
    SCHED();
    STAMP(4);
    u32 l16, l4, kwa, kza, gso[4], vo[4], wso;
    u32x4 sel;
    {
      u32 ln = lane;
      PIN(ln);
#pragma unroll
      for (int i = 0; i < 4; ++i) vo[i] = (ln >> 3) * 1024 + 16 * ((ln & 7) ^ (((ln >> 4) & 1) << 2)) + i * 7168;
#pragma unroll
      for (int i = 0; i < 2; ++i) dma4(lds0 + L_WO + cw * 8192 + i * 4096, vo, wor, ochunk(cw) * 65536 + i * 32768 + cg * 128);
      wso = ochunk(4 + cw) * 65536 + cg * 128;
      const u32 r_ = ln & 31, h_ = ln >> 5;
      l16 = ln * 16; l4 = ln * 4;
      kwa = lds0 + L_KV + cw * 1536 + r_ * 48 + h_ * 16; kza = lds0 + L_KV + cw * 1536 + r_ * 48 + 32 + h_ * 8;
      const bool hit = r_ < 16 && ((r_ >> 2) & 1) == h_;
      const u32 j = (r_ & 3) + 4 * (r_ >> 3);
#pragma unroll
      for (int e = 0; e < 4; ++e) sel[e] = (hit && (j >> 1) == e) ? ((j & 1) ? 0x3F800000u : 0x00003F80u) : 0u;
#pragma unroll
      for (int i = 0; i < 4; ++i) { gso[i] = WS_PART + ((br * 32 + gq * 4 + cw) * 16 + ((i + idx) & 3) * 4) * REC; PINS(gso[i]); }
      PIN(l16); PIN(l4); PIN(kwa); PIN(kza); PIN(sel); PINS(wso);
    }
    if (DELAY1) __builtin_amdgcn_s_sleep(DELAY1);
    STAMP(10);
    f32x16 gs[4];
    float z[4];
    {
      u32x4 g[16];
      u32 gz[16];
      auto gload = [&](int gi) {
#pragma unroll
        for (int i = 0; i < 4; ++i) {
          g[4 * gi + i] = __builtin_amdgcn_raw_buffer_load_b128(wsr, l16 + i * REC, gso[gi], AUX_SC1);
          gz[4 * gi + i] = __builtin_amdgcn_raw_buffer_load_b32(wsr, l4 + (1024 + i * REC - 1024 * (i == 3)), gso[gi] + 1024 * (i == 3), AUX_SC1);
          SCHED();
        }
      };
      auto gsum = [&](int gi) -> bool {
        u32 c = 0;
#pragma unroll
        for (int i = 0; i < 4; ++i) {
          c = sum4(c + gz[4 * gi + i], g[4 * gi + i]);
          gs[gi] = mfma(sel, g[4 * gi + i], i == 0 ? zero : gs[gi]);
          z[gi] = i == 0 ? __uint_as_float(gz[4 * gi]) : z[gi] + __uint_as_float(gz[4 * gi + i]);
        }
        PIN(z[gi]);
        const bool bad = __builtin_amdgcn_ballot_w64(pair_sum(c) != 4u * key) != 0;
        SCHED();
        return bad;
      };
      gload(0); gload(1); gload(2); gload(3);
      bool b0 = gsum(0), b1 = gsum(1), b2 = gsum(2), b3 = gsum(3);
      STAMP(11);
#pragma unroll 1
      for (u32 spin = 0; (b0 | b1 | b2 | b3) && spin < SPIN_CAP; ++spin) {
        asm volatile("" ::: "memory");
        if (b0) gload(0);
        if (b1) gload(1);
        if (b2) gload(2);
        if (b3) gload(3);
        if (b0) b0 = gsum(0);
        if (b1) b1 = gsum(1);
        if (b2) b2 = gsum(2);
        if (b3) b3 = gsum(3);
      }
    }
    __builtin_amdgcn_s_waitcnt(0x0F70);
    STAMP(5);
    {
      float tt[8];
#pragma unroll
      for (int j = 0; j < 8; ++j) tt[j] = (gs[0][j] + gs[1][j]) + (gs[2][j] + gs[3][j]);
      const float zt = (z[0] + z[1]) + (z[2] + z[3]);
      lds_w128(kwa, (u32x4){pk2(tt[0], tt[1]), pk2(tt[2], tt[3]), pk2(tt[4], tt[5]), pk2(tt[6], tt[7])});
      const u32 zz = pk2(zt, zt);
      *(LAS u32x2*)(size_t)kza = (u32x2){zz, zz};
    }
#pragma unroll
    for (int i = 0; i < 2; ++i) dma4(lds0 + L_WO + 32768 + cw * 8192 + i * 4096, vo, wor, wso + i * 32768);
    BAR();
    STAMP(6);
    {
      u32 oso[8], ob = lds0 + L_O + cw * OREC;
#pragma unroll
      for (int i = 1; i < 8; ++i) { oso[i] = WS_O + ((br * 64 + panel * 4 + cw) * 8 + ochunk(i)) * OREC; PINS(oso[i]); }
      PINS(ob);
      WAITV(0);
      BARE();
      if (DELAY3) __builtin_amdgcn_s_sleep(DELAY3);
      STAMP(8);
      auto oissue = [&](int j) { dma_orec(ob + (j % 5) * (4 * OREC), l4, l16, wsr, oso[j]); };
      oissue(1); oissue(2);
      WAITV(5); BARE(); oissue(3);
      WAITV(5); STAMP(15); BARE(); oissue(4); oissue(5);
      WAITV(10); BARE(); oissue(6);
      WAITV(10); BARE(); oissue(7);
      WAITV(10); BARE();
      WAITV(5); BARE();
      WAITV(0); BARE();
    }
    STAMP(9);
    STAMP_OUT();
  } else {
    KLOAD("");
    const int t = wave;
    const u32 l7 = lane & 7, l3 = lane >> 3, lq = lane >> 4;
    const u32 wsw = 16 * (l7 ^ ((lq & 1) << 2));
    u32 vx[4], vw[4];
#pragma unroll
    for (int i = 0; i < 4; ++i) {
      vx[i] = l3 * 1024 + 16 * (l7 ^ (((i & 1) << 2) | lq)) + i * 7168;
      vw[i] = l3 * 3072 + (wsw ^ ((i & 1) << 5)) + i * (8 * 3072 - 1024);
    }
    auto chunk = [&](u32 c) -> u32 {
      const u32 g = grp + idx;
      return c - 2 < 4 ? 2 + ((c - 2) ^ (g & 3)) : c ^ (g & 1);
    };
    const u32 xs0 = (panel * 128 + t * 32) * 1024, ws0 = (t & 1) * (32 * 3072) + (1 + (t >> 1)) * 1024 + grp * 128;
    const u32 ldst = lds0 + t * 4096;
    u32 e0 = ldst, e1 = ldst + 16384, e2 = xs0 + chunk(0) * 128, e3 = ws0 + chunk(0) * (64 * 3072);
    PINS(e0); PINS(e1); PINS(e2); PINS(e3);
#pragma unroll
    for (int i = 0; i < 4; ++i) { PIN(vx[i]); PIN(vw[i]); }
    KWAIT();
    const __amdgpu_buffer_rsrc_t xr = __builtin_amdgcn_make_buffer_rsrc(KPTR(ka, 0), 0, 2048 * 512 * 2, SRD3);
    const __amdgpu_buffer_rsrc_t wr = __builtin_amdgcn_make_buffer_rsrc(KPTR(ka, 2), 0, 512 * 1536 * 2, SRD3);
    STAMP(10);
    dma4(e0, vx, xr, e2);
    dma4(e1, vw, wr, e3);
    {
      const __amdgpu_buffer_rsrc_t pr = __builtin_amdgcn_make_buffer_rsrc(KPTR(ka, 4), 0, 32 * 256 * 2, SRD3);
      dma1(lds0 + L_WPHI + t * 1024, lane * 16, pr, (t < 2 ? grp : gq) * 2048 + (t & 1) * 1024);
    }
    STAMP(11);
    dma4(ldst + SLOT, vx, xr, xs0 + chunk(1) * 128);
    dma4(ldst + SLOT + 16384, vw, wr, ws0 + chunk(1) * (64 * 3072));
    STAMP(1);
    LANE_IDS();
    u32 key = *(const CAS u32*)((const char*)ws + WS_EPOCH + br * 256) + 1u;
    const u32 sw = (r >> 1) & 7;
    u32 xa[4];
#pragma unroll
    for (int s = 0; s < 4; ++s) { xa[s] = lds0 + (32 * t + r) * 128 + 16 * ((2 * s + h) ^ sw); PIN(xa[s]); }
    u32 fpa = lds0 + L_WPHI + 2048 + (4 * h + q) * 32 + (8 * blk + 4 * (p >> 1)) * 2;
    PIN(fpa);
    PINS(key);
    u32x4 xq[32], fa[4];
    u32 wa, wb, kra, o_off, c_off, woa, wob, vto, sto, tho;
#pragma unroll
    for (int c = 0; c < 8; ++c) {
      u32 d0, d1, s0, s1;
      if (c < 6) {
        d0 = ldst + ((c + 2) % 3) * SLOT; d1 = d0 + 16384;
        s0 = xs0 + chunk(c + 2) * 128; s1 = ws0 + chunk(c + 2) * (64 * 3072);
      } else {
        const u32 k0 = 2 * ((2 * (c - 6) + idx) & 3) + (t >> 1), k1 = (k0 + 2) & 7;
        d0 = lds0 + L_WQ1 + ((chunk(k0) + 4) & 7) * 8192 + (t & 1) * 4096; d1 = lds0 + L_WQ1 + ((chunk(k1) + 4) & 7) * 8192 + (t & 1) * 4096;
        s0 = (k0 * 64 + (t & 1) * 32) * 3072 + gq * 128; s1 = (k1 * 64 + (t & 1) * 32) * 3072 + gq * 128;
      }
      PINS(d0); PINS(d1); PINS(s0); PINS(s1);
      WAITV(8);
      BAR();
      if (c < 6) dma4(d0, vx, xr, s0); else dma4(d0, vw, wr, s0);
#pragma unroll
      for (int s = 0; s < 4; ++s) xq[4 * c + s] = lds_r128(xa[s] + (c % 3) * SLOT);
      dma4(d1, vw, wr, s1);
      if (c == 0) {
#pragma unroll
        for (int hd = 0; hd < 4; ++hd) fa[hd] = lds_tr2(fpa + hd * 512, fpa + hd * 512 + 256);
      }
      if (c == 1) {
        lds_w128(lds0 + L_V + (lane & 15) * VF + 1024, (u32x4){0x3F803F80u, 0x3F803F80u, 0x3F803F80u, 0x3F803F80u});
        const u32 sg = (lane & 4) ? 0x80008000u : 0u;
#pragma unroll
        for (int hd = 0; hd < 4; ++hd) { PIN(fa[hd]); fa[hd] ^= (u32x4){sg, sg, sg, sg}; PIN(fa[hd]); }
        wa = lds0 + L_WQ1 + (8 * h + q) * 128 + 16 * ((2 * blk + (p >> 1)) ^ ((((q >> 1) & 1) << 2) | (h << 1))) + 8 * (p & 1);
        wb = wa ^ 64;
        kra = lds0 + L_KV + (16 * h + q) * 48 + (blk ? 32 : 8 * p);
        o_off = WS_O + ((br * 64 + panel * 4 + t) * 8 + gq) * OREC + 256 + lane * 16;
        c_off = WS_O + ((br * 64 + panel * 4 + t) * 8 + gq) * OREC + lane * 4;
        woa = lds0 + L_WO + (8 * h + q) * 128 + 16 * ((2 * blk + (p & 1)) ^ (((q >> 1) & 1) << 2)) + 8 * (p >> 1);
        wob = woa ^ 64;
        vto = r * 128; sto = (idx * 16 + t * 4) * 1024;
        PIN(wa); PIN(wb); PIN(kra); PIN(o_off); PIN(c_off); PIN(woa); PIN(wob); PIN(vto); PINS(sto);
      }
    }
    TOUCH(tho, vto, wor, sto);
    STAMP(2);
    u32x4 fq[2][2][2];
    {
      f32x16 aq[2], pq[2];
      float mq[2];
      aq[0] = aq[1] = zero;
      u32x4 wf[4];
      auto ldw = [&](int n) {
        const int k = n & 31;
        const u32 o = (n < 32 ? wa : wb) + (k < 16 ? (L_WQ0 - L_WQ1) : 0) + (k & 15) * 2048;
        wf[n & 3] = lds_tr2(o, o + 512);
      };
      WAITV(1);
      BAR();
      SCHED();
      STAMP(12);
      ldw(0); ldw(1); ldw(2);
      SCHED();
      BARE();
      SCHED();
#pragma unroll
      for (int n = 0; n < 34; ++n) {
        ldw(n + 3);
        aq[n >> 5] = mfma(wf[n & 3], xq[n & 31], aq[n >> 5]);
        SGB(0x8, 1); SGB(0x100, 2);
        SCHED();
      }
#pragma unroll
      for (int hh = 0; hh < 2; ++hh) pq[hh] = mfma(fa[hh], pack8(aq[0], hh), zero);
      SCHED();
      STAMP(4);
#pragma unroll
      for (int n = 34; n < 64; ++n) {
        if (n + 3 < 64) ldw(n + 3);
        aq[1] = mfma(wf[n & 3], xq[n & 31], aq[1]);
      }
      mq[0] = negmax16(pq[0]); mq[1] = negmax16(pq[1]);
      f32x2 eq[2][8];
      sm_exp_s(pq[0], pq[1], mq, eq);
      SGB(0x8, 1); SGB(0x100, 2); SGB(0x8, 1); SGB(0x100, 2);
#pragma unroll
      for (int n = 36; n < 41; ++n) { SGB(0x8, 1); SGB(0x100, 2); SGB(0x402, 4); }
#pragma unroll
      for (int n = 41; n < 64; ++n) { SGB(0x8, 1); SGB(0x100, 2); if (n & 1) SGB(0x402, 3); else SGB(0x402, 2); }
#pragma unroll
      for (int i = 0; i < 16; ++i) PIN(eq[i >> 3][i & 7]);
      SCHED();
      STAMP(3);
#pragma unroll
      for (int hh = 0; hh < 2; ++hh) { pq[hh] = mfma(fa[2 + hh], pack8(aq[1], hh), zero); PIN(pq[hh]); }
      SCHED();
      sm_finish(eq, fq[0]);
      SCHED();
      asm volatile("s_waitcnt vmcnt(0)" : "+v"(tho)::"memory");
      mq[0] = negmax16(pq[0]); mq[1] = negmax16(pq[1]);
      PIN(mq[0]); PIN(mq[1]);
      SCHED();
#pragma unroll
      for (int i = 0; i < 4; ++i) PIN(fq[0][i >> 1][i & 1]);
      softmax16x2(pq[0], pq[1], mq, fq[1]);
#pragma unroll
      for (int i = 0; i < 4; ++i) PIN(fq[1][i >> 1][i & 1]);
    }
    STAMP(5);
    BAR();
    STAMP(6);
    u32x4 ob[2][4];
    {
      u32x4 a0[4], a1[4];
#pragma unroll
      for (int hd = 0; hd < 4; ++hd) {
        a0[hd] = lds_tr2(kra + hd * 1536, kra + hd * 1536 + 4 * 48);
        a1[hd] = lds_tr2(kra + hd * 1536 + 8 * 48, kra + hd * 1536 + 12 * 48);
      }
      SCHED();
      f32x16 y[4];
      u32 cs = 0;
      auto num = [&](int hd) {
        y[hd] = mfma(a0[hd], fq[hd >> 1][hd & 1][0], zero);
        y[hd] = mfma(a1[hd], fq[hd >> 1][hd & 1][1], y[hd]);
      };
      auto fin = [&](int hd) {
        const f32x16& yy = y[hd];
        const float rd = __builtin_amdgcn_rcpf(yy[8] + 1e-6f);
        u32 w[4];
#pragma unroll
        for (int e = 0; e < 4; ++e) { float t0 = yy[2 * e] * rd, t1 = yy[2 * e + 1] * rd; if (hd < 2) OPQ(t0); w[e] = pk2(t0, t1); }
        ob[0][hd] = (u32x4){w[0], w[1], w[2], w[3]};
        st128c(ob[0][hd], wsr, o_off + hd * 1024);
        cs = sum4(cs, ob[0][hd]);
      };
      num(0); num(1); PIN(y[0]); PIN(y[1]); SCHED();
      num(2); fin(0); SGB(0x8, 1); SGB(0x402, 6); SGB(0x8, 1); PIN(y[2]); SCHED();
      num(3); fin(1); SGB(0x8, 1); SGB(0x402, 6); SGB(0x8, 1); PIN(y[3]); SCHED();
      fin(2); fin(3);
      st32c(key - cs, wsr, c_off);
    }
    SCHED();
    STAMP(7);
    u32x4 wo[32][2];
    auto ldwo = [&](int n) {
      wo[n][0] = lds_tr2(woa + n * 2048, woa + n * 2048 + 512);
      wo[n][1] = lds_tr2(wob + n * 2048, wob + n * 2048 + 512);
    };
#pragma unroll
    for (int n = 0; n < 16; ++n) ldwo(n);
    f32x16 c0 = zero, c1 = zero;
#pragma unroll
    for (int s = 0; s < 4; ++s) { c0 = mfma(wo[s][0], ob[0][s], c0); c1 = mfma(wo[s][1], ob[0][s], c1); }
#pragma unroll
    for (int k = 4; k < 16; ++k) { PIN(wo[k][0]); PIN(wo[k][1]); }
    PIN(c0); PIN(c1);
    WAITV(0);
    BARE();
    SCHED();
#pragma unroll
    for (int n = 16; n < 24; ++n) ldwo(n);
#pragma unroll
    for (int k = 16; k < 24; ++k) { PIN(wo[k][0]); PIN(wo[k][1]); }
    const __amdgpu_buffer_rsrc_t outr = __builtin_amdgcn_make_buffer_rsrc(KPTR(kb, 0), 0, 2048 * 1024 * 2, SRD3);
    u32 oa = lds0 + L_O + t * OREC + 256 + lane * 16;
    u32 orow = ((panel * 128 + t * 32 + l3) * 1024 + br * 512 + cg * 64 + 8 * l7) * 2;
    PIN(oa); PIN(orow);
    u32 oc = oa - 256 - lane * 12, l4 = lane * 4, l16 = lane * 16, oa4 = oa + 4 * (4 * OREC), oc4 = oc + 4 * (4 * OREC);
    u32 sw1 = lds0 + t * 4096 + r * 128 + 16 * (h ^ (r & 7)), sr1 = lds0 + t * 4096 + l3 * 128 + 16 * (l7 ^ l3);
    u32 sw2 = sw1 ^ 32, sw3 = sw1 ^ 64, sw4 = sw1 ^ 96, or1 = orow + 16384, or2 = orow + 32768, or3 = orow + 49152;
    PIN(oc); PIN(l4); PIN(l16); PIN(sw1); PIN(sr1); PIN(oa4); PIN(oc4); PIN(sw2); PIN(sw3); PIN(sw4); PIN(or1); PIN(or2); PIN(or3);
    STAMP(8);
    u32 nretry = 0;
    u32 chk[2];
    auto oread = [&](int j) {
      const u32 da = j % 5 == 4 ? oa4 : oa + (j % 5) * (4 * OREC), dc = j % 5 == 4 ? oc4 : oc + (j % 5) * (4 * OREC);
#pragma unroll
      for (int s = 0; s < 4; ++s) ob[j & 1][s] = lds_r128(da + s * 1024);
      chk[j & 1] = *(LAS u32*)(size_t)dc;
    };
    auto obad = [&](int j) -> bool {
      const u32 c = sum4(sum4(chk[j & 1], ob[j & 1][0]), ob[j & 1][1]) + sum4(sum4(0u, ob[j & 1][2]), ob[j & 1][3]);
      return __builtin_amdgcn_ballot_w64(c != key) != 0;
    };
#pragma unroll
    for (int j = 1; j < 8; ++j) {
      BARE();
      SCHED();
      oread(j);
      if (j >= 2) {
#pragma unroll
        for (int s = 0; s < 4; ++s) {
          const int k = 4 * (j - 1) + s;
          c0 = mfma(wo[k][0], ob[(j - 1) & 1][s], c0);
          c1 = mfma(wo[k][1], ob[(j - 1) & 1][s], c1);
        }
        if (j >= 3 && j <= 6) { ldwo(24 + 2 * (j - 3)); ldwo(25 + 2 * (j - 3)); }
      }
      const bool bad = obad(j);
      if (j >= 3 && j <= 6) {
        SGB(0x8, 1); SGB(0x100, 3); SGB(0x8, 1); SGB(0x100, 2); SGB(0x8, 1); SGB(0x100, 3); SGB(0x8, 1); SGB(0x100, 3);
        SGB(0x8, 1); SGB(0x2, 4); SGB(0x8, 1); SGB(0x2, 5); SGB(0x8, 1); SGB(0x100, 2); SGB(0x8, 1);
      } else if (j >= 2) {
        SGB(0x8, 1); SGB(0x100, 3); SGB(0x8, 1); SGB(0x100, 2); SGB(0x8, 3); SGB(0x2, 4); SGB(0x8, 1); SGB(0x2, 5); SGB(0x8, 2);
      }
      SCHED();
      if (__builtin_expect(bad, 0)) {
#pragma unroll 1
        for (u32 spin = 0; spin < SPIN_CAP; ++spin) {
          dma_orec(lds0 + L_O + t * OREC + (j % 5) * (4 * OREC), l4, l16, wsr, WS_O + ((br * 64 + panel * 4 + t) * 8 + ochunk(j)) * OREC);
#pragma unroll 1
          for (u32 jj = j + 1; jj <= (j + AHEAD < 7 ? j + AHEAD : 7); ++jj) {
            const u32 sl = (jj >= 5 ? jj - 5 : jj) * (4 * OREC);
            u32 c = *(LAS u32*)(size_t)(oc + sl);
#pragma unroll
            for (int s = 0; s < 4; ++s) c = sum4(c, lds_r128(oa + sl + s * 1024));
            if (__builtin_amdgcn_ballot_w64(c != key) != 0)
              dma_orec(lds0 + L_O + t * OREC + sl, l4, l16, wsr, WS_O + ((br * 64 + panel * 4 + t) * 8 + ochunk(jj)) * OREC);
          }
          WAITV(0);
          oread(j);
          ++nretry;
          if (!obad(j)) break;
        }
      }
    }
#pragma unroll
    for (int s = 0; s < 4; ++s) c0 = mfma(wo[28 + s][0], ob[1][s], c0);
    PIN(c0);
    SCHED();
#pragma unroll
    for (int s = 0; s < 4; ++s) c1 = mfma(wo[28 + s][1], ob[1][s], c1);
    lds_w128(sw1, pack8(c0, 0));
    lds_w128(sw2, pack8(c0, 1));
    SGB(0x8, 2); SGB(0x2, 4); SGB(0x200, 1); SGB(0x8, 1); SGB(0x2, 4); SGB(0x200, 1); SGB(0x8, 1);
    SCHED();
    STAMP(9);
    lds_w128(sw3, pack8(c1, 0));
    lds_w128(sw4, pack8(c1, 1));
    asm volatile("" ::: "memory");
    {
      u32x4 v[4];
#pragma unroll
      for (int i = 0; i < 4; ++i) v[i] = lds_r128(sr1 + i * 1024);
      SCHED();
      st128c(v[0], outr, orow);
      st128c(v[1], outr, or1);
      st128c(v[2], outr, or2);
      st128c(v[3], outr, or3);
    }
    if (t == 0 && panel == 0 && grp == 0 && lane == 0) st32c(key, wsr, WS_EPOCH + br * 256);
    if (PROBE_N == 13) WAITV(0);
    STAMP(13);
    STAMP_OUT();
  }
}
}

extern "C" void kernel_launch(void* const* d_in, const int* in_sizes, int n_in,
                              void* d_out, int out_size, void* d_ws, size_t ws_size,
                              hipStream_t stream) {
  hh::hedgehog<<<256, 512, 0, stream>>>((const u16*)d_in[0], (const u16*)d_in[1], (const u16*)d_in[2], (const u16*)d_in[3],
                                        (const u16*)d_in[4], (const u16*)d_in[5], (const u16*)d_in[6], (const u16*)d_in[7],
                                        (u16*)d_out, (u32*)d_ws);
}
```

```cpp
#define PROBE_N 9
#define PROBE_ROLE 0
#include <hip/hip_runtime.h>
#include <stdint.h>

typedef unsigned short u16;
typedef unsigned u32;
typedef short bf16x8 __attribute__((ext_vector_type(8)));
typedef short s16x4 __attribute__((ext_vector_type(4)));
typedef float f32x16 __attribute__((ext_vector_type(16)));
typedef float f32x4 __attribute__((ext_vector_type(4)));
typedef float f32x2 __attribute__((ext_vector_type(2)));
typedef unsigned u32x8 __attribute__((ext_vector_type(8)));
typedef unsigned u32x4 __attribute__((ext_vector_type(4)));
typedef unsigned u32x2 __attribute__((ext_vector_type(2)));
typedef __bf16 bf16x2 __attribute__((ext_vector_type(2)));
#define DI __device__ __forceinline__
#define LAS __attribute__((address_space(3)))
#define CAS __attribute__((address_space(4)))
#define BC(T, x) __builtin_bit_cast(T, x)

#define DELAY1 3
#define DELAY3 5
#define CODE_PF 27648
#define AHEAD 1
#define GQ 4
#define GC 2
#define OFIRST 0

namespace hh {
constexpr int AUX_SC1 = 16;
constexpr u32 SRD3 = 0x00020000u;
constexpr u32 WS_EPOCH = 0;
constexpr u32 REC = 1280;
constexpr u32 WS_PART = 4096;
constexpr u32 OREC = 4352;
constexpr u32 WS_O = WS_PART + 2 * 32 * 16 * REC;
constexpr u32 WS_END = WS_O + 2 * 64 * 8 * OREC;
constexpr u32 WS_STAMP = 8u << 20;
constexpr u32 SLOT = 32768;
constexpr u32 L_WQ0 = 98304;
constexpr u32 L_WQ1 = 65536;
constexpr u32 L_WPHI = 163840 - 4096;
constexpr u32 L_PHI = 0;
constexpr u32 VF = 1040;
constexpr u32 L_V = 131072;
constexpr u32 L_KV = L_V + 16 * VF;
constexpr u32 L_WO = 0;
constexpr u32 L_O = 65536;
constexpr u32 SPIN_CAP = 1u << 16;

DI u32 pk2(float a, float b) { return BC(u32, __builtin_convertvector((f32x2){a, b}, bf16x2)); }
DI u32x4 pack8(const f32x16& x, int s) {
  return (u32x4){pk2(x[8 * s], x[8 * s + 1]), pk2(x[8 * s + 2], x[8 * s + 3]), pk2(x[8 * s + 4], x[8 * s + 5]), pk2(x[8 * s + 6], x[8 * s + 7])};
}
DI f32x16 mfma(u32x4 a, u32x4 b, f32x16 c) { return __builtin_amdgcn_mfma_f32_32x32x16_bf16(BC(bf16x8, a), BC(bf16x8, b), c, 0, 0, 0); }
DI f32x4 mfma16(u32x4 a, u32x4 b, f32x4 c) { return __builtin_amdgcn_mfma_f32_16x16x32_bf16(BC(bf16x8, a), BC(bf16x8, b), c, 0, 0, 0); }
DI u32x4 lds_r128(u32 a) { return *(LAS u32x4*)(size_t)a; }
DI void lds_w128(u32 a, u32x4 v) { *(LAS u32x4*)(size_t)a = v; }
DI u32x4 lds_tr2(u32 a0, u32 a1) {
  s16x4 lo = __builtin_amdgcn_ds_read_tr16_b64_v4i16((LAS s16x4*)(size_t)a0);
  s16x4 hi = __builtin_amdgcn_ds_read_tr16_b64_v4i16((LAS s16x4*)(size_t)a1);
  return BC(u32x4, __builtin_shufflevector(lo, hi, 0, 1, 2, 3, 4, 5, 6, 7));
}
DI void dma1(u32 lds, u32 voff, __amdgpu_buffer_rsrc_t srd, u32 soff) {
  asm volatile("s_mov_b32 m0, %0\n\ts_nop 0\n\tbuffer_load_dwordx4 %1, %2, %3 offen lds" ::"s"(lds), "v"(voff), "s"(srd), "s"(soff) : "memory");
}
DI void dma4(u32 lds, const u32 (&v)[4], __amdgpu_buffer_rsrc_t srd, u32 soff) {
  asm volatile("s_mov_b32 m0, %0\n\ts_nop 0\n\t"
               "buffer_load_dwordx4 %1, %5, %6 offen lds\n\t"
               "buffer_load_dwordx4 %2, %5, %6 offen offset:1024 lds\n\t"
               "buffer_load_dwordx4 %3, %5, %6 offen offset:2048 lds\n\t"
               "buffer_load_dwordx4 %4, %5, %6 offen offset:3072 lds" ::"s"(lds), "v"(v[0]), "v"(v[1]), "v"(v[2]), "v"(v[3]), "s"(srd), "s"(soff) : "memory");
}
DI void dma_orec(u32 lds, u32 l4, u32 l16, __amdgpu_buffer_rsrc_t srd, u32 soff) {
  asm volatile("s_mov_b32 m0, %0\n\ts_nop 0\n\t"
               "buffer_load_dwordx4 %2, %3, %4 offen offset:256 sc1 lds\n\t"
               "buffer_load_dwordx4 %2, %3, %4 offen offset:1280 sc1 lds\n\t"
               "buffer_load_dwordx4 %2, %3, %4 offen offset:2304 sc1 lds\n\t"
               "buffer_load_dwordx4 %2, %3, %4 offen offset:3328 sc1 lds\n\t"
               "buffer_load_dword %1, %3, %4 offen sc1 lds" ::"s"(lds), "v"(l4), "v"(l16), "s"(srd), "s"(soff) : "memory");
}
#define TOUCH(d, voff, srd, soff) asm volatile("buffer_load_dword %0, %1, %2, %3 offen" : "=v"(d) : "v"(voff), "s"(srd), "s"(soff) : "memory")
DI void st128c(u32x4 v, __amdgpu_buffer_rsrc_t srd, u32 off) { __builtin_amdgcn_raw_buffer_store_b128(v, srd, off, 0, AUX_SC1); }
DI void st32c(u32 v, __amdgpu_buffer_rsrc_t srd, u32 off) { __builtin_amdgcn_raw_buffer_store_b32(v, srd, off, 0, AUX_SC1); }
#define BAR() asm volatile("s_waitcnt lgkmcnt(0)\n\ts_barrier" ::: "memory")
#define BARE() asm volatile("s_barrier" ::: "memory")
#define OPQ(x) asm("" : "+v"(x))
#define PIN(x) asm volatile("" : "+v"(x))
#define PINS(x) asm volatile("" : "+s"(x))
#define WAITV(n) asm volatile("s_waitcnt vmcnt(" #n ")" ::: "memory")
#define SCHED() __builtin_amdgcn_sched_barrier(0)
#define SGB(m, n) __builtin_amdgcn_sched_group_barrier(m, n, 0)
DI u32 sum4(u32 c, u32x4 v) { return (c + v.x + v.y) + v.z + v.w; }
DI u32 pair_sum(u32 s) { auto t = __builtin_amdgcn_permlane32_swap(s, s, false, false); return t[0] + t[1]; }

constexpr float LOG2E = 1.44269504088896340736f;
DI float max3f(float a, float b, float c) { return __builtin_fmaxf(__builtin_fmaxf(a, b), c); }
DI float negmax16(const f32x16& x) {
  const float a = max3f(x[0], x[1], x[2]), b = max3f(x[3], x[4], x[5]), c = max3f(x[6], x[7], x[8]);
  const float d = max3f(x[9], x[10], x[11]), e = max3f(x[12], x[13], x[14]);
  return -max3f(max3f(a, b, x[15]), max3f(c, d, e), a) * LOG2E;
}
DI void sm_exp(const f32x16& p, const f32x16& q, const float (&mc)[2], f32x2 (&e)[2][8]) {
#pragma unroll
  for (int i = 0; i < 8; ++i) {
    const f32x2 t = __builtin_elementwise_fma((f32x2){p[2 * i], p[2 * i + 1]}, (f32x2){LOG2E, LOG2E}, (f32x2){mc[0], mc[0]});
    const f32x2 u = __builtin_elementwise_fma((f32x2){q[2 * i], q[2 * i + 1]}, (f32x2){LOG2E, LOG2E}, (f32x2){mc[1], mc[1]});
    e[0][i] = (f32x2){__builtin_amdgcn_exp2f(t.x), __builtin_amdgcn_exp2f(t.y)};
    e[1][i] = (f32x2){__builtin_amdgcn_exp2f(u.x), __builtin_amdgcn_exp2f(u.y)};
  }
  SGB(0x2, 4); SGB(0x400, 8); SGB(0x2, 4); SGB(0x400, 8); SGB(0x2, 6); SGB(0x400, 8); SGB(0x2, 8); SGB(0x400, 8);
}
DI void sm_exp_s(const f32x16& p, const f32x16& q, const float (&mc)[2], f32x2 (&e)[2][8]) {
#pragma unroll
  for (int i = 0; i < 16; ++i) {
    e[0][i >> 1][i & 1] = __builtin_amdgcn_exp2f(__builtin_fmaf(p[i], LOG2E, mc[0]));
    e[1][i >> 1][i & 1] = __builtin_amdgcn_exp2f(__builtin_fmaf(q[i], LOG2E, mc[1]));
  }
}
DI void sm_finish(const f32x2 (&e)[2][8], u32x4 (&o)[2][2]) {
  float r[2];
#pragma unroll
  for (int u = 0; u < 2; ++u) {
    const f32x2 s = ((e[u][0] + e[u][1]) + (e[u][2] + e[u][3])) + ((e[u][4] + e[u][5]) + (e[u][6] + e[u][7]));
    r[u] = __builtin_amdgcn_rcpf(s.x + s.y);
  }
#pragma unroll
  for (int u = 0; u < 2; ++u) {
    u32 w[8];
#pragma unroll
    for (int i = 0; i < 8; ++i) { const f32x2 t = e[u][i] * (f32x2){r[u], r[u]}; w[i] = pk2(t.x, t.y); }
    o[u][0] = (u32x4){w[0], w[1], w[2], w[3]};
    o[u][1] = (u32x4){w[4], w[5], w[6], w[7]};
  }
}
DI void softmax16x2(const f32x16& p, const f32x16& q, const float (&mc)[2], u32x4 (&o)[2][2]) {
  f32x2 e[2][8];
  sm_exp(p, q, mc, e);
  sm_finish(e, o);
}

#define STAMP(i) do { if ((i) == PROBE_N) { SCHED(); asm volatile("s_memrealtime %0\n\ts_waitcnt lgkmcnt(0)" : "=s"(pt1) :: "memory"); SCHED(); } } while (0)
#define STAMP_OUT() do { if (PROBE_N && (wave >> 2) == PROBE_ROLE) { unsigned long long d_ = pt1 - pt0; if (d_ > 100000) d_ = 100000; unsigned long long e_ = pt0 + 3000 + 8 * d_; \
  for (int it_ = 0; it_ < 1000000; ++it_) { unsigned long long t_; asm volatile("s_memrealtime %0\n\ts_waitcnt lgkmcnt(0)" : "=s"(t_) :: "memory"); if (t_ >= e_) break; } } } while (0)

__global__ __launch_bounds__(512, 2) void hedgehog(const u16* xA, const u16* wA, const u16* pA, const u16* oA,
                                                   const u16* xB, const u16* wB, const u16* pB, const u16* oB,
                                                   u16* out_, u32* ws_) {
  __shared__ __attribute__((aligned(16))) char smem[163840];
  const u32 lds0 = (u32)(size_t)(LAS char*)smem;
  unsigned long long pt0 = __builtin_amdgcn_s_memrealtime(), pt1 = pt0;
  const int bid = blockIdx.x, xcd = bid & 7, idx = bid >> 3;
  const int br = xcd >> 2;
#define KLOAD(tail)                                                                                       \
  u32x8 ka;                                                                                               \
  u32x4 kb;                                                                                               \
  asm volatile("s_load_dwordx8 %0, %2, %3\n\ts_load_dwordx4 %1, %2, 0x40" tail : "=&s"(ka), "=&s"(kb)     \
               : "s"(__builtin_amdgcn_kernarg_segment_ptr()), "s"(br * 32))
#define KWAIT() asm volatile("s_waitcnt lgkmcnt(0)" : "+s"(ka), "+s"(kb))
#define KPTR(v, i) ((void*)(((unsigned long long)(v)[(i) + 1] << 32) | (v)[i]))
  unsigned long long pcs;
  asm volatile("s_getpc_b64 %0" : "=s"(pcs));
  pcs &= ~255ull;
  const int lane = threadIdx.x & 63;
  const int wave = __builtin_amdgcn_readfirstlane(threadIdx.x >> 6);
  const int panel = (xcd & 3) * 4 + (idx & 3), grp = idx >> 2, gq = (grp + GQ) & 7, cg = (grp + GC) & 7;
  auto ochunk = [&](u32 j) -> u32 {
    const int v = ((int)j + (((int)j - 1 - ((gq - OFIRST) & 7)) >> 31) + OFIRST) & 7, z = ((int)j - 1) >> 31;
    return (u32)__builtin_amdgcn_readfirstlane((v & ~z) | (gq & z));
  };
  f32x16 zero;
#pragma unroll
  for (int i = 0; i < 16; ++i) zero[i] = 0.f;
#define LANE_IDS()                                                                            \
  u32 lane_ = lane;                                                                           \
  PIN(lane_);                                                                                 \
  const u32 r = lane_ & 31, h = lane_ >> 5;                                                   \
  const u32 blk = (lane_ >> 4) & 1, q = (lane_ & 15) >> 2, p = lane_ & 3;                     \
  u32* ws = (u32*)KPTR(kb, 2);                                                                \
  const __amdgpu_buffer_rsrc_t wsr = __builtin_amdgcn_make_buffer_rsrc(ws, 0, WS_END, SRD3);  \
  const __amdgpu_buffer_rsrc_t wor = __builtin_amdgcn_make_buffer_rsrc(KPTR(ka, 6), 0, 512 * 512 * 2, SRD3)

  if (wave >= 4) {
    const int cw = wave - 4;
    const int rh = cw >> 1, hp = cw & 1;
    KLOAD("\n\ts_waitcnt lgkmcnt(0)");
    u32 sp0, sp1, sp2, sp3;
    {
      u32 off = idx * 1024 + cw * 256;
      off = off < CODE_PF ? off : 0;
      asm volatile("s_load_dword %0, %4, %5 offset:0\n\ts_load_dword %1, %4, %5 offset:64\n\ts_load_dword %2, %4, %5 offset:128\n\ts_load_dword %3, %4, %5 offset:192"
                   : "=&s"(sp0), "=&s"(sp1), "=&s"(sp2), "=&s"(sp3) : "s"(pcs), "s"(off));
    }
    LANE_IDS();
    u32 key = *(const CAS u32*)((const char*)ws + WS_EPOCH + br * 256) + 1u;
    u32 xa[2], xb[2], wa[2], wb2[2];
    {
      const u32 r16 = lane_ & 15, g = lane_ >> 4;
#pragma unroll
      for (int s = 0; s < 2; ++s) {
        xa[s] = lds0 + (64 * rh + r16) * 128 + 16 * ((4 * s + g) ^ (r16 >> 1));
        wa[s] = lds0 + 16384 + (8 * g + q) * 128 + 16 * ((4 * hp + 2 * s + (p >> 1)) ^ ((((q >> 1) & 1) << 2) | (blk << 1))) + 8 * (p & 1);
        xb[s] = xa[s] + 2 * SLOT; wb2[s] = wa[s] + 2 * SLOT;
        PIN(xa[s]); PIN(xb[s]); PIN(wa[s]); PIN(wb2[s]);
      }
    }
    u32 fpa = lds0 + L_WPHI + hp * 1024 + (8 * h + q) * 32 + (8 * blk + 4 * (p >> 1)) * 2;
    PIN(fpa);
    PINS(key);
    f32x4 ak[4][2], av[4][2];
#pragma unroll
    for (int i = 0; i < 8; ++i) ak[i >> 1][i & 1] = av[i >> 1][i & 1] = (f32x4){0.f, 0.f, 0.f, 0.f};
    u32 vra, pra, vwa, pw0, pw1, pub16, pub4;
    u32x4 xf[2][4], wk[2][2], wv[2][2];
    auto ldx = [&](int c, int s) {
      const u32 x = c % 3 == 2 ? xb[s] : xa[s] + (c % 3) * SLOT;
#pragma unroll
      for (int i = 0; i < 4; ++i) xf[s][i] = lds_r128(x + i * 2048);
    };
    auto ldw = [&](int c, int s, int m) {
#pragma unroll
      for (int n = 0; n < 2; ++n) {
        const u32 w = (c % 3 == 2 ? wb2[n] : wa[n] + (c % 3) * SLOT) + s * 4096 + m * 8192;
        (m ? wv : wk)[s][n] = lds_tr2(w, w + 512);
      }
    };
    auto mk = [&](int s) {
#pragma unroll
      for (int i = 0; i < 8; ++i) ak[i >> 1][i & 1] = mfma16(wk[s][i & 1], xf[s][i >> 1], ak[i >> 1][i & 1]);
    };
    auto mv = [&](int s) {
#pragma unroll
      for (int i = 0; i < 8; ++i) av[i >> 1][i & 1] = mfma16(xf[s][i >> 1], wv[s][i & 1], av[i >> 1][i & 1]);
    };
    auto mix = [&](int n) {
#pragma unroll
      for (int i = 0; i < 8; ++i) {
        SGB(0x8, 1);
        if (n == 12) { if (i < 4) SGB(0x100, 2); else SGB(0x100, 1); }
        else if (n == 8) SGB(0x100, 1);
        else if (n == 4) { if (i < 4) SGB(0x100, 1); }
        else if (n < 0) SGB(0x6, 2);
      }
      SCHED();
    };
    BAR();
    asm volatile("" ::"s"(sp0), "s"(sp1), "s"(sp2), "s"(sp3));
    STAMP(1);
    ldw(0, 0, 0); ldx(0, 0); ldw(0, 0, 1);
    u32x4 fa[2];
#pragma unroll
    for (int hh = 0; hh < 2; ++hh) fa[hh] = lds_tr2(fpa + hh * 512, fpa + hh * 512 + 128);
    SCHED();
#pragma unroll
    for (int c = 0; c < 7; ++c) {
      ldw(c, 1, 0); ldx(c, 1); mk(0); mix(8);
      ldw(c, 1, 1); mv(0); mix(4);
      u32 ln = lane;
      PIN(ln);
      const u32 r_ = ln & 31, h_ = ln >> 5, q_ = (ln & 15) >> 2;
      if (c == 1) {
        const u32 sg = (ln & 4) ? 0x80008000u : 0u;
#pragma unroll
        for (int hh = 0; hh < 2; ++hh) { OPQ(fa[hh]); fa[hh] ^= (u32x4){sg, sg, sg, sg}; }
      }
      if (c == 2) vra = lds0 + L_V + (cw >> 1) * (2 * VF) + (r_ < 16 ? (ln + 16 * (cw & 1)) * 16 : 1024);
      if (c == 3) pra = lds0 + L_PHI + cw * 8192 + (4 * h_ + q_) * 64 + 16 * ((((ln >> 3) & 2) + ((ln >> 1) & 1)) ^ ((2 * h_ + (q_ >> 1)) & 3)) + 8 * (ln & 1);
      if (c == 4) {
        const u32 rec = WS_PART + ((br * 32 + grp * 4 + cw) * 16 + panel) * REC;
        pub16 = rec + ln * 16; pub4 = rec + 1024 + ln * 4;
      }
      if (c == 5) { pw0 = lds0 + L_PHI + (2 * hp) * 8192 + (64 * rh + r_) * 64 + 16 * ((2 * h_) ^ ((r_ >> 1) & 3)); pw1 = pw0 ^ 16; }
      if (c == 6) vwa = lds0 + L_V + (8 * rh + 2 * hp) * VF + ((ln & 16) * 2 + (ln & 15)) * 16 + h_ * 8;
      mk(1); mix(c >= 1 ? -1 : 0);
      if (c == 1) { PIN(fa[0]); PIN(fa[1]); }
      if (c == 2) PIN(vra);
      if (c == 3) PIN(pra);
      if (c == 4) { PIN(pub16); PIN(pub4); }
      if (c == 5) { PIN(pw0); PIN(pw1); }
      if (c == 6) PIN(vwa);
      BAR();
      ldw(c + 1, 0, 0); ldx(c + 1, 0); ldw(c + 1, 0, 1);
      mv(1); mix(12);
    }
    ldw(7, 1, 0); ldx(7, 1); ldw(7, 1, 1); mk(0); mix(12);
    mk(1); mix(0);
    __builtin_amdgcn_s_setprio(2);
    STAMP(2);
    f32x16 pk[2][2];
    {
      u32x4 kf[2][2];
      auto kpack = [&](int i) {
#pragma unroll
        for (int hh = 0; hh < 2; ++hh) {
          const f32x4 &t0 = ak[2 * i][hh], &t1 = ak[2 * i + 1][hh];
          const auto lo = __builtin_amdgcn_permlane16_swap(pk2(t0[0], t0[1]), pk2(t1[0], t1[1]), false, false);
          const auto hi = __builtin_amdgcn_permlane16_swap(pk2(t0[2], t0[3]), pk2(t1[2], t1[3]), false, false);
          kf[i][hh] = (u32x4){lo[0], hi[0], lo[1], hi[1]};
        }
      };
      mv(0); kpack(0);
      SGB(0x8, 2);
#pragma unroll
      for (int i = 0; i < 6; ++i) { SGB(0x2, 2); SGB(0x8, 1); }
      SCHED();
      pk[0][0] = mfma(fa[0], kf[0][0], zero);
      pk[0][1] = mfma(fa[1], kf[0][1], zero);
      kpack(1);
      SGB(0x8, 1); SGB(0x2, 6); SGB(0x8, 1); SGB(0x2, 6);
      PIN(pk[0][0]); PIN(pk[0][1]); PIN(kf[1][0]); PIN(kf[1][1]);
      SCHED();
      mv(1);
      pk[1][0] = mfma(fa[0], kf[1][0], zero);
      pk[1][1] = mfma(fa[1], kf[1][1], zero);
    }
    float m0[2], m1[2];
    m0[0] = negmax16(pk[0][0]); m0[1] = negmax16(pk[0][1]);
    SGB(0x8, 2);
#pragma unroll
    for (int i = 0; i < 6; ++i) { SGB(0x2, 2); SGB(0x8, 1); }
    SGB(0x8, 1); SGB(0x2, 6); SGB(0x8, 1);
#pragma unroll
    for (int i = 0; i < 8; ++i) PIN(av[i >> 1][i & 1]);
    PIN(pk[1][0]); PIN(pk[1][1]); PIN(m0[0]); PIN(m0[1]);
    SCHED();
#pragma unroll
    for (int i = 0; i < 8; ++i) {
      const f32x4& t = av[i >> 1][i & 1];
      *(LAS u32x2*)(size_t)(vwa + (4 * (i >> 2) + ((i >> 1) & 1)) * VF + (i & 1) * 256) = (u32x2){pk2(t[0], t[1]), pk2(t[2], t[3])};
    }
    SCHED();
    {
      u32x4 o[2][2];
      softmax16x2(pk[0][0], pk[0][1], m0, o);
      lds_w128(pw0, o[0][0]); lds_w128(pw1, o[0][1]);
      lds_w128(pw0 + 8192, o[1][0]); lds_w128(pw1 + 8192, o[1][1]);
      SCHED();
      m1[0] = negmax16(pk[1][0]); m1[1] = negmax16(pk[1][1]);
      PIN(m1[0]); PIN(m1[1]);
      SCHED();
      softmax16x2(pk[1][0], pk[1][1], m1, o);
      lds_w128(pw0 + 2048, o[0][0]); lds_w128(pw1 + 2048, o[0][1]);
      lds_w128(pw0 + 2048 + 8192, o[1][0]); lds_w128(pw1 + 2048 + 8192, o[1][1]);
      SGB(0x402, 26); SGB(0x200, 1); SGB(0x402, 8); SGB(0x200, 1); SGB(0x402, 8); SGB(0x200, 1); SGB(0x402, 8); SGB(0x200, 1);
      SCHED();
      BARE();
      BAR();
      SCHED();
    }
    STAMP(3);
    f32x16 kv = zero;
    {
      u32x4 a[8], b[8];
#pragma unroll
      for (int u = 0; u < 8; ++u) {
        a[u] = lds_r128(vra + ((u >> 1) * 4 + (u & 1)) * VF);
        b[u] = lds_tr2(pra + 16 * u * 64, pra + (16 * u + 8) * 64);
      }
#pragma unroll
      for (int u = 0; u < 8; ++u) kv = mfma(a[u], b[u], kv);
      SGB(0x100, 12); SGB(0x8, 1); SGB(0x100, 6); SGB(0x8, 1); SGB(0x100, 6); SGB(0x8, 6);
    }
    {
      const u32x4 w = pack8(kv, 0);
      st128c(w, wsr, pub16);
      const u32 zb = __float_as_uint(kv[8]);
      const u32 chk = key - pair_sum(sum4(h ? 0u : zb, w));
      st32c(h ? chk : zb, wsr, pub4);
    }
    SCHED();
    STAMP(4);
    u32 l16, l4, kwa, kza, gso[4], vo[4], wso;
    u32x4 sel;
    {
      u32 ln = lane;
      PIN(ln);
#pragma unroll
      for (int i = 0; i < 4; ++i) vo[i] = (ln >> 3) * 1024 + 16 * ((ln & 7) ^ (((ln >> 4) & 1) << 2)) + i * 7168;
#pragma unroll
      for (int i = 0; i < 2; ++i) dma4(lds0 + L_WO + cw * 8192 + i * 4096, vo, wor, ochunk(cw) * 65536 + i * 32768 + cg * 128);
      wso = ochunk(4 + cw) * 65536 + cg * 128;
      const u32 r_ = ln & 31, h_ = ln >> 5;
      l16 = ln * 16; l4 = ln * 4;
      kwa = lds0 + L_KV + cw * 1536 + r_ * 48 + h_ * 16; kza = lds0 + L_KV + cw * 1536 + r_ * 48 + 32 + h_ * 8;
      const bool hit = r_ < 16 && ((r_ >> 2) & 1) == h_;
      const u32 j = (r_ & 3) + 4 * (r_ >> 3);
#pragma unroll
      for (int e = 0; e < 4; ++e) sel[e] = (hit && (j >> 1) == e) ? ((j & 1) ? 0x3F800000u : 0x00003F80u) : 0u;
#pragma unroll
      for (int i = 0; i < 4; ++i) { gso[i] = WS_PART + ((br * 32 + gq * 4 + cw) * 16 + ((i + idx) & 3) * 4) * REC; PINS(gso[i]); }
      PIN(l16); PIN(l4); PIN(kwa); PIN(kza); PIN(sel); PINS(wso);
    }
    if (DELAY1) __builtin_amdgcn_s_sleep(DELAY1);
    STAMP(10);
    f32x16 gs[4];
    float z[4];
    {
      u32x4 g[16];
      u32 gz[16];
      auto gload = [&](int gi) {
#pragma unroll
        for (int i = 0; i < 4; ++i) {
          g[4 * gi + i] = __builtin_amdgcn_raw_buffer_load_b128(wsr, l16 + i * REC, gso[gi], AUX_SC1);
          gz[4 * gi + i] = __builtin_amdgcn_raw_buffer_load_b32(wsr, l4 + (1024 + i * REC - 1024 * (i == 3)), gso[gi] + 1024 * (i == 3), AUX_SC1);
          SCHED();
        }
      };
      auto gsum = [&](int gi) -> bool {
        u32 c = 0;
#pragma unroll
        for (int i = 0; i < 4; ++i) {
          c = sum4(c + gz[4 * gi + i], g[4 * gi + i]);
          gs[gi] = mfma(sel, g[4 * gi + i], i == 0 ? zero : gs[gi]);
          z[gi] = i == 0 ? __uint_as_float(gz[4 * gi]) : z[gi] + __uint_as_float(gz[4 * gi + i]);
        }
        PIN(z[gi]);
        const bool bad = __builtin_amdgcn_ballot_w64(pair_sum(c) != 4u * key) != 0;
        SCHED();
        return bad;
      };
      gload(0); gload(1); gload(2); gload(3);
      bool b0 = gsum(0), b1 = gsum(1), b2 = gsum(2), b3 = gsum(3);
      STAMP(11);
#pragma unroll 1
      for (u32 spin = 0; (b0 | b1 | b2 | b3) && spin < SPIN_CAP; ++spin) {
        asm volatile("" ::: "memory");
        if (b0) gload(0);
        if (b1) gload(1);
        if (b2) gload(2);
        if (b3) gload(3);
        if (b0) b0 = gsum(0);
        if (b1) b1 = gsum(1);
        if (b2) b2 = gsum(2);
        if (b3) b3 = gsum(3);
      }
    }
    __builtin_amdgcn_s_waitcnt(0x0F70);
    STAMP(5);
    {
      float tt[8];
#pragma unroll
      for (int j = 0; j < 8; ++j) tt[j] = (gs[0][j] + gs[1][j]) + (gs[2][j] + gs[3][j]);
      const float zt = (z[0] + z[1]) + (z[2] + z[3]);
      lds_w128(kwa, (u32x4){pk2(tt[0], tt[1]), pk2(tt[2], tt[3]), pk2(tt[4], tt[5]), pk2(tt[6], tt[7])});
      const u32 zz = pk2(zt, zt);
      *(LAS u32x2*)(size_t)kza = (u32x2){zz, zz};
    }
#pragma unroll
    for (int i = 0; i < 2; ++i) dma4(lds0 + L_WO + 32768 + cw * 8192 + i * 4096, vo, wor, wso + i * 32768);
    BAR();
    STAMP(6);
    {
      u32 oso[8], ob = lds0 + L_O + cw * OREC;
#pragma unroll
      for (int i = 1; i < 8; ++i) { oso[i] = WS_O + ((br * 64 + panel * 4 + cw) * 8 + ochunk(i)) * OREC; PINS(oso[i]); }
      PINS(ob);
      WAITV(0);
      BARE();
      if (DELAY3) __builtin_amdgcn_s_sleep(DELAY3);
      STAMP(8);
      auto oissue = [&](int j) { dma_orec(ob + (j % 5) * (4 * OREC), l4, l16, wsr, oso[j]); };
      oissue(1); oissue(2);
      WAITV(5); BARE(); oissue(3);
      WAITV(5); STAMP(15); BARE(); oissue(4); oissue(5);
      WAITV(10); BARE(); oissue(6);
      WAITV(10); BARE(); oissue(7);
      WAITV(10); BARE();
      WAITV(5); BARE();
      WAITV(0); BARE();
    }
    STAMP(9);
    STAMP_OUT();
  } else {
    KLOAD("");
    const int t = wave;
    const u32 l7 = lane & 7, l3 = lane >> 3, lq = lane >> 4;
    const u32 wsw = 16 * (l7 ^ ((lq & 1) << 2));
    u32 vx[4], vw[4];
#pragma unroll
    for (int i = 0; i < 4; ++i) {
      vx[i] = l3 * 1024 + 16 * (l7 ^ (((i & 1) << 2) | lq)) + i * 7168;
      vw[i] = l3 * 3072 + (wsw ^ ((i & 1) << 5)) + i * (8 * 3072 - 1024);
    }
    auto chunk = [&](u32 c) -> u32 {
      const u32 g = grp + idx;
      return c - 2 < 4 ? 2 + ((c - 2) ^ (g & 3)) : c ^ (g & 1);
    };
    const u32 xs0 = (panel * 128 + t * 32) * 1024, ws0 = (t & 1) * (32 * 3072) + (1 + (t >> 1)) * 1024 + grp * 128;
    const u32 ldst = lds0 + t * 4096;
    u32 e0 = ldst, e1 = ldst + 16384, e2 = xs0 + chunk(0) * 128, e3 = ws0 + chunk(0) * (64 * 3072);
    PINS(e0); PINS(e1); PINS(e2); PINS(e3);
#pragma unroll
    for (int i = 0; i < 4; ++i) { PIN(vx[i]); PIN(vw[i]); }
    KWAIT();
    const __amdgpu_buffer_rsrc_t xr = __builtin_amdgcn_make_buffer_rsrc(KPTR(ka, 0), 0, 2048 * 512 * 2, SRD3);
    const __amdgpu_buffer_rsrc_t wr = __builtin_amdgcn_make_buffer_rsrc(KPTR(ka, 2), 0, 512 * 1536 * 2, SRD3);
    STAMP(10);
    dma4(e0, vx, xr, e2);
    dma4(e1, vw, wr, e3);
    {
      const __amdgpu_buffer_rsrc_t pr = __builtin_amdgcn_make_buffer_rsrc(KPTR(ka, 4), 0, 32 * 256 * 2, SRD3);
      dma1(lds0 + L_WPHI + t * 1024, lane * 16, pr, (t < 2 ? grp : gq) * 2048 + (t & 1) * 1024);
    }
    STAMP(11);
    dma4(ldst + SLOT, vx, xr, xs0 + chunk(1) * 128);
    dma4(ldst + SLOT + 16384, vw, wr, ws0 + chunk(1) * (64 * 3072));
    STAMP(1);
    LANE_IDS();
    u32 key = *(const CAS u32*)((const char*)ws + WS_EPOCH + br * 256) + 1u;
    const u32 sw = (r >> 1) & 7;
    u32 xa[4];
#pragma unroll
    for (int s = 0; s < 4; ++s) { xa[s] = lds0 + (32 * t + r) * 128 + 16 * ((2 * s + h) ^ sw); PIN(xa[s]); }
    u32 fpa = lds0 + L_WPHI + 2048 + (4 * h + q) * 32 + (8 * blk + 4 * (p >> 1)) * 2;
    PIN(fpa);
    PINS(key);
    u32x4 xq[32], fa[4];
    u32 wa, wb, kra, o_off, c_off, woa, wob, vto, sto, tho;
#pragma unroll
    for (int c = 0; c < 8; ++c) {
      u32 d0, d1, s0, s1;
      if (c < 6) {
        d0 = ldst + ((c + 2) % 3) * SLOT; d1 = d0 + 16384;
        s0 = xs0 + chunk(c + 2) * 128; s1 = ws0 + chunk(c + 2) * (64 * 3072);
      } else {
        const u32 k0 = 2 * ((2 * (c - 6) + idx) & 3) + (t >> 1), k1 = (k0 + 2) & 7;
        d0 = lds0 + L_WQ1 + ((chunk(k0) + 4) & 7) * 8192 + (t & 1) * 4096; d1 = lds0 + L_WQ1 + ((chunk(k1) + 4) & 7) * 8192 + (t & 1) * 4096;
        s0 = (k0 * 64 + (t & 1) * 32) * 3072 + gq * 128; s1 = (k1 * 64 + (t & 1) * 32) * 3072 + gq * 128;
      }
      PINS(d0); PINS(d1); PINS(s0); PINS(s1);
      WAITV(8);
      BAR();
      if (c < 6) dma4(d0, vx, xr, s0); else dma4(d0, vw, wr, s0);
#pragma unroll
      for (int s = 0; s < 4; ++s) xq[4 * c + s] = lds_r128(xa[s] + (c % 3) * SLOT);
      dma4(d1, vw, wr, s1);
      if (c == 0) {
#pragma unroll
        for (int hd = 0; hd < 4; ++hd) fa[hd] = lds_tr2(fpa + hd * 512, fpa + hd * 512 + 256);
      }
      if (c == 1) {
        lds_w128(lds0 + L_V + (lane & 15) * VF + 1024, (u32x4){0x3F803F80u, 0x3F803F80u, 0x3F803F80u, 0x3F803F80u});
        const u32 sg = (lane & 4) ? 0x80008000u : 0u;
#pragma unroll
        for (int hd = 0; hd < 4; ++hd) { PIN(fa[hd]); fa[hd] ^= (u32x4){sg, sg, sg, sg}; PIN(fa[hd]); }
        wa = lds0 + L_WQ1 + (8 * h + q) * 128 + 16 * ((2 * blk + (p >> 1)) ^ ((((q >> 1) & 1) << 2) | (h << 1))) + 8 * (p & 1);
        wb = wa ^ 64;
        kra = lds0 + L_KV + (16 * h + q) * 48 + (blk ? 32 : 8 * p);
        o_off = WS_O + ((br * 64 + panel * 4 + t) * 8 + gq) * OREC + 256 + lane * 16;
        c_off = WS_O + ((br * 64 + panel * 4 + t) * 8 + gq) * OREC + lane * 4;
        woa = lds0 + L_WO + (8 * h + q) * 128 + 16 * ((2 * blk + (p & 1)) ^ (((q >> 1) & 1) << 2)) + 8 * (p >> 1);
        wob = woa ^ 64;
        vto = r * 128; sto = (idx * 16 + t * 4) * 1024;
        PIN(wa); PIN(wb); PIN(kra); PIN(o_off); PIN(c_off); PIN(woa); PIN(wob); PIN(vto); PINS(sto);
      }
    }
    TOUCH(tho, vto, wor, sto);
    STAMP(2);
    u32x4 fq[2][2][2];
    {
      f32x16 aq[2], pq[2];
      float mq[2];
      aq[0] = aq[1] = zero;
      u32x4 wf[4];
      auto ldw = [&](int n) {
        const int k = n & 31;
        const u32 o = (n < 32 ? wa : wb) + (k < 16 ? (L_WQ0 - L_WQ1) : 0) + (k & 15) * 2048;
        wf[n & 3] = lds_tr2(o, o + 512);
      };
      WAITV(1);
      BAR();
      SCHED();
      STAMP(12);
      ldw(0); ldw(1); ldw(2);
      SCHED();
      BARE();
      SCHED();
#pragma unroll
      for (int n = 0; n < 34; ++n) {
        ldw(n + 3);
        aq[n >> 5] = mfma(wf[n & 3], xq[n & 31], aq[n >> 5]);
        SGB(0x8, 1); SGB(0x100, 2);
        SCHED();
      }
#pragma unroll
      for (int hh = 0; hh < 2; ++hh) pq[hh] = mfma(fa[hh], pack8(aq[0], hh), zero);
      SCHED();
      STAMP(4);
#pragma unroll
      for (int n = 34; n < 64; ++n) {
        if (n + 3 < 64) ldw(n + 3);
        aq[1] = mfma(wf[n & 3], xq[n & 31], aq[1]);
      }
      mq[0] = negmax16(pq[0]); mq[1] = negmax16(pq[1]);
      f32x2 eq[2][8];
      sm_exp_s(pq[0], pq[1], mq, eq);
      SGB(0x8, 1); SGB(0x100, 2); SGB(0x8, 1); SGB(0x100, 2);
#pragma unroll
      for (int n = 36; n < 41; ++n) { SGB(0x8, 1); SGB(0x100, 2); SGB(0x402, 4); }
#pragma unroll
      for (int n = 41; n < 64; ++n) { SGB(0x8, 1); SGB(0x100, 2); if (n & 1) SGB(0x402, 3); else SGB(0x402, 2); }
#pragma unroll
      for (int i = 0; i < 16; ++i) PIN(eq[i >> 3][i & 7]);
      SCHED();
      STAMP(3);
#pragma unroll
      for (int hh = 0; hh < 2; ++hh) { pq[hh] = mfma(fa[2 + hh], pack8(aq[1], hh), zero); PIN(pq[hh]); }
      SCHED();
      sm_finish(eq, fq[0]);
      SCHED();
      asm volatile("s_waitcnt vmcnt(0)" : "+v"(tho)::"memory");
      mq[0] = negmax16(pq[0]); mq[1] = negmax16(pq[1]);
      PIN(mq[0]); PIN(mq[1]);
      SCHED();
#pragma unroll
      for (int i = 0; i < 4; ++i) PIN(fq[0][i >> 1][i & 1]);
      softmax16x2(pq[0], pq[1], mq, fq[1]);
#pragma unroll
      for (int i = 0; i < 4; ++i) PIN(fq[1][i >> 1][i & 1]);
    }
    STAMP(5);
    BAR();
    STAMP(6);
    u32x4 ob[2][4];
    {
      u32x4 a0[4], a1[4];
#pragma unroll
      for (int hd = 0; hd < 4; ++hd) {
        a0[hd] = lds_tr2(kra + hd * 1536, kra + hd * 1536 + 4 * 48);
        a1[hd] = lds_tr2(kra + hd * 1536 + 8 * 48, kra + hd * 1536 + 12 * 48);
      }
      SCHED();
      f32x16 y[4];
      u32 cs = 0;
      auto num = [&](int hd) {
        y[hd] = mfma(a0[hd], fq[hd >> 1][hd & 1][0], zero);
        y[hd] = mfma(a1[hd], fq[hd >> 1][hd & 1][1], y[hd]);
      };
      auto fin = [&](int hd) {
        const f32x16& yy = y[hd];
        const float rd = __builtin_amdgcn_rcpf(yy[8] + 1e-6f);
        u32 w[4];
#pragma unroll
        for (int e = 0; e < 4; ++e) { float t0 = yy[2 * e] * rd, t1 = yy[2 * e + 1] * rd; if (hd < 2) OPQ(t0); w[e] = pk2(t0, t1); }
        ob[0][hd] = (u32x4){w[0], w[1], w[2], w[3]};
        st128c(ob[0][hd], wsr, o_off + hd * 1024);
        cs = sum4(cs, ob[0][hd]);
      };
      num(0); num(1); PIN(y[0]); PIN(y[1]); SCHED();
      num(2); fin(0); SGB(0x8, 1); SGB(0x402, 6); SGB(0x8, 1); PIN(y[2]); SCHED();
      num(3); fin(1); SGB(0x8, 1); SGB(0x402, 6); SGB(0x8, 1); PIN(y[3]); SCHED();
      fin(2); fin(3);
      st32c(key - cs, wsr, c_off);
    }
    SCHED();
    STAMP(7);
    u32x4 wo[32][2];
    auto ldwo = [&](int n) {
      wo[n][0] = lds_tr2(woa + n * 2048, woa + n * 2048 + 512);
      wo[n][1] = lds_tr2(wob + n * 2048, wob + n * 2048 + 512);
    };
#pragma unroll
    for (int n = 0; n < 16; ++n) ldwo(n);
    f32x16 c0 = zero, c1 = zero;
#pragma unroll
    for (int s = 0; s < 4; ++s) { c0 = mfma(wo[s][0], ob[0][s], c0); c1 = mfma(wo[s][1], ob[0][s], c1); }
#pragma unroll
    for (int k = 4; k < 16; ++k) { PIN(wo[k][0]); PIN(wo[k][1]); }
    PIN(c0); PIN(c1);
    WAITV(0);
    BARE();
    SCHED();
#pragma unroll
    for (int n = 16; n < 24; ++n) ldwo(n);
#pragma unroll
    for (int k = 16; k < 24; ++k) { PIN(wo[k][0]); PIN(wo[k][1]); }
    const __amdgpu_buffer_rsrc_t outr = __builtin_amdgcn_make_buffer_rsrc(KPTR(kb, 0), 0, 2048 * 1024 * 2, SRD3);
    u32 oa = lds0 + L_O + t * OREC + 256 + lane * 16;
    u32 orow = ((panel * 128 + t * 32 + l3) * 1024 + br * 512 + cg * 64 + 8 * l7) * 2;
    PIN(oa); PIN(orow);
    u32 oc = oa - 256 - lane * 12, l4 = lane * 4, l16 = lane * 16, oa4 = oa + 4 * (4 * OREC), oc4 = oc + 4 * (4 * OREC);
    u32 sw1 = lds0 + t * 4096 + r * 128 + 16 * (h ^ (r & 7)), sr1 = lds0 + t * 4096 + l3 * 128 + 16 * (l7 ^ l3);
    u32 sw2 = sw1 ^ 32, sw3 = sw1 ^ 64, sw4 = sw1 ^ 96, or1 = orow + 16384, or2 = orow + 32768, or3 = orow + 49152;
    PIN(oc); PIN(l4); PIN(l16); PIN(sw1); PIN(sr1); PIN(oa4); PIN(oc4); PIN(sw2); PIN(sw3); PIN(sw4); PIN(or1); PIN(or2); PIN(or3);
    STAMP(8);
    u32 nretry = 0;
    u32 chk[2];
    auto oread = [&](int j) {
      const u32 da = j % 5 == 4 ? oa4 : oa + (j % 5) * (4 * OREC), dc = j % 5 == 4 ? oc4 : oc + (j % 5) * (4 * OREC);
#pragma unroll
      for (int s = 0; s < 4; ++s) ob[j & 1][s] = lds_r128(da + s * 1024);
      chk[j & 1] = *(LAS u32*)(size_t)dc;
    };
    auto obad = [&](int j) -> bool {
      const u32 c = sum4(sum4(chk[j & 1], ob[j & 1][0]), ob[j & 1][1]) + sum4(sum4(0u, ob[j & 1][2]), ob[j & 1][3]);
      return __builtin_amdgcn_ballot_w64(c != key) != 0;
    };
#pragma unroll
    for (int j = 1; j < 8; ++j) {
      BARE();
      SCHED();
      oread(j);
      if (j >= 2) {
#pragma unroll
        for (int s = 0; s < 4; ++s) {
          const int k = 4 * (j - 1) + s;
          c0 = mfma(wo[k][0], ob[(j - 1) & 1][s], c0);
          c1 = mfma(wo[k][1], ob[(j - 1) & 1][s], c1);
        }
        if (j >= 3 && j <= 6) { ldwo(24 + 2 * (j - 3)); ldwo(25 + 2 * (j - 3)); }
      }
      const bool bad = obad(j);
      if (j >= 3 && j <= 6) {
        SGB(0x8, 1); SGB(0x100, 3); SGB(0x8, 1); SGB(0x100, 2); SGB(0x8, 1); SGB(0x100, 3); SGB(0x8, 1); SGB(0x100, 3);
        SGB(0x8, 1); SGB(0x2, 4); SGB(0x8, 1); SGB(0x2, 5); SGB(0x8, 1); SGB(0x100, 2); SGB(0x8, 1);
      } else if (j >= 2) {
        SGB(0x8, 1); SGB(0x100, 3); SGB(0x8, 1); SGB(0x100, 2); SGB(0x8, 3); SGB(0x2, 4); SGB(0x8, 1); SGB(0x2, 5); SGB(0x8, 2);
      }
      SCHED();
      if (__builtin_expect(bad, 0)) {
#pragma unroll 1
        for (u32 spin = 0; spin < SPIN_CAP; ++spin) {
          dma_orec(lds0 + L_O + t * OREC + (j % 5) * (4 * OREC), l4, l16, wsr, WS_O + ((br * 64 + panel * 4 + t) * 8 + ochunk(j)) * OREC);
#pragma unroll 1
          for (u32 jj = j + 1; jj <= (j + AHEAD < 7 ? j + AHEAD : 7); ++jj) {
            const u32 sl = (jj >= 5 ? jj - 5 : jj) * (4 * OREC);
            u32 c = *(LAS u32*)(size_t)(oc + sl);
#pragma unroll
            for (int s = 0; s < 4; ++s) c = sum4(c, lds_r128(oa + sl + s * 1024));
            if (__builtin_amdgcn_ballot_w64(c != key) != 0)
              dma_orec(lds0 + L_O + t * OREC + sl, l4, l16, wsr, WS_O + ((br * 64 + panel * 4 + t) * 8 + ochunk(jj)) * OREC);
          }
          WAITV(0);
          oread(j);
          ++nretry;
          if (!obad(j)) break;
        }
      }
    }
#pragma unroll
    for (int s = 0; s < 4; ++s) c0 = mfma(wo[28 + s][0], ob[1][s], c0);
    PIN(c0);
    SCHED();
#pragma unroll
    for (int s = 0; s < 4; ++s) c1 = mfma(wo[28 + s][1], ob[1][s], c1);
    lds_w128(sw1, pack8(c0, 0));
    lds_w128(sw2, pack8(c0, 1));
    SGB(0x8, 2); SGB(0x2, 4); SGB(0x200, 1); SGB(0x8, 1); SGB(0x2, 4); SGB(0x200, 1); SGB(0x8, 1);
    SCHED();
    STAMP(9);
    lds_w128(sw3, pack8(c1, 0));
    lds_w128(sw4, pack8(c1, 1));
    asm volatile("" ::: "memory");
    {
      u32x4 v[4];
#pragma unroll
      for (int i = 0; i < 4; ++i) v[i] = lds_r128(sr1 + i * 1024);
      SCHED();
      st128c(v[0], outr, orow);
      st128c(v[1], outr, or1);
      st128c(v[2], outr, or2);
      st128c(v[3], outr, or3);
    }
    if (t == 0 && panel == 0 && grp == 0 && lane == 0) st32c(key, wsr, WS_EPOCH + br * 256);
    if (PROBE_N == 13) WAITV(0);
    STAMP(13);
    STAMP_OUT();
  }
}
}

extern "C" void kernel_launch(void* const* d_in, const int* in_sizes, int n_in,
                              void* d_out, int out_size, void* d_ws, size_t ws_size,
                              hipStream_t stream) {
  hh::hedgehog<<<256, 512, 0, stream>>>((const u16*)d_in[0], (const u16*)d_in[1], (const u16*)d_in[2], (const u16*)d_in[3],
                                        (const u16*)d_in[4], (const u16*)d_in[5], (const u16*)d_in[6], (const u16*)d_in[7],
                                        (u16*)d_out, (u32*)d_ws);
}
```

```cpp
#define PROBE_N 9
#define PROBE_ROLE 0
#include <hip/hip_runtime.h>
#include <stdint.h>

typedef unsigned short u16;
typedef unsigned u32;
typedef short bf16x8 __attribute__((ext_vector_type(8)));
typedef short s16x4 __attribute__((ext_vector_type(4)));
typedef float f32x16 __attribute__((ext_vector_type(16)));
typedef float f32x4 __attribute__((ext_vector_type(4)));
typedef float f32x2 __attribute__((ext_vector_type(2)));
typedef unsigned u32x8 __attribute__((ext_vector_type(8)));
typedef unsigned u32x4 __attribute__((ext_vector_type(4)));
typedef unsigned u32x2 __attribute__((ext_vector_type(2)));
typedef __bf16 bf16x2 __attribute__((ext_vector_type(2)));
#define DI __device__ __forceinline__
#define LAS __attribute__((address_space(3)))
#define CAS __attribute__((address_space(4)))
#define BC(T, x) __builtin_bit_cast(T, x)

#define DELAY1 3
#define DELAY3 5
#define CODE_PF 28672
#define AHEAD 1
#define GQ 1
#define GC 2
#define ORUN 0
#define OFIRST 5

namespace hh {
constexpr int AUX_SC1 = 16;
constexpr u32 SRD3 = 0x00020000u;
constexpr u32 WS_EPOCH = 0;
constexpr u32 REC = 1280;
constexpr u32 WS_PART = 4096;
constexpr u32 OREC = 4352;
constexpr u32 WS_O = WS_PART + 2 * 32 * 16 * REC;
constexpr u32 WS_END = WS_O + 2 * 64 * 8 * OREC;
constexpr u32 WS_STAMP = 8u << 20;
constexpr u32 SLOT = 32768;
constexpr u32 L_WQ0 = 98304;
constexpr u32 L_WQ1 = 65536;
constexpr u32 L_WPHI = 163840 - 4096;
constexpr u32 L_PHI = 0;
constexpr u32 VF = 1040;
constexpr u32 L_V = 131072;
constexpr u32 L_KV = L_V + 16 * VF;
constexpr u32 L_WO = 0;
constexpr u32 L_O = 65536;
constexpr u32 SPIN_CAP = 1u << 16;

DI u32 pk2(float a, float b) { return BC(u32, __builtin_convertvector((f32x2){a, b}, bf16x2)); }
DI u32x4 pack8(const f32x16& x, int s) {
  return (u32x4){pk2(x[8 * s], x[8 * s + 1]), pk2(x[8 * s + 2], x[8 * s + 3]), pk2(x[8 * s + 4], x[8 * s + 5]), pk2(x[8 * s + 6], x[8 * s + 7])};
}
DI f32x16 mfma(u32x4 a, u32x4 b, f32x16 c) { return __builtin_amdgcn_mfma_f32_32x32x16_bf16(BC(bf16x8, a), BC(bf16x8, b), c, 0, 0, 0); }
DI f32x4 mfma16(u32x4 a, u32x4 b, f32x4 c) { return __builtin_amdgcn_mfma_f32_16x16x32_bf16(BC(bf16x8, a), BC(bf16x8, b), c, 0, 0, 0); }
DI u32x4 lds_r128(u32 a) { return *(LAS u32x4*)(size_t)a; }
DI void lds_w128(u32 a, u32x4 v) { *(LAS u32x4*)(size_t)a = v; }
DI u32x4 lds_tr2(u32 a0, u32 a1) {
  s16x4 lo = __builtin_amdgcn_ds_read_tr16_b64_v4i16((LAS s16x4*)(size_t)a0);
  s16x4 hi = __builtin_amdgcn_ds_read_tr16_b64_v4i16((LAS s16x4*)(size_t)a1);
  return BC(u32x4, __builtin_shufflevector(lo, hi, 0, 1, 2, 3, 4, 5, 6, 7));
}
DI void dma1(u32 lds, u32 voff, __amdgpu_buffer_rsrc_t srd, u32 soff) {
  asm volatile("s_mov_b32 m0, %0\n\ts_nop 0\n\tbuffer_load_dwordx4 %1, %2, %3 offen lds" ::"s"(lds), "v"(voff), "s"(srd), "s"(soff) : "memory");
}
DI void dma4(u32 lds, const u32 (&v)[4], __amdgpu_buffer_rsrc_t srd, u32 soff) {
  asm volatile("s_mov_b32 m0, %0\n\ts_nop 0\n\t"
               "buffer_load_dwordx4 %1, %5, %6 offen lds\n\t"
               "buffer_load_dwordx4 %2, %5, %6 offen offset:1024 lds\n\t"
               "buffer_load_dwordx4 %3, %5, %6 offen offset:2048 lds\n\t"
               "buffer_load_dwordx4 %4, %5, %6 offen offset:3072 lds" ::"s"(lds), "v"(v[0]), "v"(v[1]), "v"(v[2]), "v"(v[3]), "s"(srd), "s"(soff) : "memory");
}
DI void dma_orec(u32 lds, u32 l4, u32 l16, __amdgpu_buffer_rsrc_t srd, u32 soff) {
  asm volatile("s_mov_b32 m0, %0\n\ts_nop 0\n\t"
               "buffer_load_dwordx4 %2, %3, %4 offen offset:256 sc1 lds\n\t"
               "buffer_load_dwordx4 %2, %3, %4 offen offset:1280 sc1 lds\n\t"
               "buffer_load_dwordx4 %2, %3, %4 offen offset:2304 sc1 lds\n\t"
               "buffer_load_dwordx4 %2, %3, %4 offen offset:3328 sc1 lds\n\t"
               "buffer_load_dword %1, %3, %4 offen sc1 lds" ::"s"(lds), "v"(l4), "v"(l16), "s"(srd), "s"(soff) : "memory");
}
#define TOUCH(d, voff, srd, soff) asm volatile("buffer_load_dword %0, %1, %2, %3 offen" : "=v"(d) : "v"(voff), "s"(srd), "s"(soff) : "memory")
DI void st128c(u32x4 v, __amdgpu_buffer_rsrc_t srd, u32 off) { __builtin_amdgcn_raw_buffer_store_b128(v, srd, off, 0, AUX_SC1); }
DI void st32c(u32 v, __amdgpu_buffer_rsrc_t srd, u32 off) { __builtin_amdgcn_raw_buffer_store_b32(v, srd, off, 0, AUX_SC1); }
#define BAR() asm volatile("s_waitcnt lgkmcnt(0)\n\ts_barrier" ::: "memory")
#define BARE() asm volatile("s_barrier" ::: "memory")
#define OPQ(x) asm("" : "+v"(x))
#define PIN(x) asm volatile("" : "+v"(x))
#define PINS(x) asm volatile("" : "+s"(x))
#define WAITV(n) asm volatile("s_waitcnt vmcnt(" #n ")" ::: "memory")
#define SCHED() __builtin_amdgcn_sched_barrier(0)
#define SGB(m, n) __builtin_amdgcn_sched_group_barrier(m, n, 0)
DI u32 sum4(u32 c, u32x4 v) { return (c + v.x + v.y) + v.z + v.w; }
DI u32 pair_sum(u32 s) { auto t = __builtin_amdgcn_permlane32_swap(s, s, false, false); return t[0] + t[1]; }

constexpr float LOG2E = 1.44269504088896340736f;
DI float max3f(float a, float b, float c) { return __builtin_fmaxf(__builtin_fmaxf(a, b), c); }
DI float negmax16(const f32x16& x) {
  const float a = max3f(x[0], x[1], x[2]), b = max3f(x[3], x[4], x[5]), c = max3f(x[6], x[7], x[8]);
  const float d = max3f(x[9], x[10], x[11]), e = max3f(x[12], x[13], x[14]);
  return -max3f(max3f(a, b, x[15]), max3f(c, d, e), a) * LOG2E;
}
DI void sm_exp(const f32x16& p, const f32x16& q, const float (&mc)[2], f32x2 (&e)[2][8]) {
#pragma unroll
  for (int i = 0; i < 8; ++i) {
    const f32x2 t = __builtin_elementwise_fma((f32x2){p[2 * i], p[2 * i + 1]}, (f32x2){LOG2E, LOG2E}, (f32x2){mc[0], mc[0]});
    const f32x2 u = __builtin_elementwise_fma((f32x2){q[2 * i], q[2 * i + 1]}, (f32x2){LOG2E, LOG2E}, (f32x2){mc[1], mc[1]});
    e[0][i] = (f32x2){__builtin_amdgcn_exp2f(t.x), __builtin_amdgcn_exp2f(t.y)};
    e[1][i] = (f32x2){__builtin_amdgcn_exp2f(u.x), __builtin_amdgcn_exp2f(u.y)};
  }
  SGB(0x2, 4); SGB(0x400, 8); SGB(0x2, 4); SGB(0x400, 8); SGB(0x2, 6); SGB(0x400, 8); SGB(0x2, 8); SGB(0x400, 8);
}
DI void sm_exp_s(const f32x16& p, const f32x16& q, const float (&mc)[2], f32x2 (&e)[2][8]) {
#pragma unroll
  for (int i = 0; i < 16; ++i) {
    e[0][i >> 1][i & 1] = __builtin_amdgcn_exp2f(__builtin_fmaf(p[i], LOG2E, mc[0]));
    e[1][i >> 1][i & 1] = __builtin_amdgcn_exp2f(__builtin_fmaf(q[i], LOG2E, mc[1]));
  }
}
DI void sm_finish(const f32x2 (&e)[2][8], u32x4 (&o)[2][2]) {
  float r[2];
#pragma unroll
  for (int u = 0; u < 2; ++u) {
    const f32x2 s = ((e[u][0] + e[u][1]) + (e[u][2] + e[u][3])) + ((e[u][4] + e[u][5]) + (e[u][6] + e[u][7]));
    r[u] = __builtin_amdgcn_rcpf(s.x + s.y);
  }
#pragma unroll
  for (int u = 0; u < 2; ++u) {
    u32 w[8];
#pragma unroll
    for (int i = 0; i < 8; ++i) { const f32x2 t = e[u][i] * (f32x2){r[u], r[u]}; w[i] = pk2(t.x, t.y); }
    o[u][0] = (u32x4){w[0], w[1], w[2], w[3]};
    o[u][1] = (u32x4){w[4], w[5], w[6], w[7]};
  }
}
DI void softmax16x2(const f32x16& p, const f32x16& q, const float (&mc)[2], u32x4 (&o)[2][2]) {
  f32x2 e[2][8];
  sm_exp(p, q, mc, e);
  sm_finish(e, o);
}

#define STAMP(i) do { if ((i) == PROBE_N) { SCHED(); asm volatile("s_memrealtime %0\n\ts_waitcnt lgkmcnt(0)" : "=s"(pt1) :: "memory"); SCHED(); } } while (0)
#define STAMP_OUT() do { if (PROBE_N && (wave >> 2) == PROBE_ROLE) { unsigned long long d_ = pt1 - pt0; if (d_ > 100000) d_ = 100000; unsigned long long e_ = pt0 + 3000 + 8 * d_; \
  for (int it_ = 0; it_ < 1000000; ++it_) { unsigned long long t_; asm volatile("s_memrealtime %0\n\ts_waitcnt lgkmcnt(0)" : "=s"(t_) :: "memory"); if (t_ >= e_) break; } } } while (0)

__global__ __launch_bounds__(512, 2) void hedgehog(const u16* xA, const u16* wA, const u16* pA, const u16* oA,
                                                   const u16* xB, const u16* wB, const u16* pB, const u16* oB,
                                                   u16* out_, u32* ws_) {
  __shared__ __attribute__((aligned(16))) char smem[163840];
  const u32 lds0 = (u32)(size_t)(LAS char*)smem;
  unsigned long long pt0 = __builtin_amdgcn_s_memrealtime(), pt1 = pt0;
  const int bid = blockIdx.x, xcd = bid & 7, idx = bid >> 3;
  const int br = xcd >> 2;
#define KLOAD(tail)                                                                                       \
  u32x8 ka;                                                                                               \
  u32x4 kb;                                                                                               \
  asm volatile("s_load_dwordx8 %0, %2, %3\n\ts_load_dwordx4 %1, %2, 0x40" tail : "=&s"(ka), "=&s"(kb)     \
               : "s"(__builtin_amdgcn_kernarg_segment_ptr()), "s"(br * 32))
#define KWAIT() asm volatile("s_waitcnt lgkmcnt(0)" : "+s"(ka), "+s"(kb))
#define KPTR(v, i) ((void*)(((unsigned long long)(v)[(i) + 1] << 32) | (v)[i]))
  unsigned long long pcs;
  asm volatile("s_getpc_b64 %0" : "=s"(pcs));
  pcs &= ~255ull;
  const int lane = threadIdx.x & 63;
  const int wave = __builtin_amdgcn_readfirstlane(threadIdx.x >> 6);
  const int panel = (xcd & 3) * 4 + (idx & 3), grp = idx >> 2, gq = (grp + GQ) & 7, cg = (grp + GC) & 7;
  auto ochunk = [&](u32 j) -> u32 {
    auto run = [&](int k) -> int {
      if (ORUN == 1) return k ^ (grp & 1);
      if (ORUN == 2) return (u32)(k - 2) < 4 ? 2 + ((k - 2) ^ (grp & 3)) : k ^ (grp & 1);
      if (ORUN == 3) return k ^ (grp & 3);
      if (ORUN == 4) return k >= 6 ? k : k >= 2 ? 2 + ((k - 2) ^ (grp & 3)) : k ^ (grp & 1);
      if (ORUN == 5) return k >= 6 ? k : k ^ (grp & 1);
      if (ORUN == 6) return k >= 6 ? k : k >= 4 ? k ^ (grp & 1) : k ^ (grp & 3);
      return k;
    };
    const int v = (run(((int)j + (((int)j - 1 - run((gq - OFIRST) & 7)) >> 31)) & 7) + OFIRST) & 7, z = ((int)j - 1) >> 31;
    return (u32)__builtin_amdgcn_readfirstlane((v & ~z) | (gq & z));
  };
  f32x16 zero;
#pragma unroll
  for (int i = 0; i < 16; ++i) zero[i] = 0.f;
#define LANE_IDS()                                                                            \
  u32 lane_ = lane;                                                                           \
  PIN(lane_);                                                                                 \
  const u32 r = lane_ & 31, h = lane_ >> 5;                                                   \
  const u32 blk = (lane_ >> 4) & 1, q = (lane_ & 15) >> 2, p = lane_ & 3;                     \
  u32* ws = (u32*)KPTR(kb, 2);                                                                \
  const __amdgpu_buffer_rsrc_t wsr = __builtin_amdgcn_make_buffer_rsrc(ws, 0, WS_END, SRD3);  \
  const __amdgpu_buffer_rsrc_t wor = __builtin_amdgcn_make_buffer_rsrc(KPTR(ka, 6), 0, 512 * 512 * 2, SRD3)

  if (wave >= 4) {
    const int cw = wave - 4;
    const int rh = cw >> 1, hp = cw & 1;
    KLOAD("\n\ts_waitcnt lgkmcnt(0)");
    u32 sp0, sp1, sp2, sp3;
    {
      u32 off = idx * 1024 + cw * 256;
      off = off < CODE_PF ? off : 0;
      asm volatile("s_load_dword %0, %4, %5 offset:0\n\ts_load_dword %1, %4, %5 offset:64\n\ts_load_dword %2, %4, %5 offset:128\n\ts_load_dword %3, %4, %5 offset:192"
                   : "=&s"(sp0), "=&s"(sp1), "=&s"(sp2), "=&s"(sp3) : "s"(pcs), "s"(off));
    }
    LANE_IDS();
    u32 key = *(const CAS u32*)((const char*)ws + WS_EPOCH + br * 256) + 1u;
    u32 xa[2], xb[2], wa[2], wb2[2];
    {
      const u32 r16 = lane_ & 15, g = lane_ >> 4;
#pragma unroll
      for (int s = 0; s < 2; ++s) {
        xa[s] = lds0 + (64 * rh + r16) * 128 + 16 * ((4 * s + g) ^ (r16 >> 1));
        wa[s] = lds0 + 16384 + (8 * g + q) * 128 + 16 * ((4 * hp + 2 * s + (p >> 1)) ^ ((((q >> 1) & 1) << 2) | (blk << 1))) + 8 * (p & 1);
        xb[s] = xa[s] + 2 * SLOT; wb2[s] = wa[s] + 2 * SLOT;
        PIN(xa[s]); PIN(xb[s]); PIN(wa[s]); PIN(wb2[s]);
      }
    }
    u32 fpa = lds0 + L_WPHI + hp * 1024 + (8 * h + q) * 32 + (8 * blk + 4 * (p >> 1)) * 2;
    PIN(fpa);
    PINS(key);
    f32x4 ak[4][2], av[4][2];
#pragma unroll
    for (int i = 0; i < 8; ++i) ak[i >> 1][i & 1] = av[i >> 1][i & 1] = (f32x4){0.f, 0.f, 0.f, 0.f};
    u32 vra, pra, vwa, pw0, pw1, pub16, pub4;
    u32x4 xf[2][4], wk[2][2], wv[2][2];
    auto ldx = [&](int c, int s) {
      const u32 x = c % 3 == 2 ? xb[s] : xa[s] + (c % 3) * SLOT;
#pragma unroll
      for (int i = 0; i < 4; ++i) xf[s][i] = lds_r128(x + i * 2048);
    };
    auto ldw = [&](int c, int s, int m) {
#pragma unroll
      for (int n = 0; n < 2; ++n) {
        const u32 w = (c % 3 == 2 ? wb2[n] : wa[n] + (c % 3) * SLOT) + s * 4096 + m * 8192;
        (m ? wv : wk)[s][n] = lds_tr2(w, w + 512);
      }
    };
    auto mk = [&](int s) {
#pragma unroll
      for (int i = 0; i < 8; ++i) ak[i >> 1][i & 1] = mfma16(wk[s][i & 1], xf[s][i >> 1], ak[i >> 1][i & 1]);
    };
    auto mv = [&](int s) {
#pragma unroll
      for (int i = 0; i < 8; ++i) av[i >> 1][i & 1] = mfma16(xf[s][i >> 1], wv[s][i & 1], av[i >> 1][i & 1]);
    };
    auto mix = [&](int n) {
#pragma unroll
      for (int i = 0; i < 8; ++i) {
        SGB(0x8, 1);
        if (n == 12) { if (i < 4) SGB(0x100, 2); else SGB(0x100, 1); }
        else if (n == 8) SGB(0x100, 1);
        else if (n == 4) { if (i < 4) SGB(0x100, 1); }
        else if (n < 0) SGB(0x6, 2);
      }
      SCHED();
    };
    BAR();
    asm volatile("" ::"s"(sp0), "s"(sp1), "s"(sp2), "s"(sp3));
    STAMP(1);
    ldw(0, 0, 0); ldx(0, 0); ldw(0, 0, 1);
    u32x4 fa[2];
#pragma unroll
    for (int hh = 0; hh < 2; ++hh) fa[hh] = lds_tr2(fpa + hh * 512, fpa + hh * 512 + 128);
    SCHED();
#pragma unroll
    for (int c = 0; c < 7; ++c) {
      ldw(c, 1, 0); ldx(c, 1); mk(0); mix(8);
      ldw(c, 1, 1); mv(0); mix(4);
      u32 ln = lane;
      PIN(ln);
      const u32 r_ = ln & 31, h_ = ln >> 5, q_ = (ln & 15) >> 2;
      if (c == 1) {
        const u32 sg = (ln & 4) ? 0x80008000u : 0u;
#pragma unroll
        for (int hh = 0; hh < 2; ++hh) { OPQ(fa[hh]); fa[hh] ^= (u32x4){sg, sg, sg, sg}; }
      }
      if (c == 2) vra = lds0 + L_V + (cw >> 1) * (2 * VF) + (r_ < 16 ? (ln + 16 * (cw & 1)) * 16 : 1024);
      if (c == 3) pra = lds0 + L_PHI + cw * 8192 + (4 * h_ + q_) * 64 + 16 * ((((ln >> 3) & 2) + ((ln >> 1) & 1)) ^ ((2 * h_ + (q_ >> 1)) & 3)) + 8 * (ln & 1);
      if (c == 4) {
        const u32 rec = WS_PART + ((br * 32 + grp * 4 + cw) * 16 + panel) * REC;
        pub16 = rec + ln * 16; pub4 = rec + 1024 + ln * 4;
      }
      if (c == 5) { pw0 = lds0 + L_PHI + (2 * hp) * 8192 + (64 * rh + r_) * 64 + 16 * ((2 * h_) ^ ((r_ >> 1) & 3)); pw1 = pw0 ^ 16; }
      if (c == 6) vwa = lds0 + L_V + (8 * rh + 2 * hp) * VF + ((ln & 16) * 2 + (ln & 15)) * 16 + h_ * 8;
      mk(1); mix(c >= 1 ? -1 : 0);
      if (c == 1) { PIN(fa[0]); PIN(fa[1]); }
      if (c == 2) PIN(vra);
      if (c == 3) PIN(pra);
      if (c == 4) { PIN(pub16); PIN(pub4); }
      if (c == 5) { PIN(pw0); PIN(pw1); }
      if (c == 6) PIN(vwa);
      BAR();
      ldw(c + 1, 0, 0); ldx(c + 1, 0); ldw(c + 1, 0, 1);
      mv(1); mix(12);
    }
    ldw(7, 1, 0); ldx(7, 1); ldw(7, 1, 1); mk(0); mix(12);
    mk(1); mix(0);
    __builtin_amdgcn_s_setprio(2);
    STAMP(2);
    f32x16 pk[2][2];
    {
      u32x4 kf[2][2];
      auto kpack = [&](int i) {
#pragma unroll
        for (int hh = 0; hh < 2; ++hh) {
          const f32x4 &t0 = ak[2 * i][hh], &t1 = ak[2 * i + 1][hh];
          const auto lo = __builtin_amdgcn_permlane16_swap(pk2(t0[0], t0[1]), pk2(t1[0], t1[1]), false, false);
          const auto hi = __builtin_amdgcn_permlane16_swap(pk2(t0[2], t0[3]), pk2(t1[2], t1[3]), false, false);
          kf[i][hh] = (u32x4){lo[0], hi[0], lo[1], hi[1]};
        }
      };
      mv(0); kpack(0);
      SGB(0x8, 2);
#pragma unroll
      for (int i = 0; i < 6; ++i) { SGB(0x2, 2); SGB(0x8, 1); }
      SCHED();
      pk[0][0] = mfma(fa[0], kf[0][0], zero);
      pk[0][1] = mfma(fa[1], kf[0][1], zero);
      kpack(1);
      SGB(0x8, 1); SGB(0x2, 6); SGB(0x8, 1); SGB(0x2, 6);
      PIN(pk[0][0]); PIN(pk[0][1]); PIN(kf[1][0]); PIN(kf[1][1]);
      SCHED();
      mv(1);
      pk[1][0] = mfma(fa[0], kf[1][0], zero);
      pk[1][1] = mfma(fa[1], kf[1][1], zero);
    }
    float m0[2], m1[2];
    m0[0] = negmax16(pk[0][0]); m0[1] = negmax16(pk[0][1]);
    SGB(0x8, 2);
#pragma unroll
    for (int i = 0; i < 6; ++i) { SGB(0x2, 2); SGB(0x8, 1); }
    SGB(0x8, 1); SGB(0x2, 6); SGB(0x8, 1);
#pragma unroll
    for (int i = 0; i < 8; ++i) PIN(av[i >> 1][i & 1]);
    PIN(pk[1][0]); PIN(pk[1][1]); PIN(m0[0]); PIN(m0[1]);
    SCHED();
#pragma unroll
    for (int i = 0; i < 8; ++i) {
      const f32x4& t = av[i >> 1][i & 1];
      *(LAS u32x2*)(size_t)(vwa + (4 * (i >> 2) + ((i >> 1) & 1)) * VF + (i & 1) * 256) = (u32x2){pk2(t[0], t[1]), pk2(t[2], t[3])};
    }
    SCHED();
    {
      u32x4 o[2][2];
      softmax16x2(pk[0][0], pk[0][1], m0, o);
      lds_w128(pw0, o[0][0]); lds_w128(pw1, o[0][1]);
      lds_w128(pw0 + 8192, o[1][0]); lds_w128(pw1 + 8192, o[1][1]);
      SCHED();
      m1[0] = negmax16(pk[1][0]); m1[1] = negmax16(pk[1][1]);
      PIN(m1[0]); PIN(m1[1]);
      SCHED();
      softmax16x2(pk[1][0], pk[1][1], m1, o);
      lds_w128(pw0 + 2048, o[0][0]); lds_w128(pw1 + 2048, o[0][1]);
      lds_w128(pw0 + 2048 + 8192, o[1][0]); lds_w128(pw1 + 2048 + 8192, o[1][1]);
      SGB(0x402, 26); SGB(0x200, 1); SGB(0x402, 8); SGB(0x200, 1); SGB(0x402, 8); SGB(0x200, 1); SGB(0x402, 8); SGB(0x200, 1);
      SCHED();
      BARE();
      BAR();
      SCHED();
    }
    STAMP(3);
    f32x16 kv = zero;
    {
      u32x4 a[8], b[8];
#pragma unroll
      for (int u = 0; u < 8; ++u) {
        a[u] = lds_r128(vra + ((u >> 1) * 4 + (u & 1)) * VF);
        b[u] = lds_tr2(pra + 16 * u * 64, pra + (16 * u + 8) * 64);
      }
#pragma unroll
      for (int u = 0; u < 8; ++u) kv = mfma(a[u], b[u], kv);
      SGB(0x100, 12); SGB(0x8, 1); SGB(0x100, 6); SGB(0x8, 1); SGB(0x100, 6); SGB(0x8, 6);
    }
    {
      const u32x4 w = pack8(kv, 0);
      st128c(w, wsr, pub16);
      const u32 zb = __float_as_uint(kv[8]);
      const u32 chk = key - pair_sum(sum4(h ? 0u : zb, w));
      st32c(h ? chk : zb, wsr, pub4);
    }
    SCHED();
    STAMP(4);
    u32 l16, l4, kwa, kza, gso[4], vo[4], wso;
    u32x4 sel;
    {
      u32 ln = lane;
      PIN(ln);
#pragma unroll
      for (int i = 0; i < 4; ++i) vo[i] = (ln >> 3) * 1024 + 16 * ((ln & 7) ^ (((ln >> 4) & 1) << 2)) + i * 7168;
#pragma unroll
      for (int i = 0; i < 2; ++i) dma4(lds0 + L_WO + cw * 8192 + i * 4096, vo, wor, ochunk(cw) * 65536 + i * 32768 + cg * 128);
      wso = ochunk(4 + cw) * 65536 + cg * 128;
      const u32 r_ = ln & 31, h_ = ln >> 5;
      l16 = ln * 16; l4 = ln * 4;
      kwa = lds0 + L_KV + cw * 1536 + r_ * 48 + h_ * 16; kza = lds0 + L_KV + cw * 1536 + r_ * 48 + 32 + h_ * 8;
      const bool hit = r_ < 16 && ((r_ >> 2) & 1) == h_;
      const u32 j = (r_ & 3) + 4 * (r_ >> 3);
#pragma unroll
      for (int e = 0; e < 4; ++e) sel[e] = (hit && (j >> 1) == e) ? ((j & 1) ? 0x3F800000u : 0x00003F80u) : 0u;
#pragma unroll
      for (int i = 0; i < 4; ++i) { gso[i] = WS_PART + ((br * 32 + gq * 4 + cw) * 16 + ((i + idx) & 3) * 4) * REC; PINS(gso[i]); }
      PIN(l16); PIN(l4); PIN(kwa); PIN(kza); PIN(sel); PINS(wso);
    }
    if (DELAY1) __builtin_amdgcn_s_sleep(DELAY1);
    STAMP(10);
    f32x16 gs[4];
    float z[4];
    {
      u32x4 g[16];
      u32 gz[16];
      auto gload = [&](int gi) {
#pragma unroll
        for (int i = 0; i < 4; ++i) {
          g[4 * gi + i] = __builtin_amdgcn_raw_buffer_load_b128(wsr, l16 + i * REC, gso[gi], AUX_SC1);
          gz[4 * gi + i] = __builtin_amdgcn_raw_buffer_load_b32(wsr, l4 + (1024 + i * REC - 1024 * (i == 3)), gso[gi] + 1024 * (i == 3), AUX_SC1);
          SCHED();
        }
      };
      auto gsum = [&](int gi) -> bool {
        u32 c = 0;
#pragma unroll
        for (int i = 0; i < 4; ++i) {
          c = sum4(c + gz[4 * gi + i], g[4 * gi + i]);
          gs[gi] = mfma(sel, g[4 * gi + i], i == 0 ? zero : gs[gi]);
          z[gi] = i == 0 ? __uint_as_float(gz[4 * gi]) : z[gi] + __uint_as_float(gz[4 * gi + i]);
        }
        PIN(z[gi]);
        const bool bad = __builtin_amdgcn_ballot_w64(pair_sum(c) != 4u * key) != 0;
        SCHED();
        return bad;
      };
      gload(0); gload(1); gload(2); gload(3);
      bool b0 = gsum(0), b1 = gsum(1), b2 = gsum(2), b3 = gsum(3);
      STAMP(11);
#pragma unroll 1
      for (u32 spin = 0; (b0 | b1 | b2 | b3) && spin < SPIN_CAP; ++spin) {
        asm volatile("" ::: "memory");
        if (b0) gload(0);
        if (b1) gload(1);
        if (b2) gload(2);
        if (b3) gload(3);
        if (b0) b0 = gsum(0);
        if (b1) b1 = gsum(1);
        if (b2) b2 = gsum(2);
        if (b3) b3 = gsum(3);
      }
    }
    __builtin_amdgcn_s_waitcnt(0x0F70);
    STAMP(5);
    {
      float tt[8];
#pragma unroll
      for (int j = 0; j < 8; ++j) tt[j] = (gs[0][j] + gs[1][j]) + (gs[2][j] + gs[3][j]);
      const float zt = (z[0] + z[1]) + (z[2] + z[3]);
      lds_w128(kwa, (u32x4){pk2(tt[0], tt[1]), pk2(tt[2], tt[3]), pk2(tt[4], tt[5]), pk2(tt[6], tt[7])});
      const u32 zz = pk2(zt, zt);
      *(LAS u32x2*)(size_t)kza = (u32x2){zz, zz};
    }
#pragma unroll
    for (int i = 0; i < 2; ++i) dma4(lds0 + L_WO + 32768 + cw * 8192 + i * 4096, vo, wor, wso + i * 32768);
    BAR();
    STAMP(6);
    {
      u32 oso[8], ob = lds0 + L_O + cw * OREC;
#pragma unroll
      for (int i = 1; i < 8; ++i) { oso[i] = WS_O + ((br * 64 + panel * 4 + cw) * 8 + ochunk(i)) * OREC; PINS(oso[i]); }
      PINS(ob);
      WAITV(0);
      BARE();
      if (DELAY3) __builtin_amdgcn_s_sleep(DELAY3);
      STAMP(8);
      auto oissue = [&](int j) { dma_orec(ob + (j % 5) * (4 * OREC), l4, l16, wsr, oso[j]); };
      oissue(1); oissue(2);
      WAITV(5); BARE(); oissue(3);
      WAITV(5); STAMP(15); BARE(); oissue(4); oissue(5);
      WAITV(10); BARE(); oissue(6);
      WAITV(10); BARE(); oissue(7);
      WAITV(10); BARE();
      WAITV(5); BARE();
      WAITV(0); BARE();
    }
    STAMP(9);
    STAMP_OUT();
  } else {
    KLOAD("");
    const int t = wave;
    const u32 l7 = lane & 7, l3 = lane >> 3, lq = lane >> 4;
    const u32 wsw = 16 * (l7 ^ ((lq & 1) << 2));
    u32 vx[4], vw[4];
#pragma unroll
    for (int i = 0; i < 4; ++i) {
      vx[i] = l3 * 1024 + 16 * (l7 ^ (((i & 1) << 2) | lq)) + i * 7168;
      vw[i] = l3 * 3072 + (wsw ^ ((i & 1) << 5)) + i * (8 * 3072 - 1024);
    }
    auto chunk = [&](u32 c) -> u32 {
      const u32 g = grp + idx;
      return c - 2 < 4 ? 2 + ((c - 2) ^ (g & 3)) : c ^ (g & 1);
    };
    const u32 xs0 = (panel * 128 + t * 32) * 1024, ws0 = (t & 1) * (32 * 3072) + (1 + (t >> 1)) * 1024 + grp * 128;
    const u32 ldst = lds0 + t * 4096;
    u32 e0 = ldst, e1 = ldst + 16384, e2 = xs0 + chunk(0) * 128, e3 = ws0 + chunk(0) * (64 * 3072);
    PINS(e0); PINS(e1); PINS(e2); PINS(e3);
#pragma unroll
    for (int i = 0; i < 4; ++i) { PIN(vx[i]); PIN(vw[i]); }
    KWAIT();
    const __amdgpu_buffer_rsrc_t xr = __builtin_amdgcn_make_buffer_rsrc(KPTR(ka, 0), 0, 2048 * 512 * 2, SRD3);
    const __amdgpu_buffer_rsrc_t wr = __builtin_amdgcn_make_buffer_rsrc(KPTR(ka, 2), 0, 512 * 1536 * 2, SRD3);
    STAMP(10);
    dma4(e0, vx, xr, e2);
    dma4(e1, vw, wr, e3);
    {
      const __amdgpu_buffer_rsrc_t pr = __builtin_amdgcn_make_buffer_rsrc(KPTR(ka, 4), 0, 32 * 256 * 2, SRD3);
      dma1(lds0 + L_WPHI + t * 1024, lane * 16, pr, (t < 2 ? grp : gq) * 2048 + (t & 1) * 1024);
    }
    STAMP(11);
    dma4(ldst + SLOT, vx, xr, xs0 + chunk(1) * 128);
    dma4(ldst + SLOT + 16384, vw, wr, ws0 + chunk(1) * (64 * 3072));
    STAMP(1);
    LANE_IDS();
    u32 key = *(const CAS u32*)((const char*)ws + WS_EPOCH + br * 256) + 1u;
    const u32 sw = (r >> 1) & 7;
    u32 xa[4];
#pragma unroll
    for (int s = 0; s < 4; ++s) { xa[s] = lds0 + (32 * t + r) * 128 + 16 * ((2 * s + h) ^ sw); PIN(xa[s]); }
    u32 fpa = lds0 + L_WPHI + 2048 + (4 * h + q) * 32 + (8 * blk + 4 * (p >> 1)) * 2;
    PIN(fpa);
    PINS(key);
    u32x4 xq[32], fa[4];
    u32 wa, wb, kra, o_off, c_off, woa, wob, vto, sto, tho;
#pragma unroll
    for (int c = 0; c < 8; ++c) {
      u32 d0, d1, s0, s1;
      if (c < 6) {
        d0 = ldst + ((c + 2) % 3) * SLOT; d1 = d0 + 16384;
        s0 = xs0 + chunk(c + 2) * 128; s1 = ws0 + chunk(c + 2) * (64 * 3072);
      } else {
        const u32 k0 = 2 * ((2 * (c - 6) + idx) & 3) + (t >> 1), k1 = (k0 + 2) & 7;
        d0 = lds0 + L_WQ1 + ((chunk(k0) + 4) & 7) * 8192 + (t & 1) * 4096; d1 = lds0 + L_WQ1 + ((chunk(k1) + 4) & 7) * 8192 + (t & 1) * 4096;
        s0 = (k0 * 64 + (t & 1) * 32) * 3072 + gq * 128; s1 = (k1 * 64 + (t & 1) * 32) * 3072 + gq * 128;
      }
      PINS(d0); PINS(d1); PINS(s0); PINS(s1);
      WAITV(8);
      BAR();
      if (c < 6) dma4(d0, vx, xr, s0); else dma4(d0, vw, wr, s0);
#pragma unroll
      for (int s = 0; s < 4; ++s) xq[4 * c + s] = lds_r128(xa[s] + (c % 3) * SLOT);
      dma4(d1, vw, wr, s1);
      if (c == 0) {
#pragma unroll
        for (int hd = 0; hd < 4; ++hd) fa[hd] = lds_tr2(fpa + hd * 512, fpa + hd * 512 + 256);
      }
      if (c == 1) {
        lds_w128(lds0 + L_V + (lane & 15) * VF + 1024, (u32x4){0x3F803F80u, 0x3F803F80u, 0x3F803F80u, 0x3F803F80u});
        const u32 sg = (lane & 4) ? 0x80008000u : 0u;
#pragma unroll
        for (int hd = 0; hd < 4; ++hd) { PIN(fa[hd]); fa[hd] ^= (u32x4){sg, sg, sg, sg}; PIN(fa[hd]); }
        wa = lds0 + L_WQ1 + (8 * h + q) * 128 + 16 * ((2 * blk + (p >> 1)) ^ ((((q >> 1) & 1) << 2) | (h << 1))) + 8 * (p & 1);
        wb = wa ^ 64;
        kra = lds0 + L_KV + (16 * h + q) * 48 + (blk ? 32 : 8 * p);
        o_off = WS_O + ((br * 64 + panel * 4 + t) * 8 + gq) * OREC + 256 + lane * 16;
        c_off = WS_O + ((br * 64 + panel * 4 + t) * 8 + gq) * OREC + lane * 4;
        woa = lds0 + L_WO + (8 * h + q) * 128 + 16 * ((2 * blk + (p & 1)) ^ (((q >> 1) & 1) << 2)) + 8 * (p >> 1);
        wob = woa ^ 64;
        vto = r * 128; sto = (idx * 16 + t * 4) * 1024;
        PIN(wa); PIN(wb); PIN(kra); PIN(o_off); PIN(c_off); PIN(woa); PIN(wob); PIN(vto); PINS(sto);
      }
    }
    TOUCH(tho, vto, wor, sto);
    STAMP(2);
    u32x4 fq[2][2][2];
    {
      f32x16 aq[2], pq[2];
      float mq[2];
      aq[0] = aq[1] = zero;
      u32x4 wf[4];
      auto ldw = [&](int n) {
        const int k = n & 31;
        const u32 o = (n < 32 ? wa : wb) + (k < 16 ? (L_WQ0 - L_WQ1) : 0) + (k & 15) * 2048;
        wf[n & 3] = lds_tr2(o, o + 512);
      };
      WAITV(1);
      BAR();
      SCHED();
      STAMP(12);
      ldw(0); ldw(1); ldw(2);
      SCHED();
      BARE();
      SCHED();
#pragma unroll
      for (int n = 0; n < 34; ++n) {
        ldw(n + 3);
        aq[n >> 5] = mfma(wf[n & 3], xq[n & 31], aq[n >> 5]);
        SGB(0x8, 1); SGB(0x100, 2);
        SCHED();
      }
#pragma unroll
      for (int hh = 0; hh < 2; ++hh) pq[hh] = mfma(fa[hh], pack8(aq[0], hh), zero);
      SCHED();
      STAMP(4);
#pragma unroll
      for (int n = 34; n < 64; ++n) {
        if (n + 3 < 64) ldw(n + 3);
        aq[1] = mfma(wf[n & 3], xq[n & 31], aq[1]);
      }
      mq[0] = negmax16(pq[0]); mq[1] = negmax16(pq[1]);
      f32x2 eq[2][8];
      sm_exp_s(pq[0], pq[1], mq, eq);
      SGB(0x8, 1); SGB(0x100, 2); SGB(0x8, 1); SGB(0x100, 2);
#pragma unroll
      for (int n = 36; n < 41; ++n) { SGB(0x8, 1); SGB(0x100, 2); SGB(0x402, 4); }
#pragma unroll
      for (int n = 41; n < 64; ++n) { SGB(0x8, 1); SGB(0x100, 2); if (n & 1) SGB(0x402, 3); else SGB(0x402, 2); }
#pragma unroll
      for (int i = 0; i < 16; ++i) PIN(eq[i >> 3][i & 7]);
      SCHED();
      STAMP(3);
#pragma unroll
      for (int hh = 0; hh < 2; ++hh) { pq[hh] = mfma(fa[2 + hh], pack8(aq[1], hh), zero); PIN(pq[hh]); }
      SCHED();
      sm_finish(eq, fq[0]);
      SCHED();
      asm volatile("s_waitcnt vmcnt(0)" : "+v"(tho)::"memory");
      mq[0] = negmax16(pq[0]); mq[1] = negmax16(pq[1]);
      PIN(mq[0]); PIN(mq[1]);
      SCHED();
#pragma unroll
      for (int i = 0; i < 4; ++i) PIN(fq[0][i >> 1][i & 1]);
      softmax16x2(pq[0], pq[1], mq, fq[1]);
#pragma unroll
      for (int i = 0; i < 4; ++i) PIN(fq[1][i >> 1][i & 1]);
    }
    STAMP(5);
    BAR();
    STAMP(6);
    u32x4 ob[2][4];
    {
      u32x4 a0[4], a1[4];
#pragma unroll
      for (int hd = 0; hd < 4; ++hd) {
        a0[hd] = lds_tr2(kra + hd * 1536, kra + hd * 1536 + 4 * 48);
        a1[hd] = lds_tr2(kra + hd * 1536 + 8 * 48, kra + hd * 1536 + 12 * 48);
      }
      SCHED();
      f32x16 y[4];
      u32 cs = 0;
      auto num = [&](int hd) {
        y[hd] = mfma(a0[hd], fq[hd >> 1][hd & 1][0], zero);
        y[hd] = mfma(a1[hd], fq[hd >> 1][hd & 1][1], y[hd]);
      };
      auto fin = [&](int hd) {
        const f32x16& yy = y[hd];
        const float rd = __builtin_amdgcn_rcpf(yy[8] + 1e-6f);
        u32 w[4];
#pragma unroll
        for (int e = 0; e < 4; ++e) { float t0 = yy[2 * e] * rd, t1 = yy[2 * e + 1] * rd; if (hd < 2) OPQ(t0); w[e] = pk2(t0, t1); }
        ob[0][hd] = (u32x4){w[0], w[1], w[2], w[3]};
        st128c(ob[0][hd], wsr, o_off + hd * 1024);
        cs = sum4(cs, ob[0][hd]);
      };
      num(0); num(1); PIN(y[0]); PIN(y[1]); SCHED();
      num(2); fin(0); SGB(0x8, 1); SGB(0x402, 6); SGB(0x8, 1); PIN(y[2]); SCHED();
      num(3); fin(1); SGB(0x8, 1); SGB(0x402, 6); SGB(0x8, 1); PIN(y[3]); SCHED();
      fin(2); fin(3);
      st32c(key - cs, wsr, c_off);
    }
    SCHED();
    STAMP(7);
    u32x4 wo[32][2];
    auto ldwo = [&](int n) {
      wo[n][0] = lds_tr2(woa + n * 2048, woa + n * 2048 + 512);
      wo[n][1] = lds_tr2(wob + n * 2048, wob + n * 2048 + 512);
    };
#pragma unroll
    for (int n = 0; n < 16; ++n) ldwo(n);
    f32x16 c0 = zero, c1 = zero;
#pragma unroll
    for (int s = 0; s < 4; ++s) { c0 = mfma(wo[s][0], ob[0][s], c0); c1 = mfma(wo[s][1], ob[0][s], c1); }
#pragma unroll
    for (int k = 4; k < 16; ++k) { PIN(wo[k][0]); PIN(wo[k][1]); }
    PIN(c0); PIN(c1);
    WAITV(0);
    BARE();
    SCHED();
#pragma unroll
    for (int n = 16; n < 24; ++n) ldwo(n);
#pragma unroll
    for (int k = 16; k < 24; ++k) { PIN(wo[k][0]); PIN(wo[k][1]); }
    const __amdgpu_buffer_rsrc_t outr = __builtin_amdgcn_make_buffer_rsrc(KPTR(kb, 0), 0, 2048 * 1024 * 2, SRD3);
    u32 oa = lds0 + L_O + t * OREC + 256 + lane * 16;
    u32 orow = ((panel * 128 + t * 32 + l3) * 1024 + br * 512 + cg * 64 + 8 * l7) * 2;
    PIN(oa); PIN(orow);
    u32 oc = oa - 256 - lane * 12, l4 = lane * 4, l16 = lane * 16, oa4 = oa + 4 * (4 * OREC), oc4 = oc + 4 * (4 * OREC);
    u32 sw1 = lds0 + t * 4096 + r * 128 + 16 * (h ^ (r & 7)), sr1 = lds0 + t * 4096 + l3 * 128 + 16 * (l7 ^ l3);
    u32 sw2 = sw1 ^ 32, sw3 = sw1 ^ 64, sw4 = sw1 ^ 96, or1 = orow + 16384, or2 = orow + 32768, or3 = orow + 49152;
    PIN(oc); PIN(l4); PIN(l16); PIN(sw1); PIN(sr1); PIN(oa4); PIN(oc4); PIN(sw2); PIN(sw3); PIN(sw4); PIN(or1); PIN(or2); PIN(or3);
    STAMP(8);
    u32 nretry = 0;
    u32 chk[2];
    auto oread = [&](int j) {
      const u32 da = j % 5 == 4 ? oa4 : oa + (j % 5) * (4 * OREC), dc = j % 5 == 4 ? oc4 : oc + (j % 5) * (4 * OREC);
#pragma unroll
      for (int s = 0; s < 4; ++s) ob[j & 1][s] = lds_r128(da + s * 1024);
      chk[j & 1] = *(LAS u32*)(size_t)dc;
    };
    auto obad = [&](int j) -> bool {
      const u32 c = sum4(sum4(chk[j & 1], ob[j & 1][0]), ob[j & 1][1]) + sum4(sum4(0u, ob[j & 1][2]), ob[j & 1][3]);
      return __builtin_amdgcn_ballot_w64(c != key) != 0;
    };
#pragma unroll
    for (int j = 1; j < 8; ++j) {
      BARE();
      SCHED();
      oread(j);
      if (j >= 2) {
#pragma unroll
        for (int s = 0; s < 4; ++s) {
          const int k = 4 * (j - 1) + s;
          c0 = mfma(wo[k][0], ob[(j - 1) & 1][s], c0);
          c1 = mfma(wo[k][1], ob[(j - 1) & 1][s], c1);
        }
        if (j >= 3 && j <= 6) { ldwo(24 + 2 * (j - 3)); ldwo(25 + 2 * (j - 3)); }
      }
      const bool bad = obad(j);
      if (j >= 3 && j <= 6) {
        SGB(0x8, 1); SGB(0x100, 3); SGB(0x8, 1); SGB(0x100, 2); SGB(0x8, 1); SGB(0x100, 3); SGB(0x8, 1); SGB(0x100, 3);
        SGB(0x8, 1); SGB(0x2, 4); SGB(0x8, 1); SGB(0x2, 5); SGB(0x8, 1); SGB(0x100, 2); SGB(0x8, 1);
      } else if (j >= 2) {
        SGB(0x8, 1); SGB(0x100, 3); SGB(0x8, 1); SGB(0x100, 2); SGB(0x8, 3); SGB(0x2, 4); SGB(0x8, 1); SGB(0x2, 5); SGB(0x8, 2);
      }
      SCHED();
      if (__builtin_expect(bad, 0)) {
#pragma unroll 1
        for (u32 spin = 0; spin < SPIN_CAP; ++spin) {
          dma_orec(lds0 + L_O + t * OREC + (j % 5) * (4 * OREC), l4, l16, wsr, WS_O + ((br * 64 + panel * 4 + t) * 8 + ochunk(j)) * OREC);
#pragma unroll 1
          for (u32 jj = j + 1; jj <= (j + AHEAD < 7 ? j + AHEAD : 7); ++jj) {
            const u32 sl = (jj >= 5 ? jj - 5 : jj) * (4 * OREC);
            u32 c = *(LAS u32*)(size_t)(oc + sl);
#pragma unroll
            for (int s = 0; s < 4; ++s) c = sum4(c, lds_r128(oa + sl + s * 1024));
            if (__builtin_amdgcn_ballot_w64(c != key) != 0)
              dma_orec(lds0 + L_O + t * OREC + sl, l4, l16, wsr, WS_O + ((br * 64 + panel * 4 + t) * 8 + ochunk(jj)) * OREC);
          }
          WAITV(0);
          oread(j);
          ++nretry;
          if (!obad(j)) break;
        }
      }
    }
#pragma unroll
    for (int s = 0; s < 4; ++s) c0 = mfma(wo[28 + s][0], ob[1][s], c0);
    PIN(c0);
    SCHED();
#pragma unroll
    for (int s = 0; s < 4; ++s) c1 = mfma(wo[28 + s][1], ob[1][s], c1);
    lds_w128(sw1, pack8(c0, 0));
    lds_w128(sw2, pack8(c0, 1));
    SGB(0x8, 2); SGB(0x2, 4); SGB(0x200, 1); SGB(0x8, 1); SGB(0x2, 4); SGB(0x200, 1); SGB(0x8, 1);
    SCHED();
    STAMP(9);
    lds_w128(sw3, pack8(c1, 0));
    lds_w128(sw4, pack8(c1, 1));
    asm volatile("" ::: "memory");
    {
      u32x4 v[4];
#pragma unroll
      for (int i = 0; i < 4; ++i) v[i] = lds_r128(sr1 + i * 1024);
      SCHED();
      st128c(v[0], outr, orow);
      st128c(v[1], outr, or1);
      st128c(v[2], outr, or2);
      st128c(v[3], outr, or3);
    }
    if (t == 0 && panel == 0 && grp == 0 && lane == 0) st32c(key, wsr, WS_EPOCH + br * 256);
    if (PROBE_N == 13) WAITV(0);
    STAMP(13);
    STAMP_OUT();
  }
}
}

extern "C" void kernel_launch(void* const* d_in, const int* in_sizes, int n_in,
                              void* d_out, int out_size, void* d_ws, size_t ws_size,
                              hipStream_t stream) {
  hh::hedgehog<<<256, 512, 0, stream>>>((const u16*)d_in[0], (const u16*)d_in[1], (const u16*)d_in[2], (const u16*)d_in[3],
                                        (const u16*)d_in[4], (const u16*)d_in[5], (const u16*)d_in[6], (const u16*)d_in[7],
                                        (u16*)d_out, (u32*)d_ws);
}
```

```cpp
#define PROBE_N 9
#define PROBE_ROLE 0
#include <hip/hip_runtime.h>
#include <stdint.h>

typedef unsigned short u16;
typedef unsigned u32;
typedef short bf16x8 __attribute__((ext_vector_type(8)));
typedef short s16x4 __attribute__((ext_vector_type(4)));
typedef float f32x16 __attribute__((ext_vector_type(16)));
typedef float f32x4 __attribute__((ext_vector_type(4)));
typedef float f32x2 __attribute__((ext_vector_type(2)));
typedef unsigned u32x8 __attribute__((ext_vector_type(8)));
typedef unsigned u32x4 __attribute__((ext_vector_type(4)));
typedef unsigned u32x2 __attribute__((ext_vector_type(2)));
typedef __bf16 bf16x2 __attribute__((ext_vector_type(2)));
#define DI __device__ __forceinline__
#define LAS __attribute__((address_space(3)))
#define CAS __attribute__((address_space(4)))
#define BC(T, x) __builtin_bit_cast(T, x)

#define DELAY1 3
#define DELAY3 5
#define CODE_PF 29184
#define AHEAD 1
#define GQ 1
#define GC 2
#define GBURST 1
#define OFIRST 5

namespace hh {
constexpr int AUX_SC1 = 16;
constexpr u32 SRD3 = 0x00020000u;
constexpr u32 WS_EPOCH = 0;
constexpr u32 REC = 1280;
constexpr u32 WS_PART = 4096;
constexpr u32 OREC = 4352;
constexpr u32 WS_O = WS_PART + 2 * 32 * 16 * REC;
constexpr u32 WS_END = WS_O + 2 * 64 * 8 * OREC;
constexpr u32 WS_STAMP = 8u << 20;
constexpr u32 SLOT = 32768;
constexpr u32 L_WQ0 = 98304;
constexpr u32 L_WQ1 = 65536;
constexpr u32 L_WPHI = 163840 - 4096;
constexpr u32 L_PHI = 0;
constexpr u32 VF = 1040;
constexpr u32 L_V = 131072;
constexpr u32 L_KV = L_V + 16 * VF;
constexpr u32 L_WO = 0;
constexpr u32 L_O = 65536;
constexpr u32 SPIN_CAP = 1u << 16;

DI u32 pk2(float a, float b) { return BC(u32, __builtin_convertvector((f32x2){a, b}, bf16x2)); }
DI u32x4 pack8(const f32x16& x, int s) {
  return (u32x4){pk2(x[8 * s], x[8 * s + 1]), pk2(x[8 * s + 2], x[8 * s + 3]), pk2(x[8 * s + 4], x[8 * s + 5]), pk2(x[8 * s + 6], x[8 * s + 7])};
}
DI f32x16 mfma(u32x4 a, u32x4 b, f32x16 c) { return __builtin_amdgcn_mfma_f32_32x32x16_bf16(BC(bf16x8, a), BC(bf16x8, b), c, 0, 0, 0); }
DI f32x4 mfma16(u32x4 a, u32x4 b, f32x4 c) { return __builtin_amdgcn_mfma_f32_16x16x32_bf16(BC(bf16x8, a), BC(bf16x8, b), c, 0, 0, 0); }
DI u32x4 lds_r128(u32 a) { return *(LAS u32x4*)(size_t)a; }
DI void lds_w128(u32 a, u32x4 v) { *(LAS u32x4*)(size_t)a = v; }
DI u32x4 lds_tr2(u32 a0, u32 a1) {
  s16x4 lo = __builtin_amdgcn_ds_read_tr16_b64_v4i16((LAS s16x4*)(size_t)a0);
  s16x4 hi = __builtin_amdgcn_ds_read_tr16_b64_v4i16((LAS s16x4*)(size_t)a1);
  return BC(u32x4, __builtin_shufflevector(lo, hi, 0, 1, 2, 3, 4, 5, 6, 7));
}
DI void dma1(u32 lds, u32 voff, __amdgpu_buffer_rsrc_t srd, u32 soff) {
  asm volatile("s_mov_b32 m0, %0\n\ts_nop 0\n\tbuffer_load_dwordx4 %1, %2, %3 offen lds" ::"s"(lds), "v"(voff), "s"(srd), "s"(soff) : "memory");
}
DI void dma4(u32 lds, const u32 (&v)[4], __amdgpu_buffer_rsrc_t srd, u32 soff) {
  asm volatile("s_mov_b32 m0, %0\n\ts_nop 0\n\t"
               "buffer_load_dwordx4 %1, %5, %6 offen lds\n\t"
               "buffer_load_dwordx4 %2, %5, %6 offen offset:1024 lds\n\t"
               "buffer_load_dwordx4 %3, %5, %6 offen offset:2048 lds\n\t"
               "buffer_load_dwordx4 %4, %5, %6 offen offset:3072 lds" ::"s"(lds), "v"(v[0]), "v"(v[1]), "v"(v[2]), "v"(v[3]), "s"(srd), "s"(soff) : "memory");
}
DI void dma_orec(u32 lds, u32 l4, u32 l16, __amdgpu_buffer_rsrc_t srd, u32 soff) {
  asm volatile("s_mov_b32 m0, %0\n\ts_nop 0\n\t"
               "buffer_load_dwordx4 %2, %3, %4 offen offset:256 sc1 lds\n\t"
               "buffer_load_dwordx4 %2, %3, %4 offen offset:1280 sc1 lds\n\t"
               "buffer_load_dwordx4 %2, %3, %4 offen offset:2304 sc1 lds\n\t"
               "buffer_load_dwordx4 %2, %3, %4 offen offset:3328 sc1 lds\n\t"
               "buffer_load_dword %1, %3, %4 offen sc1 lds" ::"s"(lds), "v"(l4), "v"(l16), "s"(srd), "s"(soff) : "memory");
}
#define TOUCH(d, voff, srd, soff) asm volatile("buffer_load_dword %0, %1, %2, %3 offen" : "=v"(d) : "v"(voff), "s"(srd), "s"(soff) : "memory")
DI void st128c(u32x4 v, __amdgpu_buffer_rsrc_t srd, u32 off) { __builtin_amdgcn_raw_buffer_store_b128(v, srd, off, 0, AUX_SC1); }
DI void st32c(u32 v, __amdgpu_buffer_rsrc_t srd, u32 off) { __builtin_amdgcn_raw_buffer_store_b32(v, srd, off, 0, AUX_SC1); }
#define BAR() asm volatile("s_waitcnt lgkmcnt(0)\n\ts_barrier" ::: "memory")
#define BARE() asm volatile("s_barrier" ::: "memory")
#define OPQ(x) asm("" : "+v"(x))
#define PIN(x) asm volatile("" : "+v"(x))
#define PINS(x) asm volatile("" : "+s"(x))
#define WAITV(n) asm volatile("s_waitcnt vmcnt(" #n ")" ::: "memory")
#define SCHED() __builtin_amdgcn_sched_barrier(0)
#define SGB(m, n) __builtin_amdgcn_sched_group_barrier(m, n, 0)
DI u32 sum4(u32 c, u32x4 v) { return (c + v.x + v.y) + v.z + v.w; }
DI u32 pair_sum(u32 s) { auto t = __builtin_amdgcn_permlane32_swap(s, s, false, false); return t[0] + t[1]; }

constexpr float LOG2E = 1.44269504088896340736f;
DI float max3f(float a, float b, float c) { return __builtin_fmaxf(__builtin_fmaxf(a, b), c); }
DI float negmax16(const f32x16& x) {
  const float a = max3f(x[0], x[1], x[2]), b = max3f(x[3], x[4], x[5]), c = max3f(x[6], x[7], x[8]);
  const float d = max3f(x[9], x[10], x[11]), e = max3f(x[12], x[13], x[14]);
  return -max3f(max3f(a, b, x[15]), max3f(c, d, e), a) * LOG2E;
}
DI void sm_exp(const f32x16& p, const f32x16& q, const float (&mc)[2], f32x2 (&e)[2][8]) {
#pragma unroll
  for (int i = 0; i < 8; ++i) {
    const f32x2 t = __builtin_elementwise_fma((f32x2){p[2 * i], p[2 * i + 1]}, (f32x2){LOG2E, LOG2E}, (f32x2){mc[0], mc[0]});
    const f32x2 u = __builtin_elementwise_fma((f32x2){q[2 * i], q[2 * i + 1]}, (f32x2){LOG2E, LOG2E}, (f32x2){mc[1], mc[1]});
    e[0][i] = (f32x2){__builtin_amdgcn_exp2f(t.x), __builtin_amdgcn_exp2f(t.y)};
    e[1][i] = (f32x2){__builtin_amdgcn_exp2f(u.x), __builtin_amdgcn_exp2f(u.y)};
  }
  SGB(0x2, 4); SGB(0x400, 8); SGB(0x2, 4); SGB(0x400, 8); SGB(0x2, 6); SGB(0x400, 8); SGB(0x2, 8); SGB(0x400, 8);
}
DI void sm_exp_s(const f32x16& p, const f32x16& q, const float (&mc)[2], f32x2 (&e)[2][8]) {
#pragma unroll
  for (int i = 0; i < 16; ++i) {
    e[0][i >> 1][i & 1] = __builtin_amdgcn_exp2f(__builtin_fmaf(p[i], LOG2E, mc[0]));
    e[1][i >> 1][i & 1] = __builtin_amdgcn_exp2f(__builtin_fmaf(q[i], LOG2E, mc[1]));
  }
}
DI void sm_finish(const f32x2 (&e)[2][8], u32x4 (&o)[2][2]) {
  float r[2];
#pragma unroll
  for (int u = 0; u < 2; ++u) {
    const f32x2 s = ((e[u][0] + e[u][1]) + (e[u][2] + e[u][3])) + ((e[u][4] + e[u][5]) + (e[u][6] + e[u][7]));
    r[u] = __builtin_amdgcn_rcpf(s.x + s.y);
  }
#pragma unroll
  for (int u = 0; u < 2; ++u) {
    u32 w[8];
#pragma unroll
    for (int i = 0; i < 8; ++i) { const f32x2 t = e[u][i] * (f32x2){r[u], r[u]}; w[i] = pk2(t.x, t.y); }
    o[u][0] = (u32x4){w[0], w[1], w[2], w[3]};
    o[u][1] = (u32x4){w[4], w[5], w[6], w[7]};
  }
}
DI void softmax16x2(const f32x16& p, const f32x16& q, const float (&mc)[2], u32x4 (&o)[2][2]) {
  f32x2 e[2][8];
  sm_exp(p, q, mc, e);
  sm_finish(e, o);
}

#define STAMP(i) do { if ((i) == PROBE_N) { SCHED(); asm volatile("s_memrealtime %0\n\ts_waitcnt lgkmcnt(0)" : "=s"(pt1) :: "memory"); SCHED(); } } while (0)
#define STAMP_OUT() do { if (PROBE_N && (wave >> 2) == PROBE_ROLE) { unsigned long long d_ = pt1 - pt0; if (d_ > 100000) d_ = 100000; unsigned long long e_ = pt0 + 3000 + 8 * d_; \
  for (int it_ = 0; it_ < 1000000; ++it_) { unsigned long long t_; asm volatile("s_memrealtime %0\n\ts_waitcnt lgkmcnt(0)" : "=s"(t_) :: "memory"); if (t_ >= e_) break; } } } while (0)

__global__ __launch_bounds__(512, 2) void hedgehog(const u16* xA, const u16* wA, const u16* pA, const u16* oA,
                                                   const u16* xB, const u16* wB, const u16* pB, const u16* oB,
                                                   u16* out_, u32* ws_) {
  __shared__ __attribute__((aligned(16))) char smem[163840];
  const u32 lds0 = (u32)(size_t)(LAS char*)smem;
  unsigned long long pt0 = __builtin_amdgcn_s_memrealtime(), pt1 = pt0;
  const int bid = blockIdx.x, xcd = bid & 7, idx = bid >> 3;
  const int br = xcd >> 2;
#define KLOAD(tail)                                                                                       \
  u32x8 ka;                                                                                               \
  u32x4 kb;                                                                                               \
  asm volatile("s_load_dwordx8 %0, %2, %3\n\ts_load_dwordx4 %1, %2, 0x40" tail : "=&s"(ka), "=&s"(kb)     \
               : "s"(__builtin_amdgcn_kernarg_segment_ptr()), "s"(br * 32))
#define KWAIT() asm volatile("s_waitcnt lgkmcnt(0)" : "+s"(ka), "+s"(kb))
#define KPTR(v, i) ((void*)(((unsigned long long)(v)[(i) + 1] << 32) | (v)[i]))
  unsigned long long pcs;
  asm volatile("s_getpc_b64 %0" : "=s"(pcs));
  pcs &= ~255ull;
  const int lane = threadIdx.x & 63;
  const int wave = __builtin_amdgcn_readfirstlane(threadIdx.x >> 6);
  const int panel = (xcd & 3) * 4 + (idx & 3), grp = idx >> 2, gq = (grp + GQ) & 7, cg = (grp + GC) & 7;
  auto ochunk = [&](u32 j) -> u32 {
    const int v = ((int)j + (((int)j - 1 - ((gq - OFIRST) & 7)) >> 31) + OFIRST) & 7, z = ((int)j - 1) >> 31;
    return (u32)__builtin_amdgcn_readfirstlane((v & ~z) | (gq & z));
  };
  f32x16 zero;
#pragma unroll
  for (int i = 0; i < 16; ++i) zero[i] = 0.f;
#define LANE_IDS()                                                                            \
  u32 lane_ = lane;                                                                           \
  PIN(lane_);                                                                                 \
  const u32 r = lane_ & 31, h = lane_ >> 5;                                                   \
  const u32 blk = (lane_ >> 4) & 1, q = (lane_ & 15) >> 2, p = lane_ & 3;                     \
  u32* ws = (u32*)KPTR(kb, 2);                                                                \
  const __amdgpu_buffer_rsrc_t wsr = __builtin_amdgcn_make_buffer_rsrc(ws, 0, WS_END, SRD3);  \
  const __amdgpu_buffer_rsrc_t wor = __builtin_amdgcn_make_buffer_rsrc(KPTR(ka, 6), 0, 512 * 512 * 2, SRD3)

  if (wave >= 4) {
    const int cw = wave - 4;
    const int rh = cw >> 1, hp = cw & 1;
    KLOAD("\n\ts_waitcnt lgkmcnt(0)");
    u32 sp0, sp1, sp2, sp3;
    {
      u32 off = idx * 1024 + cw * 256;
      off = off < CODE_PF ? off : 0;
      asm volatile("s_load_dword %0, %4, %5 offset:0\n\ts_load_dword %1, %4, %5 offset:64\n\ts_load_dword %2, %4, %5 offset:128\n\ts_load_dword %3, %4, %5 offset:192"
                   : "=&s"(sp0), "=&s"(sp1), "=&s"(sp2), "=&s"(sp3) : "s"(pcs), "s"(off));
    }
    LANE_IDS();
    u32 key = *(const CAS u32*)((const char*)ws + WS_EPOCH + br * 256) + 1u;
    u32 xa[2], xb[2], wa[2], wb2[2];
    {
      const u32 r16 = lane_ & 15, g = lane_ >> 4;
#pragma unroll
      for (int s = 0; s < 2; ++s) {
        xa[s] = lds0 + (64 * rh + r16) * 128 + 16 * ((4 * s + g) ^ (r16 >> 1));
        wa[s] = lds0 + 16384 + (8 * g + q) * 128 + 16 * ((4 * hp + 2 * s + (p >> 1)) ^ ((((q >> 1) & 1) << 2) | (blk << 1))) + 8 * (p & 1);
        xb[s] = xa[s] + 2 * SLOT; wb2[s] = wa[s] + 2 * SLOT;
        PIN(xa[s]); PIN(xb[s]); PIN(wa[s]); PIN(wb2[s]);
      }
    }
    u32 fpa = lds0 + L_WPHI + hp * 1024 + (8 * h + q) * 32 + (8 * blk + 4 * (p >> 1)) * 2;
    PIN(fpa);
    PINS(key);
    f32x4 ak[4][2], av[4][2];
#pragma unroll
    for (int i = 0; i < 8; ++i) ak[i >> 1][i & 1] = av[i >> 1][i & 1] = (f32x4){0.f, 0.f, 0.f, 0.f};
    u32 vra, pra, vwa, pw0, pw1, pub16, pub4;
    u32x4 xf[2][4], wk[2][2], wv[2][2];
    auto ldx = [&](int c, int s) {
      const u32 x = c % 3 == 2 ? xb[s] : xa[s] + (c % 3) * SLOT;
#pragma unroll
      for (int i = 0; i < 4; ++i) xf[s][i] = lds_r128(x + i * 2048);
    };
    auto ldw = [&](int c, int s, int m) {
#pragma unroll
      for (int n = 0; n < 2; ++n) {
        const u32 w = (c % 3 == 2 ? wb2[n] : wa[n] + (c % 3) * SLOT) + s * 4096 + m * 8192;
        (m ? wv : wk)[s][n] = lds_tr2(w, w + 512);
      }
    };
    auto mk = [&](int s) {
#pragma unroll
      for (int i = 0; i < 8; ++i) ak[i >> 1][i & 1] = mfma16(wk[s][i & 1], xf[s][i >> 1], ak[i >> 1][i & 1]);
    };
    auto mv = [&](int s) {
#pragma unroll
      for (int i = 0; i < 8; ++i) av[i >> 1][i & 1] = mfma16(xf[s][i >> 1], wv[s][i & 1], av[i >> 1][i & 1]);
    };
    auto mix = [&](int n) {
#pragma unroll
      for (int i = 0; i < 8; ++i) {
        SGB(0x8, 1);
        if (n == 12) { if (i < 4) SGB(0x100, 2); else SGB(0x100, 1); }
        else if (n == 8) SGB(0x100, 1);
        else if (n == 4) { if (i < 4) SGB(0x100, 1); }
        else if (n < 0) SGB(0x6, 2);
      }
      SCHED();
    };
    BAR();
    asm volatile("" ::"s"(sp0), "s"(sp1), "s"(sp2), "s"(sp3));
    STAMP(1);
    ldw(0, 0, 0); ldx(0, 0); ldw(0, 0, 1);
    u32x4 fa[2];
#pragma unroll
    for (int hh = 0; hh < 2; ++hh) fa[hh] = lds_tr2(fpa + hh * 512, fpa + hh * 512 + 128);
    SCHED();
#pragma unroll
    for (int c = 0; c < 7; ++c) {
      ldw(c, 1, 0); ldx(c, 1); mk(0); mix(8);
      ldw(c, 1, 1); mv(0); mix(4);
      u32 ln = lane;
      PIN(ln);
      const u32 r_ = ln & 31, h_ = ln >> 5, q_ = (ln & 15) >> 2;
      if (c == 1) {
        const u32 sg = (ln & 4) ? 0x80008000u : 0u;
#pragma unroll
        for (int hh = 0; hh < 2; ++hh) { OPQ(fa[hh]); fa[hh] ^= (u32x4){sg, sg, sg, sg}; }
      }
      if (c == 2) vra = lds0 + L_V + (cw >> 1) * (2 * VF) + (r_ < 16 ? (ln + 16 * (cw & 1)) * 16 : 1024);
      if (c == 3) pra = lds0 + L_PHI + cw * 8192 + (4 * h_ + q_) * 64 + 16 * ((((ln >> 3) & 2) + ((ln >> 1) & 1)) ^ ((2 * h_ + (q_ >> 1)) & 3)) + 8 * (ln & 1);
      if (c == 4) {
        const u32 rec = WS_PART + ((br * 32 + grp * 4 + cw) * 16 + panel) * REC;
        pub16 = rec + ln * 16; pub4 = rec + 1024 + ln * 4;
      }
      if (c == 5) { pw0 = lds0 + L_PHI + (2 * hp) * 8192 + (64 * rh + r_) * 64 + 16 * ((2 * h_) ^ ((r_ >> 1) & 3)); pw1 = pw0 ^ 16; }
      if (c == 6) vwa = lds0 + L_V + (8 * rh + 2 * hp) * VF + ((ln & 16) * 2 + (ln & 15)) * 16 + h_ * 8;
      mk(1); mix(c >= 1 ? -1 : 0);
      if (c == 1) { PIN(fa[0]); PIN(fa[1]); }
      if (c == 2) PIN(vra);
      if (c == 3) PIN(pra);
      if (c == 4) { PIN(pub16); PIN(pub4); }
      if (c == 5) { PIN(pw0); PIN(pw1); }
      if (c == 6) PIN(vwa);
      BAR();
      ldw(c + 1, 0, 0); ldx(c + 1, 0); ldw(c + 1, 0, 1);
      mv(1); mix(12);
    }
    ldw(7, 1, 0); ldx(7, 1); ldw(7, 1, 1); mk(0); mix(12);
    mk(1); mix(0);
    __builtin_amdgcn_s_setprio(2);
    STAMP(2);
    f32x16 pk[2][2];
    {
      u32x4 kf[2][2];
      auto kpack = [&](int i) {
#pragma unroll
        for (int hh = 0; hh < 2; ++hh) {
          const f32x4 &t0 = ak[2 * i][hh], &t1 = ak[2 * i + 1][hh];
          const auto lo = __builtin_amdgcn_permlane16_swap(pk2(t0[0], t0[1]), pk2(t1[0], t1[1]), false, false);
          const auto hi = __builtin_amdgcn_permlane16_swap(pk2(t0[2], t0[3]), pk2(t1[2], t1[3]), false, false);
          kf[i][hh] = (u32x4){lo[0], hi[0], lo[1], hi[1]};
        }
      };
      mv(0); kpack(0);
      SGB(0x8, 2);
#pragma unroll
      for (int i = 0; i < 6; ++i) { SGB(0x2, 2); SGB(0x8, 1); }
      SCHED();
      pk[0][0] = mfma(fa[0], kf[0][0], zero);
      pk[0][1] = mfma(fa[1], kf[0][1], zero);
      kpack(1);
      SGB(0x8, 1); SGB(0x2, 6); SGB(0x8, 1); SGB(0x2, 6);
      PIN(pk[0][0]); PIN(pk[0][1]); PIN(kf[1][0]); PIN(kf[1][1]);
      SCHED();
      mv(1);
      pk[1][0] = mfma(fa[0], kf[1][0], zero);
      pk[1][1] = mfma(fa[1], kf[1][1], zero);
    }
    float m0[2], m1[2];
    m0[0] = negmax16(pk[0][0]); m0[1] = negmax16(pk[0][1]);
    SGB(0x8, 2);
#pragma unroll
    for (int i = 0; i < 6; ++i) { SGB(0x2, 2); SGB(0x8, 1); }
    SGB(0x8, 1); SGB(0x2, 6); SGB(0x8, 1);
#pragma unroll
    for (int i = 0; i < 8; ++i) PIN(av[i >> 1][i & 1]);
    PIN(pk[1][0]); PIN(pk[1][1]); PIN(m0[0]); PIN(m0[1]);
    SCHED();
#pragma unroll
    for (int i = 0; i < 8; ++i) {
      const f32x4& t = av[i >> 1][i & 1];
      *(LAS u32x2*)(size_t)(vwa + (4 * (i >> 2) + ((i >> 1) & 1)) * VF + (i & 1) * 256) = (u32x2){pk2(t[0], t[1]), pk2(t[2], t[3])};
    }
    SCHED();
    {
      u32x4 o[2][2];
      softmax16x2(pk[0][0], pk[0][1], m0, o);
      lds_w128(pw0, o[0][0]); lds_w128(pw1, o[0][1]);
      lds_w128(pw0 + 8192, o[1][0]); lds_w128(pw1 + 8192, o[1][1]);
      SCHED();
      m1[0] = negmax16(pk[1][0]); m1[1] = negmax16(pk[1][1]);
      PIN(m1[0]); PIN(m1[1]);
      SCHED();
      softmax16x2(pk[1][0], pk[1][1], m1, o);
      lds_w128(pw0 + 2048, o[0][0]); lds_w128(pw1 + 2048, o[0][1]);
      lds_w128(pw0 + 2048 + 8192, o[1][0]); lds_w128(pw1 + 2048 + 8192, o[1][1]);
      SGB(0x402, 26); SGB(0x200, 1); SGB(0x402, 8); SGB(0x200, 1); SGB(0x402, 8); SGB(0x200, 1); SGB(0x402, 8); SGB(0x200, 1);
      SCHED();
      BARE();
      BAR();
      SCHED();
    }
    STAMP(3);
    f32x16 kv = zero;
    {
      u32x4 a[8], b[8];
#pragma unroll
      for (int u = 0; u < 8; ++u) {
        a[u] = lds_r128(vra + ((u >> 1) * 4 + (u & 1)) * VF);
        b[u] = lds_tr2(pra + 16 * u * 64, pra + (16 * u + 8) * 64);
      }
#pragma unroll
      for (int u = 0; u < 8; ++u) kv = mfma(a[u], b[u], kv);
      SGB(0x100, 12); SGB(0x8, 1); SGB(0x100, 6); SGB(0x8, 1); SGB(0x100, 6); SGB(0x8, 6);
    }
    {
      const u32x4 w = pack8(kv, 0);
      st128c(w, wsr, pub16);
      const u32 zb = __float_as_uint(kv[8]);
      const u32 chk = key - pair_sum(sum4(h ? 0u : zb, w));
      st32c(h ? chk : zb, wsr, pub4);
    }
    SCHED();
    STAMP(4);
    u32 l16, l4, kwa, kza, gso[4], vo[4], wso;
    u32x4 sel;
    {
      u32 ln = lane;
      PIN(ln);
#pragma unroll
      for (int i = 0; i < 4; ++i) vo[i] = (ln >> 3) * 1024 + 16 * ((ln & 7) ^ (((ln >> 4) & 1) << 2)) + i * 7168;
#pragma unroll
      for (int i = 0; i < 2; ++i) dma4(lds0 + L_WO + cw * 8192 + i * 4096, vo, wor, ochunk(cw) * 65536 + i * 32768 + cg * 128);
      wso = ochunk(4 + cw) * 65536 + cg * 128;
      const u32 r_ = ln & 31, h_ = ln >> 5;
      l16 = ln * 16; l4 = ln * 4;
      kwa = lds0 + L_KV + cw * 1536 + r_ * 48 + h_ * 16; kza = lds0 + L_KV + cw * 1536 + r_ * 48 + 32 + h_ * 8;
      const bool hit = r_ < 16 && ((r_ >> 2) & 1) == h_;
      const u32 j = (r_ & 3) + 4 * (r_ >> 3);
#pragma unroll
      for (int e = 0; e < 4; ++e) sel[e] = (hit && (j >> 1) == e) ? ((j & 1) ? 0x3F800000u : 0x00003F80u) : 0u;
#pragma unroll
      for (int i = 0; i < 4; ++i) { gso[i] = WS_PART + ((br * 32 + gq * 4 + cw) * 16 + ((i + idx) & 3) * 4) * REC; PINS(gso[i]); }
      PIN(l16); PIN(l4); PIN(kwa); PIN(kza); PIN(sel); PINS(wso);
    }
    if (DELAY1) __builtin_amdgcn_s_sleep(DELAY1);
    STAMP(10);
    f32x16 gs[4];
    float z[4];
    {
      u32x4 g[16];
      u32 gz[16];
      auto gload = [&](int gi) {
#pragma unroll
        for (int i = 0; i < 4; ++i) {
          g[4 * gi + i] = __builtin_amdgcn_raw_buffer_load_b128(wsr, l16 + i * REC, gso[gi], AUX_SC1);
          gz[4 * gi + i] = __builtin_amdgcn_raw_buffer_load_b32(wsr, l4 + (1024 + i * REC - 1024 * (i == 3)), gso[gi] + 1024 * (i == 3), AUX_SC1);
          SCHED();
        }
      };
      auto gsum = [&](int gi) -> bool {
        u32 c = 0;
        if (GBURST) { PIN(gz[4 * gi + 3]); SCHED(); }
#pragma unroll
        for (int i = 0; i < 4; ++i) {
          c = sum4(c + gz[4 * gi + i], g[4 * gi + i]);
          gs[gi] = mfma(sel, g[4 * gi + i], i == 0 ? zero : gs[gi]);
          z[gi] = i == 0 ? __uint_as_float(gz[4 * gi]) : z[gi] + __uint_as_float(gz[4 * gi + i]);
        }
        const bool bad = __builtin_amdgcn_ballot_w64(pair_sum(c) != 4u * key) != 0;
        if (GBURST) { SGB(0x8, 1); SGB(0x2, 5); SGB(0x8, 1); SGB(0x2, 5); SGB(0x8, 1); SGB(0x2, 4); SGB(0x8, 1); }
        PIN(z[gi]);
        SCHED();
        return bad;
      };
      gload(0); gload(1); gload(2); gload(3);
      bool b0 = gsum(0), b1 = gsum(1), b2 = gsum(2), b3 = gsum(3);
      STAMP(11);
#pragma unroll 1
      for (u32 spin = 0; (b0 | b1 | b2 | b3) && spin < SPIN_CAP; ++spin) {
        asm volatile("" ::: "memory");
        if (b0) gload(0);
        if (b1) gload(1);
        if (b2) gload(2);
        if (b3) gload(3);
        if (b0) b0 = gsum(0);
        if (b1) b1 = gsum(1);
        if (b2) b2 = gsum(2);
        if (b3) b3 = gsum(3);
      }
    }
    __builtin_amdgcn_s_waitcnt(0x0F70);
    STAMP(5);
    {
      float tt[8];
#pragma unroll
      for (int j = 0; j < 8; ++j) tt[j] = (gs[0][j] + gs[1][j]) + (gs[2][j] + gs[3][j]);
      const float zt = (z[0] + z[1]) + (z[2] + z[3]);
      lds_w128(kwa, (u32x4){pk2(tt[0], tt[1]), pk2(tt[2], tt[3]), pk2(tt[4], tt[5]), pk2(tt[6], tt[7])});
      const u32 zz = pk2(zt, zt);
      *(LAS u32x2*)(size_t)kza = (u32x2){zz, zz};
    }
#pragma unroll
    for (int i = 0; i < 2; ++i) dma4(lds0 + L_WO + 32768 + cw * 8192 + i * 4096, vo, wor, wso + i * 32768);
    BAR();
    STAMP(6);
    {
      SCHED();
      u32 oso[8], ob = lds0 + L_O + cw * OREC, i0 = 0;
      PINS(i0);
#pragma unroll
      for (int i = 1; i < 8; ++i) { oso[i] = WS_O + ((br * 64 + panel * 4 + cw) * 8 + ochunk(i + i0)) * OREC; PINS(oso[i]); }
      PINS(ob);
      WAITV(0);
      BARE();
      if (DELAY3) __builtin_amdgcn_s_sleep(DELAY3);
      STAMP(8);
      auto oissue = [&](int j) { dma_orec(ob + (j % 5) * (4 * OREC), l4, l16, wsr, oso[j]); };
      oissue(1); oissue(2);
      WAITV(5); BARE(); oissue(3);
      WAITV(5); STAMP(15); BARE(); oissue(4); oissue(5);
      WAITV(10); BARE(); oissue(6);
      WAITV(10); BARE(); oissue(7);
      WAITV(10); BARE();
      WAITV(5); BARE();
      WAITV(0); BARE();
    }
    STAMP(9);
    STAMP_OUT();
  } else {
    KLOAD("");
    const int t = wave;
    const u32 l7 = lane & 7, l3 = lane >> 3, lq = lane >> 4;
    const u32 wsw = 16 * (l7 ^ ((lq & 1) << 2));
    u32 vx[4], vw[4];
#pragma unroll
    for (int i = 0; i < 4; ++i) {
      vx[i] = l3 * 1024 + 16 * (l7 ^ (((i & 1) << 2) | lq)) + i * 7168;
      vw[i] = l3 * 3072 + (wsw ^ ((i & 1) << 5)) + i * (8 * 3072 - 1024);
    }
    auto chunk = [&](u32 c) -> u32 {
      const u32 g = grp + idx;
      return c - 2 < 4 ? 2 + ((c - 2) ^ (g & 3)) : c ^ (g & 1);
    };
    const u32 xs0 = (panel * 128 + t * 32) * 1024, ws0 = (t & 1) * (32 * 3072) + (1 + (t >> 1)) * 1024 + grp * 128;
    const u32 ldst = lds0 + t * 4096;
    u32 e0 = ldst, e1 = ldst + 16384, e2 = xs0 + chunk(0) * 128, e3 = ws0 + chunk(0) * (64 * 3072);
    PINS(e0); PINS(e1); PINS(e2); PINS(e3);
#pragma unroll
    for (int i = 0; i < 4; ++i) { PIN(vx[i]); PIN(vw[i]); }
    KWAIT();
    const __amdgpu_buffer_rsrc_t xr = __builtin_amdgcn_make_buffer_rsrc(KPTR(ka, 0), 0, 2048 * 512 * 2, SRD3);
    const __amdgpu_buffer_rsrc_t wr = __builtin_amdgcn_make_buffer_rsrc(KPTR(ka, 2), 0, 512 * 1536 * 2, SRD3);
    STAMP(10);
    dma4(e0, vx, xr, e2);
    dma4(e1, vw, wr, e3);
    {
      const __amdgpu_buffer_rsrc_t pr = __builtin_amdgcn_make_buffer_rsrc(KPTR(ka, 4), 0, 32 * 256 * 2, SRD3);
      dma1(lds0 + L_WPHI + t * 1024, lane * 16, pr, (t < 2 ? grp : gq) * 2048 + (t & 1) * 1024);
    }
    STAMP(11);
    dma4(ldst + SLOT, vx, xr, xs0 + chunk(1) * 128);
    dma4(ldst + SLOT + 16384, vw, wr, ws0 + chunk(1) * (64 * 3072));
    STAMP(1);
    LANE_IDS();
    u32 key = *(const CAS u32*)((const char*)ws + WS_EPOCH + br * 256) + 1u;
    const u32 sw = (r >> 1) & 7;
    u32 xa[4];
#pragma unroll
    for (int s = 0; s < 4; ++s) { xa[s] = lds0 + (32 * t + r) * 128 + 16 * ((2 * s + h) ^ sw); PIN(xa[s]); }
    u32 fpa = lds0 + L_WPHI + 2048 + (4 * h + q) * 32 + (8 * blk + 4 * (p >> 1)) * 2;
    PIN(fpa);
    PINS(key);
    u32x4 xq[32], fa[4];
    u32 wa, wb, kra, o_off, c_off, woa, wob, vto, sto, tho;
#pragma unroll
    for (int c = 0; c < 8; ++c) {
      u32 d0, d1, s0, s1;
      if (c < 6) {
        d0 = ldst + ((c + 2) % 3) * SLOT; d1 = d0 + 16384;
        s0 = xs0 + chunk(c + 2) * 128; s1 = ws0 + chunk(c + 2) * (64 * 3072);
      } else {
        const u32 k0 = 2 * ((2 * (c - 6) + idx) & 3) + (t >> 1), k1 = (k0 + 2) & 7;
        d0 = lds0 + L_WQ1 + ((chunk(k0) + 4) & 7) * 8192 + (t & 1) * 4096; d1 = lds0 + L_WQ1 + ((chunk(k1) + 4) & 7) * 8192 + (t & 1) * 4096;
        s0 = (k0 * 64 + (t & 1) * 32) * 3072 + gq * 128; s1 = (k1 * 64 + (t & 1) * 32) * 3072 + gq * 128;
      }
      PINS(d0); PINS(d1); PINS(s0); PINS(s1);
      WAITV(8);
      BAR();
      if (c < 6) dma4(d0, vx, xr, s0); else dma4(d0, vw, wr, s0);
#pragma unroll
      for (int s = 0; s < 4; ++s) xq[4 * c + s] = lds_r128(xa[s] + (c % 3) * SLOT);
      dma4(d1, vw, wr, s1);
      if (c == 0) {
#pragma unroll
        for (int hd = 0; hd < 4; ++hd) fa[hd] = lds_tr2(fpa + hd * 512, fpa + hd * 512 + 256);
      }
      if (c == 1) {
        lds_w128(lds0 + L_V + (lane & 15) * VF + 1024, (u32x4){0x3F803F80u, 0x3F803F80u, 0x3F803F80u, 0x3F803F80u});
        const u32 sg = (lane & 4) ? 0x80008000u : 0u;
#pragma unroll
        for (int hd = 0; hd < 4; ++hd) { PIN(fa[hd]); fa[hd] ^= (u32x4){sg, sg, sg, sg}; PIN(fa[hd]); }
        wa = lds0 + L_WQ1 + (8 * h + q) * 128 + 16 * ((2 * blk + (p >> 1)) ^ ((((q >> 1) & 1) << 2) | (h << 1))) + 8 * (p & 1);
        wb = wa ^ 64;
        kra = lds0 + L_KV + (16 * h + q) * 48 + (blk ? 32 : 8 * p);
        o_off = WS_O + ((br * 64 + panel * 4 + t) * 8 + gq) * OREC + 256 + lane * 16;
        c_off = WS_O + ((br * 64 + panel * 4 + t) * 8 + gq) * OREC + lane * 4;
        woa = lds0 + L_WO + (8 * h + q) * 128 + 16 * ((2 * blk + (p & 1)) ^ (((q >> 1) & 1) << 2)) + 8 * (p >> 1);
        wob = woa ^ 64;
        vto = r * 128; sto = (idx * 16 + t * 4) * 1024;
        PIN(wa); PIN(wb); PIN(kra); PIN(o_off); PIN(c_off); PIN(woa); PIN(wob); PIN(vto); PINS(sto);
      }
    }
    TOUCH(tho, vto, wor, sto);
    STAMP(2);
    u32x4 fq[2][2][2];
    {
      f32x16 aq[2], pq[2];
      float mq[2];
      aq[0] = aq[1] = zero;
      u32x4 wf[4];
      auto ldw = [&](int n) {
        const int k = n & 31;
        const u32 o = (n < 32 ? wa : wb) + (k < 16 ? (L_WQ0 - L_WQ1) : 0) + (k & 15) * 2048;
        wf[n & 3] = lds_tr2(o, o + 512);
      };
      WAITV(1);
      BAR();
      SCHED();
      STAMP(12);
      ldw(0); ldw(1); ldw(2);
      SCHED();
      BARE();
      SCHED();
#pragma unroll
      for (int n = 0; n < 34; ++n) {
        ldw(n + 3);
        aq[n >> 5] = mfma(wf[n & 3], xq[n & 31], aq[n >> 5]);
        SGB(0x8, 1); SGB(0x100, 2);
        SCHED();
      }
#pragma unroll
      for (int hh = 0; hh < 2; ++hh) pq[hh] = mfma(fa[hh], pack8(aq[0], hh), zero);
      SCHED();
      STAMP(4);
#pragma unroll
      for (int n = 34; n < 64; ++n) {
        if (n + 3 < 64) ldw(n + 3);
        aq[1] = mfma(wf[n & 3], xq[n & 31], aq[1]);
      }
      mq[0] = negmax16(pq[0]); mq[1] = negmax16(pq[1]);
      f32x2 eq[2][8];
      sm_exp_s(pq[0], pq[1], mq, eq);
      SGB(0x8, 1); SGB(0x100, 2); SGB(0x8, 1); SGB(0x100, 2);
#pragma unroll
      for (int n = 36; n < 41; ++n) { SGB(0x8, 1); SGB(0x100, 2); SGB(0x402, 4); }
#pragma unroll
      for (int n = 41; n < 64; ++n) { SGB(0x8, 1); SGB(0x100, 2); if (n & 1) SGB(0x402, 3); else SGB(0x402, 2); }
#pragma unroll
      for (int i = 0; i < 16; ++i) PIN(eq[i >> 3][i & 7]);
      SCHED();
      STAMP(3);
#pragma unroll
      for (int hh = 0; hh < 2; ++hh) { pq[hh] = mfma(fa[2 + hh], pack8(aq[1], hh), zero); PIN(pq[hh]); }
      SCHED();
      sm_finish(eq, fq[0]);
      SCHED();
      asm volatile("s_waitcnt vmcnt(0)" : "+v"(tho)::"memory");
      mq[0] = negmax16(pq[0]); mq[1] = negmax16(pq[1]);
      PIN(mq[0]); PIN(mq[1]);
      SCHED();
#pragma unroll
      for (int i = 0; i < 4; ++i) PIN(fq[0][i >> 1][i & 1]);
      softmax16x2(pq[0], pq[1], mq, fq[1]);
#pragma unroll
      for (int i = 0; i < 4; ++i) PIN(fq[1][i >> 1][i & 1]);
    }
    STAMP(5);
    BAR();
    STAMP(6);
    u32x4 ob[2][4];
    {
      u32x4 a0[4], a1[4];
#pragma unroll
      for (int hd = 0; hd < 4; ++hd) {
        a0[hd] = lds_tr2(kra + hd * 1536, kra + hd * 1536 + 4 * 48);
        a1[hd] = lds_tr2(kra + hd * 1536 + 8 * 48, kra + hd * 1536 + 12 * 48);
      }
      SCHED();
      f32x16 y[4];
      u32 cs = 0;
      auto num = [&](int hd) {
        y[hd] = mfma(a0[hd], fq[hd >> 1][hd & 1][0], zero);
        y[hd] = mfma(a1[hd], fq[hd >> 1][hd & 1][1], y[hd]);
      };
      auto fin = [&](int hd) {
        const f32x16& yy = y[hd];
        const float rd = __builtin_amdgcn_rcpf(yy[8] + 1e-6f);
        u32 w[4];
#pragma unroll
        for (int e = 0; e < 4; ++e) { float t0 = yy[2 * e] * rd, t1 = yy[2 * e + 1] * rd; if (hd < 2) OPQ(t0); w[e] = pk2(t0, t1); }
        ob[0][hd] = (u32x4){w[0], w[1], w[2], w[3]};
        st128c(ob[0][hd], wsr, o_off + hd * 1024);
        cs = sum4(cs, ob[0][hd]);
      };
      num(0); num(1); PIN(y[0]); PIN(y[1]); SCHED();
      num(2); fin(0); SGB(0x8, 1); SGB(0x402, 6); SGB(0x8, 1); PIN(y[2]); SCHED();
      num(3); fin(1); SGB(0x8, 1); SGB(0x402, 6); SGB(0x8, 1); PIN(y[3]); SCHED();
      fin(2); fin(3);
      st32c(key - cs, wsr, c_off);
    }
    SCHED();
    STAMP(7);
    u32x4 wo[32][2];
    auto ldwo = [&](int n) {
      wo[n][0] = lds_tr2(woa + n * 2048, woa + n * 2048 + 512);
      wo[n][1] = lds_tr2(wob + n * 2048, wob + n * 2048 + 512);
    };
#pragma unroll
    for (int n = 0; n < 16; ++n) ldwo(n);
    f32x16 c0 = zero, c1 = zero;
#pragma unroll
    for (int s = 0; s < 4; ++s) { c0 = mfma(wo[s][0], ob[0][s], c0); c1 = mfma(wo[s][1], ob[0][s], c1); }
#pragma unroll
    for (int k = 4; k < 16; ++k) { PIN(wo[k][0]); PIN(wo[k][1]); }
    PIN(c0); PIN(c1);
    WAITV(0);
    BARE();
    SCHED();
#pragma unroll
    for (int n = 16; n < 24; ++n) ldwo(n);
#pragma unroll
    for (int k = 16; k < 24; ++k) { PIN(wo[k][0]); PIN(wo[k][1]); }
    const __amdgpu_buffer_rsrc_t outr = __builtin_amdgcn_make_buffer_rsrc(KPTR(kb, 0), 0, 2048 * 1024 * 2, SRD3);
    u32 oa = lds0 + L_O + t * OREC + 256 + lane * 16;
    u32 orow = ((panel * 128 + t * 32 + l3) * 1024 + br * 512 + cg * 64 + 8 * l7) * 2;
    PIN(oa); PIN(orow);
    u32 oc = oa - 256 - lane * 12, l4 = lane * 4, l16 = lane * 16, oa4 = oa + 4 * (4 * OREC), oc4 = oc + 4 * (4 * OREC);
    u32 sw1 = lds0 + t * 4096 + r * 128 + 16 * (h ^ (r & 7)), sr1 = lds0 + t * 4096 + l3 * 128 + 16 * (l7 ^ l3);
    u32 sw2 = sw1 ^ 32, sw3 = sw1 ^ 64, sw4 = sw1 ^ 96, or1 = orow + 16384, or2 = orow + 32768, or3 = orow + 49152;
    PIN(oc); PIN(l4); PIN(l16); PIN(sw1); PIN(sr1); PIN(oa4); PIN(oc4); PIN(sw2); PIN(sw3); PIN(sw4); PIN(or1); PIN(or2); PIN(or3);
    STAMP(8);
    u32 nretry = 0;
    u32 chk[2];
    auto oread = [&](int j) {
      const u32 da = j % 5 == 4 ? oa4 : oa + (j % 5) * (4 * OREC), dc = j % 5 == 4 ? oc4 : oc + (j % 5) * (4 * OREC);
#pragma unroll
      for (int s = 0; s < 4; ++s) ob[j & 1][s] = lds_r128(da + s * 1024);
      chk[j & 1] = *(LAS u32*)(size_t)dc;
    };
    auto obad = [&](int j) -> bool {
      const u32 c = sum4(sum4(chk[j & 1], ob[j & 1][0]), ob[j & 1][1]) + sum4(sum4(0u, ob[j & 1][2]), ob[j & 1][3]);
      return __builtin_amdgcn_ballot_w64(c != key) != 0;
    };
#pragma unroll
    for (int j = 1; j < 8; ++j) {
      BARE();
      SCHED();
      oread(j);
      if (j >= 2) {
#pragma unroll
        for (int s = 0; s < 4; ++s) {
          const int k = 4 * (j - 1) + s;
          c0 = mfma(wo[k][0], ob[(j - 1) & 1][s], c0);
          c1 = mfma(wo[k][1], ob[(j - 1) & 1][s], c1);
        }
        if (j >= 3 && j <= 6) { ldwo(24 + 2 * (j - 3)); ldwo(25 + 2 * (j - 3)); }
      }
      const bool bad = obad(j);
      if (j >= 3 && j <= 6) {
        SGB(0x8, 1); SGB(0x100, 3); SGB(0x8, 1); SGB(0x100, 2); SGB(0x8, 1); SGB(0x100, 3); SGB(0x8, 1); SGB(0x100, 3);
        SGB(0x8, 1); SGB(0x2, 4); SGB(0x8, 1); SGB(0x2, 5); SGB(0x8, 1); SGB(0x100, 2); SGB(0x8, 1);
      } else if (j >= 2) {
        SGB(0x8, 1); SGB(0x100, 3); SGB(0x8, 1); SGB(0x100, 2); SGB(0x8, 3); SGB(0x2, 4); SGB(0x8, 1); SGB(0x2, 5); SGB(0x8, 2);
      }
      SCHED();
      if (__builtin_expect(bad, 0)) {
#pragma unroll 1
        for (u32 spin = 0; spin < SPIN_CAP; ++spin) {
          dma_orec(lds0 + L_O + t * OREC + (j % 5) * (4 * OREC), l4, l16, wsr, WS_O + ((br * 64 + panel * 4 + t) * 8 + ochunk(j)) * OREC);
          u32 jj = j + 1, jend = j + AHEAD < 7 ? j + AHEAD : 7;
          PINS(jj); PINS(jend);
#pragma unroll 1
          for (; jj <= jend; ++jj) {
            const u32 sl = (jj >= 5 ? jj - 5 : jj) * (4 * OREC);
            u32 c = *(LAS u32*)(size_t)(oc + sl);
#pragma unroll
            for (int s = 0; s < 4; ++s) c = sum4(c, lds_r128(oa + sl + s * 1024));
            if (__builtin_amdgcn_ballot_w64(c != key) != 0)
              dma_orec(lds0 + L_O + t * OREC + sl, l4, l16, wsr, WS_O + ((br * 64 + panel * 4 + t) * 8 + ochunk(jj)) * OREC);
          }
          WAITV(0);
          oread(j);
          ++nretry;
          if (!obad(j)) break;
        }
      }
    }
#pragma unroll
    for (int s = 0; s < 4; ++s) c0 = mfma(wo[28 + s][0], ob[1][s], c0);
    PIN(c0);
    SCHED();
#pragma unroll
    for (int s = 0; s < 4; ++s) c1 = mfma(wo[28 + s][1], ob[1][s], c1);
    lds_w128(sw1, pack8(c0, 0));
    lds_w128(sw2, pack8(c0, 1));
    SGB(0x8, 2); SGB(0x2, 4); SGB(0x200, 1); SGB(0x8, 1); SGB(0x2, 4); SGB(0x200, 1); SGB(0x8, 1);
    SCHED();
    STAMP(9);
    lds_w128(sw3, pack8(c1, 0));
    lds_w128(sw4, pack8(c1, 1));
    asm volatile("" ::: "memory");
    {
      u32x4 v[4];
#pragma unroll
      for (int i = 0; i < 4; ++i) v[i] = lds_r128(sr1 + i * 1024);
      SCHED();
      st128c(v[0], outr, orow);
      st128c(v[1], outr, or1);
      st128c(v[2], outr, or2);
      st128c(v[3], outr, or3);
    }
    if (t == 0 && panel == 0 && grp == 0 && lane == 0) st32c(key, wsr, WS_EPOCH + br * 256);
    if (PROBE_N == 13) WAITV(0);
    STAMP(13);
    STAMP_OUT();
  }
}
}

extern "C" void kernel_launch(void* const* d_in, const int* in_sizes, int n_in,
                              void* d_out, int out_size, void* d_ws, size_t ws_size,
                              hipStream_t stream) {
  hh::hedgehog<<<256, 512, 0, stream>>>((const u16*)d_in[0], (const u16*)d_in[1], (const u16*)d_in[2], (const u16*)d_in[3],
                                        (const u16*)d_in[4], (const u16*)d_in[5], (const u16*)d_in[6], (const u16*)d_in[7],
                                        (u16*)d_out, (u32*)d_ws);
}
```

```cpp
#include <hip/hip_runtime.h>
#include <stdint.h>

typedef unsigned short u16;
typedef unsigned u32;
typedef short bf16x8 __attribute__((ext_vector_type(8)));
typedef short s16x4 __attribute__((ext_vector_type(4)));
typedef float f32x16 __attribute__((ext_vector_type(16)));
typedef float f32x4 __attribute__((ext_vector_type(4)));
typedef float f32x2 __attribute__((ext_vector_type(2)));
typedef unsigned u32x8 __attribute__((ext_vector_type(8)));
typedef unsigned u32x4 __attribute__((ext_vector_type(4)));
typedef unsigned u32x2 __attribute__((ext_vector_type(2)));
typedef __bf16 bf16x2 __attribute__((ext_vector_type(2)));
#define DI __device__ __forceinline__
#define LAS __attribute__((address_space(3)))
#define CAS __attribute__((address_space(4)))
#define BC(T, x) __builtin_bit_cast(T, x)

namespace hh {
constexpr int AUX_SC1 = 16;
constexpr u32 SRD3 = 0x00020000u;
constexpr u32 WS_EPOCH = 0;
constexpr u32 REC = 1280;
constexpr u32 WS_PART = 4096;
constexpr u32 OREC = 4352;
constexpr u32 WS_O = WS_PART + 2 * 32 * 16 * REC;
constexpr u32 WS_END = WS_O + 2 * 64 * 8 * OREC;
constexpr u32 SLOT = 32768;
constexpr u32 L_WQ0 = 98304;
constexpr u32 L_WQ1 = 65536;
constexpr u32 L_WPHI = 163840 - 4096;
constexpr u32 L_PHI = 0;
constexpr u32 VF = 1040;
constexpr u32 L_V = 131072;
constexpr u32 L_KV = L_V + 16 * VF;
constexpr u32 L_WO = 0;
constexpr u32 L_O = 65536;
constexpr u32 SPIN_CAP = 1u << 16;
constexpr int DELAY1 = 3;
constexpr int DELAY3 = 4;
constexpr u32 CODE_PF = 29184;
constexpr int AHEAD = 1;
constexpr int GQ = 1, GC = 2, OFIRST = 5;

DI u32 pk2(float a, float b) { return BC(u32, __builtin_convertvector((f32x2){a, b}, bf16x2)); }
DI u32x4 pack8(const f32x16& x, int s) {
  return (u32x4){pk2(x[8 * s], x[8 * s + 1]), pk2(x[8 * s + 2], x[8 * s + 3]), pk2(x[8 * s + 4], x[8 * s + 5]), pk2(x[8 * s + 6], x[8 * s + 7])};
}
DI f32x16 mfma(u32x4 a, u32x4 b, f32x16 c) { return __builtin_amdgcn_mfma_f32_32x32x16_bf16(BC(bf16x8, a), BC(bf16x8, b), c, 0, 0, 0); }
DI f32x4 mfma16(u32x4 a, u32x4 b, f32x4 c) { return __builtin_amdgcn_mfma_f32_16x16x32_bf16(BC(bf16x8, a), BC(bf16x8, b), c, 0, 0, 0); }
DI u32x4 lds_r128(u32 a) { return *(LAS u32x4*)(size_t)a; }
DI void lds_w128(u32 a, u32x4 v) { *(LAS u32x4*)(size_t)a = v; }
DI u32x4 lds_tr2(u32 a0, u32 a1) {
  s16x4 lo = __builtin_amdgcn_ds_read_tr16_b64_v4i16((LAS s16x4*)(size_t)a0);
  s16x4 hi = __builtin_amdgcn_ds_read_tr16_b64_v4i16((LAS s16x4*)(size_t)a1);
  return BC(u32x4, __builtin_shufflevector(lo, hi, 0, 1, 2, 3, 4, 5, 6, 7));
}
DI void dma1(u32 lds, u32 voff, __amdgpu_buffer_rsrc_t srd, u32 soff) {
  asm volatile("s_mov_b32 m0, %0\n\ts_nop 0\n\tbuffer_load_dwordx4 %1, %2, %3 offen lds" ::"s"(lds), "v"(voff), "s"(srd), "s"(soff) : "memory");
}
DI void dma4(u32 lds, const u32 (&v)[4], __amdgpu_buffer_rsrc_t srd, u32 soff) {
  asm volatile("s_mov_b32 m0, %0\n\ts_nop 0\n\t"
               "buffer_load_dwordx4 %1, %5, %6 offen lds\n\t"
               "buffer_load_dwordx4 %2, %5, %6 offen offset:1024 lds\n\t"
               "buffer_load_dwordx4 %3, %5, %6 offen offset:2048 lds\n\t"
               "buffer_load_dwordx4 %4, %5, %6 offen offset:3072 lds" ::"s"(lds), "v"(v[0]), "v"(v[1]), "v"(v[2]), "v"(v[3]), "s"(srd), "s"(soff) : "memory");
}
DI void dma_orec(u32 lds, u32 l4, u32 l16, __amdgpu_buffer_rsrc_t srd, u32 soff) {
  asm volatile("s_mov_b32 m0, %0\n\ts_nop 0\n\t"
               "buffer_load_dwordx4 %2, %3, %4 offen offset:256 sc1 lds\n\t"
               "buffer_load_dwordx4 %2, %3, %4 offen offset:1280 sc1 lds\n\t"
               "buffer_load_dwordx4 %2, %3, %4 offen offset:2304 sc1 lds\n\t"
               "buffer_load_dwordx4 %2, %3, %4 offen offset:3328 sc1 lds\n\t"
               "buffer_load_dword %1, %3, %4 offen sc1 lds" ::"s"(lds), "v"(l4), "v"(l16), "s"(srd), "s"(soff) : "memory");
}
#define TOUCH(d, voff, srd, soff) asm volatile("buffer_load_dword %0, %1, %2, %3 offen" : "=v"(d) : "v"(voff), "s"(srd), "s"(soff) : "memory")
DI void st128c(u32x4 v, __amdgpu_buffer_rsrc_t srd, u32 off) { __builtin_amdgcn_raw_buffer_store_b128(v, srd, off, 0, AUX_SC1); }
DI void st32c(u32 v, __amdgpu_buffer_rsrc_t srd, u32 off) { __builtin_amdgcn_raw_buffer_store_b32(v, srd, off, 0, AUX_SC1); }
#define BAR() asm volatile("s_waitcnt lgkmcnt(0)\n\ts_barrier" ::: "memory")
#define BARE() asm volatile("s_barrier" ::: "memory")
#define OPQ(x) asm("" : "+v"(x))
#define PIN(x) asm volatile("" : "+v"(x))
#define PINS(x) asm volatile("" : "+s"(x))
#define WAITV(n) asm volatile("s_waitcnt vmcnt(" #n ")" ::: "memory")
#define SCHED() __builtin_amdgcn_sched_barrier(0)
#define SGB(m, n) __builtin_amdgcn_sched_group_barrier(m, n, 0)
DI u32 sum4(u32 c, u32x4 v) { return (c + v.x + v.y) + v.z + v.w; }
DI u32 pair_sum(u32 s) { auto t = __builtin_amdgcn_permlane32_swap(s, s, false, false); return t[0] + t[1]; }

constexpr float LOG2E = 1.44269504088896340736f;
DI float max3f(float a, float b, float c) { return __builtin_fmaxf(__builtin_fmaxf(a, b), c); }
DI float negmax16(const f32x16& x) {
  const float a = max3f(x[0], x[1], x[2]), b = max3f(x[3], x[4], x[5]), c = max3f(x[6], x[7], x[8]);
  const float d = max3f(x[9], x[10], x[11]), e = max3f(x[12], x[13], x[14]);
  return -max3f(max3f(a, b, x[15]), max3f(c, d, e), a) * LOG2E;
}
DI void sm_exp(const f32x16& p, const f32x16& q, const float (&mc)[2], f32x2 (&e)[2][8]) {
#pragma unroll
  for (int i = 0; i < 8; ++i) {
    const f32x2 t = __builtin_elementwise_fma((f32x2){p[2 * i], p[2 * i + 1]}, (f32x2){LOG2E, LOG2E}, (f32x2){mc[0], mc[0]});
    const f32x2 u = __builtin_elementwise_fma((f32x2){q[2 * i], q[2 * i + 1]}, (f32x2){LOG2E, LOG2E}, (f32x2){mc[1], mc[1]});
    e[0][i] = (f32x2){__builtin_amdgcn_exp2f(t.x), __builtin_amdgcn_exp2f(t.y)};
    e[1][i] = (f32x2){__builtin_amdgcn_exp2f(u.x), __builtin_amdgcn_exp2f(u.y)};
  }
  SGB(0x2, 4); SGB(0x400, 8); SGB(0x2, 4); SGB(0x400, 8); SGB(0x2, 6); SGB(0x400, 8); SGB(0x2, 8); SGB(0x400, 8);
}
DI void sm_exp_s(const f32x16& p, const f32x16& q, const float (&mc)[2], f32x2 (&e)[2][8]) {
#pragma unroll
  for (int i = 0; i < 16; ++i) {
    e[0][i >> 1][i & 1] = __builtin_amdgcn_exp2f(__builtin_fmaf(p[i], LOG2E, mc[0]));
    e[1][i >> 1][i & 1] = __builtin_amdgcn_exp2f(__builtin_fmaf(q[i], LOG2E, mc[1]));
  }
}
DI void sm_finish(const f32x2 (&e)[2][8], u32x4 (&o)[2][2]) {
  float r[2];
#pragma unroll
  for (int u = 0; u < 2; ++u) {
    const f32x2 s = ((e[u][0] + e[u][1]) + (e[u][2] + e[u][3])) + ((e[u][4] + e[u][5]) + (e[u][6] + e[u][7]));
    r[u] = __builtin_amdgcn_rcpf(s.x + s.y);
  }
#pragma unroll
  for (int u = 0; u < 2; ++u) {
    u32 w[8];
#pragma unroll
    for (int i = 0; i < 8; ++i) { const f32x2 t = e[u][i] * (f32x2){r[u], r[u]}; w[i] = pk2(t.x, t.y); }
    o[u][0] = (u32x4){w[0], w[1], w[2], w[3]};
    o[u][1] = (u32x4){w[4], w[5], w[6], w[7]};
  }
}
DI void softmax16x2(const f32x16& p, const f32x16& q, const float (&mc)[2], u32x4 (&o)[2][2]) {
  f32x2 e[2][8];
  sm_exp(p, q, mc, e);
  sm_finish(e, o);
}

__global__ __launch_bounds__(512, 2) void hedgehog(const u16* xA, const u16* wA, const u16* pA, const u16* oA,
                                                   const u16* xB, const u16* wB, const u16* pB, const u16* oB,
                                                   u16* out_, u32* ws_) {
  __shared__ __attribute__((aligned(16))) char smem[163840];
  const u32 lds0 = (u32)(size_t)(LAS char*)smem;
  const int bid = blockIdx.x, xcd = bid & 7, idx = bid >> 3;
  const int br = xcd >> 2;
#define KLOAD(tail)                                                                                       \
  u32x8 ka;                                                                                               \
  u32x4 kb;                                                                                               \
  asm volatile("s_load_dwordx8 %0, %2, %3\n\ts_load_dwordx4 %1, %2, 0x40" tail : "=&s"(ka), "=&s"(kb)     \
               : "s"(__builtin_amdgcn_kernarg_segment_ptr()), "s"(br * 32))
#define KWAIT() asm volatile("s_waitcnt lgkmcnt(0)" : "+s"(ka), "+s"(kb))
#define KPTR(v, i) ((void*)(((unsigned long long)(v)[(i) + 1] << 32) | (v)[i]))
  unsigned long long pcs;
  asm volatile("s_getpc_b64 %0" : "=s"(pcs));
  pcs &= ~255ull;
  const int lane = threadIdx.x & 63;
  const int wave = __builtin_amdgcn_readfirstlane(threadIdx.x >> 6);
  const int panel = (xcd & 3) * 4 + (idx & 3), grp = idx >> 2, gq = (grp + GQ) & 7, cg = (grp + GC) & 7;
  auto ochunk = [&](u32 j) -> u32 {
    const int v = ((int)j + (((int)j - 1 - ((gq - OFIRST) & 7)) >> 31) + OFIRST) & 7, z = ((int)j - 1) >> 31;
    return (u32)__builtin_amdgcn_readfirstlane((v & ~z) | (gq & z));
  };
  f32x16 zero;
#pragma unroll
  for (int i = 0; i < 16; ++i) zero[i] = 0.f;
#define LANE_IDS()                                                                            \
  u32 lane_ = lane;                                                                           \
  PIN(lane_);                                                                                 \
  const u32 r = lane_ & 31, h = lane_ >> 5;                                                   \
  const u32 blk = (lane_ >> 4) & 1, q = (lane_ & 15) >> 2, p = lane_ & 3;                     \
  u32* ws = (u32*)KPTR(kb, 2);                                                                \
  const __amdgpu_buffer_rsrc_t wsr = __builtin_amdgcn_make_buffer_rsrc(ws, 0, WS_END, SRD3);  \
  const __amdgpu_buffer_rsrc_t wor = __builtin_amdgcn_make_buffer_rsrc(KPTR(ka, 6), 0, 512 * 512 * 2, SRD3)

  if (wave >= 4) {
    const int cw = wave - 4;
    const int rh = cw >> 1, hp = cw & 1;
    KLOAD("\n\ts_waitcnt lgkmcnt(0)");
    u32 sp0, sp1, sp2, sp3;
    {
      u32 off = idx * 1024 + cw * 256;
      off = off < CODE_PF ? off : 0;
      asm volatile("s_load_dword %0, %4, %5 offset:0\n\ts_load_dword %1, %4, %5 offset:64\n\ts_load_dword %2, %4, %5 offset:128\n\ts_load_dword %3, %4, %5 offset:192"
                   : "=&s"(sp0), "=&s"(sp1), "=&s"(sp2), "=&s"(sp3) : "s"(pcs), "s"(off));
    }
    LANE_IDS();
    u32 key = *(const CAS u32*)((const char*)ws + WS_EPOCH + br * 256) + 1u;
    u32 xa[2], xb[2], wa[2], wb2[2];
    {
      const u32 r16 = lane_ & 15, g = lane_ >> 4;
#pragma unroll
      for (int s = 0; s < 2; ++s) {
        xa[s] = lds0 + (64 * rh + r16) * 128 + 16 * ((4 * s + g) ^ (r16 >> 1));
        wa[s] = lds0 + 16384 + (8 * g + q) * 128 + 16 * ((4 * hp + 2 * s + (p >> 1)) ^ ((((q >> 1) & 1) << 2) | (blk << 1))) + 8 * (p & 1);
        xb[s] = xa[s] + 2 * SLOT; wb2[s] = wa[s] + 2 * SLOT;
        PIN(xa[s]); PIN(xb[s]); PIN(wa[s]); PIN(wb2[s]);
      }
    }
    u32 fpa = lds0 + L_WPHI + hp * 1024 + (8 * h + q) * 32 + (8 * blk + 4 * (p >> 1)) * 2;
    PIN(fpa);
    PINS(key);
    f32x4 ak[4][2], av[4][2];
#pragma unroll
    for (int i = 0; i < 8; ++i) ak[i >> 1][i & 1] = av[i >> 1][i & 1] = (f32x4){0.f, 0.f, 0.f, 0.f};
    u32 vra, pra, vwa, pw0, pw1, pub16, pub4;
    u32x4 xf[2][4], wk[2][2], wv[2][2];
    auto ldx = [&](int c, int s) {
      const u32 x = c % 3 == 2 ? xb[s] : xa[s] + (c % 3) * SLOT;
#pragma unroll
      for (int i = 0; i < 4; ++i) xf[s][i] = lds_r128(x + i * 2048);
    };
    auto ldw = [&](int c, int s, int m) {
#pragma unroll
      for (int n = 0; n < 2; ++n) {
        const u32 w = (c % 3 == 2 ? wb2[n] : wa[n] + (c % 3) * SLOT) + s * 4096 + m * 8192;
        (m ? wv : wk)[s][n] = lds_tr2(w, w + 512);
      }
    };
    auto mk = [&](int s) {
#pragma unroll
      for (int i = 0; i < 8; ++i) ak[i >> 1][i & 1] = mfma16(wk[s][i & 1], xf[s][i >> 1], ak[i >> 1][i & 1]);
    };
    auto mv = [&](int s) {
#pragma unroll
      for (int i = 0; i < 8; ++i) av[i >> 1][i & 1] = mfma16(xf[s][i >> 1], wv[s][i & 1], av[i >> 1][i & 1]);
    };
    auto mix = [&](int n) {
#pragma unroll
      for (int i = 0; i < 8; ++i) {
        SGB(0x8, 1);
        if (n == 12) { if (i < 4) SGB(0x100, 2); else SGB(0x100, 1); }
        else if (n == 8) SGB(0x100, 1);
        else if (n == 4) { if (i < 4) SGB(0x100, 1); }
        else if (n < 0) SGB(0x6, 2);
      }
      SCHED();
    };
    BAR();
    asm volatile("" ::"s"(sp0), "s"(sp1), "s"(sp2), "s"(sp3));
    ldw(0, 0, 0); ldx(0, 0); ldw(0, 0, 1);
    u32x4 fa[2];
#pragma unroll
    for (int hh = 0; hh < 2; ++hh) fa[hh] = lds_tr2(fpa + hh * 512, fpa + hh * 512 + 128);
    SCHED();
#pragma unroll
    for (int c = 0; c < 7; ++c) {
      ldw(c, 1, 0); ldx(c, 1); mk(0); mix(8);
      ldw(c, 1, 1); mv(0); mix(4);
      u32 ln = lane;
      PIN(ln);
      const u32 r_ = ln & 31, h_ = ln >> 5, q_ = (ln & 15) >> 2;
      if (c == 1) {
        const u32 sg = (ln & 4) ? 0x80008000u : 0u;
#pragma unroll
        for (int hh = 0; hh < 2; ++hh) { OPQ(fa[hh]); fa[hh] ^= (u32x4){sg, sg, sg, sg}; }
      }
      if (c == 2) vra = lds0 + L_V + (cw >> 1) * (2 * VF) + (r_ < 16 ? (ln + 16 * (cw & 1)) * 16 : 1024);
      if (c == 3) pra = lds0 + L_PHI + cw * 8192 + (4 * h_ + q_) * 64 + 16 * ((((ln >> 3) & 2) + ((ln >> 1) & 1)) ^ ((2 * h_ + (q_ >> 1)) & 3)) + 8 * (ln & 1);
      if (c == 4) {
        const u32 rec = WS_PART + ((br * 32 + grp * 4 + cw) * 16 + panel) * REC;
        pub16 = rec + ln * 16; pub4 = rec + 1024 + ln * 4;
      }
      if (c == 5) { pw0 = lds0 + L_PHI + (2 * hp) * 8192 + (64 * rh + r_) * 64 + 16 * ((2 * h_) ^ ((r_ >> 1) & 3)); pw1 = pw0 ^ 16; }
      if (c == 6) vwa = lds0 + L_V + (8 * rh + 2 * hp) * VF + ((ln & 16) * 2 + (ln & 15)) * 16 + h_ * 8;
      mk(1); mix(c >= 1 ? -1 : 0);
      if (c == 1) { PIN(fa[0]); PIN(fa[1]); }
      if (c == 2) PIN(vra);
      if (c == 3) PIN(pra);
      if (c == 4) { PIN(pub16); PIN(pub4); }
      if (c == 5) { PIN(pw0); PIN(pw1); }
      if (c == 6) PIN(vwa);
      BAR();
      ldw(c + 1, 0, 0); ldx(c + 1, 0); ldw(c + 1, 0, 1);
      mv(1); mix(12);
    }
    ldw(7, 1, 0); ldx(7, 1); ldw(7, 1, 1); mk(0); mix(12);
    mk(1); mix(0);
    __builtin_amdgcn_s_setprio(2);
    f32x16 pk[2][2];
    {
      u32x4 kf[2][2];
      auto kpack = [&](int i) {
#pragma unroll
        for (int hh = 0; hh < 2; ++hh) {
          const f32x4 &t0 = ak[2 * i][hh], &t1 = ak[2 * i + 1][hh];
          const auto lo = __builtin_amdgcn_permlane16_swap(pk2(t0[0], t0[1]), pk2(t1[0], t1[1]), false, false);
          const auto hi = __builtin_amdgcn_permlane16_swap(pk2(t0[2], t0[3]), pk2(t1[2], t1[3]), false, false);
          kf[i][hh] = (u32x4){lo[0], hi[0], lo[1], hi[1]};
        }
      };
      mv(0); kpack(0);
      SGB(0x8, 2);
#pragma unroll
      for (int i = 0; i < 6; ++i) { SGB(0x2, 2); SGB(0x8, 1); }
      SCHED();
      pk[0][0] = mfma(fa[0], kf[0][0], zero);
      pk[0][1] = mfma(fa[1], kf[0][1], zero);
      kpack(1);
      SGB(0x8, 1); SGB(0x2, 6); SGB(0x8, 1); SGB(0x2, 6);
      PIN(pk[0][0]); PIN(pk[0][1]); PIN(kf[1][0]); PIN(kf[1][1]);
      SCHED();
      mv(1);
      pk[1][0] = mfma(fa[0], kf[1][0], zero);
      pk[1][1] = mfma(fa[1], kf[1][1], zero);
    }
    float m0[2], m1[2];
    m0[0] = negmax16(pk[0][0]); m0[1] = negmax16(pk[0][1]);
    SGB(0x8, 2);
#pragma unroll
    for (int i = 0; i < 6; ++i) { SGB(0x2, 2); SGB(0x8, 1); }
    SGB(0x8, 1); SGB(0x2, 6); SGB(0x8, 1);
#pragma unroll
    for (int i = 0; i < 8; ++i) PIN(av[i >> 1][i & 1]);
    PIN(pk[1][0]); PIN(pk[1][1]); PIN(m0[0]); PIN(m0[1]);
    SCHED();
#pragma unroll
    for (int i = 0; i < 8; ++i) {
      const f32x4& t = av[i >> 1][i & 1];
      *(LAS u32x2*)(size_t)(vwa + (4 * (i >> 2) + ((i >> 1) & 1)) * VF + (i & 1) * 256) = (u32x2){pk2(t[0], t[1]), pk2(t[2], t[3])};
    }
    SCHED();
    {
      u32x4 o[2][2];
      softmax16x2(pk[0][0], pk[0][1], m0, o);
      lds_w128(pw0, o[0][0]); lds_w128(pw1, o[0][1]);
      lds_w128(pw0 + 8192, o[1][0]); lds_w128(pw1 + 8192, o[1][1]);
      SCHED();
      m1[0] = negmax16(pk[1][0]); m1[1] = negmax16(pk[1][1]);
      PIN(m1[0]); PIN(m1[1]);
      SCHED();
      softmax16x2(pk[1][0], pk[1][1], m1, o);
      lds_w128(pw0 + 2048, o[0][0]); lds_w128(pw1 + 2048, o[0][1]);
      lds_w128(pw0 + 2048 + 8192, o[1][0]); lds_w128(pw1 + 2048 + 8192, o[1][1]);
      SGB(0x402, 26); SGB(0x200, 1); SGB(0x402, 8); SGB(0x200, 1); SGB(0x402, 8); SGB(0x200, 1); SGB(0x402, 8); SGB(0x200, 1);
      SCHED();
      BARE();
      BAR();
      SCHED();
    }
    f32x16 kv = zero;
    {
      u32x4 a[8], b[8];
#pragma unroll
      for (int u = 0; u < 8; ++u) {
        a[u] = lds_r128(vra + ((u >> 1) * 4 + (u & 1)) * VF);
        b[u] = lds_tr2(pra + 16 * u * 64, pra + (16 * u + 8) * 64);
      }
#pragma unroll
      for (int u = 0; u < 8; ++u) kv = mfma(a[u], b[u], kv);
      SGB(0x100, 12); SGB(0x8, 1); SGB(0x100, 6); SGB(0x8, 1); SGB(0x100, 6); SGB(0x8, 6);
    }
    {
      const u32x4 w = pack8(kv, 0);
      st128c(w, wsr, pub16);
      const u32 zb = __float_as_uint(kv[8]);
      const u32 chk = key - pair_sum(sum4(h ? 0u : zb, w));
      st32c(h ? chk : zb, wsr, pub4);
    }
    SCHED();
    u32 l16, l4, kwa, kza, gso[4], vo[4], wso;
    u32x4 sel;
    {
      u32 ln = lane;
      PIN(ln);
#pragma unroll
      for (int i = 0; i < 4; ++i) vo[i] = (ln >> 3) * 1024 + 16 * ((ln & 7) ^ (((ln >> 4) & 1) << 2)) + i * 7168;
#pragma unroll
      for (int i = 0; i < 2; ++i) dma4(lds0 + L_WO + cw * 8192 + i * 4096, vo, wor, ochunk(cw) * 65536 + i * 32768 + cg * 128);
      wso = ochunk(4 + cw) * 65536 + cg * 128;
      const u32 r_ = ln & 31, h_ = ln >> 5;
      l16 = ln * 16; l4 = ln * 4;
      kwa = lds0 + L_KV + cw * 1536 + r_ * 48 + h_ * 16; kza = lds0 + L_KV + cw * 1536 + r_ * 48 + 32 + h_ * 8;
      const bool hit = r_ < 16 && ((r_ >> 2) & 1) == h_;
      const u32 j = (r_ & 3) + 4 * (r_ >> 3);
#pragma unroll
      for (int e = 0; e < 4; ++e) sel[e] = (hit && (j >> 1) == e) ? ((j & 1) ? 0x3F800000u : 0x00003F80u) : 0u;
#pragma unroll
      for (int i = 0; i < 4; ++i) { gso[i] = WS_PART + ((br * 32 + gq * 4 + cw) * 16 + ((i + idx) & 3) * 4) * REC; PINS(gso[i]); }
      PIN(l16); PIN(l4); PIN(kwa); PIN(kza); PIN(sel); PINS(wso);
    }
    __builtin_amdgcn_s_sleep(DELAY1);
    f32x16 gs[4];
    float z[4];
    {
      u32x4 g[16];
      u32 gz[16];
      auto gload = [&](int gi) {
#pragma unroll
        for (int i = 0; i < 4; ++i) {
          g[4 * gi + i] = __builtin_amdgcn_raw_buffer_load_b128(wsr, l16 + i * REC, gso[gi], AUX_SC1);
          gz[4 * gi + i] = __builtin_amdgcn_raw_buffer_load_b32(wsr, l4 + (1024 + i * REC - 1024 * (i == 3)), gso[gi] + 1024 * (i == 3), AUX_SC1);
          SCHED();
        }
      };
      auto gsum = [&](int gi) -> bool {
        u32 c = 0;
        PIN(gz[4 * gi + 3]); SCHED();
#pragma unroll
        for (int i = 0; i < 4; ++i) {
          c = sum4(c + gz[4 * gi + i], g[4 * gi + i]);
          gs[gi] = mfma(sel, g[4 * gi + i], i == 0 ? zero : gs[gi]);
          z[gi] = i == 0 ? __uint_as_float(gz[4 * gi]) : z[gi] + __uint_as_float(gz[4 * gi + i]);
        }
        const bool bad = __builtin_amdgcn_ballot_w64(pair_sum(c) != 4u * key) != 0;
        SGB(0x8, 1); SGB(0x2, 5); SGB(0x8, 1); SGB(0x2, 5); SGB(0x8, 1); SGB(0x2, 4); SGB(0x8, 1);
        PIN(z[gi]);
        SCHED();
        return bad;
      };
      gload(0); gload(1); gload(2); gload(3);
      bool b0 = gsum(0), b1 = gsum(1), b2 = gsum(2), b3 = gsum(3);
#pragma unroll 1
      for (u32 spin = 0; (b0 | b1 | b2 | b3) && spin < SPIN_CAP; ++spin) {
        asm volatile("" ::: "memory");
        if (b0) gload(0);
        if (b1) gload(1);
        if (b2) gload(2);
        if (b3) gload(3);
        if (b0) b0 = gsum(0);
        if (b1) b1 = gsum(1);
        if (b2) b2 = gsum(2);
        if (b3) b3 = gsum(3);
      }
    }
    __builtin_amdgcn_s_waitcnt(0x0F70);
    {
      float tt[8];
#pragma unroll
      for (int j = 0; j < 8; ++j) tt[j] = (gs[0][j] + gs[1][j]) + (gs[2][j] + gs[3][j]);
      const float zt = (z[0] + z[1]) + (z[2] + z[3]);
      lds_w128(kwa, (u32x4){pk2(tt[0], tt[1]), pk2(tt[2], tt[3]), pk2(tt[4], tt[5]), pk2(tt[6], tt[7])});
      const u32 zz = pk2(zt, zt);
      *(LAS u32x2*)(size_t)kza = (u32x2){zz, zz};
    }
#pragma unroll
    for (int i = 0; i < 2; ++i) dma4(lds0 + L_WO + 32768 + cw * 8192 + i * 4096, vo, wor, wso + i * 32768);
    BAR();
    {
      SCHED();
      u32 oso[8], ob = lds0 + L_O + cw * OREC, i0 = 0;
      PINS(i0);
#pragma unroll
      for (int i = 1; i < 8; ++i) { oso[i] = WS_O + ((br * 64 + panel * 4 + cw) * 8 + ochunk(i + i0)) * OREC; PINS(oso[i]); }
      PINS(ob);
      WAITV(0);
      BARE();
      __builtin_amdgcn_s_sleep(DELAY3);
      auto oissue = [&](int j) { dma_orec(ob + (j % 5) * (4 * OREC), l4, l16, wsr, oso[j]); };
      oissue(1); oissue(2);
      WAITV(5); BARE(); oissue(3);
      WAITV(5); BARE(); oissue(4); oissue(5);
      WAITV(10); BARE(); oissue(6);
      WAITV(10); BARE(); oissue(7);
      WAITV(10); BARE();
      WAITV(5); BARE();
      WAITV(0); BARE();
    }
  } else {
    KLOAD("");
    const int t = wave;
    const u32 l7 = lane & 7, l3 = lane >> 3, lq = lane >> 4;
    const u32 wsw = 16 * (l7 ^ ((lq & 1) << 2));
    u32 vx[4], vw[4];
#pragma unroll
    for (int i = 0; i < 4; ++i) {
      vx[i] = l3 * 1024 + 16 * (l7 ^ (((i & 1) << 2) | lq)) + i * 7168;
      vw[i] = l3 * 3072 + (wsw ^ ((i & 1) << 5)) + i * (8 * 3072 - 1024);
    }
    auto chunk = [&](u32 c) -> u32 {
      const u32 g = grp + idx;
      return c - 2 < 4 ? 2 + ((c - 2) ^ (g & 3)) : c ^ (g & 1);
    };
    const u32 xs0 = (panel * 128 + t * 32) * 1024, ws0 = (t & 1) * (32 * 3072) + (1 + (t >> 1)) * 1024 + grp * 128;
    const u32 ldst = lds0 + t * 4096;
    u32 e0 = ldst, e1 = ldst + 16384, e2 = xs0 + chunk(0) * 128, e3 = ws0 + chunk(0) * (64 * 3072);
    PINS(e0); PINS(e1); PINS(e2); PINS(e3);
#pragma unroll
    for (int i = 0; i < 4; ++i) { PIN(vx[i]); PIN(vw[i]); }
    KWAIT();
    const __amdgpu_buffer_rsrc_t xr = __builtin_amdgcn_make_buffer_rsrc(KPTR(ka, 0), 0, 2048 * 512 * 2, SRD3);
    const __amdgpu_buffer_rsrc_t wr = __builtin_amdgcn_make_buffer_rsrc(KPTR(ka, 2), 0, 512 * 1536 * 2, SRD3);
    dma4(e0, vx, xr, e2);
    dma4(e1, vw, wr, e3);
    {
      const __amdgpu_buffer_rsrc_t pr = __builtin_amdgcn_make_buffer_rsrc(KPTR(ka, 4), 0, 32 * 256 * 2, SRD3);
      dma1(lds0 + L_WPHI + t * 1024, lane * 16, pr, (t < 2 ? grp : gq) * 2048 + (t & 1) * 1024);
    }
    dma4(ldst + SLOT, vx, xr, xs0 + chunk(1) * 128);
    dma4(ldst + SLOT + 16384, vw, wr, ws0 + chunk(1) * (64 * 3072));
    LANE_IDS();
    u32 key = *(const CAS u32*)((const char*)ws + WS_EPOCH + br * 256) + 1u;
    const u32 sw = (r >> 1) & 7;
    u32 xa[4];
#pragma unroll
    for (int s = 0; s < 4; ++s) { xa[s] = lds0 + (32 * t + r) * 128 + 16 * ((2 * s + h) ^ sw); PIN(xa[s]); }
    u32 fpa = lds0 + L_WPHI + 2048 + (4 * h + q) * 32 + (8 * blk + 4 * (p >> 1)) * 2;
    PIN(fpa);
    PINS(key);
    u32x4 xq[32], fa[4];
    u32 wa, wb, kra, o_off, c_off, woa, wob, vto, sto, tho;
#pragma unroll
    for (int c = 0; c < 8; ++c) {
      u32 d0, d1, s0, s1;
      if (c < 6) {
        d0 = ldst + ((c + 2) % 3) * SLOT; d1 = d0 + 16384;
        s0 = xs0 + chunk(c + 2) * 128; s1 = ws0 + chunk(c + 2) * (64 * 3072);
      } else {
        const u32 k0 = 2 * ((2 * (c - 6) + idx) & 3) + (t >> 1), k1 = (k0 + 2) & 7;
        d0 = lds0 + L_WQ1 + ((chunk(k0) + 4) & 7) * 8192 + (t & 1) * 4096; d1 = lds0 + L_WQ1 + ((chunk(k1) + 4) & 7) * 8192 + (t & 1) * 4096;
        s0 = (k0 * 64 + (t & 1) * 32) * 3072 + gq * 128; s1 = (k1 * 64 + (t & 1) * 32) * 3072 + gq * 128;
      }
      PINS(d0); PINS(d1); PINS(s0); PINS(s1);
      WAITV(8);
      BAR();
      if (c < 6) dma4(d0, vx, xr, s0); else dma4(d0, vw, wr, s0);
#pragma unroll
      for (int s = 0; s < 4; ++s) xq[4 * c + s] = lds_r128(xa[s] + (c % 3) * SLOT);
      dma4(d1, vw, wr, s1);
      if (c == 0) {
#pragma unroll
        for (int hd = 0; hd < 4; ++hd) fa[hd] = lds_tr2(fpa + hd * 512, fpa + hd * 512 + 256);
      }
      if (c == 1) {
        lds_w128(lds0 + L_V + (lane & 15) * VF + 1024, (u32x4){0x3F803F80u, 0x3F803F80u, 0x3F803F80u, 0x3F803F80u});
        const u32 sg = (lane & 4) ? 0x80008000u : 0u;
#pragma unroll
        for (int hd = 0; hd < 4; ++hd) { PIN(fa[hd]); fa[hd] ^= (u32x4){sg, sg, sg, sg}; PIN(fa[hd]); }
        wa = lds0 + L_WQ1 + (8 * h + q) * 128 + 16 * ((2 * blk + (p >> 1)) ^ ((((q >> 1) & 1) << 2) | (h << 1))) + 8 * (p & 1);
        wb = wa ^ 64;
        kra = lds0 + L_KV + (16 * h + q) * 48 + (blk ? 32 : 8 * p);
        o_off = WS_O + ((br * 64 + panel * 4 + t) * 8 + gq) * OREC + 256 + lane * 16;
        c_off = WS_O + ((br * 64 + panel * 4 + t) * 8 + gq) * OREC + lane * 4;
        woa = lds0 + L_WO + (8 * h + q) * 128 + 16 * ((2 * blk + (p & 1)) ^ (((q >> 1) & 1) << 2)) + 8 * (p >> 1);
        wob = woa ^ 64;
        vto = r * 128; sto = (idx * 16 + t * 4) * 1024;
        PIN(wa); PIN(wb); PIN(kra); PIN(o_off); PIN(c_off); PIN(woa); PIN(wob); PIN(vto); PINS(sto);
      }
    }
    TOUCH(tho, vto, wor, sto);
    u32x4 fq[2][2][2];
    {
      f32x16 aq[2], pq[2];
      float mq[2];
      aq[0] = aq[1] = zero;
      u32x4 wf[4];
      auto ldw = [&](int n) {
        const int k = n & 31;
        const u32 o = (n < 32 ? wa : wb) + (k < 16 ? (L_WQ0 - L_WQ1) : 0) + (k & 15) * 2048;
        wf[n & 3] = lds_tr2(o, o + 512);
      };
      WAITV(1);
      BAR();
      SCHED();
      ldw(0); ldw(1); ldw(2);
      SCHED();
      BARE();
      SCHED();
#pragma unroll
      for (int n = 0; n < 34; ++n) {
        ldw(n + 3);
        aq[n >> 5] = mfma(wf[n & 3], xq[n & 31], aq[n >> 5]);
        SGB(0x8, 1); SGB(0x100, 2);
        SCHED();
      }
#pragma unroll
      for (int hh = 0; hh < 2; ++hh) pq[hh] = mfma(fa[hh], pack8(aq[0], hh), zero);
      SCHED();
#pragma unroll
      for (int n = 34; n < 64; ++n) {
        if (n + 3 < 64) ldw(n + 3);
        aq[1] = mfma(wf[n & 3], xq[n & 31], aq[1]);
      }
      mq[0] = negmax16(pq[0]); mq[1] = negmax16(pq[1]);
      f32x2 eq[2][8];
      sm_exp_s(pq[0], pq[1], mq, eq);
      SGB(0x8, 1); SGB(0x100, 2); SGB(0x8, 1); SGB(0x100, 2);
#pragma unroll
      for (int n = 36; n < 41; ++n) { SGB(0x8, 1); SGB(0x100, 2); SGB(0x402, 4); }
#pragma unroll
      for (int n = 41; n < 64; ++n) { SGB(0x8, 1); SGB(0x100, 2); if (n & 1) SGB(0x402, 3); else SGB(0x402, 2); }
#pragma unroll
      for (int i = 0; i < 16; ++i) PIN(eq[i >> 3][i & 7]);
      SCHED();
#pragma unroll
      for (int hh = 0; hh < 2; ++hh) { pq[hh] = mfma(fa[2 + hh], pack8(aq[1], hh), zero); PIN(pq[hh]); }
      SCHED();
      sm_finish(eq, fq[0]);
      SCHED();
      asm volatile("s_waitcnt vmcnt(0)" : "+v"(tho)::"memory");
      mq[0] = negmax16(pq[0]); mq[1] = negmax16(pq[1]);
      PIN(mq[0]); PIN(mq[1]);
      SCHED();
#pragma unroll
      for (int i = 0; i < 4; ++i) PIN(fq[0][i >> 1][i & 1]);
      softmax16x2(pq[0], pq[1], mq, fq[1]);
#pragma unroll
      for (int i = 0; i < 4; ++i) PIN(fq[1][i >> 1][i & 1]);
    }
    BAR();
    u32x4 ob[2][4];
    {
      u32x4 a0[4], a1[4];
#pragma unroll
      for (int hd = 0; hd < 4; ++hd) {
        a0[hd] = lds_tr2(kra + hd * 1536, kra + hd * 1536 + 4 * 48);
        a1[hd] = lds_tr2(kra + hd * 1536 + 8 * 48, kra + hd * 1536 + 12 * 48);
      }
      SCHED();
      f32x16 y[4];
      u32 cs = 0;
      auto num = [&](int hd) {
        y[hd] = mfma(a0[hd], fq[hd >> 1][hd & 1][0], zero);
        y[hd] = mfma(a1[hd], fq[hd >> 1][hd & 1][1], y[hd]);
      };
      auto fin = [&](int hd) {
        const f32x16& yy = y[hd];
        const float rd = __builtin_amdgcn_rcpf(yy[8] + 1e-6f);
        u32 w[4];
#pragma unroll
        for (int e = 0; e < 4; ++e) { float t0 = yy[2 * e] * rd, t1 = yy[2 * e + 1] * rd; if (hd < 2) OPQ(t0); w[e] = pk2(t0, t1); }
        ob[0][hd] = (u32x4){w[0], w[1], w[2], w[3]};
        st128c(ob[0][hd], wsr, o_off + hd * 1024);
        cs = sum4(cs, ob[0][hd]);
      };
      num(0); num(1); PIN(y[0]); PIN(y[1]); SCHED();
      num(2); fin(0); SGB(0x8, 1); SGB(0x402, 6); SGB(0x8, 1); PIN(y[2]); SCHED();
      num(3); fin(1); SGB(0x8, 1); SGB(0x402, 6); SGB(0x8, 1); PIN(y[3]); SCHED();
      fin(2); fin(3);
      st32c(key - cs, wsr, c_off);
    }
    SCHED();
    u32x4 wo[32][2];
    auto ldwo = [&](int n) {
      wo[n][0] = lds_tr2(woa + n * 2048, woa + n * 2048 + 512);
      wo[n][1] = lds_tr2(wob + n * 2048, wob + n * 2048 + 512);
    };
#pragma unroll
    for (int n = 0; n < 16; ++n) ldwo(n);
    f32x16 c0 = zero, c1 = zero;
#pragma unroll
    for (int s = 0; s < 4; ++s) { c0 = mfma(wo[s][0], ob[0][s], c0); c1 = mfma(wo[s][1], ob[0][s], c1); }
#pragma unroll
    for (int k = 4; k < 16; ++k) { PIN(wo[k][0]); PIN(wo[k][1]); }
    PIN(c0); PIN(c1);
    WAITV(0);
    BARE();
    SCHED();
#pragma unroll
    for (int n = 16; n < 24; ++n) ldwo(n);
#pragma unroll
    for (int k = 16; k < 24; ++k) { PIN(wo[k][0]); PIN(wo[k][1]); }
    const __amdgpu_buffer_rsrc_t outr = __builtin_amdgcn_make_buffer_rsrc(KPTR(kb, 0), 0, 2048 * 1024 * 2, SRD3);
    u32 oa = lds0 + L_O + t * OREC + 256 + lane * 16;
    u32 orow = ((panel * 128 + t * 32 + l3) * 1024 + br * 512 + cg * 64 + 8 * l7) * 2;
    PIN(oa); PIN(orow);
    u32 oc = oa - 256 - lane * 12, l4 = lane * 4, l16 = lane * 16, oa4 = oa + 4 * (4 * OREC), oc4 = oc + 4 * (4 * OREC);
    u32 sw1 = lds0 + t * 4096 + r * 128 + 16 * (h ^ (r & 7)), sr1 = lds0 + t * 4096 + l3 * 128 + 16 * (l7 ^ l3);
    u32 sw2 = sw1 ^ 32, sw3 = sw1 ^ 64, sw4 = sw1 ^ 96, or1 = orow + 16384, or2 = orow + 32768, or3 = orow + 49152;
    PIN(oc); PIN(l4); PIN(l16); PIN(sw1); PIN(sr1); PIN(oa4); PIN(oc4); PIN(sw2); PIN(sw3); PIN(sw4); PIN(or1); PIN(or2); PIN(or3);
    u32 chk[2];
    auto oread = [&](int j) {
      const u32 da = j % 5 == 4 ? oa4 : oa + (j % 5) * (4 * OREC), dc = j % 5 == 4 ? oc4 : oc + (j % 5) * (4 * OREC);
#pragma unroll
      for (int s = 0; s < 4; ++s) ob[j & 1][s] = lds_r128(da + s * 1024);
      chk[j & 1] = *(LAS u32*)(size_t)dc;
    };
    auto obad = [&](int j) -> bool {
      const u32 c = sum4(sum4(chk[j & 1], ob[j & 1][0]), ob[j & 1][1]) + sum4(sum4(0u, ob[j & 1][2]), ob[j & 1][3]);
      return __builtin_amdgcn_ballot_w64(c != key) != 0;
    };
#pragma unroll
    for (int j = 1; j < 8; ++j) {
      BARE();
      SCHED();
      oread(j);
      if (j >= 2) {
#pragma unroll
        for (int s = 0; s < 4; ++s) {
          const int k = 4 * (j - 1) + s;
          c0 = mfma(wo[k][0], ob[(j - 1) & 1][s], c0);
          c1 = mfma(wo[k][1], ob[(j - 1) & 1][s], c1);
        }
        if (j >= 3 && j <= 6) { ldwo(24 + 2 * (j - 3)); ldwo(25 + 2 * (j - 3)); }
      }
      const bool bad = obad(j);
      if (j >= 3 && j <= 6) {
        SGB(0x8, 1); SGB(0x100, 3); SGB(0x8, 1); SGB(0x100, 2); SGB(0x8, 1); SGB(0x100, 3); SGB(0x8, 1); SGB(0x100, 3);
        SGB(0x8, 1); SGB(0x2, 4); SGB(0x8, 1); SGB(0x2, 5); SGB(0x8, 1); SGB(0x100, 2); SGB(0x8, 1);
      } else if (j >= 2) {
        SGB(0x8, 1); SGB(0x100, 3); SGB(0x8, 1); SGB(0x100, 2); SGB(0x8, 3); SGB(0x2, 4); SGB(0x8, 1); SGB(0x2, 5); SGB(0x8, 2);
      }
      SCHED();
      if (__builtin_expect(bad, 0)) {
#pragma unroll 1
        for (u32 spin = 0; spin < SPIN_CAP; ++spin) {
          dma_orec(lds0 + L_O + t * OREC + (j % 5) * (4 * OREC), l4, l16, wsr, WS_O + ((br * 64 + panel * 4 + t) * 8 + ochunk(j)) * OREC);
          u32 jj = j + 1, jend = j + AHEAD < 7 ? j + AHEAD : 7;
          PINS(jj); PINS(jend);
#pragma unroll 1
          for (; jj <= jend; ++jj) {
            const u32 sl = (jj >= 5 ? jj - 5 : jj) * (4 * OREC);
            u32 c = *(LAS u32*)(size_t)(oc + sl);
#pragma unroll
            for (int s = 0; s < 4; ++s) c = sum4(c, lds_r128(oa + sl + s * 1024));
            if (__builtin_amdgcn_ballot_w64(c != key) != 0)
              dma_orec(lds0 + L_O + t * OREC + sl, l4, l16, wsr, WS_O + ((br * 64 + panel * 4 + t) * 8 + ochunk(jj)) * OREC);
          }
          WAITV(0);
          oread(j);
          if (!obad(j)) break;
        }
      }
    }
#pragma unroll
    for (int s = 0; s < 4; ++s) c0 = mfma(wo[28 + s][0], ob[1][s], c0);
    PIN(c0);
    SCHED();
#pragma unroll
    for (int s = 0; s < 4; ++s) c1 = mfma(wo[28 + s][1], ob[1][s], c1);
    lds_w128(sw1, pack8(c0, 0));
    lds_w128(sw2, pack8(c0, 1));
    SGB(0x8, 2); SGB(0x2, 4); SGB(0x200, 1); SGB(0x8, 1); SGB(0x2, 4); SGB(0x200, 1); SGB(0x8, 1);
    SCHED();
    lds_w128(sw3, pack8(c1, 0));
    lds_w128(sw4, pack8(c1, 1));
    asm volatile("" ::: "memory");
    {
      u32x4 v[4];
#pragma unroll
      for (int i = 0; i < 4; ++i) v[i] = lds_r128(sr1 + i * 1024);
      SCHED();
      st128c(v[0], outr, orow);
      st128c(v[1], outr, or1);
      st128c(v[2], outr, or2);
      st128c(v[3], outr, or3);
    }
    if (t == 0 && panel == 0 && grp == 0 && lane == 0) st32c(key, wsr, WS_EPOCH + br * 256);
  }
}
}

extern "C" void kernel_launch(void* const* d_in, const int* in_sizes, int n_in,
                              void* d_out, int out_size, void* d_ws, size_t ws_size,
                              hipStream_t stream) {
  hh::hedgehog<<<256, 512, 0, stream>>>((const u16*)d_in[0], (const u16*)d_in[1], (const u16*)d_in[2], (const u16*)d_in[3],
                                        (const u16*)d_in[4], (const u16*)d_in[5], (const u16*)d_in[6], (const u16*)d_in[7],
                                        (u16*)d_out, (u32*)d_ws);
}
```

```cpp
#define WARM 1
#define PROBE_N 12
#define PROBE_ROLE 0
#include <hip/hip_runtime.h>
#include <stdint.h>

typedef unsigned short u16;
typedef unsigned u32;
typedef short bf16x8 __attribute__((ext_vector_type(8)));
typedef short s16x4 __attribute__((ext_vector_type(4)));
typedef float f32x16 __attribute__((ext_vector_type(16)));
typedef float f32x4 __attribute__((ext_vector_type(4)));
typedef float f32x2 __attribute__((ext_vector_type(2)));
typedef unsigned u32x8 __attribute__((ext_vector_type(8)));
typedef unsigned u32x4 __attribute__((ext_vector_type(4)));
typedef unsigned u32x2 __attribute__((ext_vector_type(2)));
typedef __bf16 bf16x2 __attribute__((ext_vector_type(2)));
#define DI __device__ __forceinline__
#define LAS __attribute__((address_space(3)))
#define CAS __attribute__((address_space(4)))
#define BC(T, x) __builtin_bit_cast(T, x)

#define DELAY1 3
#define DELAY3 4
#define CODE_PF 29184
#define AHEAD 1
#define GQ 1
#define GC 2
#define GBURST 1
#define OFIRST 5

namespace hh {
constexpr int AUX_SC1 = 16;
constexpr u32 SRD3 = 0x00020000u;
constexpr u32 WS_EPOCH = 0;
constexpr u32 REC = 1280;
constexpr u32 WS_PART = 4096;
constexpr u32 OREC = 4352;
constexpr u32 WS_O = WS_PART + 2 * 32 * 16 * REC;
constexpr u32 WS_END = WS_O + 2 * 64 * 8 * OREC;
constexpr u32 WS_STAMP = 8u << 20;
constexpr u32 SLOT = 32768;
constexpr u32 L_WQ0 = 98304;
constexpr u32 L_WQ1 = 65536;
constexpr u32 L_WPHI = 163840 - 4096;
constexpr u32 L_PHI = 0;
constexpr u32 VF = 1040;
constexpr u32 L_V = 131072;
constexpr u32 L_KV = L_V + 16 * VF;
constexpr u32 L_WO = 0;
constexpr u32 L_O = 65536;
constexpr u32 SPIN_CAP = 1u << 16;

DI u32 pk2(float a, float b) { return BC(u32, __builtin_convertvector((f32x2){a, b}, bf16x2)); }
DI u32x4 pack8(const f32x16& x, int s) {
  return (u32x4){pk2(x[8 * s], x[8 * s + 1]), pk2(x[8 * s + 2], x[8 * s + 3]), pk2(x[8 * s + 4], x[8 * s + 5]), pk2(x[8 * s + 6], x[8 * s + 7])};
}
DI f32x16 mfma(u32x4 a, u32x4 b, f32x16 c) { return __builtin_amdgcn_mfma_f32_32x32x16_bf16(BC(bf16x8, a), BC(bf16x8, b), c, 0, 0, 0); }
DI f32x4 mfma16(u32x4 a, u32x4 b, f32x4 c) { return __builtin_amdgcn_mfma_f32_16x16x32_bf16(BC(bf16x8, a), BC(bf16x8, b), c, 0, 0, 0); }
DI u32x4 lds_r128(u32 a) { return *(LAS u32x4*)(size_t)a; }
DI void lds_w128(u32 a, u32x4 v) { *(LAS u32x4*)(size_t)a = v; }
DI u32x4 lds_tr2(u32 a0, u32 a1) {
  s16x4 lo = __builtin_amdgcn_ds_read_tr16_b64_v4i16((LAS s16x4*)(size_t)a0);
  s16x4 hi = __builtin_amdgcn_ds_read_tr16_b64_v4i16((LAS s16x4*)(size_t)a1);
  return BC(u32x4, __builtin_shufflevector(lo, hi, 0, 1, 2, 3, 4, 5, 6, 7));
}
DI void dma1(u32 lds, u32 voff, __amdgpu_buffer_rsrc_t srd, u32 soff) {
  asm volatile("s_mov_b32 m0, %0\n\ts_nop 0\n\tbuffer_load_dwordx4 %1, %2, %3 offen lds" ::"s"(lds), "v"(voff), "s"(srd), "s"(soff) : "memory");
}
DI void dma4(u32 lds, const u32 (&v)[4], __amdgpu_buffer_rsrc_t srd, u32 soff) {
  asm volatile("s_mov_b32 m0, %0\n\ts_nop 0\n\t"
               "buffer_load_dwordx4 %1, %5, %6 offen lds\n\t"
               "buffer_load_dwordx4 %2, %5, %6 offen offset:1024 lds\n\t"
               "buffer_load_dwordx4 %3, %5, %6 offen offset:2048 lds\n\t"
               "buffer_load_dwordx4 %4, %5, %6 offen offset:3072 lds" ::"s"(lds), "v"(v[0]), "v"(v[1]), "v"(v[2]), "v"(v[3]), "s"(srd), "s"(soff) : "memory");
}
DI void dma_orec(u32 lds, u32 l4, u32 l16, __amdgpu_buffer_rsrc_t srd, u32 soff) {
  asm volatile("s_mov_b32 m0, %0\n\ts_nop 0\n\t"
               "buffer_load_dwordx4 %2, %3, %4 offen offset:256 sc1 lds\n\t"
               "buffer_load_dwordx4 %2, %3, %4 offen offset:1280 sc1 lds\n\t"
               "buffer_load_dwordx4 %2, %3, %4 offen offset:2304 sc1 lds\n\t"
               "buffer_load_dwordx4 %2, %3, %4 offen offset:3328 sc1 lds\n\t"
               "buffer_load_dword %1, %3, %4 offen sc1 lds" ::"s"(lds), "v"(l4), "v"(l16), "s"(srd), "s"(soff) : "memory");
}
#define TOUCH(d, voff, srd, soff) asm volatile("buffer_load_dword %0, %1, %2, %3 offen" : "=v"(d) : "v"(voff), "s"(srd), "s"(soff) : "memory")
DI void st128c(u32x4 v, __amdgpu_buffer_rsrc_t srd, u32 off) { __builtin_amdgcn_raw_buffer_store_b128(v, srd, off, 0, AUX_SC1); }
DI void st32c(u32 v, __amdgpu_buffer_rsrc_t srd, u32 off) { __builtin_amdgcn_raw_buffer_store_b32(v, srd, off, 0, AUX_SC1); }
#define BAR() asm volatile("s_waitcnt lgkmcnt(0)\n\ts_barrier" ::: "memory")
#define BARE() asm volatile("s_barrier" ::: "memory")
#define OPQ(x) asm("" : "+v"(x))
#define PIN(x) asm volatile("" : "+v"(x))
#define PINS(x) asm volatile("" : "+s"(x))
#define WAITV(n) asm volatile("s_waitcnt vmcnt(" #n ")" ::: "memory")
#define SCHED() __builtin_amdgcn_sched_barrier(0)
#define SGB(m, n) __builtin_amdgcn_sched_group_barrier(m, n, 0)
DI u32 sum4(u32 c, u32x4 v) { return (c + v.x + v.y) + v.z + v.w; }
DI u32 pair_sum(u32 s) { auto t = __builtin_amdgcn_permlane32_swap(s, s, false, false); return t[0] + t[1]; }

constexpr float LOG2E = 1.44269504088896340736f;
DI float max3f(float a, float b, float c) { return __builtin_fmaxf(__builtin_fmaxf(a, b), c); }
DI float negmax16(const f32x16& x) {
  const float a = max3f(x[0], x[1], x[2]), b = max3f(x[3], x[4], x[5]), c = max3f(x[6], x[7], x[8]);
  const float d = max3f(x[9], x[10], x[11]), e = max3f(x[12], x[13], x[14]);
  return -max3f(max3f(a, b, x[15]), max3f(c, d, e), a) * LOG2E;
}
DI void sm_exp(const f32x16& p, const f32x16& q, const float (&mc)[2], f32x2 (&e)[2][8]) {
#pragma unroll
  for (int i = 0; i < 8; ++i) {
    const f32x2 t = __builtin_elementwise_fma((f32x2){p[2 * i], p[2 * i + 1]}, (f32x2){LOG2E, LOG2E}, (f32x2){mc[0], mc[0]});
    const f32x2 u = __builtin_elementwise_fma((f32x2){q[2 * i], q[2 * i + 1]}, (f32x2){LOG2E, LOG2E}, (f32x2){mc[1], mc[1]});
    e[0][i] = (f32x2){__builtin_amdgcn_exp2f(t.x), __builtin_amdgcn_exp2f(t.y)};
    e[1][i] = (f32x2){__builtin_amdgcn_exp2f(u.x), __builtin_amdgcn_exp2f(u.y)};
  }
  SGB(0x2, 4); SGB(0x400, 8); SGB(0x2, 4); SGB(0x400, 8); SGB(0x2, 6); SGB(0x400, 8); SGB(0x2, 8); SGB(0x400, 8);
}
DI void sm_exp_s(const f32x16& p, const f32x16& q, const float (&mc)[2], f32x2 (&e)[2][8]) {
#pragma unroll
  for (int i = 0; i < 16; ++i) {
    e[0][i >> 1][i & 1] = __builtin_amdgcn_exp2f(__builtin_fmaf(p[i], LOG2E, mc[0]));
    e[1][i >> 1][i & 1] = __builtin_amdgcn_exp2f(__builtin_fmaf(q[i], LOG2E, mc[1]));
  }
}
DI void sm_finish(const f32x2 (&e)[2][8], u32x4 (&o)[2][2]) {
  float r[2];
#pragma unroll
  for (int u = 0; u < 2; ++u) {
    const f32x2 s = ((e[u][0] + e[u][1]) + (e[u][2] + e[u][3])) + ((e[u][4] + e[u][5]) + (e[u][6] + e[u][7]));
    r[u] = __builtin_amdgcn_rcpf(s.x + s.y);
  }
#pragma unroll
  for (int u = 0; u < 2; ++u) {
    u32 w[8];
#pragma unroll
    for (int i = 0; i < 8; ++i) { const f32x2 t = e[u][i] * (f32x2){r[u], r[u]}; w[i] = pk2(t.x, t.y); }
    o[u][0] = (u32x4){w[0], w[1], w[2], w[3]};
    o[u][1] = (u32x4){w[4], w[5], w[6], w[7]};
  }
}
DI void softmax16x2(const f32x16& p, const f32x16& q, const float (&mc)[2], u32x4 (&o)[2][2]) {
  f32x2 e[2][8];
  sm_exp(p, q, mc, e);
  sm_finish(e, o);
}

#define STAMP(i) do { if ((i) == PROBE_N) { SCHED(); asm volatile("s_memrealtime %0\n\ts_waitcnt lgkmcnt(0)" : "=s"(pt1) :: "memory"); SCHED(); } } while (0)
#define STAMP_OUT() do { if (PROBE_N && (wave >> 2) == PROBE_ROLE) { unsigned long long d_ = pt1 - pt0; if (d_ > 100000) d_ = 100000; unsigned long long e_ = pt0 + 3000 + 8 * d_; \
  for (int it_ = 0; it_ < 1000000; ++it_) { unsigned long long t_; asm volatile("s_memrealtime %0\n\ts_waitcnt lgkmcnt(0)" : "=s"(t_) :: "memory"); if (t_ >= e_) break; } } } while (0)

__global__ __launch_bounds__(512, 2) void hedgehog(const u16* xA, const u16* wA, const u16* pA, const u16* oA,
                                                   const u16* xB, const u16* wB, const u16* pB, const u16* oB,
                                                   u16* out_, u32* ws_) {
  __shared__ __attribute__((aligned(16))) char smem[163840];
  const u32 lds0 = (u32)(size_t)(LAS char*)smem;
  unsigned long long pt0 = __builtin_amdgcn_s_memrealtime(), pt1 = pt0;
  const int bid = blockIdx.x, xcd = bid & 7, idx = bid >> 3;
  const int br = xcd >> 2;
#define KLOAD(tail)                                                                                       \
  u32x8 ka;                                                                                               \
  u32x4 kb;                                                                                               \
  asm volatile("s_load_dwordx8 %0, %2, %3\n\ts_load_dwordx4 %1, %2, 0x40" tail : "=&s"(ka), "=&s"(kb)     \
               : "s"(__builtin_amdgcn_kernarg_segment_ptr()), "s"(br * 32))
#define KWAIT() asm volatile("s_waitcnt lgkmcnt(0)" : "+s"(ka), "+s"(kb))
#define KPTR(v, i) ((void*)(((unsigned long long)(v)[(i) + 1] << 32) | (v)[i]))
  unsigned long long pcs;
  asm volatile("s_getpc_b64 %0" : "=s"(pcs));
  pcs &= ~255ull;
  const int lane = threadIdx.x & 63;
  const int wave = __builtin_amdgcn_readfirstlane(threadIdx.x >> 6);
  const int panel = (xcd & 3) * 4 + (idx & 3), grp = idx >> 2, gq = (grp + GQ) & 7, cg = (grp + GC) & 7;
  auto ochunk = [&](u32 j) -> u32 {
    const int v = ((int)j + (((int)j - 1 - ((gq - OFIRST) & 7)) >> 31) + OFIRST) & 7, z = ((int)j - 1) >> 31;
    return (u32)__builtin_amdgcn_readfirstlane((v & ~z) | (gq & z));
  };
  f32x16 zero;
#pragma unroll
  for (int i = 0; i < 16; ++i) zero[i] = 0.f;
#define LANE_IDS()                                                                            \
  u32 lane_ = lane;                                                                           \
  PIN(lane_);                                                                                 \
  const u32 r = lane_ & 31, h = lane_ >> 5;                                                   \
  const u32 blk = (lane_ >> 4) & 1, q = (lane_ & 15) >> 2, p = lane_ & 3;                     \
  u32* ws = (u32*)KPTR(kb, 2);                                                                \
  const __amdgpu_buffer_rsrc_t wsr = __builtin_amdgcn_make_buffer_rsrc(ws, 0, WS_END, SRD3);  \
  const __amdgpu_buffer_rsrc_t wor = __builtin_amdgcn_make_buffer_rsrc(KPTR(ka, 6), 0, 512 * 512 * 2, SRD3)

  if (wave >= 4) {
    const int cw = wave - 4;
    const int rh = cw >> 1, hp = cw & 1;
    KLOAD("\n\ts_waitcnt lgkmcnt(0)");
    u32 sp0, sp1, sp2, sp3;
    {
      u32 off = idx * 1024 + cw * 256;
      off = off < CODE_PF ? off : 0;
      asm volatile("s_load_dword %0, %4, %5 offset:0\n\ts_load_dword %1, %4, %5 offset:64\n\ts_load_dword %2, %4, %5 offset:128\n\ts_load_dword %3, %4, %5 offset:192"
                   : "=&s"(sp0), "=&s"(sp1), "=&s"(sp2), "=&s"(sp3) : "s"(pcs), "s"(off));
    }
    LANE_IDS();
    u32 key = *(const CAS u32*)((const char*)ws + WS_EPOCH + br * 256) + 1u;
    u32 xa[2], xb[2], wa[2], wb2[2];
    {
      const u32 r16 = lane_ & 15, g = lane_ >> 4;
#pragma unroll
      for (int s = 0; s < 2; ++s) {
        xa[s] = lds0 + (64 * rh + r16) * 128 + 16 * ((4 * s + g) ^ (r16 >> 1));
        wa[s] = lds0 + 16384 + (8 * g + q) * 128 + 16 * ((4 * hp + 2 * s + (p >> 1)) ^ ((((q >> 1) & 1) << 2) | (blk << 1))) + 8 * (p & 1);
        xb[s] = xa[s] + 2 * SLOT; wb2[s] = wa[s] + 2 * SLOT;
        PIN(xa[s]); PIN(xb[s]); PIN(wa[s]); PIN(wb2[s]);
      }
    }
    u32 fpa = lds0 + L_WPHI + hp * 1024 + (8 * h + q) * 32 + (8 * blk + 4 * (p >> 1)) * 2;
    PIN(fpa);
    if (!WARM) PINS(key);
    f32x4 ak[4][2], av[4][2];
#pragma unroll
    for (int i = 0; i < 8; ++i) ak[i >> 1][i & 1] = av[i >> 1][i & 1] = (f32x4){0.f, 0.f, 0.f, 0.f};
    u32 vra, pra, vwa, pw0, pw1, pub16, pub4;
    u32x4 xf[2][4], wk[2][2], wv[2][2];
    auto ldx = [&](int c, int s) {
      const u32 x = c % 3 == 2 ? xb[s] : xa[s] + (c % 3) * SLOT;
#pragma unroll
      for (int i = 0; i < 4; ++i) xf[s][i] = lds_r128(x + i * 2048);
    };
    auto ldw = [&](int c, int s, int m) {
#pragma unroll
      for (int n = 0; n < 2; ++n) {
        const u32 w = (c % 3 == 2 ? wb2[n] : wa[n] + (c % 3) * SLOT) + s * 4096 + m * 8192;
        (m ? wv : wk)[s][n] = lds_tr2(w, w + 512);
      }
    };
    auto mk = [&](int s) {
#pragma unroll
      for (int i = 0; i < 8; ++i) ak[i >> 1][i & 1] = mfma16(wk[s][i & 1], xf[s][i >> 1], ak[i >> 1][i & 1]);
    };
    auto mv = [&](int s) {
#pragma unroll
      for (int i = 0; i < 8; ++i) av[i >> 1][i & 1] = mfma16(xf[s][i >> 1], wv[s][i & 1], av[i >> 1][i & 1]);
    };
    auto mix = [&](int n) {
#pragma unroll
      for (int i = 0; i < 8; ++i) {
        SGB(0x8, 1);
        if (n == 12) { if (i < 4) SGB(0x100, 2); else SGB(0x100, 1); }
        else if (n == 8) SGB(0x100, 1);
        else if (n == 4) { if (i < 4) SGB(0x100, 1); }
        else if (n < 0) SGB(0x6, 2);
      }
      SCHED();
    };
    if (WARM) {
      u32 hsh = lane * 2654435761u + 12345u;
      u32x4 a = {(hsh & 0x3FFF3FFFu) | 0x30003000u, ((hsh * 7u) & 0xBFFFBFFFu) | 0x30003000u, ((hsh * 13u) & 0x3FFFBFFFu) | 0x30003000u, ((hsh * 29u) & 0xBFFF3FFFu) | 0x30003000u};
      u32x4 b = {a.y, a.w, a.x, a.z};
      f32x16 w0 = zero, w1 = zero;
#pragma unroll 1
      for (int i = 0; i < WARM; ++i) { w0 = mfma(a, b, w0); w1 = mfma(b, a, w1); w0 = mfma(b, b, w0); w1 = mfma(a, a, w1); PIN(w0); PIN(w1); }
    }
    BAR();
    asm volatile("" ::"s"(sp0), "s"(sp1), "s"(sp2), "s"(sp3));
    if (WARM) PINS(key);
    STAMP(1);
    ldw(0, 0, 0); ldx(0, 0); ldw(0, 0, 1);
    u32x4 fa[2];
#pragma unroll
    for (int hh = 0; hh < 2; ++hh) fa[hh] = lds_tr2(fpa + hh * 512, fpa + hh * 512 + 128);
    SCHED();
#pragma unroll
    for (int c = 0; c < 7; ++c) {
      ldw(c, 1, 0); ldx(c, 1); mk(0); mix(8);
      ldw(c, 1, 1); mv(0); mix(4);
      u32 ln = lane;
      PIN(ln);
      const u32 r_ = ln & 31, h_ = ln >> 5, q_ = (ln & 15) >> 2;
      if (c == 1) {
        const u32 sg = (ln & 4) ? 0x80008000u : 0u;
#pragma unroll
        for (int hh = 0; hh < 2; ++hh) { OPQ(fa[hh]); fa[hh] ^= (u32x4){sg, sg, sg, sg}; }
      }
      if (c == 2) vra = lds0 + L_V + (cw >> 1) * (2 * VF) + (r_ < 16 ? (ln + 16 * (cw & 1)) * 16 : 1024);
      if (c == 3) pra = lds0 + L_PHI + cw * 8192 + (4 * h_ + q_) * 64 + 16 * ((((ln >> 3) & 2) + ((ln >> 1) & 1)) ^ ((2 * h_ + (q_ >> 1)) & 3)) + 8 * (ln & 1);
      if (c == 4) {
        const u32 rec = WS_PART + ((br * 32 + grp * 4 + cw) * 16 + panel) * REC;
        pub16 = rec + ln * 16; pub4 = rec + 1024 + ln * 4;
      }
      if (c == 5) { pw0 = lds0 + L_PHI + (2 * hp) * 8192 + (64 * rh + r_) * 64 + 16 * ((2 * h_) ^ ((r_ >> 1) & 3)); pw1 = pw0 ^ 16; }
      if (c == 6) vwa = lds0 + L_V + (8 * rh + 2 * hp) * VF + ((ln & 16) * 2 + (ln & 15)) * 16 + h_ * 8;
      mk(1); mix(c >= 1 ? -1 : 0);
      if (c == 1) { PIN(fa[0]); PIN(fa[1]); }
      if (c == 2) PIN(vra);
      if (c == 3) PIN(pra);
      if (c == 4) { PIN(pub16); PIN(pub4); }
      if (c == 5) { PIN(pw0); PIN(pw1); }
      if (c == 6) PIN(vwa);
      BAR();
      ldw(c + 1, 0, 0); ldx(c + 1, 0); ldw(c + 1, 0, 1);
      mv(1); mix(12);
    }
    ldw(7, 1, 0); ldx(7, 1); ldw(7, 1, 1); mk(0); mix(12);
    mk(1); mix(0);
    __builtin_amdgcn_s_setprio(2);
    STAMP(2);
    f32x16 pk[2][2];
    {
      u32x4 kf[2][2];
      auto kpack = [&](int i) {
#pragma unroll
        for (int hh = 0; hh < 2; ++hh) {
          const f32x4 &t0 = ak[2 * i][hh], &t1 = ak[2 * i + 1][hh];
          const auto lo = __builtin_amdgcn_permlane16_swap(pk2(t0[0], t0[1]), pk2(t1[0], t1[1]), false, false);
          const auto hi = __builtin_amdgcn_permlane16_swap(pk2(t0[2], t0[3]), pk2(t1[2], t1[3]), false, false);
          kf[i][hh] = (u32x4){lo[0], hi[0], lo[1], hi[1]};
        }
      };
      mv(0); kpack(0);
      SGB(0x8, 2);
#pragma unroll
      for (int i = 0; i < 6; ++i) { SGB(0x2, 2); SGB(0x8, 1); }
      SCHED();
      pk[0][0] = mfma(fa[0], kf[0][0], zero);
      pk[0][1] = mfma(fa[1], kf[0][1], zero);
      kpack(1);
      SGB(0x8, 1); SGB(0x2, 6); SGB(0x8, 1); SGB(0x2, 6);
      PIN(pk[0][0]); PIN(pk[0][1]); PIN(kf[1][0]); PIN(kf[1][1]);
      SCHED();
      mv(1);
      pk[1][0] = mfma(fa[0], kf[1][0], zero);
      pk[1][1] = mfma(fa[1], kf[1][1], zero);
    }
    float m0[2], m1[2];
    m0[0] = negmax16(pk[0][0]); m0[1] = negmax16(pk[0][1]);
    SGB(0x8, 2);
#pragma unroll
    for (int i = 0; i < 6; ++i) { SGB(0x2, 2); SGB(0x8, 1); }
    SGB(0x8, 1); SGB(0x2, 6); SGB(0x8, 1);
#pragma unroll
    for (int i = 0; i < 8; ++i) PIN(av[i >> 1][i & 1]);
    PIN(pk[1][0]); PIN(pk[1][1]); PIN(m0[0]); PIN(m0[1]);
    SCHED();
#pragma unroll
    for (int i = 0; i < 8; ++i) {
      const f32x4& t = av[i >> 1][i & 1];
      *(LAS u32x2*)(size_t)(vwa + (4 * (i >> 2) + ((i >> 1) & 1)) * VF + (i & 1) * 256) = (u32x2){pk2(t[0], t[1]), pk2(t[2], t[3])};
    }
    SCHED();
    {
      u32x4 o[2][2];
      softmax16x2(pk[0][0], pk[0][1], m0, o);
      lds_w128(pw0, o[0][0]); lds_w128(pw1, o[0][1]);
      lds_w128(pw0 + 8192, o[1][0]); lds_w128(pw1 + 8192, o[1][1]);
      SCHED();
      m1[0] = negmax16(pk[1][0]); m1[1] = negmax16(pk[1][1]);
      PIN(m1[0]); PIN(m1[1]);
      SCHED();
      softmax16x2(pk[1][0], pk[1][1], m1, o);
      lds_w128(pw0 + 2048, o[0][0]); lds_w128(pw1 + 2048, o[0][1]);
      lds_w128(pw0 + 2048 + 8192, o[1][0]); lds_w128(pw1 + 2048 + 8192, o[1][1]);
      SGB(0x402, 26); SGB(0x200, 1); SGB(0x402, 8); SGB(0x200, 1); SGB(0x402, 8); SGB(0x200, 1); SGB(0x402, 8); SGB(0x200, 1);
      SCHED();
      BARE();
      BAR();
      SCHED();
    }
    STAMP(3);
    f32x16 kv = zero;
    {
      u32x4 a[8], b[8];
#pragma unroll
      for (int u = 0; u < 8; ++u) {
        a[u] = lds_r128(vra + ((u >> 1) * 4 + (u & 1)) * VF);
        b[u] = lds_tr2(pra + 16 * u * 64, pra + (16 * u + 8) * 64);
      }
#pragma unroll
      for (int u = 0; u < 8; ++u) kv = mfma(a[u], b[u], kv);
      SGB(0x100, 12); SGB(0x8, 1); SGB(0x100, 6); SGB(0x8, 1); SGB(0x100, 6); SGB(0x8, 6);
    }
    {
      const u32x4 w = pack8(kv, 0);
      st128c(w, wsr, pub16);
      const u32 zb = __float_as_uint(kv[8]);
      const u32 chk = key - pair_sum(sum4(h ? 0u : zb, w));
      st32c(h ? chk : zb, wsr, pub4);
    }
    SCHED();
    STAMP(4);
    u32 l16, l4, kwa, kza, gso[4], vo[4], wso;
    u32x4 sel;
    {
      u32 ln = lane;
      PIN(ln);
#pragma unroll
      for (int i = 0; i < 4; ++i) vo[i] = (ln >> 3) * 1024 + 16 * ((ln & 7) ^ (((ln >> 4) & 1) << 2)) + i * 7168;
#pragma unroll
      for (int i = 0; i < 2; ++i) dma4(lds0 + L_WO + cw * 8192 + i * 4096, vo, wor, ochunk(cw) * 65536 + i * 32768 + cg * 128);
      wso = ochunk(4 + cw) * 65536 + cg * 128;
      const u32 r_ = ln & 31, h_ = ln >> 5;
      l16 = ln * 16; l4 = ln * 4;
      kwa = lds0 + L_KV + cw * 1536 + r_ * 48 + h_ * 16; kza = lds0 + L_KV + cw * 1536 + r_ * 48 + 32 + h_ * 8;
      const bool hit = r_ < 16 && ((r_ >> 2) & 1) == h_;
      const u32 j = (r_ & 3) + 4 * (r_ >> 3);
#pragma unroll
      for (int e = 0; e < 4; ++e) sel[e] = (hit && (j >> 1) == e) ? ((j & 1) ? 0x3F800000u : 0x00003F80u) : 0u;
#pragma unroll
      for (int i = 0; i < 4; ++i) { gso[i] = WS_PART + ((br * 32 + gq * 4 + cw) * 16 + ((i + idx) & 3) * 4) * REC; PINS(gso[i]); }
      PIN(l16); PIN(l4); PIN(kwa); PIN(kza); PIN(sel); PINS(wso);
    }
    if (DELAY1) __builtin_amdgcn_s_sleep(DELAY1);
    STAMP(10);
    f32x16 gs[4];
    float z[4];
    {
      u32x4 g[16];
      u32 gz[16];
      auto gload = [&](int gi) {
#pragma unroll
        for (int i = 0; i < 4; ++i) {
          g[4 * gi + i] = __builtin_amdgcn_raw_buffer_load_b128(wsr, l16 + i * REC, gso[gi], AUX_SC1);
          gz[4 * gi + i] = __builtin_amdgcn_raw_buffer_load_b32(wsr, l4 + (1024 + i * REC - 1024 * (i == 3)), gso[gi] + 1024 * (i == 3), AUX_SC1);
          SCHED();
        }
      };
      auto gsum = [&](int gi) -> bool {
        u32 c = 0;
        if (GBURST) { PIN(gz[4 * gi + 3]); SCHED(); }
#pragma unroll
        for (int i = 0; i < 4; ++i) {
          c = sum4(c + gz[4 * gi + i], g[4 * gi + i]);
          gs[gi] = mfma(sel, g[4 * gi + i], i == 0 ? zero : gs[gi]);
          z[gi] = i == 0 ? __uint_as_float(gz[4 * gi]) : z[gi] + __uint_as_float(gz[4 * gi + i]);
        }
        const bool bad = __builtin_amdgcn_ballot_w64(pair_sum(c) != 4u * key) != 0;
        if (GBURST) { SGB(0x8, 1); SGB(0x2, 5); SGB(0x8, 1); SGB(0x2, 5); SGB(0x8, 1); SGB(0x2, 4); SGB(0x8, 1); }
        PIN(z[gi]);
        SCHED();
        return bad;
      };
      gload(0); gload(1); gload(2); gload(3);
      bool b0 = gsum(0), b1 = gsum(1), b2 = gsum(2), b3 = gsum(3);
      STAMP(11);
#pragma unroll 1
      for (u32 spin = 0; (b0 | b1 | b2 | b3) && spin < SPIN_CAP; ++spin) {
        asm volatile("" ::: "memory");
        if (b0) gload(0);
        if (b1) gload(1);
        if (b2) gload(2);
        if (b3) gload(3);
        if (b0) b0 = gsum(0);
        if (b1) b1 = gsum(1);
        if (b2) b2 = gsum(2);
        if (b3) b3 = gsum(3);
      }
    }
    __builtin_amdgcn_s_waitcnt(0x0F70);
    STAMP(5);
    {
      float tt[8];
#pragma unroll
      for (int j = 0; j < 8; ++j) tt[j] = (gs[0][j] + gs[1][j]) + (gs[2][j] + gs[3][j]);
      const float zt = (z[0] + z[1]) + (z[2] + z[3]);
      lds_w128(kwa, (u32x4){pk2(tt[0], tt[1]), pk2(tt[2], tt[3]), pk2(tt[4], tt[5]), pk2(tt[6], tt[7])});
      const u32 zz = pk2(zt, zt);
      *(LAS u32x2*)(size_t)kza = (u32x2){zz, zz};
    }
#pragma unroll
    for (int i = 0; i < 2; ++i) dma4(lds0 + L_WO + 32768 + cw * 8192 + i * 4096, vo, wor, wso + i * 32768);
    BAR();
    STAMP(6);
    {
      SCHED();
      u32 oso[8], ob = lds0 + L_O + cw * OREC, i0 = 0;
      PINS(i0);
#pragma unroll
      for (int i = 1; i < 8; ++i) { oso[i] = WS_O + ((br * 64 + panel * 4 + cw) * 8 + ochunk(i + i0)) * OREC; PINS(oso[i]); }
      PINS(ob);
      WAITV(0);
      BARE();
      if (DELAY3) __builtin_amdgcn_s_sleep(DELAY3);
      STAMP(8);
      auto oissue = [&](int j) { dma_orec(ob + (j % 5) * (4 * OREC), l4, l16, wsr, oso[j]); };
      oissue(1); oissue(2);
      WAITV(5); BARE(); oissue(3);
      WAITV(5); STAMP(15); BARE(); oissue(4); oissue(5);
      WAITV(10); BARE(); oissue(6);
      WAITV(10); BARE(); oissue(7);
      WAITV(10); BARE();
      WAITV(5); BARE();
      WAITV(0); BARE();
    }
    STAMP(9);
    STAMP_OUT();
  } else {
    KLOAD("");
    const int t = wave;
    const u32 l7 = lane & 7, l3 = lane >> 3, lq = lane >> 4;
    const u32 wsw = 16 * (l7 ^ ((lq & 1) << 2));
    u32 vx[4], vw[4];
#pragma unroll
    for (int i = 0; i < 4; ++i) {
      vx[i] = l3 * 1024 + 16 * (l7 ^ (((i & 1) << 2) | lq)) + i * 7168;
      vw[i] = l3 * 3072 + (wsw ^ ((i & 1) << 5)) + i * (8 * 3072 - 1024);
    }
    auto chunk = [&](u32 c) -> u32 {
      const u32 g = grp + idx;
      return c - 2 < 4 ? 2 + ((c - 2) ^ (g & 3)) : c ^ (g & 1);
    };
    const u32 xs0 = (panel * 128 + t * 32) * 1024, ws0 = (t & 1) * (32 * 3072) + (1 + (t >> 1)) * 1024 + grp * 128;
    const u32 ldst = lds0 + t * 4096;
    u32 e0 = ldst, e1 = ldst + 16384, e2 = xs0 + chunk(0) * 128, e3 = ws0 + chunk(0) * (64 * 3072);
    PINS(e0); PINS(e1); PINS(e2); PINS(e3);
#pragma unroll
    for (int i = 0; i < 4; ++i) { PIN(vx[i]); PIN(vw[i]); }
    KWAIT();
    const __amdgpu_buffer_rsrc_t xr = __builtin_amdgcn_make_buffer_rsrc(KPTR(ka, 0), 0, 2048 * 512 * 2, SRD3);
    const __amdgpu_buffer_rsrc_t wr = __builtin_amdgcn_make_buffer_rsrc(KPTR(ka, 2), 0, 512 * 1536 * 2, SRD3);
    STAMP(10);
    dma4(e0, vx, xr, e2);
    dma4(e1, vw, wr, e3);
    {
      const __amdgpu_buffer_rsrc_t pr = __builtin_amdgcn_make_buffer_rsrc(KPTR(ka, 4), 0, 32 * 256 * 2, SRD3);
      dma1(lds0 + L_WPHI + t * 1024, lane * 16, pr, (t < 2 ? grp : gq) * 2048 + (t & 1) * 1024);
    }
    STAMP(11);
    dma4(ldst + SLOT, vx, xr, xs0 + chunk(1) * 128);
    dma4(ldst + SLOT + 16384, vw, wr, ws0 + chunk(1) * (64 * 3072));
    STAMP(1);
    LANE_IDS();
    u32 key = *(const CAS u32*)((const char*)ws + WS_EPOCH + br * 256) + 1u;
    const u32 sw = (r >> 1) & 7;
    u32 xa[4];
#pragma unroll
    for (int s = 0; s < 4; ++s) { xa[s] = lds0 + (32 * t + r) * 128 + 16 * ((2 * s + h) ^ sw); PIN(xa[s]); }
    u32 fpa = lds0 + L_WPHI + 2048 + (4 * h + q) * 32 + (8 * blk + 4 * (p >> 1)) * 2;
    PIN(fpa);
    PINS(key);
    u32x4 xq[32], fa[4];
    u32 wa, wb, kra, o_off, c_off, woa, wob, vto, sto, tho;
#pragma unroll
    for (int c = 0; c < 8; ++c) {
      u32 d0, d1, s0, s1;
      if (c < 6) {
        d0 = ldst + ((c + 2) % 3) * SLOT; d1 = d0 + 16384;
        s0 = xs0 + chunk(c + 2) * 128; s1 = ws0 + chunk(c + 2) * (64 * 3072);
      } else {
        const u32 k0 = 2 * ((2 * (c - 6) + idx) & 3) + (t >> 1), k1 = (k0 + 2) & 7;
        d0 = lds0 + L_WQ1 + ((chunk(k0) + 4) & 7) * 8192 + (t & 1) * 4096; d1 = lds0 + L_WQ1 + ((chunk(k1) + 4) & 7) * 8192 + (t & 1) * 4096;
        s0 = (k0 * 64 + (t & 1) * 32) * 3072 + gq * 128; s1 = (k1 * 64 + (t & 1) * 32) * 3072 + gq * 128;
      }
      PINS(d0); PINS(d1); PINS(s0); PINS(s1);
      WAITV(8);
      BAR();
      if (c < 6) dma4(d0, vx, xr, s0); else dma4(d0, vw, wr, s0);
#pragma unroll
      for (int s = 0; s < 4; ++s) xq[4 * c + s] = lds_r128(xa[s] + (c % 3) * SLOT);
      dma4(d1, vw, wr, s1);
      if (c == 0) {
#pragma unroll
        for (int hd = 0; hd < 4; ++hd) fa[hd] = lds_tr2(fpa + hd * 512, fpa + hd * 512 + 256);
      }
      if (c == 1) {
        lds_w128(lds0 + L_V + (lane & 15) * VF + 1024, (u32x4){0x3F803F80u, 0x3F803F80u, 0x3F803F80u, 0x3F803F80u});
        const u32 sg = (lane & 4) ? 0x80008000u : 0u;
#pragma unroll
        for (int hd = 0; hd < 4; ++hd) { PIN(fa[hd]); fa[hd] ^= (u32x4){sg, sg, sg, sg}; PIN(fa[hd]); }
        wa = lds0 + L_WQ1 + (8 * h + q) * 128 + 16 * ((2 * blk + (p >> 1)) ^ ((((q >> 1) & 1) << 2) | (h << 1))) + 8 * (p & 1);
        wb = wa ^ 64;
        kra = lds0 + L_KV + (16 * h + q) * 48 + (blk ? 32 : 8 * p);
        o_off = WS_O + ((br * 64 + panel * 4 + t) * 8 + gq) * OREC + 256 + lane * 16;
        c_off = WS_O + ((br * 64 + panel * 4 + t) * 8 + gq) * OREC + lane * 4;
        woa = lds0 + L_WO + (8 * h + q) * 128 + 16 * ((2 * blk + (p & 1)) ^ (((q >> 1) & 1) << 2)) + 8 * (p >> 1);
        wob = woa ^ 64;
        vto = r * 128; sto = (idx * 16 + t * 4) * 1024;
        PIN(wa); PIN(wb); PIN(kra); PIN(o_off); PIN(c_off); PIN(woa); PIN(wob); PIN(vto); PINS(sto);
      }
    }
    TOUCH(tho, vto, wor, sto);
    STAMP(2);
    u32x4 fq[2][2][2];
    {
      f32x16 aq[2], pq[2];
      float mq[2];
      aq[0] = aq[1] = zero;
      u32x4 wf[4];
      auto ldw = [&](int n) {
        const int k = n & 31;
        const u32 o = (n < 32 ? wa : wb) + (k < 16 ? (L_WQ0 - L_WQ1) : 0) + (k & 15) * 2048;
        wf[n & 3] = lds_tr2(o, o + 512);
      };
      WAITV(1);
      BAR();
      SCHED();
      STAMP(12);
      ldw(0); ldw(1); ldw(2);
      SCHED();
      BARE();
      SCHED();
#pragma unroll
      for (int n = 0; n < 34; ++n) {
        ldw(n + 3);
        aq[n >> 5] = mfma(wf[n & 3], xq[n & 31], aq[n >> 5]);
        SGB(0x8, 1); SGB(0x100, 2);
        SCHED();
      }
#pragma unroll
      for (int hh = 0; hh < 2; ++hh) pq[hh] = mfma(fa[hh], pack8(aq[0], hh), zero);
      SCHED();
      STAMP(4);
#pragma unroll
      for (int n = 34; n < 64; ++n) {
        if (n + 3 < 64) ldw(n + 3);
        aq[1] = mfma(wf[n & 3], xq[n & 31], aq[1]);
      }
      mq[0] = negmax16(pq[0]); mq[1] = negmax16(pq[1]);
      f32x2 eq[2][8];
      sm_exp_s(pq[0], pq[1], mq, eq);
      SGB(0x8, 1); SGB(0x100, 2); SGB(0x8, 1); SGB(0x100, 2);
#pragma unroll
      for (int n = 36; n < 41; ++n) { SGB(0x8, 1); SGB(0x100, 2); SGB(0x402, 4); }
#pragma unroll
      for (int n = 41; n < 64; ++n) { SGB(0x8, 1); SGB(0x100, 2); if (n & 1) SGB(0x402, 3); else SGB(0x402, 2); }
#pragma unroll
      for (int i = 0; i < 16; ++i) PIN(eq[i >> 3][i & 7]);
      SCHED();
      STAMP(3);
#pragma unroll
      for (int hh = 0; hh < 2; ++hh) { pq[hh] = mfma(fa[2 + hh], pack8(aq[1], hh), zero); PIN(pq[hh]); }
      SCHED();
      sm_finish(eq, fq[0]);
      SCHED();
      asm volatile("s_waitcnt vmcnt(0)" : "+v"(tho)::"memory");
      mq[0] = negmax16(pq[0]); mq[1] = negmax16(pq[1]);
      PIN(mq[0]); PIN(mq[1]);
      SCHED();
#pragma unroll
      for (int i = 0; i < 4; ++i) PIN(fq[0][i >> 1][i & 1]);
      softmax16x2(pq[0], pq[1], mq, fq[1]);
#pragma unroll
      for (int i = 0; i < 4; ++i) PIN(fq[1][i >> 1][i & 1]);
    }
    STAMP(5);
    BAR();
    STAMP(6);
    u32x4 ob[2][4];
    {
      u32x4 a0[4], a1[4];
#pragma unroll
      for (int hd = 0; hd < 4; ++hd) {
        a0[hd] = lds_tr2(kra + hd * 1536, kra + hd * 1536 + 4 * 48);
        a1[hd] = lds_tr2(kra + hd * 1536 + 8 * 48, kra + hd * 1536 + 12 * 48);
      }
      SCHED();
      f32x16 y[4];
      u32 cs = 0;
      auto num = [&](int hd) {
        y[hd] = mfma(a0[hd], fq[hd >> 1][hd & 1][0], zero);
        y[hd] = mfma(a1[hd], fq[hd >> 1][hd & 1][1], y[hd]);
      };
      auto fin = [&](int hd) {
        const f32x16& yy = y[hd];
        const float rd = __builtin_amdgcn_rcpf(yy[8] + 1e-6f);
        u32 w[4];
#pragma unroll
        for (int e = 0; e < 4; ++e) { float t0 = yy[2 * e] * rd, t1 = yy[2 * e + 1] * rd; if (hd < 2) OPQ(t0); w[e] = pk2(t0, t1); }
        ob[0][hd] = (u32x4){w[0], w[1], w[2], w[3]};
        st128c(ob[0][hd], wsr, o_off + hd * 1024);
        cs = sum4(cs, ob[0][hd]);
      };
      num(0); num(1); PIN(y[0]); PIN(y[1]); SCHED();
      num(2); fin(0); SGB(0x8, 1); SGB(0x402, 6); SGB(0x8, 1); PIN(y[2]); SCHED();
      num(3); fin(1); SGB(0x8, 1); SGB(0x402, 6); SGB(0x8, 1); PIN(y[3]); SCHED();
      fin(2); fin(3);
      st32c(key - cs, wsr, c_off);
    }
    SCHED();
    STAMP(7);
    u32x4 wo[32][2];
    auto ldwo = [&](int n) {
      wo[n][0] = lds_tr2(woa + n * 2048, woa + n * 2048 + 512);
      wo[n][1] = lds_tr2(wob + n * 2048, wob + n * 2048 + 512);
    };
#pragma unroll
    for (int n = 0; n < 16; ++n) ldwo(n);
    f32x16 c0 = zero, c1 = zero;
#pragma unroll
    for (int s = 0; s < 4; ++s) { c0 = mfma(wo[s][0], ob[0][s], c0); c1 = mfma(wo[s][1], ob[0][s], c1); }
#pragma unroll
    for (int k = 4; k < 16; ++k) { PIN(wo[k][0]); PIN(wo[k][1]); }
    PIN(c0); PIN(c1);
    WAITV(0);
    BARE();
    SCHED();
#pragma unroll
    for (int n = 16; n < 24; ++n) ldwo(n);
#pragma unroll
    for (int k = 16; k < 24; ++k) { PIN(wo[k][0]); PIN(wo[k][1]); }
    const __amdgpu_buffer_rsrc_t outr = __builtin_amdgcn_make_buffer_rsrc(KPTR(kb, 0), 0, 2048 * 1024 * 2, SRD3);
    u32 oa = lds0 + L_O + t * OREC + 256 + lane * 16;
    u32 orow = ((panel * 128 + t * 32 + l3) * 1024 + br * 512 + cg * 64 + 8 * l7) * 2;
    PIN(oa); PIN(orow);
    u32 oc = oa - 256 - lane * 12, l4 = lane * 4, l16 = lane * 16, oa4 = oa + 4 * (4 * OREC), oc4 = oc + 4 * (4 * OREC);
    u32 sw1 = lds0 + t * 4096 + r * 128 + 16 * (h ^ (r & 7)), sr1 = lds0 + t * 4096 + l3 * 128 + 16 * (l7 ^ l3);
    u32 sw2 = sw1 ^ 32, sw3 = sw1 ^ 64, sw4 = sw1 ^ 96, or1 = orow + 16384, or2 = orow + 32768, or3 = orow + 49152;
    PIN(oc); PIN(l4); PIN(l16); PIN(sw1); PIN(sr1); PIN(oa4); PIN(oc4); PIN(sw2); PIN(sw3); PIN(sw4); PIN(or1); PIN(or2); PIN(or3);
    STAMP(8);
    u32 nretry = 0;
    u32 chk[2];
    auto oread = [&](int j) {
      const u32 da = j % 5 == 4 ? oa4 : oa + (j % 5) * (4 * OREC), dc = j % 5 == 4 ? oc4 : oc + (j % 5) * (4 * OREC);
#pragma unroll
      for (int s = 0; s < 4; ++s) ob[j & 1][s] = lds_r128(da + s * 1024);
      chk[j & 1] = *(LAS u32*)(size_t)dc;
    };
    auto obad = [&](int j) -> bool {
      const u32 c = sum4(sum4(chk[j & 1], ob[j & 1][0]), ob[j & 1][1]) + sum4(sum4(0u, ob[j & 1][2]), ob[j & 1][3]);
      return __builtin_amdgcn_ballot_w64(c != key) != 0;
    };
#pragma unroll
    for (int j = 1; j < 8; ++j) {
      BARE();
      SCHED();
      oread(j);
      if (j >= 2) {
#pragma unroll
        for (int s = 0; s < 4; ++s) {
          const int k = 4 * (j - 1) + s;
          c0 = mfma(wo[k][0], ob[(j - 1) & 1][s], c0);
          c1 = mfma(wo[k][1], ob[(j - 1) & 1][s], c1);
        }
        if (j >= 3 && j <= 6) { ldwo(24 + 2 * (j - 3)); ldwo(25 + 2 * (j - 3)); }
      }
      const bool bad = obad(j);
      if (j >= 3 && j <= 6) {
        SGB(0x8, 1); SGB(0x100, 3); SGB(0x8, 1); SGB(0x100, 2); SGB(0x8, 1); SGB(0x100, 3); SGB(0x8, 1); SGB(0x100, 3);
        SGB(0x8, 1); SGB(0x2, 4); SGB(0x8, 1); SGB(0x2, 5); SGB(0x8, 1); SGB(0x100, 2); SGB(0x8, 1);
      } else if (j >= 2) {
        SGB(0x8, 1); SGB(0x100, 3); SGB(0x8, 1); SGB(0x100, 2); SGB(0x8, 3); SGB(0x2, 4); SGB(0x8, 1); SGB(0x2, 5); SGB(0x8, 2);
      }
      SCHED();
      if (__builtin_expect(bad, 0)) {
#pragma unroll 1
        for (u32 spin = 0; spin < SPIN_CAP; ++spin) {
          dma_orec(lds0 + L_O + t * OREC + (j % 5) * (4 * OREC), l4, l16, wsr, WS_O + ((br * 64 + panel * 4 + t) * 8 + ochunk(j)) * OREC);
          u32 jj = j + 1, jend = j + AHEAD < 7 ? j + AHEAD : 7;
          PINS(jj); PINS(jend);
#pragma unroll 1
          for (; jj <= jend; ++jj) {
            const u32 sl = (jj >= 5 ? jj - 5 : jj) * (4 * OREC);
            u32 c = *(LAS u32*)(size_t)(oc + sl);
#pragma unroll
            for (int s = 0; s < 4; ++s) c = sum4(c, lds_r128(oa + sl + s * 1024));
            if (__builtin_amdgcn_ballot_w64(c != key) != 0)
              dma_orec(lds0 + L_O + t * OREC + sl, l4, l16, wsr, WS_O + ((br * 64 + panel * 4 + t) * 8 + ochunk(jj)) * OREC);
          }
          WAITV(0);
          oread(j);
          ++nretry;
          if (!obad(j)) break;
        }
      }
    }
#pragma unroll
    for (int s = 0; s < 4; ++s) c0 = mfma(wo[28 + s][0], ob[1][s], c0);
    PIN(c0);
    SCHED();
#pragma unroll
    for (int s = 0; s < 4; ++s) c1 = mfma(wo[28 + s][1], ob[1][s], c1);
    lds_w128(sw1, pack8(c0, 0));
    lds_w128(sw2, pack8(c0, 1));
    SGB(0x8, 2); SGB(0x2, 4); SGB(0x200, 1); SGB(0x8, 1); SGB(0x2, 4); SGB(0x200, 1); SGB(0x8, 1);
    SCHED();
    STAMP(9);
    lds_w128(sw3, pack8(c1, 0));
    lds_w128(sw4, pack8(c1, 1));
    asm volatile("" ::: "memory");
    {
      u32x4 v[4];
#pragma unroll
      for (int i = 0; i < 4; ++i) v[i] = lds_r128(sr1 + i * 1024);
      SCHED();
      st128c(v[0], outr, orow);
      st128c(v[1], outr, or1);
      st128c(v[2], outr, or2);
      st128c(v[3], outr, or3);
    }
    if (t == 0 && panel == 0 && grp == 0 && lane == 0) st32c(key, wsr, WS_EPOCH + br * 256);
    if (PROBE_N == 13) WAITV(0);
    STAMP(13);
    STAMP_OUT();
  }
}
}

extern "C" void kernel_launch(void* const* d_in, const int* in_sizes, int n_in,
                              void* d_out, int out_size, void* d_ws, size_t ws_size,
                              hipStream_t stream) {
  hh::hedgehog<<<256, 512, 0, stream>>>((const u16*)d_in[0], (const u16*)d_in[1], (const u16*)d_in[2], (const u16*)d_in[3],
                                        (const u16*)d_in[4], (const u16*)d_in[5], (const u16*)d_in[6], (const u16*)d_in[7],
                                        (u16*)d_out, (u32*)d_ws);
}
```

```cpp
#define PROBE_N 12
#define PROBE_ROLE 0
#include <hip/hip_runtime.h>
#include <stdint.h>

typedef unsigned short u16;
typedef unsigned u32;
typedef short bf16x8 __attribute__((ext_vector_type(8)));
typedef short s16x4 __attribute__((ext_vector_type(4)));
typedef float f32x16 __attribute__((ext_vector_type(16)));
typedef float f32x4 __attribute__((ext_vector_type(4)));
typedef float f32x2 __attribute__((ext_vector_type(2)));
typedef unsigned u32x8 __attribute__((ext_vector_type(8)));
typedef unsigned u32x4 __attribute__((ext_vector_type(4)));
typedef unsigned u32x2 __attribute__((ext_vector_type(2)));
typedef __bf16 bf16x2 __attribute__((ext_vector_type(2)));
#define DI __device__ __forceinline__
#define LAS __attribute__((address_space(3)))
#define CAS __attribute__((address_space(4)))
#define BC(T, x) __builtin_bit_cast(T, x)

#define DELAY1 3
#define DELAY3 4
#define CODE_PF 29184
#define AHEAD 1
#define GQ 1
#define GC 2
#define GBURST 1
#define WARM 0
#define LATE1 2
#define OFIRST 5

namespace hh {
constexpr int AUX_SC1 = 16;
constexpr u32 SRD3 = 0x00020000u;
constexpr u32 WS_EPOCH = 0;
constexpr u32 REC = 1280;
constexpr u32 WS_PART = 4096;
constexpr u32 OREC = 4352;
constexpr u32 WS_O = WS_PART + 2 * 32 * 16 * REC;
constexpr u32 WS_END = WS_O + 2 * 64 * 8 * OREC;
constexpr u32 WS_STAMP = 8u << 20;
constexpr u32 SLOT = 32768;
constexpr u32 L_WQ0 = 98304;
constexpr u32 L_WQ1 = 65536;
constexpr u32 L_WPHI = 163840 - 4096;
constexpr u32 L_PHI = 0;
constexpr u32 VF = 1040;
constexpr u32 L_V = 131072;
constexpr u32 L_KV = L_V + 16 * VF;
constexpr u32 L_WO = 0;
constexpr u32 L_O = 65536;
constexpr u32 SPIN_CAP = 1u << 16;

DI u32 pk2(float a, float b) { return BC(u32, __builtin_convertvector((f32x2){a, b}, bf16x2)); }
DI u32x4 pack8(const f32x16& x, int s) {
  return (u32x4){pk2(x[8 * s], x[8 * s + 1]), pk2(x[8 * s + 2], x[8 * s + 3]), pk2(x[8 * s + 4], x[8 * s + 5]), pk2(x[8 * s + 6], x[8 * s + 7])};
}
DI f32x16 mfma(u32x4 a, u32x4 b, f32x16 c) { return __builtin_amdgcn_mfma_f32_32x32x16_bf16(BC(bf16x8, a), BC(bf16x8, b), c, 0, 0, 0); }
DI f32x4 mfma16(u32x4 a, u32x4 b, f32x4 c) { return __builtin_amdgcn_mfma_f32_16x16x32_bf16(BC(bf16x8, a), BC(bf16x8, b), c, 0, 0, 0); }
DI u32x4 lds_r128(u32 a) { return *(LAS u32x4*)(size_t)a; }
DI void lds_w128(u32 a, u32x4 v) { *(LAS u32x4*)(size_t)a = v; }
DI u32x4 lds_tr2(u32 a0, u32 a1) {
  s16x4 lo = __builtin_amdgcn_ds_read_tr16_b64_v4i16((LAS s16x4*)(size_t)a0);
  s16x4 hi = __builtin_amdgcn_ds_read_tr16_b64_v4i16((LAS s16x4*)(size_t)a1);
  return BC(u32x4, __builtin_shufflevector(lo, hi, 0, 1, 2, 3, 4, 5, 6, 7));
}
DI void dma1(u32 lds, u32 voff, __amdgpu_buffer_rsrc_t srd, u32 soff) {
  asm volatile("s_mov_b32 m0, %0\n\ts_nop 0\n\tbuffer_load_dwordx4 %1, %2, %3 offen lds" ::"s"(lds), "v"(voff), "s"(srd), "s"(soff) : "memory");
}
DI void dma4(u32 lds, const u32 (&v)[4], __amdgpu_buffer_rsrc_t srd, u32 soff) {
  asm volatile("s_mov_b32 m0, %0\n\ts_nop 0\n\t"
               "buffer_load_dwordx4 %1, %5, %6 offen lds\n\t"
               "buffer_load_dwordx4 %2, %5, %6 offen offset:1024 lds\n\t"
               "buffer_load_dwordx4 %3, %5, %6 offen offset:2048 lds\n\t"
               "buffer_load_dwordx4 %4, %5, %6 offen offset:3072 lds" ::"s"(lds), "v"(v[0]), "v"(v[1]), "v"(v[2]), "v"(v[3]), "s"(srd), "s"(soff) : "memory");
}
DI void dma_orec(u32 lds, u32 l4, u32 l16, __amdgpu_buffer_rsrc_t srd, u32 soff) {
  asm volatile("s_mov_b32 m0, %0\n\ts_nop 0\n\t"
               "buffer_load_dwordx4 %2, %3, %4 offen offset:256 sc1 lds\n\t"
               "buffer_load_dwordx4 %2, %3, %4 offen offset:1280 sc1 lds\n\t"
               "buffer_load_dwordx4 %2, %3, %4 offen offset:2304 sc1 lds\n\t"
               "buffer_load_dwordx4 %2, %3, %4 offen offset:3328 sc1 lds\n\t"
               "buffer_load_dword %1, %3, %4 offen sc1 lds" ::"s"(lds), "v"(l4), "v"(l16), "s"(srd), "s"(soff) : "memory");
}
#define TOUCH(d, voff, srd, soff) asm volatile("buffer_load_dword %0, %1, %2, %3 offen" : "=v"(d) : "v"(voff), "s"(srd), "s"(soff) : "memory")
DI void st128c(u32x4 v, __amdgpu_buffer_rsrc_t srd, u32 off) { __builtin_amdgcn_raw_buffer_store_b128(v, srd, off, 0, AUX_SC1); }
DI void st32c(u32 v, __amdgpu_buffer_rsrc_t srd, u32 off) { __builtin_amdgcn_raw_buffer_store_b32(v, srd, off, 0, AUX_SC1); }
#define BAR() asm volatile("s_waitcnt lgkmcnt(0)\n\ts_barrier" ::: "memory")
#define BARE() asm volatile("s_barrier" ::: "memory")
#define OPQ(x) asm("" : "+v"(x))
#define PIN(x) asm volatile("" : "+v"(x))
#define PINS(x) asm volatile("" : "+s"(x))
#define WAITV(n) asm volatile("s_waitcnt vmcnt(" #n ")" ::: "memory")
#define SCHED() __builtin_amdgcn_sched_barrier(0)
#define SGB(m, n) __builtin_amdgcn_sched_group_barrier(m, n, 0)
DI u32 sum4(u32 c, u32x4 v) { return (c + v.x + v.y) + v.z + v.w; }
DI u32 pair_sum(u32 s) { auto t = __builtin_amdgcn_permlane32_swap(s, s, false, false); return t[0] + t[1]; }

constexpr float LOG2E = 1.44269504088896340736f;
DI float max3f(float a, float b, float c) { return __builtin_fmaxf(__builtin_fmaxf(a, b), c); }
DI float negmax16(const f32x16& x) {
  const float a = max3f(x[0], x[1], x[2]), b = max3f(x[3], x[4], x[5]), c = max3f(x[6], x[7], x[8]);
  const float d = max3f(x[9], x[10], x[11]), e = max3f(x[12], x[13], x[14]);
  return -max3f(max3f(a, b, x[15]), max3f(c, d, e), a) * LOG2E;
}
DI void sm_exp(const f32x16& p, const f32x16& q, const float (&mc)[2], f32x2 (&e)[2][8]) {
#pragma unroll
  for (int i = 0; i < 8; ++i) {
    const f32x2 t = __builtin_elementwise_fma((f32x2){p[2 * i], p[2 * i + 1]}, (f32x2){LOG2E, LOG2E}, (f32x2){mc[0], mc[0]});
    const f32x2 u = __builtin_elementwise_fma((f32x2){q[2 * i], q[2 * i + 1]}, (f32x2){LOG2E, LOG2E}, (f32x2){mc[1], mc[1]});
    e[0][i] = (f32x2){__builtin_amdgcn_exp2f(t.x), __builtin_amdgcn_exp2f(t.y)};
    e[1][i] = (f32x2){__builtin_amdgcn_exp2f(u.x), __builtin_amdgcn_exp2f(u.y)};
  }
  SGB(0x2, 4); SGB(0x400, 8); SGB(0x2, 4); SGB(0x400, 8); SGB(0x2, 6); SGB(0x400, 8); SGB(0x2, 8); SGB(0x400, 8);
}
DI void sm_exp_s(const f32x16& p, const f32x16& q, const float (&mc)[2], f32x2 (&e)[2][8]) {
#pragma unroll
  for (int i = 0; i < 16; ++i) {
    e[0][i >> 1][i & 1] = __builtin_amdgcn_exp2f(__builtin_fmaf(p[i], LOG2E, mc[0]));
    e[1][i >> 1][i & 1] = __builtin_amdgcn_exp2f(__builtin_fmaf(q[i], LOG2E, mc[1]));
  }
}
DI void sm_finish(const f32x2 (&e)[2][8], u32x4 (&o)[2][2]) {
  float r[2];
#pragma unroll
  for (int u = 0; u < 2; ++u) {
    const f32x2 s = ((e[u][0] + e[u][1]) + (e[u][2] + e[u][3])) + ((e[u][4] + e[u][5]) + (e[u][6] + e[u][7]));
    r[u] = __builtin_amdgcn_rcpf(s.x + s.y);
  }
#pragma unroll
  for (int u = 0; u < 2; ++u) {
    u32 w[8];
#pragma unroll
    for (int i = 0; i < 8; ++i) { const f32x2 t = e[u][i] * (f32x2){r[u], r[u]}; w[i] = pk2(t.x, t.y); }
    o[u][0] = (u32x4){w[0], w[1], w[2], w[3]};
    o[u][1] = (u32x4){w[4], w[5], w[6], w[7]};
  }
}
DI void softmax16x2(const f32x16& p, const f32x16& q, const float (&mc)[2], u32x4 (&o)[2][2]) {
  f32x2 e[2][8];
  sm_exp(p, q, mc, e);
  sm_finish(e, o);
}

#define STAMP(i) do { if ((i) == PROBE_N) { SCHED(); asm volatile("s_memrealtime %0\n\ts_waitcnt lgkmcnt(0)" : "=s"(pt1) :: "memory"); SCHED(); } } while (0)
#define STAMP_OUT() do { if (PROBE_N && (wave >> 2) == PROBE_ROLE) { unsigned long long d_ = pt1 - pt0; if (d_ > 100000) d_ = 100000; unsigned long long e_ = pt0 + 3000 + 8 * d_; \
  for (int it_ = 0; it_ < 1000000; ++it_) { unsigned long long t_; asm volatile("s_memrealtime %0\n\ts_waitcnt lgkmcnt(0)" : "=s"(t_) :: "memory"); if (t_ >= e_) break; } } } while (0)

__global__ __launch_bounds__(512, 2) void hedgehog(const u16* xA, const u16* wA, const u16* pA, const u16* oA,
                                                   const u16* xB, const u16* wB, const u16* pB, const u16* oB,
                                                   u16* out_, u32* ws_) {
  __shared__ __attribute__((aligned(16))) char smem[163840];
  const u32 lds0 = (u32)(size_t)(LAS char*)smem;
  unsigned long long pt0 = __builtin_amdgcn_s_memrealtime(), pt1 = pt0;
  const int bid = blockIdx.x, xcd = bid & 7, idx = bid >> 3;
  const int br = xcd >> 2;
#define KLOAD(tail)                                                                                       \
  u32x8 ka;                                                                                               \
  u32x4 kb;                                                                                               \
  asm volatile("s_load_dwordx8 %0, %2, %3\n\ts_load_dwordx4 %1, %2, 0x40" tail : "=&s"(ka), "=&s"(kb)     \
               : "s"(__builtin_amdgcn_kernarg_segment_ptr()), "s"(br * 32))
#define KWAIT() asm volatile("s_waitcnt lgkmcnt(0)" : "+s"(ka), "+s"(kb))
#define KPTR(v, i) ((void*)(((unsigned long long)(v)[(i) + 1] << 32) | (v)[i]))
  unsigned long long pcs;
  asm volatile("s_getpc_b64 %0" : "=s"(pcs));
  pcs &= ~255ull;
  const int lane = threadIdx.x & 63;
  const int wave = __builtin_amdgcn_readfirstlane(threadIdx.x >> 6);
  const int panel = (xcd & 3) * 4 + (idx & 3), grp = idx >> 2, gq = (grp + GQ) & 7, cg = (grp + GC) & 7;
  auto ochunk = [&](u32 j) -> u32 {
    const int v = ((int)j + (((int)j - 1 - ((gq - OFIRST) & 7)) >> 31) + OFIRST) & 7, z = ((int)j - 1) >> 31;
    return (u32)__builtin_amdgcn_readfirstlane((v & ~z) | (gq & z));
  };
  f32x16 zero;
#pragma unroll
  for (int i = 0; i < 16; ++i) zero[i] = 0.f;
#define LANE_IDS()                                                                            \
  u32 lane_ = lane;                                                                           \
  PIN(lane_);                                                                                 \
  const u32 r = lane_ & 31, h = lane_ >> 5;                                                   \
  const u32 blk = (lane_ >> 4) & 1, q = (lane_ & 15) >> 2, p = lane_ & 3;                     \
  u32* ws = (u32*)KPTR(kb, 2);                                                                \
  const __amdgpu_buffer_rsrc_t wsr = __builtin_amdgcn_make_buffer_rsrc(ws, 0, WS_END, SRD3);  \
  const __amdgpu_buffer_rsrc_t wor = __builtin_amdgcn_make_buffer_rsrc(KPTR(ka, 6), 0, 512 * 512 * 2, SRD3)

  if (wave >= 4) {
    const int cw = wave - 4;
    const int rh = cw >> 1, hp = cw & 1;
    KLOAD("\n\ts_waitcnt lgkmcnt(0)");
    u32 sp0, sp1, sp2, sp3;
    {
      u32 off = idx * 1024 + cw * 256;
      off = off < CODE_PF ? off : 0;
      asm volatile("s_load_dword %0, %4, %5 offset:0\n\ts_load_dword %1, %4, %5 offset:64\n\ts_load_dword %2, %4, %5 offset:128\n\ts_load_dword %3, %4, %5 offset:192"
                   : "=&s"(sp0), "=&s"(sp1), "=&s"(sp2), "=&s"(sp3) : "s"(pcs), "s"(off));
    }
    LANE_IDS();
    u32 key = *(const CAS u32*)((const char*)ws + WS_EPOCH + br * 256) + 1u;
    u32 xa[2], xb[2], wa[2], wb2[2];
    {
      const u32 r16 = lane_ & 15, g = lane_ >> 4;
#pragma unroll
      for (int s = 0; s < 2; ++s) {
        xa[s] = lds0 + (64 * rh + r16) * 128 + 16 * ((4 * s + g) ^ (r16 >> 1));
        wa[s] = lds0 + 16384 + (8 * g + q) * 128 + 16 * ((4 * hp + 2 * s + (p >> 1)) ^ ((((q >> 1) & 1) << 2) | (blk << 1))) + 8 * (p & 1);
        xb[s] = xa[s] + 2 * SLOT; wb2[s] = wa[s] + 2 * SLOT;
        PIN(xa[s]); PIN(xb[s]); PIN(wa[s]); PIN(wb2[s]);
      }
    }
    u32 fpa = lds0 + L_WPHI + hp * 1024 + (8 * h + q) * 32 + (8 * blk + 4 * (p >> 1)) * 2;
    PIN(fpa);
    if (!WARM) PINS(key);
    f32x4 ak[4][2], av[4][2];
#pragma unroll
    for (int i = 0; i < 8; ++i) ak[i >> 1][i & 1] = av[i >> 1][i & 1] = (f32x4){0.f, 0.f, 0.f, 0.f};
    u32 vra, pra, vwa, pw0, pw1, pub16, pub4;
    u32x4 xf[2][4], wk[2][2], wv[2][2];
    auto ldx = [&](int c, int s) {
      const u32 x = c % 3 == 2 ? xb[s] : xa[s] + (c % 3) * SLOT;
#pragma unroll
      for (int i = 0; i < 4; ++i) xf[s][i] = lds_r128(x + i * 2048);
    };
    auto ldw = [&](int c, int s, int m) {
#pragma unroll
      for (int n = 0; n < 2; ++n) {
        const u32 w = (c % 3 == 2 ? wb2[n] : wa[n] + (c % 3) * SLOT) + s * 4096 + m * 8192;
        (m ? wv : wk)[s][n] = lds_tr2(w, w + 512);
      }
    };
    auto mk = [&](int s) {
#pragma unroll
      for (int i = 0; i < 8; ++i) ak[i >> 1][i & 1] = mfma16(wk[s][i & 1], xf[s][i >> 1], ak[i >> 1][i & 1]);
    };
    auto mv = [&](int s) {
#pragma unroll
      for (int i = 0; i < 8; ++i) av[i >> 1][i & 1] = mfma16(xf[s][i >> 1], wv[s][i & 1], av[i >> 1][i & 1]);
    };
    auto mix = [&](int n) {
#pragma unroll
      for (int i = 0; i < 8; ++i) {
        SGB(0x8, 1);
        if (n == 12) { if (i < 4) SGB(0x100, 2); else SGB(0x100, 1); }
        else if (n == 8) SGB(0x100, 1);
        else if (n == 4) { if (i < 4) SGB(0x100, 1); }
        else if (n < 0) SGB(0x6, 2);
      }
      SCHED();
    };
    if (WARM) {
      u32 hsh = lane * 2654435761u + 12345u;
      u32x4 a = {(hsh & 0x3FFF3FFFu) | 0x30003000u, ((hsh * 7u) & 0xBFFFBFFFu) | 0x30003000u, ((hsh * 13u) & 0x3FFFBFFFu) | 0x30003000u, ((hsh * 29u) & 0xBFFF3FFFu) | 0x30003000u};
      u32x4 b = {a.y, a.w, a.x, a.z};
      f32x16 w0 = zero, w1 = zero;
#pragma unroll 1
      for (int i = 0; i < WARM; ++i) { w0 = mfma(a, b, w0); w1 = mfma(b, a, w1); w0 = mfma(b, b, w0); w1 = mfma(a, a, w1); PIN(w0); PIN(w1); }
    }
    BAR();
    asm volatile("" ::"s"(sp0), "s"(sp1), "s"(sp2), "s"(sp3));
    if (WARM) PINS(key);
    STAMP(1);
    ldw(0, 0, 0); ldx(0, 0); ldw(0, 0, 1);
    u32x4 fa[2];
#pragma unroll
    for (int hh = 0; hh < 2; ++hh) fa[hh] = lds_tr2(fpa + hh * 512, fpa + hh * 512 + 128);
    SCHED();
#pragma unroll
    for (int c = 0; c < 7; ++c) {
      ldw(c, 1, 0); ldx(c, 1); mk(0); mix(8);
      ldw(c, 1, 1); mv(0); mix(4);
      u32 ln = lane;
      PIN(ln);
      const u32 r_ = ln & 31, h_ = ln >> 5, q_ = (ln & 15) >> 2;
      if (c == 1) {
        const u32 sg = (ln & 4) ? 0x80008000u : 0u;
#pragma unroll
        for (int hh = 0; hh < 2; ++hh) { OPQ(fa[hh]); fa[hh] ^= (u32x4){sg, sg, sg, sg}; }
      }
      if (c == 2) vra = lds0 + L_V + (cw >> 1) * (2 * VF) + (r_ < 16 ? (ln + 16 * (cw & 1)) * 16 : 1024);
      if (c == 3) pra = lds0 + L_PHI + cw * 8192 + (4 * h_ + q_) * 64 + 16 * ((((ln >> 3) & 2) + ((ln >> 1) & 1)) ^ ((2 * h_ + (q_ >> 1)) & 3)) + 8 * (ln & 1);
      if (c == 4) {
        const u32 rec = WS_PART + ((br * 32 + grp * 4 + cw) * 16 + panel) * REC;
        pub16 = rec + ln * 16; pub4 = rec + 1024 + ln * 4;
      }
      if (c == 5) { pw0 = lds0 + L_PHI + (2 * hp) * 8192 + (64 * rh + r_) * 64 + 16 * ((2 * h_) ^ ((r_ >> 1) & 3)); pw1 = pw0 ^ 16; }
      if (c == 6) vwa = lds0 + L_V + (8 * rh + 2 * hp) * VF + ((ln & 16) * 2 + (ln & 15)) * 16 + h_ * 8;
      mk(1); mix(c >= 1 ? -1 : 0);
      if (c == 1) { PIN(fa[0]); PIN(fa[1]); }
      if (c == 2) PIN(vra);
      if (c == 3) PIN(pra);
      if (c == 4) { PIN(pub16); PIN(pub4); }
      if (c == 5) { PIN(pw0); PIN(pw1); }
      if (c == 6) PIN(vwa);
      BAR();
      ldw(c + 1, 0, 0); ldx(c + 1, 0); ldw(c + 1, 0, 1);
      mv(1); mix(12);
    }
    ldw(7, 1, 0); ldx(7, 1); ldw(7, 1, 1); mk(0); mix(12);
    mk(1); mix(0);
    __builtin_amdgcn_s_setprio(2);
    STAMP(2);
    f32x16 pk[2][2];
    {
      u32x4 kf[2][2];
      auto kpack = [&](int i) {
#pragma unroll
        for (int hh = 0; hh < 2; ++hh) {
          const f32x4 &t0 = ak[2 * i][hh], &t1 = ak[2 * i + 1][hh];
          const auto lo = __builtin_amdgcn_permlane16_swap(pk2(t0[0], t0[1]), pk2(t1[0], t1[1]), false, false);
          const auto hi = __builtin_amdgcn_permlane16_swap(pk2(t0[2], t0[3]), pk2(t1[2], t1[3]), false, false);
          kf[i][hh] = (u32x4){lo[0], hi[0], lo[1], hi[1]};
        }
      };
      mv(0); kpack(0);
      SGB(0x8, 2);
#pragma unroll
      for (int i = 0; i < 6; ++i) { SGB(0x2, 2); SGB(0x8, 1); }
      SCHED();
      pk[0][0] = mfma(fa[0], kf[0][0], zero);
      pk[0][1] = mfma(fa[1], kf[0][1], zero);
      kpack(1);
      SGB(0x8, 1); SGB(0x2, 6); SGB(0x8, 1); SGB(0x2, 6);
      PIN(pk[0][0]); PIN(pk[0][1]); PIN(kf[1][0]); PIN(kf[1][1]);
      SCHED();
      mv(1);
      pk[1][0] = mfma(fa[0], kf[1][0], zero);
      pk[1][1] = mfma(fa[1], kf[1][1], zero);
    }
    float m0[2], m1[2];
    m0[0] = negmax16(pk[0][0]); m0[1] = negmax16(pk[0][1]);
    SGB(0x8, 2);
#pragma unroll
    for (int i = 0; i < 6; ++i) { SGB(0x2, 2); SGB(0x8, 1); }
    SGB(0x8, 1); SGB(0x2, 6); SGB(0x8, 1);
#pragma unroll
    for (int i = 0; i < 8; ++i) PIN(av[i >> 1][i & 1]);
    PIN(pk[1][0]); PIN(pk[1][1]); PIN(m0[0]); PIN(m0[1]);
    SCHED();
#pragma unroll
    for (int i = 0; i < 8; ++i) {
      const f32x4& t = av[i >> 1][i & 1];
      *(LAS u32x2*)(size_t)(vwa + (4 * (i >> 2) + ((i >> 1) & 1)) * VF + (i & 1) * 256) = (u32x2){pk2(t[0], t[1]), pk2(t[2], t[3])};
    }
    SCHED();
    {
      u32x4 o[2][2];
      softmax16x2(pk[0][0], pk[0][1], m0, o);
      lds_w128(pw0, o[0][0]); lds_w128(pw1, o[0][1]);
      lds_w128(pw0 + 8192, o[1][0]); lds_w128(pw1 + 8192, o[1][1]);
      SCHED();
      m1[0] = negmax16(pk[1][0]); m1[1] = negmax16(pk[1][1]);
      PIN(m1[0]); PIN(m1[1]);
      SCHED();
      softmax16x2(pk[1][0], pk[1][1], m1, o);
      lds_w128(pw0 + 2048, o[0][0]); lds_w128(pw1 + 2048, o[0][1]);
      lds_w128(pw0 + 2048 + 8192, o[1][0]); lds_w128(pw1 + 2048 + 8192, o[1][1]);
      SGB(0x402, 26); SGB(0x200, 1); SGB(0x402, 8); SGB(0x200, 1); SGB(0x402, 8); SGB(0x200, 1); SGB(0x402, 8); SGB(0x200, 1);
      SCHED();
      BARE();
      BAR();
      SCHED();
    }
    STAMP(3);
    f32x16 kv = zero;
    {
      u32x4 a[8], b[8];
#pragma unroll
      for (int u = 0; u < 8; ++u) {
        a[u] = lds_r128(vra + ((u >> 1) * 4 + (u & 1)) * VF);
        b[u] = lds_tr2(pra + 16 * u * 64, pra + (16 * u + 8) * 64);
      }
#pragma unroll
      for (int u = 0; u < 8; ++u) kv = mfma(a[u], b[u], kv);
      SGB(0x100, 12); SGB(0x8, 1); SGB(0x100, 6); SGB(0x8, 1); SGB(0x100, 6); SGB(0x8, 6);
    }
    {
      const u32x4 w = pack8(kv, 0);
      st128c(w, wsr, pub16);
      const u32 zb = __float_as_uint(kv[8]);
      const u32 chk = key - pair_sum(sum4(h ? 0u : zb, w));
      st32c(h ? chk : zb, wsr, pub4);
    }
    SCHED();
    STAMP(4);
    u32 l16, l4, kwa, kza, gso[4], vo[4], wso;
    u32x4 sel;
    {
      u32 ln = lane;
      PIN(ln);
#pragma unroll
      for (int i = 0; i < 4; ++i) vo[i] = (ln >> 3) * 1024 + 16 * ((ln & 7) ^ (((ln >> 4) & 1) << 2)) + i * 7168;
#pragma unroll
      for (int i = 0; i < 2; ++i) dma4(lds0 + L_WO + cw * 8192 + i * 4096, vo, wor, ochunk(cw) * 65536 + i * 32768 + cg * 128);
      wso = ochunk(4 + cw) * 65536 + cg * 128;
      const u32 r_ = ln & 31, h_ = ln >> 5;
      l16 = ln * 16; l4 = ln * 4;
      kwa = lds0 + L_KV + cw * 1536 + r_ * 48 + h_ * 16; kza = lds0 + L_KV + cw * 1536 + r_ * 48 + 32 + h_ * 8;
      const bool hit = r_ < 16 && ((r_ >> 2) & 1) == h_;
      const u32 j = (r_ & 3) + 4 * (r_ >> 3);
#pragma unroll
      for (int e = 0; e < 4; ++e) sel[e] = (hit && (j >> 1) == e) ? ((j & 1) ? 0x3F800000u : 0x00003F80u) : 0u;
#pragma unroll
      for (int i = 0; i < 4; ++i) { gso[i] = WS_PART + ((br * 32 + gq * 4 + cw) * 16 + ((i + idx) & 3) * 4) * REC; PINS(gso[i]); }
      PIN(l16); PIN(l4); PIN(kwa); PIN(kza); PIN(sel); PINS(wso);
    }
    if (DELAY1) __builtin_amdgcn_s_sleep(DELAY1);
    STAMP(10);
    f32x16 gs[4];
    float z[4];
    {
      u32x4 g[16];
      u32 gz[16];
      auto gload = [&](int gi) {
#pragma unroll
        for (int i = 0; i < 4; ++i) {
          g[4 * gi + i] = __builtin_amdgcn_raw_buffer_load_b128(wsr, l16 + i * REC, gso[gi], AUX_SC1);
          gz[4 * gi + i] = __builtin_amdgcn_raw_buffer_load_b32(wsr, l4 + (1024 + i * REC - 1024 * (i == 3)), gso[gi] + 1024 * (i == 3), AUX_SC1);
          SCHED();
        }
      };
      auto gsum = [&](int gi) -> bool {
        u32 c = 0;
        if (GBURST) { PIN(gz[4 * gi + 3]); SCHED(); }
#pragma unroll
        for (int i = 0; i < 4; ++i) {
          c = sum4(c + gz[4 * gi + i], g[4 * gi + i]);
          gs[gi] = mfma(sel, g[4 * gi + i], i == 0 ? zero : gs[gi]);
          z[gi] = i == 0 ? __uint_as_float(gz[4 * gi]) : z[gi] + __uint_as_float(gz[4 * gi + i]);
        }
        const bool bad = __builtin_amdgcn_ballot_w64(pair_sum(c) != 4u * key) != 0;
        if (GBURST) { SGB(0x8, 1); SGB(0x2, 5); SGB(0x8, 1); SGB(0x2, 5); SGB(0x8, 1); SGB(0x2, 4); SGB(0x8, 1); }
        PIN(z[gi]);
        SCHED();
        return bad;
      };
      gload(0); gload(1); gload(2); gload(3);
      bool b0 = gsum(0), b1 = gsum(1), b2 = gsum(2), b3 = gsum(3);
      STAMP(11);
#pragma unroll 1
      for (u32 spin = 0; (b0 | b1 | b2 | b3) && spin < SPIN_CAP; ++spin) {
        asm volatile("" ::: "memory");
        if (b0) gload(0);
        if (b1) gload(1);
        if (b2) gload(2);
        if (b3) gload(3);
        if (b0) b0 = gsum(0);
        if (b1) b1 = gsum(1);
        if (b2) b2 = gsum(2);
        if (b3) b3 = gsum(3);
      }
    }
    __builtin_amdgcn_s_waitcnt(0x0F70);
    STAMP(5);
    {
      float tt[8];
#pragma unroll
      for (int j = 0; j < 8; ++j) tt[j] = (gs[0][j] + gs[1][j]) + (gs[2][j] + gs[3][j]);
      const float zt = (z[0] + z[1]) + (z[2] + z[3]);
      lds_w128(kwa, (u32x4){pk2(tt[0], tt[1]), pk2(tt[2], tt[3]), pk2(tt[4], tt[5]), pk2(tt[6], tt[7])});
      const u32 zz = pk2(zt, zt);
      *(LAS u32x2*)(size_t)kza = (u32x2){zz, zz};
    }
#pragma unroll
    for (int i = 0; i < 2; ++i) dma4(lds0 + L_WO + 32768 + cw * 8192 + i * 4096, vo, wor, wso + i * 32768);
    BAR();
    STAMP(6);
    {
      SCHED();
      u32 oso[8], ob = lds0 + L_O + cw * OREC, i0 = 0;
      PINS(i0);
#pragma unroll
      for (int i = 1; i < 8; ++i) { oso[i] = WS_O + ((br * 64 + panel * 4 + cw) * 8 + ochunk(i + i0)) * OREC; PINS(oso[i]); }
      PINS(ob);
      WAITV(0);
      BARE();
      if (DELAY3) __builtin_amdgcn_s_sleep(DELAY3);
      STAMP(8);
      auto oissue = [&](int j) { dma_orec(ob + (j % 5) * (4 * OREC), l4, l16, wsr, oso[j]); };
      oissue(1); oissue(2);
      WAITV(5); BARE(); oissue(3);
      WAITV(5); STAMP(15); BARE(); oissue(4); oissue(5);
      WAITV(10); BARE(); oissue(6);
      WAITV(10); BARE(); oissue(7);
      WAITV(10); BARE();
      WAITV(5); BARE();
      WAITV(0); BARE();
    }
    STAMP(9);
    STAMP_OUT();
  } else {
    KLOAD("");
    const int t = wave;
    const u32 l7 = lane & 7, l3 = lane >> 3, lq = lane >> 4;
    const u32 wsw = 16 * (l7 ^ ((lq & 1) << 2));
    u32 vx[4], vw[4];
#pragma unroll
    for (int i = 0; i < 4; ++i) {
      vx[i] = l3 * 1024 + 16 * (l7 ^ (((i & 1) << 2) | lq)) + i * 7168;
      vw[i] = l3 * 3072 + (wsw ^ ((i & 1) << 5)) + i * (8 * 3072 - 1024);
    }
    auto chunk = [&](u32 c) -> u32 {
      const u32 g = grp + idx;
      return c - 2 < 4 ? 2 + ((c - 2) ^ (g & 3)) : c ^ (g & 1);
    };
    const u32 xs0 = (panel * 128 + t * 32) * 1024, ws0 = (t & 1) * (32 * 3072) + (1 + (t >> 1)) * 1024 + grp * 128;
    const u32 ldst = lds0 + t * 4096;
    u32 e0 = ldst, e1 = ldst + 16384, e2 = xs0 + chunk(0) * 128, e3 = ws0 + chunk(0) * (64 * 3072);
    PINS(e0); PINS(e1); PINS(e2); PINS(e3);
#pragma unroll
    for (int i = 0; i < 4; ++i) { PIN(vx[i]); PIN(vw[i]); }
    KWAIT();
    const __amdgpu_buffer_rsrc_t xr = __builtin_amdgcn_make_buffer_rsrc(KPTR(ka, 0), 0, 2048 * 512 * 2, SRD3);
    const __amdgpu_buffer_rsrc_t wr = __builtin_amdgcn_make_buffer_rsrc(KPTR(ka, 2), 0, 512 * 1536 * 2, SRD3);
    STAMP(10);
    dma4(e0, vx, xr, e2);
    dma4(e1, vw, wr, e3);
    {
      const __amdgpu_buffer_rsrc_t pr = __builtin_amdgcn_make_buffer_rsrc(KPTR(ka, 4), 0, 32 * 256 * 2, SRD3);
      dma1(lds0 + L_WPHI + t * 1024, lane * 16, pr, (t < 2 ? grp : gq) * 2048 + (t & 1) * 1024);
    }
    STAMP(11);
    if (!LATE1) {
      dma4(ldst + SLOT, vx, xr, xs0 + chunk(1) * 128);
      dma4(ldst + SLOT + 16384, vw, wr, ws0 + chunk(1) * (64 * 3072));
    }
    LANE_IDS();
    u32 key = *(const CAS u32*)((const char*)ws + WS_EPOCH + br * 256);
    const u32 sw = (r >> 1) & 7;
    u32 xa[4];
#pragma unroll
    for (int s = 0; s < 4; ++s) { xa[s] = lds0 + (32 * t + r) * 128 + 16 * ((2 * s + h) ^ sw); PIN(xa[s]); }
    u32 fpa = lds0 + L_WPHI + 2048 + (4 * h + q) * 32 + (8 * blk + 4 * (p >> 1)) * 2;
    PIN(fpa);
    if (LATE1 != 2) { key += 1u; PINS(key); }
    u32x4 xq[32], fa[4];
    u32 wa, wb, kra, o_off, c_off, woa, wob, vto, sto, tho;
#pragma unroll
    for (int c = 0; c < 8; ++c) {
      u32 d0, d1, s0, s1;
      if (c < 6) {
        d0 = ldst + ((c + 2) % 3) * SLOT; d1 = d0 + 16384;
        s0 = xs0 + chunk(c + 2) * 128; s1 = ws0 + chunk(c + 2) * (64 * 3072);
      } else {
        const u32 k0 = 2 * ((2 * (c - 6) + idx) & 3) + (t >> 1), k1 = (k0 + 2) & 7;
        d0 = lds0 + L_WQ1 + ((chunk(k0) + 4) & 7) * 8192 + (t & 1) * 4096; d1 = lds0 + L_WQ1 + ((chunk(k1) + 4) & 7) * 8192 + (t & 1) * 4096;
        s0 = (k0 * 64 + (t & 1) * 32) * 3072 + gq * 128; s1 = (k1 * 64 + (t & 1) * 32) * 3072 + gq * 128;
      }
      PINS(d0); PINS(d1); PINS(s0); PINS(s1);
      if (LATE1 && c == 0) {
        SCHED();
        dma4(ldst + SLOT, vx, xr, xs0 + chunk(1) * 128);
        dma4(ldst + SLOT + 16384, vw, wr, ws0 + chunk(1) * (64 * 3072));
        STAMP(1);
        if (LATE1 == 2) { PINS(key); key += 1u; }
      }
      WAITV(8);
      BAR();
      if (c < 6) dma4(d0, vx, xr, s0); else dma4(d0, vw, wr, s0);
#pragma unroll
      for (int s = 0; s < 4; ++s) xq[4 * c + s] = lds_r128(xa[s] + (c % 3) * SLOT);
      dma4(d1, vw, wr, s1);
      if (c == 0) {
#pragma unroll
        for (int hd = 0; hd < 4; ++hd) fa[hd] = lds_tr2(fpa + hd * 512, fpa + hd * 512 + 256);
      }
      if (c == 1) {
        lds_w128(lds0 + L_V + (lane & 15) * VF + 1024, (u32x4){0x3F803F80u, 0x3F803F80u, 0x3F803F80u, 0x3F803F80u});
        const u32 sg = (lane & 4) ? 0x80008000u : 0u;
#pragma unroll
        for (int hd = 0; hd < 4; ++hd) { PIN(fa[hd]); fa[hd] ^= (u32x4){sg, sg, sg, sg}; PIN(fa[hd]); }
        wa = lds0 + L_WQ1 + (8 * h + q) * 128 + 16 * ((2 * blk + (p >> 1)) ^ ((((q >> 1) & 1) << 2) | (h << 1))) + 8 * (p & 1);
        wb = wa ^ 64;
        kra = lds0 + L_KV + (16 * h + q) * 48 + (blk ? 32 : 8 * p);
        o_off = WS_O + ((br * 64 + panel * 4 + t) * 8 + gq) * OREC + 256 + lane * 16;
        c_off = WS_O + ((br * 64 + panel * 4 + t) * 8 + gq) * OREC + lane * 4;
        woa = lds0 + L_WO + (8 * h + q) * 128 + 16 * ((2 * blk + (p & 1)) ^ (((q >> 1) & 1) << 2)) + 8 * (p >> 1);
        wob = woa ^ 64;
        vto = r * 128; sto = (idx * 16 + t * 4) * 1024;
        PIN(wa); PIN(wb); PIN(kra); PIN(o_off); PIN(c_off); PIN(woa); PIN(wob); PIN(vto); PINS(sto);
      }
    }
    TOUCH(tho, vto, wor, sto);
    STAMP(2);
    u32x4 fq[2][2][2];
    {
      f32x16 aq[2], pq[2];
      float mq[2];
      aq[0] = aq[1] = zero;
      u32x4 wf[4];
      auto ldw = [&](int n) {
        const int k = n & 31;
        const u32 o = (n < 32 ? wa : wb) + (k < 16 ? (L_WQ0 - L_WQ1) : 0) + (k & 15) * 2048;
        wf[n & 3] = lds_tr2(o, o + 512);
      };
      WAITV(1);
      BAR();
      SCHED();
      STAMP(12);
      ldw(0); ldw(1); ldw(2);
      SCHED();
      BARE();
      SCHED();
#pragma unroll
      for (int n = 0; n < 34; ++n) {
        ldw(n + 3);
        aq[n >> 5] = mfma(wf[n & 3], xq[n & 31], aq[n >> 5]);
        SGB(0x8, 1); SGB(0x100, 2);
        SCHED();
      }
#pragma unroll
      for (int hh = 0; hh < 2; ++hh) pq[hh] = mfma(fa[hh], pack8(aq[0], hh), zero);
      SCHED();
      STAMP(4);
#pragma unroll
      for (int n = 34; n < 64; ++n) {
        if (n + 3 < 64) ldw(n + 3);
        aq[1] = mfma(wf[n & 3], xq[n & 31], aq[1]);
      }
      mq[0] = negmax16(pq[0]); mq[1] = negmax16(pq[1]);
      f32x2 eq[2][8];
      sm_exp_s(pq[0], pq[1], mq, eq);
      SGB(0x8, 1); SGB(0x100, 2); SGB(0x8, 1); SGB(0x100, 2);
#pragma unroll
      for (int n = 36; n < 41; ++n) { SGB(0x8, 1); SGB(0x100, 2); SGB(0x402, 4); }
#pragma unroll
      for (int n = 41; n < 64; ++n) { SGB(0x8, 1); SGB(0x100, 2); if (n & 1) SGB(0x402, 3); else SGB(0x402, 2); }
#pragma unroll
      for (int i = 0; i < 16; ++i) PIN(eq[i >> 3][i & 7]);
      SCHED();
      STAMP(3);
#pragma unroll
      for (int hh = 0; hh < 2; ++hh) { pq[hh] = mfma(fa[2 + hh], pack8(aq[1], hh), zero); PIN(pq[hh]); }
      SCHED();
      sm_finish(eq, fq[0]);
      SCHED();
      asm volatile("s_waitcnt vmcnt(0)" : "+v"(tho)::"memory");
      mq[0] = negmax16(pq[0]); mq[1] = negmax16(pq[1]);
      PIN(mq[0]); PIN(mq[1]);
      SCHED();
#pragma unroll
      for (int i = 0; i < 4; ++i) PIN(fq[0][i >> 1][i & 1]);
      softmax16x2(pq[0], pq[1], mq, fq[1]);
#pragma unroll
      for (int i = 0; i < 4; ++i) PIN(fq[1][i >> 1][i & 1]);
    }
    STAMP(5);
    BAR();
    STAMP(6);
    u32x4 ob[2][4];
    {
      u32x4 a0[4], a1[4];
#pragma unroll
      for (int hd = 0; hd < 4; ++hd) {
        a0[hd] = lds_tr2(kra + hd * 1536, kra + hd * 1536 + 4 * 48);
        a1[hd] = lds_tr2(kra + hd * 1536 + 8 * 48, kra + hd * 1536 + 12 * 48);
      }
      SCHED();
      f32x16 y[4];
      u32 cs = 0;
      auto num = [&](int hd) {
        y[hd] = mfma(a0[hd], fq[hd >> 1][hd & 1][0], zero);
        y[hd] = mfma(a1[hd], fq[hd >> 1][hd & 1][1], y[hd]);
      };
      auto fin = [&](int hd) {
        const f32x16& yy = y[hd];
        const float rd = __builtin_amdgcn_rcpf(yy[8] + 1e-6f);
        u32 w[4];
#pragma unroll
        for (int e = 0; e < 4; ++e) { float t0 = yy[2 * e] * rd, t1 = yy[2 * e + 1] * rd; if (hd < 2) OPQ(t0); w[e] = pk2(t0, t1); }
        ob[0][hd] = (u32x4){w[0], w[1], w[2], w[3]};
        st128c(ob[0][hd], wsr, o_off + hd * 1024);
        cs = sum4(cs, ob[0][hd]);
      };
      num(0); num(1); PIN(y[0]); PIN(y[1]); SCHED();
      num(2); fin(0); SGB(0x8, 1); SGB(0x402, 6); SGB(0x8, 1); PIN(y[2]); SCHED();
      num(3); fin(1); SGB(0x8, 1); SGB(0x402, 6); SGB(0x8, 1); PIN(y[3]); SCHED();
      fin(2); fin(3);
      st32c(key - cs, wsr, c_off);
    }
    SCHED();
    STAMP(7);
    u32x4 wo[32][2];
    auto ldwo = [&](int n) {
      wo[n][0] = lds_tr2(woa + n * 2048, woa + n * 2048 + 512);
      wo[n][1] = lds_tr2(wob + n * 2048, wob + n * 2048 + 512);
    };
#pragma unroll
    for (int n = 0; n < 16; ++n) ldwo(n);
    f32x16 c0 = zero, c1 = zero;
#pragma unroll
    for (int s = 0; s < 4; ++s) { c0 = mfma(wo[s][0], ob[0][s], c0); c1 = mfma(wo[s][1], ob[0][s], c1); }
#pragma unroll
    for (int k = 4; k < 16; ++k) { PIN(wo[k][0]); PIN(wo[k][1]); }
    PIN(c0); PIN(c1);
    WAITV(0);
    BARE();
    SCHED();
#pragma unroll
    for (int n = 16; n < 24; ++n) ldwo(n);
#pragma unroll
    for (int k = 16; k < 24; ++k) { PIN(wo[k][0]); PIN(wo[k][1]); }
    const __amdgpu_buffer_rsrc_t outr = __builtin_amdgcn_make_buffer_rsrc(KPTR(kb, 0), 0, 2048 * 1024 * 2, SRD3);
    u32 oa = lds0 + L_O + t * OREC + 256 + lane * 16;
    u32 orow = ((panel * 128 + t * 32 + l3) * 1024 + br * 512 + cg * 64 + 8 * l7) * 2;
    PIN(oa); PIN(orow);
    u32 oc = oa - 256 - lane * 12, l4 = lane * 4, l16 = lane * 16, oa4 = oa + 4 * (4 * OREC), oc4 = oc + 4 * (4 * OREC);
    u32 sw1 = lds0 + t * 4096 + r * 128 + 16 * (h ^ (r & 7)), sr1 = lds0 + t * 4096 + l3 * 128 + 16 * (l7 ^ l3);
    u32 sw2 = sw1 ^ 32, sw3 = sw1 ^ 64, sw4 = sw1 ^ 96, or1 = orow + 16384, or2 = orow + 32768, or3 = orow + 49152;
    PIN(oc); PIN(l4); PIN(l16); PIN(sw1); PIN(sr1); PIN(oa4); PIN(oc4); PIN(sw2); PIN(sw3); PIN(sw4); PIN(or1); PIN(or2); PIN(or3);
    STAMP(8);
    u32 nretry = 0;
    u32 chk[2];
    auto oread = [&](int j) {
      const u32 da = j % 5 == 4 ? oa4 : oa + (j % 5) * (4 * OREC), dc = j % 5 == 4 ? oc4 : oc + (j % 5) * (4 * OREC);
#pragma unroll
      for (int s = 0; s < 4; ++s) ob[j & 1][s] = lds_r128(da + s * 1024);
      chk[j & 1] = *(LAS u32*)(size_t)dc;
    };
    auto obad = [&](int j) -> bool {
      const u32 c = sum4(sum4(chk[j & 1], ob[j & 1][0]), ob[j & 1][1]) + sum4(sum4(0u, ob[j & 1][2]), ob[j & 1][3]);
      return __builtin_amdgcn_ballot_w64(c != key) != 0;
    };
#pragma unroll
    for (int j = 1; j < 8; ++j) {
      BARE();
      SCHED();
      oread(j);
      if (j >= 2) {
#pragma unroll
        for (int s = 0; s < 4; ++s) {
          const int k = 4 * (j - 1) + s;
          c0 = mfma(wo[k][0], ob[(j - 1) & 1][s], c0);
          c1 = mfma(wo[k][1], ob[(j - 1) & 1][s], c1);
        }
        if (j >= 3 && j <= 6) { ldwo(24 + 2 * (j - 3)); ldwo(25 + 2 * (j - 3)); }
      }
      const bool bad = obad(j);
      if (j >= 3 && j <= 6) {
        SGB(0x8, 1); SGB(0x100, 3); SGB(0x8, 1); SGB(0x100, 2); SGB(0x8, 1); SGB(0x100, 3); SGB(0x8, 1); SGB(0x100, 3);
        SGB(0x8, 1); SGB(0x2, 4); SGB(0x8, 1); SGB(0x2, 5); SGB(0x8, 1); SGB(0x100, 2); SGB(0x8, 1);
      } else if (j >= 2) {
        SGB(0x8, 1); SGB(0x100, 3); SGB(0x8, 1); SGB(0x100, 2); SGB(0x8, 3); SGB(0x2, 4); SGB(0x8, 1); SGB(0x2, 5); SGB(0x8, 2);
      }
      SCHED();
      if (__builtin_expect(bad, 0)) {
#pragma unroll 1
        for (u32 spin = 0; spin < SPIN_CAP; ++spin) {
          dma_orec(lds0 + L_O + t * OREC + (j % 5) * (4 * OREC), l4, l16, wsr, WS_O + ((br * 64 + panel * 4 + t) * 8 + ochunk(j)) * OREC);
          u32 jj = j + 1, jend = j + AHEAD < 7 ? j + AHEAD : 7;
          PINS(jj); PINS(jend);
#pragma unroll 1
          for (; jj <= jend; ++jj) {
            const u32 sl = (jj >= 5 ? jj - 5 : jj) * (4 * OREC);
            u32 c = *(LAS u32*)(size_t)(oc + sl);
#pragma unroll
            for (int s = 0; s < 4; ++s) c = sum4(c, lds_r128(oa + sl + s * 1024));
            if (__builtin_amdgcn_ballot_w64(c != key) != 0)
              dma_orec(lds0 + L_O + t * OREC + sl, l4, l16, wsr, WS_O + ((br * 64 + panel * 4 + t) * 8 + ochunk(jj)) * OREC);
          }
          WAITV(0);
          oread(j);
          ++nretry;
          if (!obad(j)) break;
        }
      }
    }
#pragma unroll
    for (int s = 0; s < 4; ++s) c0 = mfma(wo[28 + s][0], ob[1][s], c0);
    PIN(c0);
    SCHED();
#pragma unroll
    for (int s = 0; s < 4; ++s) c1 = mfma(wo[28 + s][1], ob[1][s], c1);
    lds_w128(sw1, pack8(c0, 0));
    lds_w128(sw2, pack8(c0, 1));
    SGB(0x8, 2); SGB(0x2, 4); SGB(0x200, 1); SGB(0x8, 1); SGB(0x2, 4); SGB(0x200, 1); SGB(0x8, 1);
    SCHED();
    STAMP(9);
    lds_w128(sw3, pack8(c1, 0));
    lds_w128(sw4, pack8(c1, 1));
    asm volatile("" ::: "memory");
    {
      u32x4 v[4];
#pragma unroll
      for (int i = 0; i < 4; ++i) v[i] = lds_r128(sr1 + i * 1024);
      SCHED();
      st128c(v[0], outr, orow);
      st128c(v[1], outr, or1);
      st128c(v[2], outr, or2);
      st128c(v[3], outr, or3);
    }
    if (t == 0 && panel == 0 && grp == 0 && lane == 0) st32c(key, wsr, WS_EPOCH + br * 256);
    if (PROBE_N == 13) WAITV(0);
    STAMP(13);
    STAMP_OUT();
  }
}
}

extern "C" void kernel_launch(void* const* d_in, const int* in_sizes, int n_in,
                              void* d_out, int out_size, void* d_ws, size_t ws_size,
                              hipStream_t stream) {
  hh::hedgehog<<<256, 512, 0, stream>>>((const u16*)d_in[0], (const u16*)d_in[1], (const u16*)d_in[2], (const u16*)d_in[3],
                                        (const u16*)d_in[4], (const u16*)d_in[5], (const u16*)d_in[6], (const u16*)d_in[7],
                                        (u16*)d_out, (u32*)d_ws);
}
```

```cpp
#define PROBE_N 9
#define PROBE_ROLE 0
#include <hip/hip_runtime.h>
#include <stdint.h>

typedef unsigned short u16;
typedef unsigned u32;
typedef short bf16x8 __attribute__((ext_vector_type(8)));
typedef short s16x4 __attribute__((ext_vector_type(4)));
typedef float f32x16 __attribute__((ext_vector_type(16)));
typedef float f32x4 __attribute__((ext_vector_type(4)));
typedef float f32x2 __attribute__((ext_vector_type(2)));
typedef unsigned u32x8 __attribute__((ext_vector_type(8)));
typedef unsigned u32x4 __attribute__((ext_vector_type(4)));
typedef unsigned u32x2 __attribute__((ext_vector_type(2)));
typedef __bf16 bf16x2 __attribute__((ext_vector_type(2)));
#define DI __device__ __forceinline__
#define LAS __attribute__((address_space(3)))
#define CAS __attribute__((address_space(4)))
#define BC(T, x) __builtin_bit_cast(T, x)

#define DELAY1 3
#define DELAY3 4
#define CODE_PF 29184
#define AHEAD 1
#define GQ 1
#define GC 2
#define GBURST 1
#define WARM 0
#define RSLEEP 0
#define OFIRST 5

namespace hh {
constexpr int AUX_SC1 = 16;
constexpr u32 SRD3 = 0x00020000u;
constexpr u32 WS_EPOCH = 0;
constexpr u32 REC = 1280;
constexpr u32 WS_PART = 4096;
constexpr u32 OREC = 4352;
constexpr u32 WS_O = WS_PART + 2 * 32 * 16 * REC;
constexpr u32 WS_END = WS_O + 2 * 64 * 8 * OREC;
constexpr u32 WS_STAMP = 8u << 20;
constexpr u32 SLOT = 32768;
constexpr u32 L_WQ0 = 98304;
constexpr u32 L_WQ1 = 65536;
constexpr u32 L_WPHI = 163840 - 4096;
constexpr u32 L_PHI = 0;
constexpr u32 VF = 1040;
constexpr u32 L_V = 131072;
constexpr u32 L_KV = L_V + 16 * VF;
constexpr u32 L_WO = 0;
constexpr u32 L_O = 65536;
constexpr u32 SPIN_CAP = 1u << 16;

DI u32 pk2(float a, float b) { return BC(u32, __builtin_convertvector((f32x2){a, b}, bf16x2)); }
DI u32x4 pack8(const f32x16& x, int s) {
  return (u32x4){pk2(x[8 * s], x[8 * s + 1]), pk2(x[8 * s + 2], x[8 * s + 3]), pk2(x[8 * s + 4], x[8 * s + 5]), pk2(x[8 * s + 6], x[8 * s + 7])};
}
DI f32x16 mfma(u32x4 a, u32x4 b, f32x16 c) { return __builtin_amdgcn_mfma_f32_32x32x16_bf16(BC(bf16x8, a), BC(bf16x8, b), c, 0, 0, 0); }
DI f32x4 mfma16(u32x4 a, u32x4 b, f32x4 c) { return __builtin_amdgcn_mfma_f32_16x16x32_bf16(BC(bf16x8, a), BC(bf16x8, b), c, 0, 0, 0); }
DI u32x4 lds_r128(u32 a) { return *(LAS u32x4*)(size_t)a; }
DI void lds_w128(u32 a, u32x4 v) { *(LAS u32x4*)(size_t)a = v; }
DI u32x4 lds_tr2(u32 a0, u32 a1) {
  s16x4 lo = __builtin_amdgcn_ds_read_tr16_b64_v4i16((LAS s16x4*)(size_t)a0);
  s16x4 hi = __builtin_amdgcn_ds_read_tr16_b64_v4i16((LAS s16x4*)(size_t)a1);
  return BC(u32x4, __builtin_shufflevector(lo, hi, 0, 1, 2, 3, 4, 5, 6, 7));
}
DI void dma1(u32 lds, u32 voff, __amdgpu_buffer_rsrc_t srd, u32 soff) {
  asm volatile("s_mov_b32 m0, %0\n\ts_nop 0\n\tbuffer_load_dwordx4 %1, %2, %3 offen lds" ::"s"(lds), "v"(voff), "s"(srd), "s"(soff) : "memory");
}
DI void dma4(u32 lds, const u32 (&v)[4], __amdgpu_buffer_rsrc_t srd, u32 soff) {
  asm volatile("s_mov_b32 m0, %0\n\ts_nop 0\n\t"
               "buffer_load_dwordx4 %1, %5, %6 offen lds\n\t"
               "buffer_load_dwordx4 %2, %5, %6 offen offset:1024 lds\n\t"
               "buffer_load_dwordx4 %3, %5, %6 offen offset:2048 lds\n\t"
               "buffer_load_dwordx4 %4, %5, %6 offen offset:3072 lds" ::"s"(lds), "v"(v[0]), "v"(v[1]), "v"(v[2]), "v"(v[3]), "s"(srd), "s"(soff) : "memory");
}
DI void dma_orec(u32 lds, u32 l4, u32 l16, __amdgpu_buffer_rsrc_t srd, u32 soff) {
  asm volatile("s_mov_b32 m0, %0\n\ts_nop 0\n\t"
               "buffer_load_dwordx4 %2, %3, %4 offen offset:256 sc1 lds\n\t"
               "buffer_load_dwordx4 %2, %3, %4 offen offset:1280 sc1 lds\n\t"
               "buffer_load_dwordx4 %2, %3, %4 offen offset:2304 sc1 lds\n\t"
               "buffer_load_dwordx4 %2, %3, %4 offen offset:3328 sc1 lds\n\t"
               "buffer_load_dword %1, %3, %4 offen sc1 lds" ::"s"(lds), "v"(l4), "v"(l16), "s"(srd), "s"(soff) : "memory");
}
#define TOUCH(d, voff, srd, soff) asm volatile("buffer_load_dword %0, %1, %2, %3 offen" : "=v"(d) : "v"(voff), "s"(srd), "s"(soff) : "memory")
DI void st128c(u32x4 v, __amdgpu_buffer_rsrc_t srd, u32 off) { __builtin_amdgcn_raw_buffer_store_b128(v, srd, off, 0, AUX_SC1); }
DI void st32c(u32 v, __amdgpu_buffer_rsrc_t srd, u32 off) { __builtin_amdgcn_raw_buffer_store_b32(v, srd, off, 0, AUX_SC1); }
#define BAR() asm volatile("s_waitcnt lgkmcnt(0)\n\ts_barrier" ::: "memory")
#define BARE() asm volatile("s_barrier" ::: "memory")
#define OPQ(x) asm("" : "+v"(x))
#define PIN(x) asm volatile("" : "+v"(x))
#define PINS(x) asm volatile("" : "+s"(x))
#define WAITV(n) asm volatile("s_waitcnt vmcnt(" #n ")" ::: "memory")
#define SCHED() __builtin_amdgcn_sched_barrier(0)
#define SGB(m, n) __builtin_amdgcn_sched_group_barrier(m, n, 0)
DI u32 sum4(u32 c, u32x4 v) { return (c + v.x + v.y) + v.z + v.w; }
DI u32 pair_sum(u32 s) { auto t = __builtin_amdgcn_permlane32_swap(s, s, false, false); return t[0] + t[1]; }

constexpr float LOG2E = 1.44269504088896340736f;
DI float max3f(float a, float b, float c) { return __builtin_fmaxf(__builtin_fmaxf(a, b), c); }
DI float negmax16(const f32x16& x) {
  const float a = max3f(x[0], x[1], x[2]), b = max3f(x[3], x[4], x[5]), c = max3f(x[6], x[7], x[8]);
  const float d = max3f(x[9], x[10], x[11]), e = max3f(x[12], x[13], x[14]);
  return -max3f(max3f(a, b, x[15]), max3f(c, d, e), a) * LOG2E;
}
DI void sm_exp(const f32x16& p, const f32x16& q, const float (&mc)[2], f32x2 (&e)[2][8]) {
#pragma unroll
  for (int i = 0; i < 8; ++i) {
    const f32x2 t = __builtin_elementwise_fma((f32x2){p[2 * i], p[2 * i + 1]}, (f32x2){LOG2E, LOG2E}, (f32x2){mc[0], mc[0]});
    const f32x2 u = __builtin_elementwise_fma((f32x2){q[2 * i], q[2 * i + 1]}, (f32x2){LOG2E, LOG2E}, (f32x2){mc[1], mc[1]});
    e[0][i] = (f32x2){__builtin_amdgcn_exp2f(t.x), __builtin_amdgcn_exp2f(t.y)};
    e[1][i] = (f32x2){__builtin_amdgcn_exp2f(u.x), __builtin_amdgcn_exp2f(u.y)};
  }
  SGB(0x2, 4); SGB(0x400, 8); SGB(0x2, 4); SGB(0x400, 8); SGB(0x2, 6); SGB(0x400, 8); SGB(0x2, 8); SGB(0x400, 8);
}
DI void sm_exp_s(const f32x16& p, const f32x16& q, const float (&mc)[2], f32x2 (&e)[2][8]) {
#pragma unroll
  for (int i = 0; i < 16; ++i) {
    e[0][i >> 1][i & 1] = __builtin_amdgcn_exp2f(__builtin_fmaf(p[i], LOG2E, mc[0]));
    e[1][i >> 1][i & 1] = __builtin_amdgcn_exp2f(__builtin_fmaf(q[i], LOG2E, mc[1]));
  }
}
DI void sm_finish(const f32x2 (&e)[2][8], u32x4 (&o)[2][2]) {
  float r[2];
#pragma unroll
  for (int u = 0; u < 2; ++u) {
    const f32x2 s = ((e[u][0] + e[u][1]) + (e[u][2] + e[u][3])) + ((e[u][4] + e[u][5]) + (e[u][6] + e[u][7]));
    r[u] = __builtin_amdgcn_rcpf(s.x + s.y);
  }
#pragma unroll
  for (int u = 0; u < 2; ++u) {
    u32 w[8];
#pragma unroll
    for (int i = 0; i < 8; ++i) { const f32x2 t = e[u][i] * (f32x2){r[u], r[u]}; w[i] = pk2(t.x, t.y); }
    o[u][0] = (u32x4){w[0], w[1], w[2], w[3]};
    o[u][1] = (u32x4){w[4], w[5], w[6], w[7]};
  }
}
DI void softmax16x2(const f32x16& p, const f32x16& q, const float (&mc)[2], u32x4 (&o)[2][2]) {
  f32x2 e[2][8];
  sm_exp(p, q, mc, e);
  sm_finish(e, o);
}

#define STAMP(i) do { if ((i) == PROBE_N) { SCHED(); asm volatile("s_memrealtime %0\n\ts_waitcnt lgkmcnt(0)" : "=s"(pt1) :: "memory"); SCHED(); } } while (0)
#define STAMP_OUT() do { if (PROBE_N && (wave >> 2) == PROBE_ROLE) { unsigned long long d_ = pt1 - pt0; if (d_ > 100000) d_ = 100000; unsigned long long e_ = pt0 + 3000 + 8 * d_; \
  for (int it_ = 0; it_ < 1000000; ++it_) { unsigned long long t_; asm volatile("s_memrealtime %0\n\ts_waitcnt lgkmcnt(0)" : "=s"(t_) :: "memory"); if (t_ >= e_) break; } } } while (0)

__global__ __launch_bounds__(512, 2) void hedgehog(const u16* xA, const u16* wA, const u16* pA, const u16* oA,
                                                   const u16* xB, const u16* wB, const u16* pB, const u16* oB,
                                                   u16* out_, u32* ws_) {
  __shared__ __attribute__((aligned(16))) char smem[163840];
  const u32 lds0 = (u32)(size_t)(LAS char*)smem;
  unsigned long long pt0 = __builtin_amdgcn_s_memrealtime(), pt1 = pt0;
  const int bid = blockIdx.x, xcd = bid & 7, idx = bid >> 3;
  const int br = xcd >> 2;
#define KLOAD(tail)                                                                                       \
  u32x8 ka;                                                                                               \
  u32x4 kb;                                                                                               \
  asm volatile("s_load_dwordx8 %0, %2, %3\n\ts_load_dwordx4 %1, %2, 0x40" tail : "=&s"(ka), "=&s"(kb)     \
               : "s"(__builtin_amdgcn_kernarg_segment_ptr()), "s"(br * 32))
#define KWAIT() asm volatile("s_waitcnt lgkmcnt(0)" : "+s"(ka), "+s"(kb))
#define KPTR(v, i) ((void*)(((unsigned long long)(v)[(i) + 1] << 32) | (v)[i]))
  unsigned long long pcs;
  asm volatile("s_getpc_b64 %0" : "=s"(pcs));
  pcs &= ~255ull;
  const int lane = threadIdx.x & 63;
  const int wave = __builtin_amdgcn_readfirstlane(threadIdx.x >> 6);
  const int panel = (xcd & 3) * 4 + (idx & 3), grp = idx >> 2, gq = (grp + GQ) & 7, cg = (grp + GC) & 7;
  auto ochunk = [&](u32 j) -> u32 {
    const int v = ((int)j + (((int)j - 1 - ((gq - OFIRST) & 7)) >> 31) + OFIRST) & 7, z = ((int)j - 1) >> 31;
    return (u32)__builtin_amdgcn_readfirstlane((v & ~z) | (gq & z));
  };
  f32x16 zero;
#pragma unroll
  for (int i = 0; i < 16; ++i) zero[i] = 0.f;
#define LANE_IDS()                                                                            \
  u32 lane_ = lane;                                                                           \
  PIN(lane_);                                                                                 \
  const u32 r = lane_ & 31, h = lane_ >> 5;                                                   \
  const u32 blk = (lane_ >> 4) & 1, q = (lane_ & 15) >> 2, p = lane_ & 3;                     \
  u32* ws = (u32*)KPTR(kb, 2);                                                                \
  const __amdgpu_buffer_rsrc_t wsr = __builtin_amdgcn_make_buffer_rsrc(ws, 0, WS_END, SRD3);  \
  const __amdgpu_buffer_rsrc_t wor = __builtin_amdgcn_make_buffer_rsrc(KPTR(ka, 6), 0, 512 * 512 * 2, SRD3)

  if (wave >= 4) {
    const int cw = wave - 4;
    const int rh = cw >> 1, hp = cw & 1;
    KLOAD("\n\ts_waitcnt lgkmcnt(0)");
    u32 sp0, sp1, sp2, sp3;
    {
      u32 off = idx * 1024 + cw * 256;
      off = off < CODE_PF ? off : 0;
      asm volatile("s_load_dword %0, %4, %5 offset:0\n\ts_load_dword %1, %4, %5 offset:64\n\ts_load_dword %2, %4, %5 offset:128\n\ts_load_dword %3, %4, %5 offset:192"
                   : "=&s"(sp0), "=&s"(sp1), "=&s"(sp2), "=&s"(sp3) : "s"(pcs), "s"(off));
    }
    LANE_IDS();
    u32 key = *(const CAS u32*)((const char*)ws + WS_EPOCH + br * 256) + 1u;
    u32 xa[2], xb[2], wa[2], wb2[2];
    {
      const u32 r16 = lane_ & 15, g = lane_ >> 4;
#pragma unroll
      for (int s = 0; s < 2; ++s) {
        xa[s] = lds0 + (64 * rh + r16) * 128 + 16 * ((4 * s + g) ^ (r16 >> 1));
        wa[s] = lds0 + 16384 + (8 * g + q) * 128 + 16 * ((4 * hp + 2 * s + (p >> 1)) ^ ((((q >> 1) & 1) << 2) | (blk << 1))) + 8 * (p & 1);
        xb[s] = xa[s] + 2 * SLOT; wb2[s] = wa[s] + 2 * SLOT;
        PIN(xa[s]); PIN(xb[s]); PIN(wa[s]); PIN(wb2[s]);
      }
    }
    u32 fpa = lds0 + L_WPHI + hp * 1024 + (8 * h + q) * 32 + (8 * blk + 4 * (p >> 1)) * 2;
    PIN(fpa);
    if (!WARM) PINS(key);
    f32x4 ak[4][2], av[4][2];
#pragma unroll
    for (int i = 0; i < 8; ++i) ak[i >> 1][i & 1] = av[i >> 1][i & 1] = (f32x4){0.f, 0.f, 0.f, 0.f};
    u32 vra, pra, vwa, pw0, pw1, pub16, pub4;
    u32x4 xf[2][4], wk[2][2], wv[2][2];
    auto ldx = [&](int c, int s) {
      const u32 x = c % 3 == 2 ? xb[s] : xa[s] + (c % 3) * SLOT;
#pragma unroll
      for (int i = 0; i < 4; ++i) xf[s][i] = lds_r128(x + i * 2048);
    };
    auto ldw = [&](int c, int s, int m) {
#pragma unroll
      for (int n = 0; n < 2; ++n) {
        const u32 w = (c % 3 == 2 ? wb2[n] : wa[n] + (c % 3) * SLOT) + s * 4096 + m * 8192;
        (m ? wv : wk)[s][n] = lds_tr2(w, w + 512);
      }
    };
    auto mk = [&](int s) {
#pragma unroll
      for (int i = 0; i < 8; ++i) ak[i >> 1][i & 1] = mfma16(wk[s][i & 1], xf[s][i >> 1], ak[i >> 1][i & 1]);
    };
    auto mv = [&](int s) {
#pragma unroll
      for (int i = 0; i < 8; ++i) av[i >> 1][i & 1] = mfma16(xf[s][i >> 1], wv[s][i & 1], av[i >> 1][i & 1]);
    };
    auto mix = [&](int n) {
#pragma unroll
      for (int i = 0; i < 8; ++i) {
        SGB(0x8, 1);
        if (n == 12) { if (i < 4) SGB(0x100, 2); else SGB(0x100, 1); }
        else if (n == 8) SGB(0x100, 1);
        else if (n == 4) { if (i < 4) SGB(0x100, 1); }
        else if (n < 0) SGB(0x6, 2);
      }
      SCHED();
    };
    if (WARM) {
      u32 hsh = lane * 2654435761u + 12345u;
      u32x4 a = {(hsh & 0x3FFF3FFFu) | 0x30003000u, ((hsh * 7u) & 0xBFFFBFFFu) | 0x30003000u, ((hsh * 13u) & 0x3FFFBFFFu) | 0x30003000u, ((hsh * 29u) & 0xBFFF3FFFu) | 0x30003000u};
      u32x4 b = {a.y, a.w, a.x, a.z};
      f32x16 w0 = zero, w1 = zero;
#pragma unroll 1
      for (int i = 0; i < WARM; ++i) { w0 = mfma(a, b, w0); w1 = mfma(b, a, w1); w0 = mfma(b, b, w0); w1 = mfma(a, a, w1); PIN(w0); PIN(w1); }
    }
    BAR();
    asm volatile("" ::"s"(sp0), "s"(sp1), "s"(sp2), "s"(sp3));
    if (WARM) PINS(key);
    STAMP(1);
    ldw(0, 0, 0); ldx(0, 0); ldw(0, 0, 1);
    u32x4 fa[2];
#pragma unroll
    for (int hh = 0; hh < 2; ++hh) fa[hh] = lds_tr2(fpa + hh * 512, fpa + hh * 512 + 128);
    SCHED();
#pragma unroll
    for (int c = 0; c < 7; ++c) {
      ldw(c, 1, 0); ldx(c, 1); mk(0); mix(8);
      ldw(c, 1, 1); mv(0); mix(4);
      u32 ln = lane;
      PIN(ln);
      const u32 r_ = ln & 31, h_ = ln >> 5, q_ = (ln & 15) >> 2;
      if (c == 1) {
        const u32 sg = (ln & 4) ? 0x80008000u : 0u;
#pragma unroll
        for (int hh = 0; hh < 2; ++hh) { OPQ(fa[hh]); fa[hh] ^= (u32x4){sg, sg, sg, sg}; }
      }
      if (c == 2) vra = lds0 + L_V + (cw >> 1) * (2 * VF) + (r_ < 16 ? (ln + 16 * (cw & 1)) * 16 : 1024);
      if (c == 3) pra = lds0 + L_PHI + cw * 8192 + (4 * h_ + q_) * 64 + 16 * ((((ln >> 3) & 2) + ((ln >> 1) & 1)) ^ ((2 * h_ + (q_ >> 1)) & 3)) + 8 * (ln & 1);
      if (c == 4) {
        const u32 rec = WS_PART + ((br * 32 + grp * 4 + cw) * 16 + panel) * REC;
        pub16 = rec + ln * 16; pub4 = rec + 1024 + ln * 4;
      }
      if (c == 5) { pw0 = lds0 + L_PHI + (2 * hp) * 8192 + (64 * rh + r_) * 64 + 16 * ((2 * h_) ^ ((r_ >> 1) & 3)); pw1 = pw0 ^ 16; }
      if (c == 6) vwa = lds0 + L_V + (8 * rh + 2 * hp) * VF + ((ln & 16) * 2 + (ln & 15)) * 16 + h_ * 8;
      mk(1); mix(c >= 1 ? -1 : 0);
      if (c == 1) { PIN(fa[0]); PIN(fa[1]); }
      if (c == 2) PIN(vra);
      if (c == 3) PIN(pra);
      if (c == 4) { PIN(pub16); PIN(pub4); }
      if (c == 5) { PIN(pw0); PIN(pw1); }
      if (c == 6) PIN(vwa);
      BAR();
      ldw(c + 1, 0, 0); ldx(c + 1, 0); ldw(c + 1, 0, 1);
      mv(1); mix(12);
    }
    ldw(7, 1, 0); ldx(7, 1); ldw(7, 1, 1); mk(0); mix(12);
    mk(1); mix(0);
    __builtin_amdgcn_s_setprio(2);
    STAMP(2);
    f32x16 pk[2][2];
    {
      u32x4 kf[2][2];
      auto kpack = [&](int i) {
#pragma unroll
        for (int hh = 0; hh < 2; ++hh) {
          const f32x4 &t0 = ak[2 * i][hh], &t1 = ak[2 * i + 1][hh];
          const auto lo = __builtin_amdgcn_permlane16_swap(pk2(t0[0], t0[1]), pk2(t1[0], t1[1]), false, false);
          const auto hi = __builtin_amdgcn_permlane16_swap(pk2(t0[2], t0[3]), pk2(t1[2], t1[3]), false, false);
          kf[i][hh] = (u32x4){lo[0], hi[0], lo[1], hi[1]};
        }
      };
      mv(0); kpack(0);
      SGB(0x8, 2);
#pragma unroll
      for (int i = 0; i < 6; ++i) { SGB(0x2, 2); SGB(0x8, 1); }
      SCHED();
      pk[0][0] = mfma(fa[0], kf[0][0], zero);
      pk[0][1] = mfma(fa[1], kf[0][1], zero);
      kpack(1);
      SGB(0x8, 1); SGB(0x2, 6); SGB(0x8, 1); SGB(0x2, 6);
      PIN(pk[0][0]); PIN(pk[0][1]); PIN(kf[1][0]); PIN(kf[1][1]);
      SCHED();
      mv(1);
      pk[1][0] = mfma(fa[0], kf[1][0], zero);
      pk[1][1] = mfma(fa[1], kf[1][1], zero);
    }
    float m0[2], m1[2];
    m0[0] = negmax16(pk[0][0]); m0[1] = negmax16(pk[0][1]);
    SGB(0x8, 2);
#pragma unroll
    for (int i = 0; i < 6; ++i) { SGB(0x2, 2); SGB(0x8, 1); }
    SGB(0x8, 1); SGB(0x2, 6); SGB(0x8, 1);
#pragma unroll
    for (int i = 0; i < 8; ++i) PIN(av[i >> 1][i & 1]);
    PIN(pk[1][0]); PIN(pk[1][1]); PIN(m0[0]); PIN(m0[1]);
    SCHED();
#pragma unroll
    for (int i = 0; i < 8; ++i) {
      const f32x4& t = av[i >> 1][i & 1];
      *(LAS u32x2*)(size_t)(vwa + (4 * (i >> 2) + ((i >> 1) & 1)) * VF + (i & 1) * 256) = (u32x2){pk2(t[0], t[1]), pk2(t[2], t[3])};
    }
    SCHED();
    {
      u32x4 o[2][2];
      softmax16x2(pk[0][0], pk[0][1], m0, o);
      lds_w128(pw0, o[0][0]); lds_w128(pw1, o[0][1]);
      lds_w128(pw0 + 8192, o[1][0]); lds_w128(pw1 + 8192, o[1][1]);
      SCHED();
      m1[0] = negmax16(pk[1][0]); m1[1] = negmax16(pk[1][1]);
      PIN(m1[0]); PIN(m1[1]);
      SCHED();
      softmax16x2(pk[1][0], pk[1][1], m1, o);
      lds_w128(pw0 + 2048, o[0][0]); lds_w128(pw1 + 2048, o[0][1]);
      lds_w128(pw0 + 2048 + 8192, o[1][0]); lds_w128(pw1 + 2048 + 8192, o[1][1]);
      SGB(0x402, 26); SGB(0x200, 1); SGB(0x402, 8); SGB(0x200, 1); SGB(0x402, 8); SGB(0x200, 1); SGB(0x402, 8); SGB(0x200, 1);
      SCHED();
      BARE();
      BAR();
      SCHED();
    }
    STAMP(3);
    f32x16 kv = zero;
    {
      u32x4 a[8], b[8];
#pragma unroll
      for (int u = 0; u < 8; ++u) {
        a[u] = lds_r128(vra + ((u >> 1) * 4 + (u & 1)) * VF);
        b[u] = lds_tr2(pra + 16 * u * 64, pra + (16 * u + 8) * 64);
      }
#pragma unroll
      for (int u = 0; u < 8; ++u) kv = mfma(a[u], b[u], kv);
      SGB(0x100, 12); SGB(0x8, 1); SGB(0x100, 6); SGB(0x8, 1); SGB(0x100, 6); SGB(0x8, 6);
    }
    {
      const u32x4 w = pack8(kv, 0);
      st128c(w, wsr, pub16);
      const u32 zb = __float_as_uint(kv[8]);
      const u32 chk = key - pair_sum(sum4(h ? 0u : zb, w));
      st32c(h ? chk : zb, wsr, pub4);
    }
    SCHED();
    STAMP(4);
    u32 l16, l4, kwa, kza, gso[4], vo[4], wso;
    u32x4 sel;
    {
      u32 ln = lane;
      PIN(ln);
#pragma unroll
      for (int i = 0; i < 4; ++i) vo[i] = (ln >> 3) * 1024 + 16 * ((ln & 7) ^ (((ln >> 4) & 1) << 2)) + i * 7168;
#pragma unroll
      for (int i = 0; i < 2; ++i) dma4(lds0 + L_WO + cw * 8192 + i * 4096, vo, wor, ochunk(cw) * 65536 + i * 32768 + cg * 128);
      wso = ochunk(4 + cw) * 65536 + cg * 128;
      const u32 r_ = ln & 31, h_ = ln >> 5;
      l16 = ln * 16; l4 = ln * 4;
      kwa = lds0 + L_KV + cw * 1536 + r_ * 48 + h_ * 16; kza = lds0 + L_KV + cw * 1536 + r_ * 48 + 32 + h_ * 8;
      const bool hit = r_ < 16 && ((r_ >> 2) & 1) == h_;
      const u32 j = (r_ & 3) + 4 * (r_ >> 3);
#pragma unroll
      for (int e = 0; e < 4; ++e) sel[e] = (hit && (j >> 1) == e) ? ((j & 1) ? 0x3F800000u : 0x00003F80u) : 0u;
#pragma unroll
      for (int i = 0; i < 4; ++i) { gso[i] = WS_PART + ((br * 32 + gq * 4 + cw) * 16 + ((i + idx) & 3) * 4) * REC; PINS(gso[i]); }
      PIN(l16); PIN(l4); PIN(kwa); PIN(kza); PIN(sel); PINS(wso);
    }
    if (DELAY1) __builtin_amdgcn_s_sleep(DELAY1);
    STAMP(10);
    f32x16 gs[4];
    float z[4];
    {
      u32x4 g[16];
      u32 gz[16];
      auto gload = [&](int gi) {
#pragma unroll
        for (int i = 0; i < 4; ++i) {
          g[4 * gi + i] = __builtin_amdgcn_raw_buffer_load_b128(wsr, l16 + i * REC, gso[gi], AUX_SC1);
          gz[4 * gi + i] = __builtin_amdgcn_raw_buffer_load_b32(wsr, l4 + (1024 + i * REC - 1024 * (i == 3)), gso[gi] + 1024 * (i == 3), AUX_SC1);
          SCHED();
        }
      };
      auto gsum = [&](int gi) -> bool {
        u32 c = 0;
        if (GBURST) { PIN(gz[4 * gi + 3]); SCHED(); }
#pragma unroll
        for (int i = 0; i < 4; ++i) {
          c = sum4(c + gz[4 * gi + i], g[4 * gi + i]);
          gs[gi] = mfma(sel, g[4 * gi + i], i == 0 ? zero : gs[gi]);
          z[gi] = i == 0 ? __uint_as_float(gz[4 * gi]) : z[gi] + __uint_as_float(gz[4 * gi + i]);
        }
        const bool bad = __builtin_amdgcn_ballot_w64(pair_sum(c) != 4u * key) != 0;
        if (GBURST) { SGB(0x8, 1); SGB(0x2, 5); SGB(0x8, 1); SGB(0x2, 5); SGB(0x8, 1); SGB(0x2, 4); SGB(0x8, 1); }
        PIN(z[gi]);
        SCHED();
        return bad;
      };
      gload(0); gload(1); gload(2); gload(3);
      bool b0 = gsum(0), b1 = gsum(1), b2 = gsum(2), b3 = gsum(3);
      STAMP(11);
#pragma unroll 1
      for (u32 spin = 0; (b0 | b1 | b2 | b3) && spin < SPIN_CAP; ++spin) {
        asm volatile("" ::: "memory");
        if (RSLEEP) __builtin_amdgcn_s_sleep(RSLEEP);
        if (b0) gload(0);
        if (b1) gload(1);
        if (b2) gload(2);
        if (b3) gload(3);
        if (b0) b0 = gsum(0);
        if (b1) b1 = gsum(1);
        if (b2) b2 = gsum(2);
        if (b3) b3 = gsum(3);
      }
    }
    __builtin_amdgcn_s_waitcnt(0x0F70);
    STAMP(5);
    {
      float tt[8];
#pragma unroll
      for (int j = 0; j < 8; ++j) tt[j] = (gs[0][j] + gs[1][j]) + (gs[2][j] + gs[3][j]);
      const float zt = (z[0] + z[1]) + (z[2] + z[3]);
      lds_w128(kwa, (u32x4){pk2(tt[0], tt[1]), pk2(tt[2], tt[3]), pk2(tt[4], tt[5]), pk2(tt[6], tt[7])});
      const u32 zz = pk2(zt, zt);
      *(LAS u32x2*)(size_t)kza = (u32x2){zz, zz};
    }
#pragma unroll
    for (int i = 0; i < 2; ++i) dma4(lds0 + L_WO + 32768 + cw * 8192 + i * 4096, vo, wor, wso + i * 32768);
    BAR();
    STAMP(6);
    {
      SCHED();
      u32 oso[8], ob = lds0 + L_O + cw * OREC, i0 = 0;
      PINS(i0);
#pragma unroll
      for (int i = 1; i < 8; ++i) { oso[i] = WS_O + ((br * 64 + panel * 4 + cw) * 8 + ochunk(i + i0)) * OREC; PINS(oso[i]); }
      PINS(ob);
      WAITV(0);
      BARE();
      if (DELAY3) __builtin_amdgcn_s_sleep(DELAY3);
      STAMP(8);
      auto oissue = [&](int j) { dma_orec(ob + (j % 5) * (4 * OREC), l4, l16, wsr, oso[j]); };
      oissue(1); oissue(2);
      WAITV(5); BARE(); oissue(3);
      WAITV(5); STAMP(15); BARE(); oissue(4); oissue(5);
      WAITV(10); BARE(); oissue(6);
      WAITV(10); BARE(); oissue(7);
      WAITV(10); BARE();
      WAITV(5); BARE();
      WAITV(0); BARE();
    }
    STAMP(9);
    STAMP_OUT();
  } else {
    KLOAD("");
    const int t = wave;
    const u32 l7 = lane & 7, l3 = lane >> 3, lq = lane >> 4;
    const u32 wsw = 16 * (l7 ^ ((lq & 1) << 2));
    u32 vx[4], vw[4];
#pragma unroll
    for (int i = 0; i < 4; ++i) {
      vx[i] = l3 * 1024 + 16 * (l7 ^ (((i & 1) << 2) | lq)) + i * 7168;
      vw[i] = l3 * 3072 + (wsw ^ ((i & 1) << 5)) + i * (8 * 3072 - 1024);
    }
    auto chunk = [&](u32 c) -> u32 {
      const u32 g = grp + idx;
      return c - 2 < 4 ? 2 + ((c - 2) ^ (g & 3)) : c ^ (g & 1);
    };
    const u32 xs0 = (panel * 128 + t * 32) * 1024, ws0 = (t & 1) * (32 * 3072) + (1 + (t >> 1)) * 1024 + grp * 128;
    const u32 ldst = lds0 + t * 4096;
    u32 e0 = ldst, e1 = ldst + 16384, e2 = xs0 + chunk(0) * 128, e3 = ws0 + chunk(0) * (64 * 3072);
    PINS(e0); PINS(e1); PINS(e2); PINS(e3);
#pragma unroll
    for (int i = 0; i < 4; ++i) { PIN(vx[i]); PIN(vw[i]); }
    KWAIT();
    const __amdgpu_buffer_rsrc_t xr = __builtin_amdgcn_make_buffer_rsrc(KPTR(ka, 0), 0, 2048 * 512 * 2, SRD3);
    const __amdgpu_buffer_rsrc_t wr = __builtin_amdgcn_make_buffer_rsrc(KPTR(ka, 2), 0, 512 * 1536 * 2, SRD3);
    STAMP(10);
    dma4(e0, vx, xr, e2);
    dma4(e1, vw, wr, e3);
    {
      const __amdgpu_buffer_rsrc_t pr = __builtin_amdgcn_make_buffer_rsrc(KPTR(ka, 4), 0, 32 * 256 * 2, SRD3);
      dma1(lds0 + L_WPHI + t * 1024, lane * 16, pr, (t < 2 ? grp : gq) * 2048 + (t & 1) * 1024);
    }
    STAMP(11);
    dma4(ldst + SLOT, vx, xr, xs0 + chunk(1) * 128);
    dma4(ldst + SLOT + 16384, vw, wr, ws0 + chunk(1) * (64 * 3072));
    STAMP(1);
    LANE_IDS();
    u32 key = *(const CAS u32*)((const char*)ws + WS_EPOCH + br * 256) + 1u;
    const u32 sw = (r >> 1) & 7;
    u32 xa[4];
#pragma unroll
    for (int s = 0; s < 4; ++s) { xa[s] = lds0 + (32 * t + r) * 128 + 16 * ((2 * s + h) ^ sw); PIN(xa[s]); }
    u32 fpa = lds0 + L_WPHI + 2048 + (4 * h + q) * 32 + (8 * blk + 4 * (p >> 1)) * 2;
    PIN(fpa);
    PINS(key);
    u32x4 xq[32], fa[4];
    u32 wa, wb, kra, o_off, c_off, woa, wob, vto, sto, tho;
#pragma unroll
    for (int c = 0; c < 8; ++c) {
      u32 d0, d1, s0, s1;
      if (c < 6) {
        d0 = ldst + ((c + 2) % 3) * SLOT; d1 = d0 + 16384;
        s0 = xs0 + chunk(c + 2) * 128; s1 = ws0 + chunk(c + 2) * (64 * 3072);
      } else {
        const u32 k0 = 2 * ((2 * (c - 6) + idx) & 3) + (t >> 1), k1 = (k0 + 2) & 7;
        d0 = lds0 + L_WQ1 + ((chunk(k0) + 4) & 7) * 8192 + (t & 1) * 4096; d1 = lds0 + L_WQ1 + ((chunk(k1) + 4) & 7) * 8192 + (t & 1) * 4096;
        s0 = (k0 * 64 + (t & 1) * 32) * 3072 + gq * 128; s1 = (k1 * 64 + (t & 1) * 32) * 3072 + gq * 128;
      }
      PINS(d0); PINS(d1); PINS(s0); PINS(s1);
      WAITV(8);
      BAR();
      if (c < 6) dma4(d0, vx, xr, s0); else dma4(d0, vw, wr, s0);
#pragma unroll
      for (int s = 0; s < 4; ++s) xq[4 * c + s] = lds_r128(xa[s] + (c % 3) * SLOT);
      dma4(d1, vw, wr, s1);
      if (c == 0) {
#pragma unroll
        for (int hd = 0; hd < 4; ++hd) fa[hd] = lds_tr2(fpa + hd * 512, fpa + hd * 512 + 256);
      }
      if (c == 1) {
        lds_w128(lds0 + L_V + (lane & 15) * VF + 1024, (u32x4){0x3F803F80u, 0x3F803F80u, 0x3F803F80u, 0x3F803F80u});
        const u32 sg = (lane & 4) ? 0x80008000u : 0u;
#pragma unroll
        for (int hd = 0; hd < 4; ++hd) { PIN(fa[hd]); fa[hd] ^= (u32x4){sg, sg, sg, sg}; PIN(fa[hd]); }
        wa = lds0 + L_WQ1 + (8 * h + q) * 128 + 16 * ((2 * blk + (p >> 1)) ^ ((((q >> 1) & 1) << 2) | (h << 1))) + 8 * (p & 1);
        wb = wa ^ 64;
        kra = lds0 + L_KV + (16 * h + q) * 48 + (blk ? 32 : 8 * p);
        o_off = WS_O + ((br * 64 + panel * 4 + t) * 8 + gq) * OREC + 256 + lane * 16;
        c_off = WS_O + ((br * 64 + panel * 4 + t) * 8 + gq) * OREC + lane * 4;
        woa = lds0 + L_WO + (8 * h + q) * 128 + 16 * ((2 * blk + (p & 1)) ^ (((q >> 1) & 1) << 2)) + 8 * (p >> 1);
        wob = woa ^ 64;
        vto = r * 128; sto = (idx * 16 + t * 4) * 1024;
        PIN(wa); PIN(wb); PIN(kra); PIN(o_off); PIN(c_off); PIN(woa); PIN(wob); PIN(vto); PINS(sto);
      }
    }
    TOUCH(tho, vto, wor, sto);
    STAMP(2);
    u32x4 fq[2][2][2];
    {
      f32x16 aq[2], pq[2];
      float mq[2];
      aq[0] = aq[1] = zero;
      u32x4 wf[4];
      auto ldw = [&](int n) {
        const int k = n & 31;
        const u32 o = (n < 32 ? wa : wb) + (k < 16 ? (L_WQ0 - L_WQ1) : 0) + (k & 15) * 2048;
        wf[n & 3] = lds_tr2(o, o + 512);
      };
      WAITV(1);
      BAR();
      SCHED();
      STAMP(12);
      ldw(0); ldw(1); ldw(2);
      SCHED();
      BARE();
      SCHED();
#pragma unroll
      for (int n = 0; n < 34; ++n) {
        ldw(n + 3);
        aq[n >> 5] = mfma(wf[n & 3], xq[n & 31], aq[n >> 5]);
        SGB(0x8, 1); SGB(0x100, 2);
        SCHED();
      }
#pragma unroll
      for (int hh = 0; hh < 2; ++hh) pq[hh] = mfma(fa[hh], pack8(aq[0], hh), zero);
      SCHED();
      STAMP(4);
#pragma unroll
      for (int n = 34; n < 64; ++n) {
        if (n + 3 < 64) ldw(n + 3);
        aq[1] = mfma(wf[n & 3], xq[n & 31], aq[1]);
      }
      mq[0] = negmax16(pq[0]); mq[1] = negmax16(pq[1]);
      f32x2 eq[2][8];
      sm_exp_s(pq[0], pq[1], mq, eq);
      SGB(0x8, 1); SGB(0x100, 2); SGB(0x8, 1); SGB(0x100, 2);
#pragma unroll
      for (int n = 36; n < 41; ++n) { SGB(0x8, 1); SGB(0x100, 2); SGB(0x402, 4); }
#pragma unroll
      for (int n = 41; n < 64; ++n) { SGB(0x8, 1); SGB(0x100, 2); if (n & 1) SGB(0x402, 3); else SGB(0x402, 2); }
#pragma unroll
      for (int i = 0; i < 16; ++i) PIN(eq[i >> 3][i & 7]);
      SCHED();
      STAMP(3);
#pragma unroll
      for (int hh = 0; hh < 2; ++hh) { pq[hh] = mfma(fa[2 + hh], pack8(aq[1], hh), zero); PIN(pq[hh]); }
      SCHED();
      sm_finish(eq, fq[0]);
      SCHED();
      asm volatile("s_waitcnt vmcnt(0)" : "+v"(tho)::"memory");
      mq[0] = negmax16(pq[0]); mq[1] = negmax16(pq[1]);
      PIN(mq[0]); PIN(mq[1]);
      SCHED();
#pragma unroll
      for (int i = 0; i < 4; ++i) PIN(fq[0][i >> 1][i & 1]);
      softmax16x2(pq[0], pq[1], mq, fq[1]);
#pragma unroll
      for (int i = 0; i < 4; ++i) PIN(fq[1][i >> 1][i & 1]);
    }
    STAMP(5);
    BAR();
    STAMP(6);
    u32x4 ob[2][4];
    {
      u32x4 a0[4], a1[4];
#pragma unroll
      for (int hd = 0; hd < 4; ++hd) {
        a0[hd] = lds_tr2(kra + hd * 1536, kra + hd * 1536 + 4 * 48);
        a1[hd] = lds_tr2(kra + hd * 1536 + 8 * 48, kra + hd * 1536 + 12 * 48);
      }
      SCHED();
      f32x16 y[4];
      u32 cs = 0;
      auto num = [&](int hd) {
        y[hd] = mfma(a0[hd], fq[hd >> 1][hd & 1][0], zero);
        y[hd] = mfma(a1[hd], fq[hd >> 1][hd & 1][1], y[hd]);
      };
      auto fin = [&](int hd) {
        const f32x16& yy = y[hd];
        const float rd = __builtin_amdgcn_rcpf(yy[8] + 1e-6f);
        u32 w[4];
#pragma unroll
        for (int e = 0; e < 4; ++e) { float t0 = yy[2 * e] * rd, t1 = yy[2 * e + 1] * rd; if (hd < 2) OPQ(t0); w[e] = pk2(t0, t1); }
        ob[0][hd] = (u32x4){w[0], w[1], w[2], w[3]};
        st128c(ob[0][hd], wsr, o_off + hd * 1024);
        cs = sum4(cs, ob[0][hd]);
      };
      num(0); num(1); PIN(y[0]); PIN(y[1]); SCHED();
      num(2); fin(0); SGB(0x8, 1); SGB(0x402, 6); SGB(0x8, 1); PIN(y[2]); SCHED();
      num(3); fin(1); SGB(0x8, 1); SGB(0x402, 6); SGB(0x8, 1); PIN(y[3]); SCHED();
      fin(2); fin(3);
      st32c(key - cs, wsr, c_off);
    }
    SCHED();
    STAMP(7);
    u32x4 wo[32][2];
    auto ldwo = [&](int n) {
      wo[n][0] = lds_tr2(woa + n * 2048, woa + n * 2048 + 512);
      wo[n][1] = lds_tr2(wob + n * 2048, wob + n * 2048 + 512);
    };
#pragma unroll
    for (int n = 0; n < 16; ++n) ldwo(n);
    f32x16 c0 = zero, c1 = zero;
#pragma unroll
    for (int s = 0; s < 4; ++s) { c0 = mfma(wo[s][0], ob[0][s], c0); c1 = mfma(wo[s][1], ob[0][s], c1); }
#pragma unroll
    for (int k = 4; k < 16; ++k) { PIN(wo[k][0]); PIN(wo[k][1]); }
    PIN(c0); PIN(c1);
    WAITV(0);
    BARE();
    SCHED();
#pragma unroll
    for (int n = 16; n < 24; ++n) ldwo(n);
#pragma unroll
    for (int k = 16; k < 24; ++k) { PIN(wo[k][0]); PIN(wo[k][1]); }
    const __amdgpu_buffer_rsrc_t outr = __builtin_amdgcn_make_buffer_rsrc(KPTR(kb, 0), 0, 2048 * 1024 * 2, SRD3);
    u32 oa = lds0 + L_O + t * OREC + 256 + lane * 16;
    u32 orow = ((panel * 128 + t * 32 + l3) * 1024 + br * 512 + cg * 64 + 8 * l7) * 2;
    PIN(oa); PIN(orow);
    u32 oc = oa - 256 - lane * 12, l4 = lane * 4, l16 = lane * 16, oa4 = oa + 4 * (4 * OREC), oc4 = oc + 4 * (4 * OREC);
    u32 sw1 = lds0 + t * 4096 + r * 128 + 16 * (h ^ (r & 7)), sr1 = lds0 + t * 4096 + l3 * 128 + 16 * (l7 ^ l3);
    u32 sw2 = sw1 ^ 32, sw3 = sw1 ^ 64, sw4 = sw1 ^ 96, or1 = orow + 16384, or2 = orow + 32768, or3 = orow + 49152;
    PIN(oc); PIN(l4); PIN(l16); PIN(sw1); PIN(sr1); PIN(oa4); PIN(oc4); PIN(sw2); PIN(sw3); PIN(sw4); PIN(or1); PIN(or2); PIN(or3);
    STAMP(8);
    u32 nretry = 0;
    u32 chk[2];
    auto oread = [&](int j) {
      const u32 da = j % 5 == 4 ? oa4 : oa + (j % 5) * (4 * OREC), dc = j % 5 == 4 ? oc4 : oc + (j % 5) * (4 * OREC);
#pragma unroll
      for (int s = 0; s < 4; ++s) ob[j & 1][s] = lds_r128(da + s * 1024);
      chk[j & 1] = *(LAS u32*)(size_t)dc;
    };
    auto obad = [&](int j) -> bool {
      const u32 c = sum4(sum4(chk[j & 1], ob[j & 1][0]), ob[j & 1][1]) + sum4(sum4(0u, ob[j & 1][2]), ob[j & 1][3]);
      return __builtin_amdgcn_ballot_w64(c != key) != 0;
    };
#pragma unroll
    for (int j = 1; j < 8; ++j) {
      BARE();
      SCHED();
      oread(j);
      if (j >= 2) {
#pragma unroll
        for (int s = 0; s < 4; ++s) {
          const int k = 4 * (j - 1) + s;
          c0 = mfma(wo[k][0], ob[(j - 1) & 1][s], c0);
          c1 = mfma(wo[k][1], ob[(j - 1) & 1][s], c1);
        }
        if (j >= 3 && j <= 6) { ldwo(24 + 2 * (j - 3)); ldwo(25 + 2 * (j - 3)); }
      }
      const bool bad = obad(j);
      if (j >= 3 && j <= 6) {
        SGB(0x8, 1); SGB(0x100, 3); SGB(0x8, 1); SGB(0x100, 2); SGB(0x8, 1); SGB(0x100, 3); SGB(0x8, 1); SGB(0x100, 3);
        SGB(0x8, 1); SGB(0x2, 4); SGB(0x8, 1); SGB(0x2, 5); SGB(0x8, 1); SGB(0x100, 2); SGB(0x8, 1);
      } else if (j >= 2) {
        SGB(0x8, 1); SGB(0x100, 3); SGB(0x8, 1); SGB(0x100, 2); SGB(0x8, 3); SGB(0x2, 4); SGB(0x8, 1); SGB(0x2, 5); SGB(0x8, 2);
      }
      SCHED();
      if (__builtin_expect(bad, 0)) {
#pragma unroll 1
        for (u32 spin = 0; spin < SPIN_CAP; ++spin) {
          dma_orec(lds0 + L_O + t * OREC + (j % 5) * (4 * OREC), l4, l16, wsr, WS_O + ((br * 64 + panel * 4 + t) * 8 + ochunk(j)) * OREC);
          u32 jj = j + 1, jend = j + AHEAD < 7 ? j + AHEAD : 7;
          PINS(jj); PINS(jend);
#pragma unroll 1
          for (; jj <= jend; ++jj) {
            const u32 sl = (jj >= 5 ? jj - 5 : jj) * (4 * OREC);
            u32 c = *(LAS u32*)(size_t)(oc + sl);
#pragma unroll
            for (int s = 0; s < 4; ++s) c = sum4(c, lds_r128(oa + sl + s * 1024));
            if (__builtin_amdgcn_ballot_w64(c != key) != 0)
              dma_orec(lds0 + L_O + t * OREC + sl, l4, l16, wsr, WS_O + ((br * 64 + panel * 4 + t) * 8 + ochunk(jj)) * OREC);
          }
          WAITV(0);
          oread(j);
          ++nretry;
          if (!obad(j)) break;
        }
      }
    }
#pragma unroll
    for (int s = 0; s < 4; ++s) c0 = mfma(wo[28 + s][0], ob[1][s], c0);
    PIN(c0);
    SCHED();
#pragma unroll
    for (int s = 0; s < 4; ++s) c1 = mfma(wo[28 + s][1], ob[1][s], c1);
    lds_w128(sw1, pack8(c0, 0));
    lds_w128(sw2, pack8(c0, 1));
    SGB(0x8, 2); SGB(0x2, 4); SGB(0x200, 1); SGB(0x8, 1); SGB(0x2, 4); SGB(0x200, 1); SGB(0x8, 1);
    SCHED();
    STAMP(9);
    lds_w128(sw3, pack8(c1, 0));
    lds_w128(sw4, pack8(c1, 1));
    asm volatile("" ::: "memory");
    {
      u32x4 v[4];
#pragma unroll
      for (int i = 0; i < 4; ++i) v[i] = lds_r128(sr1 + i * 1024);
      SCHED();
      st128c(v[0], outr, orow);
      st128c(v[1], outr, or1);
      st128c(v[2], outr, or2);
      st128c(v[3], outr, or3);
    }
    if (t == 0 && panel == 0 && grp == 0 && lane == 0) st32c(key, wsr, WS_EPOCH + br * 256);
    if (PROBE_N == 13) WAITV(0);
    STAMP(13);
    STAMP_OUT();
  }
}
}

extern "C" void kernel_launch(void* const* d_in, const int* in_sizes, int n_in,
                              void* d_out, int out_size, void* d_ws, size_t ws_size,
                              hipStream_t stream) {
  hh::hedgehog<<<256, 512, 0, stream>>>((const u16*)d_in[0], (const u16*)d_in[1], (const u16*)d_in[2], (const u16*)d_in[3],
                                        (const u16*)d_in[4], (const u16*)d_in[5], (const u16*)d_in[6], (const u16*)d_in[7],
                                        (u16*)d_out, (u32*)d_ws);
}
```

```cpp
#define PROBE_N 9
#define PROBE_ROLE 0
#include <hip/hip_runtime.h>
#include <stdint.h>

typedef unsigned short u16;
typedef unsigned u32;
typedef short bf16x8 __attribute__((ext_vector_type(8)));
typedef short s16x4 __attribute__((ext_vector_type(4)));
typedef float f32x16 __attribute__((ext_vector_type(16)));
typedef float f32x4 __attribute__((ext_vector_type(4)));
typedef float f32x2 __attribute__((ext_vector_type(2)));
typedef unsigned u32x8 __attribute__((ext_vector_type(8)));
typedef unsigned u32x4 __attribute__((ext_vector_type(4)));
typedef unsigned u32x2 __attribute__((ext_vector_type(2)));
typedef __bf16 bf16x2 __attribute__((ext_vector_type(2)));
#define DI __device__ __forceinline__
#define LAS __attribute__((address_space(3)))
#define CAS __attribute__((address_space(4)))
#define BC(T, x) __builtin_bit_cast(T, x)

#define DELAY1 3
#define DELAY3 4
#define CODE_PF 29184
#define AHEAD 1
#define GQ 1
#define GC 2
#define GBURST 1
#define WARM 0
#define RSLEEP 0
#define GORD 0
#define OFIRST 5

namespace hh {
constexpr int AUX_SC1 = 16;
constexpr u32 SRD3 = 0x00020000u;
constexpr u32 WS_EPOCH = 0;
constexpr u32 REC = 1280;
constexpr u32 WS_PART = 4096;
constexpr u32 OREC = 4352;
constexpr u32 WS_O = WS_PART + 2 * 32 * 16 * REC;
constexpr u32 WS_END = WS_O + 2 * 64 * 8 * OREC;
constexpr u32 WS_STAMP = 8u << 20;
constexpr u32 SLOT = 32768;
constexpr u32 L_WQ0 = 98304;
constexpr u32 L_WQ1 = 65536;
constexpr u32 L_WPHI = 163840 - 4096;
constexpr u32 L_PHI = 0;
constexpr u32 VF = 1040;
constexpr u32 L_V = 131072;
constexpr u32 L_KV = L_V + 16 * VF;
constexpr u32 L_WO = 0;
constexpr u32 L_O = 65536;
constexpr u32 SPIN_CAP = 1u << 16;

DI u32 pk2(float a, float b) { return BC(u32, __builtin_convertvector((f32x2){a, b}, bf16x2)); }
DI u32x4 pack8(const f32x16& x, int s) {
  return (u32x4){pk2(x[8 * s], x[8 * s + 1]), pk2(x[8 * s + 2], x[8 * s + 3]), pk2(x[8 * s + 4], x[8 * s + 5]), pk2(x[8 * s + 6], x[8 * s + 7])};
}
DI f32x16 mfma(u32x4 a, u32x4 b, f32x16 c) { return __builtin_amdgcn_mfma_f32_32x32x16_bf16(BC(bf16x8, a), BC(bf16x8, b), c, 0, 0, 0); }
DI f32x4 mfma16(u32x4 a, u32x4 b, f32x4 c) { return __builtin_amdgcn_mfma_f32_16x16x32_bf16(BC(bf16x8, a), BC(bf16x8, b), c, 0, 0, 0); }
DI u32x4 lds_r128(u32 a) { return *(LAS u32x4*)(size_t)a; }
DI void lds_w128(u32 a, u32x4 v) { *(LAS u32x4*)(size_t)a = v; }
DI u32x4 lds_tr2(u32 a0, u32 a1) {
  s16x4 lo = __builtin_amdgcn_ds_read_tr16_b64_v4i16((LAS s16x4*)(size_t)a0);
  s16x4 hi = __builtin_amdgcn_ds_read_tr16_b64_v4i16((LAS s16x4*)(size_t)a1);
  return BC(u32x4, __builtin_shufflevector(lo, hi, 0, 1, 2, 3, 4, 5, 6, 7));
}
DI void dma1(u32 lds, u32 voff, __amdgpu_buffer_rsrc_t srd, u32 soff) {
  asm volatile("s_mov_b32 m0, %0\n\ts_nop 0\n\tbuffer_load_dwordx4 %1, %2, %3 offen lds" ::"s"(lds), "v"(voff), "s"(srd), "s"(soff) : "memory");
}
DI void dma4(u32 lds, const u32 (&v)[4], __amdgpu_buffer_rsrc_t srd, u32 soff) {
  asm volatile("s_mov_b32 m0, %0\n\ts_nop 0\n\t"
               "buffer_load_dwordx4 %1, %5, %6 offen lds\n\t"
               "buffer_load_dwordx4 %2, %5, %6 offen offset:1024 lds\n\t"
               "buffer_load_dwordx4 %3, %5, %6 offen offset:2048 lds\n\t"
               "buffer_load_dwordx4 %4, %5, %6 offen offset:3072 lds" ::"s"(lds), "v"(v[0]), "v"(v[1]), "v"(v[2]), "v"(v[3]), "s"(srd), "s"(soff) : "memory");
}
DI void dma_orec(u32 lds, u32 l4, u32 l16, __amdgpu_buffer_rsrc_t srd, u32 soff) {
  asm volatile("s_mov_b32 m0, %0\n\ts_nop 0\n\t"
               "buffer_load_dwordx4 %2, %3, %4 offen offset:256 sc1 lds\n\t"
               "buffer_load_dwordx4 %2, %3, %4 offen offset:1280 sc1 lds\n\t"
               "buffer_load_dwordx4 %2, %3, %4 offen offset:2304 sc1 lds\n\t"
               "buffer_load_dwordx4 %2, %3, %4 offen offset:3328 sc1 lds\n\t"
               "buffer_load_dword %1, %3, %4 offen sc1 lds" ::"s"(lds), "v"(l4), "v"(l16), "s"(srd), "s"(soff) : "memory");
}
#define TOUCH(d, voff, srd, soff) asm volatile("buffer_load_dword %0, %1, %2, %3 offen" : "=v"(d) : "v"(voff), "s"(srd), "s"(soff) : "memory")
DI void st128c(u32x4 v, __amdgpu_buffer_rsrc_t srd, u32 off) { __builtin_amdgcn_raw_buffer_store_b128(v, srd, off, 0, AUX_SC1); }
DI void st32c(u32 v, __amdgpu_buffer_rsrc_t srd, u32 off) { __builtin_amdgcn_raw_buffer_store_b32(v, srd, off, 0, AUX_SC1); }
#define BAR() asm volatile("s_waitcnt lgkmcnt(0)\n\ts_barrier" ::: "memory")
#define BARE() asm volatile("s_barrier" ::: "memory")
#define OPQ(x) asm("" : "+v"(x))
#define PIN(x) asm volatile("" : "+v"(x))
#define PINS(x) asm volatile("" : "+s"(x))
#define WAITV(n) asm volatile("s_waitcnt vmcnt(" #n ")" ::: "memory")
#define SCHED() __builtin_amdgcn_sched_barrier(0)
#define SGB(m, n) __builtin_amdgcn_sched_group_barrier(m, n, 0)
DI u32 sum4(u32 c, u32x4 v) { return (c + v.x + v.y) + v.z + v.w; }
DI u32 pair_sum(u32 s) { auto t = __builtin_amdgcn_permlane32_swap(s, s, false, false); return t[0] + t[1]; }

constexpr float LOG2E = 1.44269504088896340736f;
DI float max3f(float a, float b, float c) { return __builtin_fmaxf(__builtin_fmaxf(a, b), c); }
DI float negmax16(const f32x16& x) {
  const float a = max3f(x[0], x[1], x[2]), b = max3f(x[3], x[4], x[5]), c = max3f(x[6], x[7], x[8]);
  const float d = max3f(x[9], x[10], x[11]), e = max3f(x[12], x[13], x[14]);
  return -max3f(max3f(a, b, x[15]), max3f(c, d, e), a) * LOG2E;
}
DI void sm_exp(const f32x16& p, const f32x16& q, const float (&mc)[2], f32x2 (&e)[2][8]) {
#pragma unroll
  for (int i = 0; i < 8; ++i) {
    const f32x2 t = __builtin_elementwise_fma((f32x2){p[2 * i], p[2 * i + 1]}, (f32x2){LOG2E, LOG2E}, (f32x2){mc[0], mc[0]});
    const f32x2 u = __builtin_elementwise_fma((f32x2){q[2 * i], q[2 * i + 1]}, (f32x2){LOG2E, LOG2E}, (f32x2){mc[1], mc[1]});
    e[0][i] = (f32x2){__builtin_amdgcn_exp2f(t.x), __builtin_amdgcn_exp2f(t.y)};
    e[1][i] = (f32x2){__builtin_amdgcn_exp2f(u.x), __builtin_amdgcn_exp2f(u.y)};
  }
  SGB(0x2, 4); SGB(0x400, 8); SGB(0x2, 4); SGB(0x400, 8); SGB(0x2, 6); SGB(0x400, 8); SGB(0x2, 8); SGB(0x400, 8);
}
DI void sm_exp_s(const f32x16& p, const f32x16& q, const float (&mc)[2], f32x2 (&e)[2][8]) {
#pragma unroll
  for (int i = 0; i < 16; ++i) {
    e[0][i >> 1][i & 1] = __builtin_amdgcn_exp2f(__builtin_fmaf(p[i], LOG2E, mc[0]));
    e[1][i >> 1][i & 1] = __builtin_amdgcn_exp2f(__builtin_fmaf(q[i], LOG2E, mc[1]));
  }
}
DI void sm_finish(const f32x2 (&e)[2][8], u32x4 (&o)[2][2]) {
  float r[2];
#pragma unroll
  for (int u = 0; u < 2; ++u) {
    const f32x2 s = ((e[u][0] + e[u][1]) + (e[u][2] + e[u][3])) + ((e[u][4] + e[u][5]) + (e[u][6] + e[u][7]));
    r[u] = __builtin_amdgcn_rcpf(s.x + s.y);
  }
#pragma unroll
  for (int u = 0; u < 2; ++u) {
    u32 w[8];
#pragma unroll
    for (int i = 0; i < 8; ++i) { const f32x2 t = e[u][i] * (f32x2){r[u], r[u]}; w[i] = pk2(t.x, t.y); }
    o[u][0] = (u32x4){w[0], w[1], w[2], w[3]};
    o[u][1] = (u32x4){w[4], w[5], w[6], w[7]};
  }
}
DI void softmax16x2(const f32x16& p, const f32x16& q, const float (&mc)[2], u32x4 (&o)[2][2]) {
  f32x2 e[2][8];
  sm_exp(p, q, mc, e);
  sm_finish(e, o);
}

#define STAMP(i) do { if ((i) == PROBE_N) { SCHED(); asm volatile("s_memrealtime %0\n\ts_waitcnt lgkmcnt(0)" : "=s"(pt1) :: "memory"); SCHED(); } } while (0)
#define STAMP_OUT() do { if (PROBE_N && (wave >> 2) == PROBE_ROLE) { unsigned long long d_ = pt1 - pt0; if (d_ > 100000) d_ = 100000; unsigned long long e_ = pt0 + 3000 + 8 * d_; \
  for (int it_ = 0; it_ < 1000000; ++it_) { unsigned long long t_; asm volatile("s_memrealtime %0\n\ts_waitcnt lgkmcnt(0)" : "=s"(t_) :: "memory"); if (t_ >= e_) break; } } } while (0)

__global__ __launch_bounds__(512, 2) void hedgehog(const u16* xA, const u16* wA, const u16* pA, const u16* oA,
                                                   const u16* xB, const u16* wB, const u16* pB, const u16* oB,
                                                   u16* out_, u32* ws_) {
  __shared__ __attribute__((aligned(16))) char smem[163840];
  const u32 lds0 = (u32)(size_t)(LAS char*)smem;
  unsigned long long pt0 = __builtin_amdgcn_s_memrealtime(), pt1 = pt0;
  const int bid = blockIdx.x, xcd = bid & 7, idx = bid >> 3;
  const int br = xcd >> 2;
#define KLOAD(tail)                                                                                       \
  u32x8 ka;                                                                                               \
  u32x4 kb;                                                                                               \
  asm volatile("s_load_dwordx8 %0, %2, %3\n\ts_load_dwordx4 %1, %2, 0x40" tail : "=&s"(ka), "=&s"(kb)     \
               : "s"(__builtin_amdgcn_kernarg_segment_ptr()), "s"(br * 32))
#define KWAIT() asm volatile("s_waitcnt lgkmcnt(0)" : "+s"(ka), "+s"(kb))
#define KPTR(v, i) ((void*)(((unsigned long long)(v)[(i) + 1] << 32) | (v)[i]))
  unsigned long long pcs;
  asm volatile("s_getpc_b64 %0" : "=s"(pcs));
  pcs &= ~255ull;
  const int lane = threadIdx.x & 63;
  const int wave = __builtin_amdgcn_readfirstlane(threadIdx.x >> 6);
  const int panel = (xcd & 3) * 4 + (idx & 3), grp = idx >> 2, gq = (grp + GQ) & 7, cg = (grp + GC) & 7;
  auto ochunk = [&](u32 j) -> u32 {
    const int v = ((int)j + (((int)j - 1 - ((gq - OFIRST) & 7)) >> 31) + OFIRST) & 7, z = ((int)j - 1) >> 31;
    return (u32)__builtin_amdgcn_readfirstlane((v & ~z) | (gq & z));
  };
  f32x16 zero;
#pragma unroll
  for (int i = 0; i < 16; ++i) zero[i] = 0.f;
#define LANE_IDS()                                                                            \
  u32 lane_ = lane;                                                                           \
  PIN(lane_);                                                                                 \
  const u32 r = lane_ & 31, h = lane_ >> 5;                                                   \
  const u32 blk = (lane_ >> 4) & 1, q = (lane_ & 15) >> 2, p = lane_ & 3;                     \
  u32* ws = (u32*)KPTR(kb, 2);                                                                \
  const __amdgpu_buffer_rsrc_t wsr = __builtin_amdgcn_make_buffer_rsrc(ws, 0, WS_END, SRD3);  \
  const __amdgpu_buffer_rsrc_t wor = __builtin_amdgcn_make_buffer_rsrc(KPTR(ka, 6), 0, 512 * 512 * 2, SRD3)

  if (wave >= 4) {
    const int cw = wave - 4;
    const int rh = cw >> 1, hp = cw & 1;
    KLOAD("\n\ts_waitcnt lgkmcnt(0)");
    u32 sp0, sp1, sp2, sp3;
    {
      u32 off = idx * 1024 + cw * 256;
      off = off < CODE_PF ? off : 0;
      asm volatile("s_load_dword %0, %4, %5 offset:0\n\ts_load_dword %1, %4, %5 offset:64\n\ts_load_dword %2, %4, %5 offset:128\n\ts_load_dword %3, %4, %5 offset:192"
                   : "=&s"(sp0), "=&s"(sp1), "=&s"(sp2), "=&s"(sp3) : "s"(pcs), "s"(off));
    }
    LANE_IDS();
    u32 key = *(const CAS u32*)((const char*)ws + WS_EPOCH + br * 256) + 1u;
    u32 xa[2], xb[2], wa[2], wb2[2];
    {
      const u32 r16 = lane_ & 15, g = lane_ >> 4;
#pragma unroll
      for (int s = 0; s < 2; ++s) {
        xa[s] = lds0 + (64 * rh + r16) * 128 + 16 * ((4 * s + g) ^ (r16 >> 1));
        wa[s] = lds0 + 16384 + (8 * g + q) * 128 + 16 * ((4 * hp + 2 * s + (p >> 1)) ^ ((((q >> 1) & 1) << 2) | (blk << 1))) + 8 * (p & 1);
        xb[s] = xa[s] + 2 * SLOT; wb2[s] = wa[s] + 2 * SLOT;
        PIN(xa[s]); PIN(xb[s]); PIN(wa[s]); PIN(wb2[s]);
      }
    }
    u32 fpa = lds0 + L_WPHI + hp * 1024 + (8 * h + q) * 32 + (8 * blk + 4 * (p >> 1)) * 2;
    PIN(fpa);
    if (!WARM) PINS(key);
    f32x4 ak[4][2], av[4][2];
#pragma unroll
    for (int i = 0; i < 8; ++i) ak[i >> 1][i & 1] = av[i >> 1][i & 1] = (f32x4){0.f, 0.f, 0.f, 0.f};
    u32 vra, pra, vwa, pw0, pw1, pub16, pub4;
    u32x4 xf[2][4], wk[2][2], wv[2][2];
    auto ldx = [&](int c, int s) {
      const u32 x = c % 3 == 2 ? xb[s] : xa[s] + (c % 3) * SLOT;
#pragma unroll
      for (int i = 0; i < 4; ++i) xf[s][i] = lds_r128(x + i * 2048);
    };
    auto ldw = [&](int c, int s, int m) {
#pragma unroll
      for (int n = 0; n < 2; ++n) {
        const u32 w = (c % 3 == 2 ? wb2[n] : wa[n] + (c % 3) * SLOT) + s * 4096 + m * 8192;
        (m ? wv : wk)[s][n] = lds_tr2(w, w + 512);
      }
    };
    auto mk = [&](int s) {
#pragma unroll
      for (int i = 0; i < 8; ++i) ak[i >> 1][i & 1] = mfma16(wk[s][i & 1], xf[s][i >> 1], ak[i >> 1][i & 1]);
    };
    auto mv = [&](int s) {
#pragma unroll
      for (int i = 0; i < 8; ++i) av[i >> 1][i & 1] = mfma16(xf[s][i >> 1], wv[s][i & 1], av[i >> 1][i & 1]);
    };
    auto mix = [&](int n) {
#pragma unroll
      for (int i = 0; i < 8; ++i) {
        SGB(0x8, 1);
        if (n == 12) { if (i < 4) SGB(0x100, 2); else SGB(0x100, 1); }
        else if (n == 8) SGB(0x100, 1);
        else if (n == 4) { if (i < 4) SGB(0x100, 1); }
        else if (n < 0) SGB(0x6, 2);
      }
      SCHED();
    };
    if (WARM) {
      u32 hsh = lane * 2654435761u + 12345u;
      u32x4 a = {(hsh & 0x3FFF3FFFu) | 0x30003000u, ((hsh * 7u) & 0xBFFFBFFFu) | 0x30003000u, ((hsh * 13u) & 0x3FFFBFFFu) | 0x30003000u, ((hsh * 29u) & 0xBFFF3FFFu) | 0x30003000u};
      u32x4 b = {a.y, a.w, a.x, a.z};
      f32x16 w0 = zero, w1 = zero;
#pragma unroll 1
      for (int i = 0; i < WARM; ++i) { w0 = mfma(a, b, w0); w1 = mfma(b, a, w1); w0 = mfma(b, b, w0); w1 = mfma(a, a, w1); PIN(w0); PIN(w1); }
    }
    BAR();
    asm volatile("" ::"s"(sp0), "s"(sp1), "s"(sp2), "s"(sp3));
    if (WARM) PINS(key);
    STAMP(1);
    ldw(0, 0, 0); ldx(0, 0); ldw(0, 0, 1);
    u32x4 fa[2];
#pragma unroll
    for (int hh = 0; hh < 2; ++hh) fa[hh] = lds_tr2(fpa + hh * 512, fpa + hh * 512 + 128);
    SCHED();
#pragma unroll
    for (int c = 0; c < 7; ++c) {
      ldw(c, 1, 0); ldx(c, 1); mk(0); mix(8);
      ldw(c, 1, 1); mv(0); mix(4);
      u32 ln = lane;
      PIN(ln);
      const u32 r_ = ln & 31, h_ = ln >> 5, q_ = (ln & 15) >> 2;
      if (c == 1) {
        const u32 sg = (ln & 4) ? 0x80008000u : 0u;
#pragma unroll
        for (int hh = 0; hh < 2; ++hh) { OPQ(fa[hh]); fa[hh] ^= (u32x4){sg, sg, sg, sg}; }
      }
      if (c == 2) vra = lds0 + L_V + (cw >> 1) * (2 * VF) + (r_ < 16 ? (ln + 16 * (cw & 1)) * 16 : 1024);
      if (c == 3) pra = lds0 + L_PHI + cw * 8192 + (4 * h_ + q_) * 64 + 16 * ((((ln >> 3) & 2) + ((ln >> 1) & 1)) ^ ((2 * h_ + (q_ >> 1)) & 3)) + 8 * (ln & 1);
      if (c == 4) {
        const u32 rec = WS_PART + ((br * 32 + grp * 4 + cw) * 16 + panel) * REC;
        pub16 = rec + ln * 16; pub4 = rec + 1024 + ln * 4;
      }
      if (c == 5) { pw0 = lds0 + L_PHI + (2 * hp) * 8192 + (64 * rh + r_) * 64 + 16 * ((2 * h_) ^ ((r_ >> 1) & 3)); pw1 = pw0 ^ 16; }
      if (c == 6) vwa = lds0 + L_V + (8 * rh + 2 * hp) * VF + ((ln & 16) * 2 + (ln & 15)) * 16 + h_ * 8;
      mk(1); mix(c >= 1 ? -1 : 0);
      if (c == 1) { PIN(fa[0]); PIN(fa[1]); }
      if (c == 2) PIN(vra);
      if (c == 3) PIN(pra);
      if (c == 4) { PIN(pub16); PIN(pub4); }
      if (c == 5) { PIN(pw0); PIN(pw1); }
      if (c == 6) PIN(vwa);
      BAR();
      ldw(c + 1, 0, 0); ldx(c + 1, 0); ldw(c + 1, 0, 1);
      mv(1); mix(12);
    }
    ldw(7, 1, 0); ldx(7, 1); ldw(7, 1, 1); mk(0); mix(12);
    mk(1); mix(0);
    __builtin_amdgcn_s_setprio(2);
    STAMP(2);
    f32x16 pk[2][2];
    {
      u32x4 kf[2][2];
      auto kpack = [&](int i) {
#pragma unroll
        for (int hh = 0; hh < 2; ++hh) {
          const f32x4 &t0 = ak[2 * i][hh], &t1 = ak[2 * i + 1][hh];
          const auto lo = __builtin_amdgcn_permlane16_swap(pk2(t0[0], t0[1]), pk2(t1[0], t1[1]), false, false);
          const auto hi = __builtin_amdgcn_permlane16_swap(pk2(t0[2], t0[3]), pk2(t1[2], t1[3]), false, false);
          kf[i][hh] = (u32x4){lo[0], hi[0], lo[1], hi[1]};
        }
      };
      mv(0); kpack(0);
      SGB(0x8, 2);
#pragma unroll
      for (int i = 0; i < 6; ++i) { SGB(0x2, 2); SGB(0x8, 1); }
      SCHED();
      pk[0][0] = mfma(fa[0], kf[0][0], zero);
      pk[0][1] = mfma(fa[1], kf[0][1], zero);
      kpack(1);
      SGB(0x8, 1); SGB(0x2, 6); SGB(0x8, 1); SGB(0x2, 6);
      PIN(pk[0][0]); PIN(pk[0][1]); PIN(kf[1][0]); PIN(kf[1][1]);
      SCHED();
      mv(1);
      pk[1][0] = mfma(fa[0], kf[1][0], zero);
      pk[1][1] = mfma(fa[1], kf[1][1], zero);
    }
    float m0[2], m1[2];
    m0[0] = negmax16(pk[0][0]); m0[1] = negmax16(pk[0][1]);
    SGB(0x8, 2);
#pragma unroll
    for (int i = 0; i < 6; ++i) { SGB(0x2, 2); SGB(0x8, 1); }
    SGB(0x8, 1); SGB(0x2, 6); SGB(0x8, 1);
#pragma unroll
    for (int i = 0; i < 8; ++i) PIN(av[i >> 1][i & 1]);
    PIN(pk[1][0]); PIN(pk[1][1]); PIN(m0[0]); PIN(m0[1]);
    SCHED();
#pragma unroll
    for (int i = 0; i < 8; ++i) {
      const f32x4& t = av[i >> 1][i & 1];
      *(LAS u32x2*)(size_t)(vwa + (4 * (i >> 2) + ((i >> 1) & 1)) * VF + (i & 1) * 256) = (u32x2){pk2(t[0], t[1]), pk2(t[2], t[3])};
    }
    SCHED();
    {
      u32x4 o[2][2];
      softmax16x2(pk[0][0], pk[0][1], m0, o);
      lds_w128(pw0, o[0][0]); lds_w128(pw1, o[0][1]);
      lds_w128(pw0 + 8192, o[1][0]); lds_w128(pw1 + 8192, o[1][1]);
      SCHED();
      m1[0] = negmax16(pk[1][0]); m1[1] = negmax16(pk[1][1]);
      PIN(m1[0]); PIN(m1[1]);
      SCHED();
      softmax16x2(pk[1][0], pk[1][1], m1, o);
      lds_w128(pw0 + 2048, o[0][0]); lds_w128(pw1 + 2048, o[0][1]);
      lds_w128(pw0 + 2048 + 8192, o[1][0]); lds_w128(pw1 + 2048 + 8192, o[1][1]);
      SGB(0x402, 26); SGB(0x200, 1); SGB(0x402, 8); SGB(0x200, 1); SGB(0x402, 8); SGB(0x200, 1); SGB(0x402, 8); SGB(0x200, 1);
      SCHED();
      BARE();
      BAR();
      SCHED();
    }
    STAMP(3);
    f32x16 kv = zero;
    {
      u32x4 a[8], b[8];
#pragma unroll
      for (int u = 0; u < 8; ++u) {
        a[u] = lds_r128(vra + ((u >> 1) * 4 + (u & 1)) * VF);
        b[u] = lds_tr2(pra + 16 * u * 64, pra + (16 * u + 8) * 64);
      }
#pragma unroll
      for (int u = 0; u < 8; ++u) kv = mfma(a[u], b[u], kv);
      SGB(0x100, 12); SGB(0x8, 1); SGB(0x100, 6); SGB(0x8, 1); SGB(0x100, 6); SGB(0x8, 6);
    }
    {
      const u32x4 w = pack8(kv, 0);
      st128c(w, wsr, pub16);
      const u32 zb = __float_as_uint(kv[8]);
      const u32 chk = key - pair_sum(sum4(h ? 0u : zb, w));
      st32c(h ? chk : zb, wsr, pub4);
    }
    SCHED();
    STAMP(4);
    u32 l16, l4, kwa, kza, gso[4], vo[4], wso;
    u32x4 sel;
    {
      u32 ln = lane;
      PIN(ln);
#pragma unroll
      for (int i = 0; i < 4; ++i) vo[i] = (ln >> 3) * 1024 + 16 * ((ln & 7) ^ (((ln >> 4) & 1) << 2)) + i * 7168;
#pragma unroll
      for (int i = 0; i < 2; ++i) dma4(lds0 + L_WO + cw * 8192 + i * 4096, vo, wor, ochunk(cw) * 65536 + i * 32768 + cg * 128);
      wso = ochunk(4 + cw) * 65536 + cg * 128;
      const u32 r_ = ln & 31, h_ = ln >> 5;
      l16 = ln * 16; l4 = ln * 4;
      kwa = lds0 + L_KV + cw * 1536 + r_ * 48 + h_ * 16; kza = lds0 + L_KV + cw * 1536 + r_ * 48 + 32 + h_ * 8;
      const bool hit = r_ < 16 && ((r_ >> 2) & 1) == h_;
      const u32 j = (r_ & 3) + 4 * (r_ >> 3);
#pragma unroll
      for (int e = 0; e < 4; ++e) sel[e] = (hit && (j >> 1) == e) ? ((j & 1) ? 0x3F800000u : 0x00003F80u) : 0u;
#pragma unroll
      for (int i = 0; i < 4; ++i) {
        u32 k = i + (idx & 3); k = k >= 3 ? k - 3 : k; k = k >= 3 ? k - 3 : k;
        const u32 gsel = GORD == 0 ? (i + idx) & 3 : GORD == 1 ? (xcd + 1 + i) & 3 : (i == 3 ? xcd : xcd + 1 + k) & 3;
        gso[i] = WS_PART + ((br * 32 + gq * 4 + cw) * 16 + gsel * 4) * REC; PINS(gso[i]);
      }
      PIN(l16); PIN(l4); PIN(kwa); PIN(kza); PIN(sel); PINS(wso);
    }
    if (DELAY1) __builtin_amdgcn_s_sleep(DELAY1);
    STAMP(10);
    f32x16 gs[4];
    float z[4];
    {
      u32x4 g[16];
      u32 gz[16];
      auto gload = [&](int gi) {
#pragma unroll
        for (int i = 0; i < 4; ++i) {
          g[4 * gi + i] = __builtin_amdgcn_raw_buffer_load_b128(wsr, l16 + i * REC, gso[gi], AUX_SC1);
          gz[4 * gi + i] = __builtin_amdgcn_raw_buffer_load_b32(wsr, l4 + (1024 + i * REC - 1024 * (i == 3)), gso[gi] + 1024 * (i == 3), AUX_SC1);
          SCHED();
        }
      };
      auto gsum = [&](int gi) -> bool {
        u32 c = 0;
        if (GBURST) { PIN(gz[4 * gi + 3]); SCHED(); }
#pragma unroll
        for (int i = 0; i < 4; ++i) {
          c = sum4(c + gz[4 * gi + i], g[4 * gi + i]);
          gs[gi] = mfma(sel, g[4 * gi + i], i == 0 ? zero : gs[gi]);
          z[gi] = i == 0 ? __uint_as_float(gz[4 * gi]) : z[gi] + __uint_as_float(gz[4 * gi + i]);
        }
        const bool bad = __builtin_amdgcn_ballot_w64(pair_sum(c) != 4u * key) != 0;
        if (GBURST) { SGB(0x8, 1); SGB(0x2, 5); SGB(0x8, 1); SGB(0x2, 5); SGB(0x8, 1); SGB(0x2, 4); SGB(0x8, 1); }
        PIN(z[gi]);
        SCHED();
        return bad;
      };
      gload(0); gload(1); gload(2); gload(3);
      bool b0 = gsum(0), b1 = gsum(1), b2 = gsum(2), b3 = gsum(3);
      STAMP(11);
#pragma unroll 1
      for (u32 spin = 0; (b0 | b1 | b2 | b3) && spin < SPIN_CAP; ++spin) {
        asm volatile("" ::: "memory");
        if (RSLEEP) __builtin_amdgcn_s_sleep(RSLEEP);
        if (b0) gload(0);
        if (b1) gload(1);
        if (b2) gload(2);
        if (b3) gload(3);
        if (b0) b0 = gsum(0);
        if (b1) b1 = gsum(1);
        if (b2) b2 = gsum(2);
        if (b3) b3 = gsum(3);
      }
    }
    __builtin_amdgcn_s_waitcnt(0x0F70);
    STAMP(5);
    {
      float tt[8];
#pragma unroll
      for (int j = 0; j < 8; ++j) tt[j] = (gs[0][j] + gs[1][j]) + (gs[2][j] + gs[3][j]);
      const float zt = (z[0] + z[1]) + (z[2] + z[3]);
      lds_w128(kwa, (u32x4){pk2(tt[0], tt[1]), pk2(tt[2], tt[3]), pk2(tt[4], tt[5]), pk2(tt[6], tt[7])});
      const u32 zz = pk2(zt, zt);
      *(LAS u32x2*)(size_t)kza = (u32x2){zz, zz};
    }
#pragma unroll
    for (int i = 0; i < 2; ++i) dma4(lds0 + L_WO + 32768 + cw * 8192 + i * 4096, vo, wor, wso + i * 32768);
    BAR();
    STAMP(6);
    {
      SCHED();
      u32 oso[8], ob = lds0 + L_O + cw * OREC, i0 = 0;
      PINS(i0);
#pragma unroll
      for (int i = 1; i < 8; ++i) { oso[i] = WS_O + ((br * 64 + panel * 4 + cw) * 8 + ochunk(i + i0)) * OREC; PINS(oso[i]); }
      PINS(ob);
      WAITV(0);
      BARE();
      if (DELAY3) __builtin_amdgcn_s_sleep(DELAY3);
      STAMP(8);
      auto oissue = [&](int j) { dma_orec(ob + (j % 5) * (4 * OREC), l4, l16, wsr, oso[j]); };
      oissue(1); oissue(2);
      WAITV(5); BARE(); oissue(3);
      WAITV(5); STAMP(15); BARE(); oissue(4); oissue(5);
      WAITV(10); BARE(); oissue(6);
      WAITV(10); BARE(); oissue(7);
      WAITV(10); BARE();
      WAITV(5); BARE();
      WAITV(0); BARE();
    }
    STAMP(9);
    STAMP_OUT();
  } else {
    KLOAD("");
    const int t = wave;
    const u32 l7 = lane & 7, l3 = lane >> 3, lq = lane >> 4;
    const u32 wsw = 16 * (l7 ^ ((lq & 1) << 2));
    u32 vx[4], vw[4];
#pragma unroll
    for (int i = 0; i < 4; ++i) {
      vx[i] = l3 * 1024 + 16 * (l7 ^ (((i & 1) << 2) | lq)) + i * 7168;
      vw[i] = l3 * 3072 + (wsw ^ ((i & 1) << 5)) + i * (8 * 3072 - 1024);
    }
    auto chunk = [&](u32 c) -> u32 {
      const u32 g = grp + idx;
      return c - 2 < 4 ? 2 + ((c - 2) ^ (g & 3)) : c ^ (g & 1);
    };
    const u32 xs0 = (panel * 128 + t * 32) * 1024, ws0 = (t & 1) * (32 * 3072) + (1 + (t >> 1)) * 1024 + grp * 128;
    const u32 ldst = lds0 + t * 4096;
    u32 e0 = ldst, e1 = ldst + 16384, e2 = xs0 + chunk(0) * 128, e3 = ws0 + chunk(0) * (64 * 3072);
    PINS(e0); PINS(e1); PINS(e2); PINS(e3);
#pragma unroll
    for (int i = 0; i < 4; ++i) { PIN(vx[i]); PIN(vw[i]); }
    KWAIT();
    const __amdgpu_buffer_rsrc_t xr = __builtin_amdgcn_make_buffer_rsrc(KPTR(ka, 0), 0, 2048 * 512 * 2, SRD3);
    const __amdgpu_buffer_rsrc_t wr = __builtin_amdgcn_make_buffer_rsrc(KPTR(ka, 2), 0, 512 * 1536 * 2, SRD3);
    STAMP(10);
    dma4(e0, vx, xr, e2);
    dma4(e1, vw, wr, e3);
    {
      const __amdgpu_buffer_rsrc_t pr = __builtin_amdgcn_make_buffer_rsrc(KPTR(ka, 4), 0, 32 * 256 * 2, SRD3);
      dma1(lds0 + L_WPHI + t * 1024, lane * 16, pr, (t < 2 ? grp : gq) * 2048 + (t & 1) * 1024);
    }
    STAMP(11);
    dma4(ldst + SLOT, vx, xr, xs0 + chunk(1) * 128);
    dma4(ldst + SLOT + 16384, vw, wr, ws0 + chunk(1) * (64 * 3072));
    STAMP(1);
    LANE_IDS();
    u32 key = *(const CAS u32*)((const char*)ws + WS_EPOCH + br * 256) + 1u;
    const u32 sw = (r >> 1) & 7;
    u32 xa[4];
#pragma unroll
    for (int s = 0; s < 4; ++s) { xa[s] = lds0 + (32 * t + r) * 128 + 16 * ((2 * s + h) ^ sw); PIN(xa[s]); }
    u32 fpa = lds0 + L_WPHI + 2048 + (4 * h + q) * 32 + (8 * blk + 4 * (p >> 1)) * 2;
    PIN(fpa);
    PINS(key);
    u32x4 xq[32], fa[4];
    u32 wa, wb, kra, o_off, c_off, woa, wob, vto, sto, tho;
#pragma unroll
    for (int c = 0; c < 8; ++c) {
      u32 d0, d1, s0, s1;
      if (c < 6) {
        d0 = ldst + ((c + 2) % 3) * SLOT; d1 = d0 + 16384;
        s0 = xs0 + chunk(c + 2) * 128; s1 = ws0 + chunk(c + 2) * (64 * 3072);
      } else {
        const u32 k0 = 2 * ((2 * (c - 6) + idx) & 3) + (t >> 1), k1 = (k0 + 2) & 7;
        d0 = lds0 + L_WQ1 + ((chunk(k0) + 4) & 7) * 8192 + (t & 1) * 4096; d1 = lds0 + L_WQ1 + ((chunk(k1) + 4) & 7) * 8192 + (t & 1) * 4096;
        s0 = (k0 * 64 + (t & 1) * 32) * 3072 + gq * 128; s1 = (k1 * 64 + (t & 1) * 32) * 3072 + gq * 128;
      }
      PINS(d0); PINS(d1); PINS(s0); PINS(s1);
      WAITV(8);
      BAR();
      if (c < 6) dma4(d0, vx, xr, s0); else dma4(d0, vw, wr, s0);
#pragma unroll
      for (int s = 0; s < 4; ++s) xq[4 * c + s] = lds_r128(xa[s] + (c % 3) * SLOT);
      dma4(d1, vw, wr, s1);
      if (c == 0) {
#pragma unroll
        for (int hd = 0; hd < 4; ++hd) fa[hd] = lds_tr2(fpa + hd * 512, fpa + hd * 512 + 256);
      }
      if (c == 1) {
        lds_w128(lds0 + L_V + (lane & 15) * VF + 1024, (u32x4){0x3F803F80u, 0x3F803F80u, 0x3F803F80u, 0x3F803F80u});
        const u32 sg = (lane & 4) ? 0x80008000u : 0u;
#pragma unroll
        for (int hd = 0; hd < 4; ++hd) { PIN(fa[hd]); fa[hd] ^= (u32x4){sg, sg, sg, sg}; PIN(fa[hd]); }
        wa = lds0 + L_WQ1 + (8 * h + q) * 128 + 16 * ((2 * blk + (p >> 1)) ^ ((((q >> 1) & 1) << 2) | (h << 1))) + 8 * (p & 1);
        wb = wa ^ 64;
        kra = lds0 + L_KV + (16 * h + q) * 48 + (blk ? 32 : 8 * p);
        o_off = WS_O + ((br * 64 + panel * 4 + t) * 8 + gq) * OREC + 256 + lane * 16;
        c_off = WS_O + ((br * 64 + panel * 4 + t) * 8 + gq) * OREC + lane * 4;
        woa = lds0 + L_WO + (8 * h + q) * 128 + 16 * ((2 * blk + (p & 1)) ^ (((q >> 1) & 1) << 2)) + 8 * (p >> 1);
        wob = woa ^ 64;
        vto = r * 128; sto = (idx * 16 + t * 4) * 1024;
        PIN(wa); PIN(wb); PIN(kra); PIN(o_off); PIN(c_off); PIN(woa); PIN(wob); PIN(vto); PINS(sto);
      }
    }
    TOUCH(tho, vto, wor, sto);
    STAMP(2);
    u32x4 fq[2][2][2];
    {
      f32x16 aq[2], pq[2];
      float mq[2];
      aq[0] = aq[1] = zero;
      u32x4 wf[4];
      auto ldw = [&](int n) {
        const int k = n & 31;
        const u32 o = (n < 32 ? wa : wb) + (k < 16 ? (L_WQ0 - L_WQ1) : 0) + (k & 15) * 2048;
        wf[n & 3] = lds_tr2(o, o + 512);
      };
      WAITV(1);
      BAR();
      SCHED();
      STAMP(12);
      ldw(0); ldw(1); ldw(2);
      SCHED();
      BARE();
      SCHED();
#pragma unroll
      for (int n = 0; n < 34; ++n) {
        ldw(n + 3);
        aq[n >> 5] = mfma(wf[n & 3], xq[n & 31], aq[n >> 5]);
        SGB(0x8, 1); SGB(0x100, 2);
        SCHED();
      }
#pragma unroll
      for (int hh = 0; hh < 2; ++hh) pq[hh] = mfma(fa[hh], pack8(aq[0], hh), zero);
      SCHED();
      STAMP(4);
#pragma unroll
      for (int n = 34; n < 64; ++n) {
        if (n + 3 < 64) ldw(n + 3);
        aq[1] = mfma(wf[n & 3], xq[n & 31], aq[1]);
      }
      mq[0] = negmax16(pq[0]); mq[1] = negmax16(pq[1]);
      f32x2 eq[2][8];
      sm_exp_s(pq[0], pq[1], mq, eq);
      SGB(0x8, 1); SGB(0x100, 2); SGB(0x8, 1); SGB(0x100, 2);
#pragma unroll
      for (int n = 36; n < 41; ++n) { SGB(0x8, 1); SGB(0x100, 2); SGB(0x402, 4); }
#pragma unroll
      for (int n = 41; n < 64; ++n) { SGB(0x8, 1); SGB(0x100, 2); if (n & 1) SGB(0x402, 3); else SGB(0x402, 2); }
#pragma unroll
      for (int i = 0; i < 16; ++i) PIN(eq[i >> 3][i & 7]);
      SCHED();
      STAMP(3);
#pragma unroll
      for (int hh = 0; hh < 2; ++hh) { pq[hh] = mfma(fa[2 + hh], pack8(aq[1], hh), zero); PIN(pq[hh]); }
      SCHED();
      sm_finish(eq, fq[0]);
      SCHED();
      asm volatile("s_waitcnt vmcnt(0)" : "+v"(tho)::"memory");
      mq[0] = negmax16(pq[0]); mq[1] = negmax16(pq[1]);
      PIN(mq[0]); PIN(mq[1]);
      SCHED();
#pragma unroll
      for (int i = 0; i < 4; ++i) PIN(fq[0][i >> 1][i & 1]);
      softmax16x2(pq[0], pq[1], mq, fq[1]);
#pragma unroll
      for (int i = 0; i < 4; ++i) PIN(fq[1][i >> 1][i & 1]);
    }
    STAMP(5);
    BAR();
    STAMP(6);
    u32x4 ob[2][4];
    {
      u32x4 a0[4], a1[4];
#pragma unroll
      for (int hd = 0; hd < 4; ++hd) {
        a0[hd] = lds_tr2(kra + hd * 1536, kra + hd * 1536 + 4 * 48);
        a1[hd] = lds_tr2(kra + hd * 1536 + 8 * 48, kra + hd * 1536 + 12 * 48);
      }
      SCHED();
      f32x16 y[4];
      u32 cs = 0;
      auto num = [&](int hd) {
        y[hd] = mfma(a0[hd], fq[hd >> 1][hd & 1][0], zero);
        y[hd] = mfma(a1[hd], fq[hd >> 1][hd & 1][1], y[hd]);
      };
      auto fin = [&](int hd) {
        const f32x16& yy = y[hd];
        const float rd = __builtin_amdgcn_rcpf(yy[8] + 1e-6f);
        u32 w[4];
#pragma unroll
        for (int e = 0; e < 4; ++e) { float t0 = yy[2 * e] * rd, t1 = yy[2 * e + 1] * rd; if (hd < 2) OPQ(t0); w[e] = pk2(t0, t1); }
        ob[0][hd] = (u32x4){w[0], w[1], w[2], w[3]};
        st128c(ob[0][hd], wsr, o_off + hd * 1024);
        cs = sum4(cs, ob[0][hd]);
      };
      num(0); num(1); PIN(y[0]); PIN(y[1]); SCHED();
      num(2); fin(0); SGB(0x8, 1); SGB(0x402, 6); SGB(0x8, 1); PIN(y[2]); SCHED();
      num(3); fin(1); SGB(0x8, 1); SGB(0x402, 6); SGB(0x8, 1); PIN(y[3]); SCHED();
      fin(2); fin(3);
      st32c(key - cs, wsr, c_off);
    }
    SCHED();
    STAMP(7);
    u32x4 wo[32][2];
    auto ldwo = [&](int n) {
      wo[n][0] = lds_tr2(woa + n * 2048, woa + n * 2048 + 512);
      wo[n][1] = lds_tr2(wob + n * 2048, wob + n * 2048 + 512);
    };
#pragma unroll
    for (int n = 0; n < 16; ++n) ldwo(n);
    f32x16 c0 = zero, c1 = zero;
#pragma unroll
    for (int s = 0; s < 4; ++s) { c0 = mfma(wo[s][0], ob[0][s], c0); c1 = mfma(wo[s][1], ob[0][s], c1); }
#pragma unroll
    for (int k = 4; k < 16; ++k) { PIN(wo[k][0]); PIN(wo[k][1]); }
    PIN(c0); PIN(c1);
    WAITV(0);
    BARE();
    SCHED();
#pragma unroll
    for (int n = 16; n < 24; ++n) ldwo(n);
#pragma unroll
    for (int k = 16; k < 24; ++k) { PIN(wo[k][0]); PIN(wo[k][1]); }
    const __amdgpu_buffer_rsrc_t outr = __builtin_amdgcn_make_buffer_rsrc(KPTR(kb, 0), 0, 2048 * 1024 * 2, SRD3);
    u32 oa = lds0 + L_O + t * OREC + 256 + lane * 16;
    u32 orow = ((panel * 128 + t * 32 + l3) * 1024 + br * 512 + cg * 64 + 8 * l7) * 2;
    PIN(oa); PIN(orow);
    u32 oc = oa - 256 - lane * 12, l4 = lane * 4, l16 = lane * 16, oa4 = oa + 4 * (4 * OREC), oc4 = oc + 4 * (4 * OREC);
    u32 sw1 = lds0 + t * 4096 + r * 128 + 16 * (h ^ (r & 7)), sr1 = lds0 + t * 4096 + l3 * 128 + 16 * (l7 ^ l3);
    u32 sw2 = sw1 ^ 32, sw3 = sw1 ^ 64, sw4 = sw1 ^ 96, or1 = orow + 16384, or2 = orow + 32768, or3 = orow + 49152;
    PIN(oc); PIN(l4); PIN(l16); PIN(sw1); PIN(sr1); PIN(oa4); PIN(oc4); PIN(sw2); PIN(sw3); PIN(sw4); PIN(or1); PIN(or2); PIN(or3);
    STAMP(8);
    u32 nretry = 0;
    u32 chk[2];
    auto oread = [&](int j) {
      const u32 da = j % 5 == 4 ? oa4 : oa + (j % 5) * (4 * OREC), dc = j % 5 == 4 ? oc4 : oc + (j % 5) * (4 * OREC);
#pragma unroll
      for (int s = 0; s < 4; ++s) ob[j & 1][s] = lds_r128(da + s * 1024);
      chk[j & 1] = *(LAS u32*)(size_t)dc;
    };
    auto obad = [&](int j) -> bool {
      const u32 c = sum4(sum4(chk[j & 1], ob[j & 1][0]), ob[j & 1][1]) + sum4(sum4(0u, ob[j & 1][2]), ob[j & 1][3]);
      return __builtin_amdgcn_ballot_w64(c != key) != 0;
    };
#pragma unroll
    for (int j = 1; j < 8; ++j) {
      BARE();
      SCHED();
      oread(j);
      if (j >= 2) {
#pragma unroll
        for (int s = 0; s < 4; ++s) {
          const int k = 4 * (j - 1) + s;
          c0 = mfma(wo[k][0], ob[(j - 1) & 1][s], c0);
          c1 = mfma(wo[k][1], ob[(j - 1) & 1][s], c1);
        }
        if (j >= 3 && j <= 6) { ldwo(24 + 2 * (j - 3)); ldwo(25 + 2 * (j - 3)); }
      }
      const bool bad = obad(j);
      if (j >= 3 && j <= 6) {
        SGB(0x8, 1); SGB(0x100, 3); SGB(0x8, 1); SGB(0x100, 2); SGB(0x8, 1); SGB(0x100, 3); SGB(0x8, 1); SGB(0x100, 3);
        SGB(0x8, 1); SGB(0x2, 4); SGB(0x8, 1); SGB(0x2, 5); SGB(0x8, 1); SGB(0x100, 2); SGB(0x8, 1);
      } else if (j >= 2) {
        SGB(0x8, 1); SGB(0x100, 3); SGB(0x8, 1); SGB(0x100, 2); SGB(0x8, 3); SGB(0x2, 4); SGB(0x8, 1); SGB(0x2, 5); SGB(0x8, 2);
      }
      SCHED();
      if (__builtin_expect(bad, 0)) {
#pragma unroll 1
        for (u32 spin = 0; spin < SPIN_CAP; ++spin) {
          dma_orec(lds0 + L_O + t * OREC + (j % 5) * (4 * OREC), l4, l16, wsr, WS_O + ((br * 64 + panel * 4 + t) * 8 + ochunk(j)) * OREC);
          u32 jj = j + 1, jend = j + AHEAD < 7 ? j + AHEAD : 7;
          PINS(jj); PINS(jend);
#pragma unroll 1
          for (; jj <= jend; ++jj) {
            const u32 sl = (jj >= 5 ? jj - 5 : jj) * (4 * OREC);
            u32 c = *(LAS u32*)(size_t)(oc + sl);
#pragma unroll
            for (int s = 0; s < 4; ++s) c = sum4(c, lds_r128(oa + sl + s * 1024));
            if (__builtin_amdgcn_ballot_w64(c != key) != 0)
              dma_orec(lds0 + L_O + t * OREC + sl, l4, l16, wsr, WS_O + ((br * 64 + panel * 4 + t) * 8 + ochunk(jj)) * OREC);
          }
          WAITV(0);
          oread(j);
          ++nretry;
          if (!obad(j)) break;
        }
      }
    }
#pragma unroll
    for (int s = 0; s < 4; ++s) c0 = mfma(wo[28 + s][0], ob[1][s], c0);
    PIN(c0);
    SCHED();
#pragma unroll
    for (int s = 0; s < 4; ++s) c1 = mfma(wo[28 + s][1], ob[1][s], c1);
    lds_w128(sw1, pack8(c0, 0));
    lds_w128(sw2, pack8(c0, 1));
    SGB(0x8, 2); SGB(0x2, 4); SGB(0x200, 1); SGB(0x8, 1); SGB(0x2, 4); SGB(0x200, 1); SGB(0x8, 1);
    SCHED();
    STAMP(9);
    lds_w128(sw3, pack8(c1, 0));
    lds_w128(sw4, pack8(c1, 1));
    asm volatile("" ::: "memory");
    {
      u32x4 v[4];
#pragma unroll
      for (int i = 0; i < 4; ++i) v[i] = lds_r128(sr1 + i * 1024);
      SCHED();
      st128c(v[0], outr, orow);
      st128c(v[1], outr, or1);
      st128c(v[2], outr, or2);
      st128c(v[3], outr, or3);
    }
    if (t == 0 && panel == 0 && grp == 0 && lane == 0) st32c(key, wsr, WS_EPOCH + br * 256);
    if (PROBE_N == 13) WAITV(0);
    STAMP(13);
    STAMP_OUT();
  }
}
}

extern "C" void kernel_launch(void* const* d_in, const int* in_sizes, int n_in,
                              void* d_out, int out_size, void* d_ws, size_t ws_size,
                              hipStream_t stream) {
  hh::hedgehog<<<256, 512, 0, stream>>>((const u16*)d_in[0], (const u16*)d_in[1], (const u16*)d_in[2], (const u16*)d_in[3],
                                        (const u16*)d_in[4], (const u16*)d_in[5], (const u16*)d_in[6], (const u16*)d_in[7],
                                        (u16*)d_out, (u32*)d_ws);
}
```

```cpp
#define BARA 0
#define PROBE_N 9
#define PROBE_ROLE 0
#include <hip/hip_runtime.h>
#include <stdint.h>

typedef unsigned short u16;
typedef unsigned u32;
typedef short bf16x8 __attribute__((ext_vector_type(8)));
typedef short s16x4 __attribute__((ext_vector_type(4)));
typedef float f32x16 __attribute__((ext_vector_type(16)));
typedef float f32x4 __attribute__((ext_vector_type(4)));
typedef float f32x2 __attribute__((ext_vector_type(2)));
typedef unsigned u32x8 __attribute__((ext_vector_type(8)));
typedef unsigned u32x4 __attribute__((ext_vector_type(4)));
typedef unsigned u32x2 __attribute__((ext_vector_type(2)));
typedef __bf16 bf16x2 __attribute__((ext_vector_type(2)));
#define DI __device__ __forceinline__
#define LAS __attribute__((address_space(3)))
#define CAS __attribute__((address_space(4)))
#define BC(T, x) __builtin_bit_cast(T, x)

#define DELAY1 3
#define DELAY3 4
#define CODE_PF 29184
#define AHEAD 1
#define GQ 1
#define GC 2
#define GBURST 1
#define WARM 0
#define RSLEEP 0
#define GORD 0
#define B0 0
#define OFIRST 5

namespace hh {
constexpr int AUX_SC1 = 16;
constexpr u32 SRD3 = 0x00020000u;
constexpr u32 WS_EPOCH = 0;
constexpr u32 REC = 1280;
constexpr u32 WS_PART = 4096;
constexpr u32 OREC = 4352;
constexpr u32 WS_O = WS_PART + 2 * 32 * 16 * REC;
constexpr u32 WS_END = WS_O + 2 * 64 * 8 * OREC;
constexpr u32 WS_STAMP = 8u << 20;
constexpr u32 SLOT = 32768;
constexpr u32 L_WQ0 = 98304;
constexpr u32 L_WQ1 = 65536;
constexpr u32 L_WPHI = 163840 - 4096;
constexpr u32 L_PHI = 0;
constexpr u32 VF = 1040;
constexpr u32 L_V = 131072;
constexpr u32 L_KV = L_V + 16 * VF;
constexpr u32 L_WO = 0;
constexpr u32 L_WA = BARA ? 32768 : 0, L_WB = BARA ? 0 : 32768;
constexpr u32 L_O = 65536;
constexpr u32 SPIN_CAP = 1u << 16;

DI u32 pk2(float a, float b) { return BC(u32, __builtin_convertvector((f32x2){a, b}, bf16x2)); }
DI u32x4 pack8(const f32x16& x, int s) {
  return (u32x4){pk2(x[8 * s], x[8 * s + 1]), pk2(x[8 * s + 2], x[8 * s + 3]), pk2(x[8 * s + 4], x[8 * s + 5]), pk2(x[8 * s + 6], x[8 * s + 7])};
}
DI f32x16 mfma(u32x4 a, u32x4 b, f32x16 c) { return __builtin_amdgcn_mfma_f32_32x32x16_bf16(BC(bf16x8, a), BC(bf16x8, b), c, 0, 0, 0); }
DI f32x4 mfma16(u32x4 a, u32x4 b, f32x4 c) { return __builtin_amdgcn_mfma_f32_16x16x32_bf16(BC(bf16x8, a), BC(bf16x8, b), c, 0, 0, 0); }
DI u32x4 lds_r128(u32 a) { return *(LAS u32x4*)(size_t)a; }
DI void lds_w128(u32 a, u32x4 v) { *(LAS u32x4*)(size_t)a = v; }
DI u32x4 lds_tr2(u32 a0, u32 a1) {
  s16x4 lo = __builtin_amdgcn_ds_read_tr16_b64_v4i16((LAS s16x4*)(size_t)a0);
  s16x4 hi = __builtin_amdgcn_ds_read_tr16_b64_v4i16((LAS s16x4*)(size_t)a1);
  return BC(u32x4, __builtin_shufflevector(lo, hi, 0, 1, 2, 3, 4, 5, 6, 7));
}
DI void dma1(u32 lds, u32 voff, __amdgpu_buffer_rsrc_t srd, u32 soff) {
  asm volatile("s_mov_b32 m0, %0\n\ts_nop 0\n\tbuffer_load_dwordx4 %1, %2, %3 offen lds" ::"s"(lds), "v"(voff), "s"(srd), "s"(soff) : "memory");
}
DI void dma4(u32 lds, const u32 (&v)[4], __amdgpu_buffer_rsrc_t srd, u32 soff) {
  asm volatile("s_mov_b32 m0, %0\n\ts_nop 0\n\t"
               "buffer_load_dwordx4 %1, %5, %6 offen lds\n\t"
               "buffer_load_dwordx4 %2, %5, %6 offen offset:1024 lds\n\t"
               "buffer_load_dwordx4 %3, %5, %6 offen offset:2048 lds\n\t"
               "buffer_load_dwordx4 %4, %5, %6 offen offset:3072 lds" ::"s"(lds), "v"(v[0]), "v"(v[1]), "v"(v[2]), "v"(v[3]), "s"(srd), "s"(soff) : "memory");
}
DI void dma_orec(u32 lds, u32 l4, u32 l16, __amdgpu_buffer_rsrc_t srd, u32 soff) {
  asm volatile("s_mov_b32 m0, %0\n\ts_nop 0\n\t"
               "buffer_load_dwordx4 %2, %3, %4 offen offset:256 sc1 lds\n\t"
               "buffer_load_dwordx4 %2, %3, %4 offen offset:1280 sc1 lds\n\t"
               "buffer_load_dwordx4 %2, %3, %4 offen offset:2304 sc1 lds\n\t"
               "buffer_load_dwordx4 %2, %3, %4 offen offset:3328 sc1 lds\n\t"
               "buffer_load_dword %1, %3, %4 offen sc1 lds" ::"s"(lds), "v"(l4), "v"(l16), "s"(srd), "s"(soff) : "memory");
}
#define TOUCH(d, voff, srd, soff) asm volatile("buffer_load_dword %0, %1, %2, %3 offen" : "=v"(d) : "v"(voff), "s"(srd), "s"(soff) : "memory")
DI void st128c(u32x4 v, __amdgpu_buffer_rsrc_t srd, u32 off) { __builtin_amdgcn_raw_buffer_store_b128(v, srd, off, 0, AUX_SC1); }
DI void st32c(u32 v, __amdgpu_buffer_rsrc_t srd, u32 off) { __builtin_amdgcn_raw_buffer_store_b32(v, srd, off, 0, AUX_SC1); }
#define BAR() asm volatile("s_waitcnt lgkmcnt(0)\n\ts_barrier" ::: "memory")
#define BARE() asm volatile("s_barrier" ::: "memory")
#define OPQ(x) asm("" : "+v"(x))
#define PIN(x) asm volatile("" : "+v"(x))
#define PINS(x) asm volatile("" : "+s"(x))
#define WAITV(n) asm volatile("s_waitcnt vmcnt(" #n ")" ::: "memory")
#define SCHED() __builtin_amdgcn_sched_barrier(0)
#define SGB(m, n) __builtin_amdgcn_sched_group_barrier(m, n, 0)
DI u32 sum4(u32 c, u32x4 v) { return (c + v.x + v.y) + v.z + v.w; }
DI u32 pair_sum(u32 s) { auto t = __builtin_amdgcn_permlane32_swap(s, s, false, false); return t[0] + t[1]; }

constexpr float LOG2E = 1.44269504088896340736f;
DI float max3f(float a, float b, float c) { return __builtin_fmaxf(__builtin_fmaxf(a, b), c); }
DI float negmax16(const f32x16& x) {
  const float a = max3f(x[0], x[1], x[2]), b = max3f(x[3], x[4], x[5]), c = max3f(x[6], x[7], x[8]);
  const float d = max3f(x[9], x[10], x[11]), e = max3f(x[12], x[13], x[14]);
  return -max3f(max3f(a, b, x[15]), max3f(c, d, e), a) * LOG2E;
}
DI void sm_exp(const f32x16& p, const f32x16& q, const float (&mc)[2], f32x2 (&e)[2][8]) {
#pragma unroll
  for (int i = 0; i < 8; ++i) {
    const f32x2 t = __builtin_elementwise_fma((f32x2){p[2 * i], p[2 * i + 1]}, (f32x2){LOG2E, LOG2E}, (f32x2){mc[0], mc[0]});
    const f32x2 u = __builtin_elementwise_fma((f32x2){q[2 * i], q[2 * i + 1]}, (f32x2){LOG2E, LOG2E}, (f32x2){mc[1], mc[1]});
    e[0][i] = (f32x2){__builtin_amdgcn_exp2f(t.x), __builtin_amdgcn_exp2f(t.y)};
    e[1][i] = (f32x2){__builtin_amdgcn_exp2f(u.x), __builtin_amdgcn_exp2f(u.y)};
  }
  SGB(0x2, 4); SGB(0x400, 8); SGB(0x2, 4); SGB(0x400, 8); SGB(0x2, 6); SGB(0x400, 8); SGB(0x2, 8); SGB(0x400, 8);
}
DI void sm_exp_s(const f32x16& p, const f32x16& q, const float (&mc)[2], f32x2 (&e)[2][8]) {
#pragma unroll
  for (int i = 0; i < 16; ++i) {
    e[0][i >> 1][i & 1] = __builtin_amdgcn_exp2f(__builtin_fmaf(p[i], LOG2E, mc[0]));
    e[1][i >> 1][i & 1] = __builtin_amdgcn_exp2f(__builtin_fmaf(q[i], LOG2E, mc[1]));
  }
}
DI void sm_finish(const f32x2 (&e)[2][8], u32x4 (&o)[2][2]) {
  float r[2];
#pragma unroll
  for (int u = 0; u < 2; ++u) {
    const f32x2 s = ((e[u][0] + e[u][1]) + (e[u][2] + e[u][3])) + ((e[u][4] + e[u][5]) + (e[u][6] + e[u][7]));
    r[u] = __builtin_amdgcn_rcpf(s.x + s.y);
  }
#pragma unroll
  for (int u = 0; u < 2; ++u) {
    u32 w[8];
#pragma unroll
    for (int i = 0; i < 8; ++i) { const f32x2 t = e[u][i] * (f32x2){r[u], r[u]}; w[i] = pk2(t.x, t.y); }
    o[u][0] = (u32x4){w[0], w[1], w[2], w[3]};
    o[u][1] = (u32x4){w[4], w[5], w[6], w[7]};
  }
}
DI void softmax16x2(const f32x16& p, const f32x16& q, const float (&mc)[2], u32x4 (&o)[2][2]) {
  f32x2 e[2][8];
  sm_exp(p, q, mc, e);
  sm_finish(e, o);
}

#define STAMP(i) do { if ((i) == PROBE_N) { SCHED(); asm volatile("s_memrealtime %0\n\ts_waitcnt lgkmcnt(0)" : "=s"(pt1) :: "memory"); SCHED(); } } while (0)
#define STAMP_OUT() do { if (PROBE_N && (wave >> 2) == PROBE_ROLE) { unsigned long long d_ = pt1 - pt0; if (d_ > 100000) d_ = 100000; unsigned long long e_ = pt0 + 3000 + 8 * d_; \
  for (int it_ = 0; it_ < 1000000; ++it_) { unsigned long long t_; asm volatile("s_memrealtime %0\n\ts_waitcnt lgkmcnt(0)" : "=s"(t_) :: "memory"); if (t_ >= e_) break; } } } while (0)

__global__ __launch_bounds__(512, 2) void hedgehog(const u16* xA, const u16* wA, const u16* pA, const u16* oA,
                                                   const u16* xB, const u16* wB, const u16* pB, const u16* oB,
                                                   u16* out_, u32* ws_) {
  __shared__ __attribute__((aligned(16))) char smem[163840];
  const u32 lds0 = (u32)(size_t)(LAS char*)smem;
  unsigned long long pt0 = __builtin_amdgcn_s_memrealtime(), pt1 = pt0;
  const int bid = blockIdx.x, xcd = bid & 7, idx = bid >> 3;
  const int br = xcd >> 2;
#define KLOAD(tail)                                                                                       \
  u32x8 ka;                                                                                               \
  u32x4 kb;                                                                                               \
  asm volatile("s_load_dwordx8 %0, %2, %3\n\ts_load_dwordx4 %1, %2, 0x40" tail : "=&s"(ka), "=&s"(kb)     \
               : "s"(__builtin_amdgcn_kernarg_segment_ptr()), "s"(br * 32))
#define KWAIT() asm volatile("s_waitcnt lgkmcnt(0)" : "+s"(ka), "+s"(kb))
#define KPTR(v, i) ((void*)(((unsigned long long)(v)[(i) + 1] << 32) | (v)[i]))
  unsigned long long pcs;
  asm volatile("s_getpc_b64 %0" : "=s"(pcs));
  pcs &= ~255ull;
  const int lane = threadIdx.x & 63;
  const int wave = __builtin_amdgcn_readfirstlane(threadIdx.x >> 6);
  const int panel = (xcd & 3) * 4 + (idx & 3), grp = idx >> 2, gq = (grp + GQ) & 7, cg = (grp + GC) & 7;
  auto ochunk = [&](u32 j) -> u32 {
    const int v = ((int)j + (((int)j - 1 - ((gq - OFIRST) & 7)) >> 31) + OFIRST) & 7, z = ((int)j - 1) >> 31;
    return (u32)__builtin_amdgcn_readfirstlane((v & ~z) | (gq & z));
  };
  f32x16 zero;
#pragma unroll
  for (int i = 0; i < 16; ++i) zero[i] = 0.f;
#define LANE_IDS()                                                                            \
  u32 lane_ = lane;                                                                           \
  PIN(lane_);                                                                                 \
  const u32 r = lane_ & 31, h = lane_ >> 5;                                                   \
  const u32 blk = (lane_ >> 4) & 1, q = (lane_ & 15) >> 2, p = lane_ & 3;                     \
  u32* ws = (u32*)KPTR(kb, 2);                                                                \
  const __amdgpu_buffer_rsrc_t wsr = __builtin_amdgcn_make_buffer_rsrc(ws, 0, WS_END, SRD3);  \
  const __amdgpu_buffer_rsrc_t wor = __builtin_amdgcn_make_buffer_rsrc(KPTR(ka, 6), 0, 512 * 512 * 2, SRD3)

  if (wave >= 4) {
    const int cw = wave - 4;
    const int rh = cw >> 1, hp = cw & 1;
    KLOAD("\n\ts_waitcnt lgkmcnt(0)");
    u32 sp0, sp1, sp2, sp3;
    {
      u32 off = idx * 1024 + cw * 256;
      off = off < CODE_PF ? off : 0;
      asm volatile("s_load_dword %0, %4, %5 offset:0\n\ts_load_dword %1, %4, %5 offset:64\n\ts_load_dword %2, %4, %5 offset:128\n\ts_load_dword %3, %4, %5 offset:192"
                   : "=&s"(sp0), "=&s"(sp1), "=&s"(sp2), "=&s"(sp3) : "s"(pcs), "s"(off));
    }
    LANE_IDS();
    u32 key = *(const CAS u32*)((const char*)ws + WS_EPOCH + br * 256) + 1u;
    u32 xa[2], xb[2], wa[2], wb2[2];
    {
      const u32 r16 = lane_ & 15, g = lane_ >> 4;
#pragma unroll
      for (int s = 0; s < 2; ++s) {
        xa[s] = lds0 + (64 * rh + r16) * 128 + 16 * ((4 * s + g) ^ (r16 >> 1));
        wa[s] = lds0 + 16384 + (8 * g + q) * 128 + 16 * ((4 * hp + 2 * s + (p >> 1)) ^ ((((q >> 1) & 1) << 2) | (blk << 1))) + 8 * (p & 1);
        xb[s] = xa[s] + 2 * SLOT; wb2[s] = wa[s] + 2 * SLOT;
        PIN(xa[s]); PIN(xb[s]); PIN(wa[s]); PIN(wb2[s]);
      }
    }
    u32 fpa = lds0 + L_WPHI + hp * 1024 + (8 * h + q) * 32 + (8 * blk + 4 * (p >> 1)) * 2;
    PIN(fpa);
    if (!WARM) PINS(key);
    f32x4 ak[4][2], av[4][2];
#pragma unroll
    for (int i = 0; i < 8; ++i) ak[i >> 1][i & 1] = av[i >> 1][i & 1] = (f32x4){0.f, 0.f, 0.f, 0.f};
    u32 vra, pra, vwa, pw0, pw1, pub16, pub4;
    u32x4 xf[2][4], wk[2][2], wv[2][2];
    auto ldx = [&](int c, int s) {
      const u32 x = c % 3 == 2 ? xb[s] : xa[s] + (c % 3) * SLOT;
#pragma unroll
      for (int i = 0; i < 4; ++i) xf[s][i] = lds_r128(x + i * 2048);
    };
    auto ldw = [&](int c, int s, int m) {
#pragma unroll
      for (int n = 0; n < 2; ++n) {
        const u32 w = (c % 3 == 2 ? wb2[n] : wa[n] + (c % 3) * SLOT) + s * 4096 + m * 8192;
        (m ? wv : wk)[s][n] = lds_tr2(w, w + 512);
      }
    };
    auto mk = [&](int s) {
#pragma unroll
      for (int i = 0; i < 8; ++i) ak[i >> 1][i & 1] = mfma16(wk[s][i & 1], xf[s][i >> 1], ak[i >> 1][i & 1]);
    };
    auto mv = [&](int s) {
#pragma unroll
      for (int i = 0; i < 8; ++i) av[i >> 1][i & 1] = mfma16(xf[s][i >> 1], wv[s][i & 1], av[i >> 1][i & 1]);
    };
    auto mix = [&](int n) {
#pragma unroll
      for (int i = 0; i < 8; ++i) {
        SGB(0x8, 1);
        if (n == 12) { if (i < 4) SGB(0x100, 2); else SGB(0x100, 1); }
        else if (n == 8) SGB(0x100, 1);
        else if (n == 4) { if (i < 4) SGB(0x100, 1); }
        else if (n < 0) SGB(0x6, 2);
      }
      SCHED();
    };
    if (WARM) {
      u32 hsh = lane * 2654435761u + 12345u;
      u32x4 a = {(hsh & 0x3FFF3FFFu) | 0x30003000u, ((hsh * 7u) & 0xBFFFBFFFu) | 0x30003000u, ((hsh * 13u) & 0x3FFFBFFFu) | 0x30003000u, ((hsh * 29u) & 0xBFFF3FFFu) | 0x30003000u};
      u32x4 b = {a.y, a.w, a.x, a.z};
      f32x16 w0 = zero, w1 = zero;
#pragma unroll 1
      for (int i = 0; i < WARM; ++i) { w0 = mfma(a, b, w0); w1 = mfma(b, a, w1); w0 = mfma(b, b, w0); w1 = mfma(a, a, w1); PIN(w0); PIN(w1); }
    }
    BAR();
    asm volatile("" ::"s"(sp0), "s"(sp1), "s"(sp2), "s"(sp3));
    if (WARM) PINS(key);
    STAMP(1);
    ldw(0, 0, 0); ldx(0, 0); ldw(0, 0, 1);
    u32x4 fa[2];
#pragma unroll
    for (int hh = 0; hh < 2; ++hh) fa[hh] = lds_tr2(fpa + hh * 512, fpa + hh * 512 + 128);
    SCHED();
#pragma unroll
    for (int c = 0; c < 7; ++c) {
      ldw(c, 1, 0); ldx(c, 1); mk(0); mix(8);
      ldw(c, 1, 1); mv(0); mix(4);
      u32 ln = lane;
      PIN(ln);
      const u32 r_ = ln & 31, h_ = ln >> 5, q_ = (ln & 15) >> 2;
      if (c == 1) {
        const u32 sg = (ln & 4) ? 0x80008000u : 0u;
#pragma unroll
        for (int hh = 0; hh < 2; ++hh) { OPQ(fa[hh]); fa[hh] ^= (u32x4){sg, sg, sg, sg}; }
      }
      if (c == 2) vra = lds0 + L_V + (cw >> 1) * (2 * VF) + (r_ < 16 ? (ln + 16 * (cw & 1)) * 16 : 1024);
      if (c == 3) pra = lds0 + L_PHI + cw * 8192 + (4 * h_ + q_) * 64 + 16 * ((((ln >> 3) & 2) + ((ln >> 1) & 1)) ^ ((2 * h_ + (q_ >> 1)) & 3)) + 8 * (ln & 1);
      if (c == 4) {
        const u32 rec = WS_PART + ((br * 32 + grp * 4 + cw) * 16 + panel) * REC;
        pub16 = rec + ln * 16; pub4 = rec + 1024 + ln * 4;
      }
      if (c == 5) { pw0 = lds0 + L_PHI + (2 * hp) * 8192 + (64 * rh + r_) * 64 + 16 * ((2 * h_) ^ ((r_ >> 1) & 3)); pw1 = pw0 ^ 16; }
      if (c == 6) vwa = lds0 + L_V + (8 * rh + 2 * hp) * VF + ((ln & 16) * 2 + (ln & 15)) * 16 + h_ * 8;
      mk(1); mix(c >= 1 ? -1 : 0);
      if (c == 1) { PIN(fa[0]); PIN(fa[1]); }
      if (c == 2) PIN(vra);
      if (c == 3) PIN(pra);
      if (c == 4) { PIN(pub16); PIN(pub4); }
      if (c == 5) { PIN(pw0); PIN(pw1); }
      if (c == 6) PIN(vwa);
      BAR();
      ldw(c + 1, 0, 0); ldx(c + 1, 0); ldw(c + 1, 0, 1);
      mv(1); mix(12);
    }
    ldw(7, 1, 0); ldx(7, 1); ldw(7, 1, 1); mk(0); mix(12);
    mk(1); mix(0);
    __builtin_amdgcn_s_setprio(2);
    STAMP(2);
    f32x16 pk[2][2];
    {
      u32x4 kf[2][2];
      auto kpack = [&](int i) {
#pragma unroll
        for (int hh = 0; hh < 2; ++hh) {
          const f32x4 &t0 = ak[2 * i][hh], &t1 = ak[2 * i + 1][hh];
          const auto lo = __builtin_amdgcn_permlane16_swap(pk2(t0[0], t0[1]), pk2(t1[0], t1[1]), false, false);
          const auto hi = __builtin_amdgcn_permlane16_swap(pk2(t0[2], t0[3]), pk2(t1[2], t1[3]), false, false);
          kf[i][hh] = (u32x4){lo[0], hi[0], lo[1], hi[1]};
        }
      };
      mv(0); kpack(0);
      SGB(0x8, 2);
#pragma unroll
      for (int i = 0; i < 6; ++i) { SGB(0x2, 2); SGB(0x8, 1); }
      SCHED();
      pk[0][0] = mfma(fa[0], kf[0][0], zero);
      pk[0][1] = mfma(fa[1], kf[0][1], zero);
      kpack(1);
      SGB(0x8, 1); SGB(0x2, 6); SGB(0x8, 1); SGB(0x2, 6);
      PIN(pk[0][0]); PIN(pk[0][1]); PIN(kf[1][0]); PIN(kf[1][1]);
      SCHED();
      mv(1);
      pk[1][0] = mfma(fa[0], kf[1][0], zero);
      pk[1][1] = mfma(fa[1], kf[1][1], zero);
    }
    float m0[2], m1[2];
    m0[0] = negmax16(pk[0][0]); m0[1] = negmax16(pk[0][1]);
    SGB(0x8, 2);
#pragma unroll
    for (int i = 0; i < 6; ++i) { SGB(0x2, 2); SGB(0x8, 1); }
    SGB(0x8, 1); SGB(0x2, 6); SGB(0x8, 1);
#pragma unroll
    for (int i = 0; i < 8; ++i) PIN(av[i >> 1][i & 1]);
    PIN(pk[1][0]); PIN(pk[1][1]); PIN(m0[0]); PIN(m0[1]);
    SCHED();
#pragma unroll
    for (int i = 0; i < 8; ++i) {
      const f32x4& t = av[i >> 1][i & 1];
      *(LAS u32x2*)(size_t)(vwa + (4 * (i >> 2) + ((i >> 1) & 1)) * VF + (i & 1) * 256) = (u32x2){pk2(t[0], t[1]), pk2(t[2], t[3])};
    }
    SCHED();
    if (BARA == 2) { BARE(); SCHED(); }
    {
      u32x4 o[2][2];
      softmax16x2(pk[0][0], pk[0][1], m0, o);
      lds_w128(pw0, o[0][0]); lds_w128(pw1, o[0][1]);
      lds_w128(pw0 + 8192, o[1][0]); lds_w128(pw1 + 8192, o[1][1]);
      SCHED();
      m1[0] = negmax16(pk[1][0]); m1[1] = negmax16(pk[1][1]);
      PIN(m1[0]); PIN(m1[1]);
      SCHED();
      if (BARA == 1) { BARE(); SCHED(); }
      softmax16x2(pk[1][0], pk[1][1], m1, o);
      lds_w128(pw0 + 2048, o[0][0]); lds_w128(pw1 + 2048, o[0][1]);
      lds_w128(pw0 + 2048 + 8192, o[1][0]); lds_w128(pw1 + 2048 + 8192, o[1][1]);
      SGB(0x402, 26); SGB(0x200, 1); SGB(0x402, 8); SGB(0x200, 1); SGB(0x402, 8); SGB(0x200, 1); SGB(0x402, 8); SGB(0x200, 1);
      SCHED();
      BARE();
      BAR();
      SCHED();
    }
    STAMP(3);
    f32x16 kv = zero;
    {
      u32x4 a[8], b[8];
#pragma unroll
      for (int u = 0; u < 8; ++u) {
        a[u] = lds_r128(vra + ((u >> 1) * 4 + (u & 1)) * VF);
        b[u] = lds_tr2(pra + 16 * u * 64, pra + (16 * u + 8) * 64);
      }
#pragma unroll
      for (int u = 0; u < 8; ++u) kv = mfma(a[u], b[u], kv);
      SGB(0x100, 12); SGB(0x8, 1); SGB(0x100, 6); SGB(0x8, 1); SGB(0x100, 6); SGB(0x8, 6);
    }
    {
      const u32x4 w = pack8(kv, 0);
      st128c(w, wsr, pub16);
      const u32 zb = __float_as_uint(kv[8]);
      const u32 chk = key - pair_sum(sum4(h ? 0u : zb, w));
      st32c(h ? chk : zb, wsr, pub4);
    }
    SCHED();
    STAMP(4);
    u32 l16, l4, kwa, kza, gso[4], vo[4], wso;
    u32x4 sel;
    {
      u32 ln = lane;
      PIN(ln);
#pragma unroll
      for (int i = 0; i < 4; ++i) vo[i] = (ln >> 3) * 1024 + 16 * ((ln & 7) ^ (((ln >> 4) & 1) << 2)) + i * 7168;
      if (!BARA) {
#pragma unroll
        for (int i = 0; i < 2; ++i) dma4(lds0 + L_WA + cw * 8192 + i * 4096, vo, wor, ochunk(cw) * 65536 + i * 32768 + cg * 128);
      } else { PIN(vo[0]); PIN(vo[1]); PIN(vo[2]); PIN(vo[3]); }
      wso = ochunk(4 + cw) * 65536 + cg * 128;
      const u32 r_ = ln & 31, h_ = ln >> 5;
      l16 = ln * 16; l4 = ln * 4;
      kwa = lds0 + L_KV + cw * 1536 + r_ * 48 + h_ * 16; kza = lds0 + L_KV + cw * 1536 + r_ * 48 + 32 + h_ * 8;
      const bool hit = r_ < 16 && ((r_ >> 2) & 1) == h_;
      const u32 j = (r_ & 3) + 4 * (r_ >> 3);
#pragma unroll
      for (int e = 0; e < 4; ++e) sel[e] = (hit && (j >> 1) == e) ? ((j & 1) ? 0x3F800000u : 0x00003F80u) : 0u;
#pragma unroll
      for (int i = 0; i < 4; ++i) {
        u32 k = i + (idx & 3); k = k >= 3 ? k - 3 : k; k = k >= 3 ? k - 3 : k;
        const u32 gsel = GORD == 0 ? (i + idx) & 3 : GORD == 1 ? (xcd + 1 + i) & 3 : (i == 3 ? xcd : xcd + 1 + k) & 3;
        gso[i] = WS_PART + ((br * 32 + gq * 4 + cw) * 16 + gsel * 4) * REC; PINS(gso[i]);
      }
      PIN(l16); PIN(l4); PIN(kwa); PIN(kza); PIN(sel); PINS(wso);
    }
    if (DELAY1) __builtin_amdgcn_s_sleep(DELAY1);
    STAMP(10);
    f32x16 gs[4];
    float z[4];
    {
      u32x4 g[16];
      u32 gz[16];
      auto gload = [&](int gi) {
#pragma unroll
        for (int i = 0; i < 4; ++i) {
          g[4 * gi + i] = __builtin_amdgcn_raw_buffer_load_b128(wsr, l16 + i * REC, gso[gi], AUX_SC1);
          gz[4 * gi + i] = __builtin_amdgcn_raw_buffer_load_b32(wsr, l4 + (1024 + i * REC - 1024 * (i == 3)), gso[gi] + 1024 * (i == 3), AUX_SC1);
          SCHED();
        }
      };
      auto gsum = [&](int gi) -> bool {
        u32 c = 0;
        if (GBURST) { PIN(gz[4 * gi + 3]); SCHED(); }
#pragma unroll
        for (int i = 0; i < 4; ++i) {
          c = sum4(c + gz[4 * gi + i], g[4 * gi + i]);
          gs[gi] = mfma(sel, g[4 * gi + i], i == 0 ? zero : gs[gi]);
          z[gi] = i == 0 ? __uint_as_float(gz[4 * gi]) : z[gi] + __uint_as_float(gz[4 * gi + i]);
        }
        const bool bad = __builtin_amdgcn_ballot_w64(pair_sum(c) != 4u * key) != 0;
        if (GBURST) { SGB(0x8, 1); SGB(0x2, 5); SGB(0x8, 1); SGB(0x2, 5); SGB(0x8, 1); SGB(0x2, 4); SGB(0x8, 1); }
        PIN(z[gi]);
        SCHED();
        return bad;
      };
      gload(0); gload(1); gload(2); gload(3);
      bool b0 = gsum(0), b1 = gsum(1), b2 = gsum(2), b3 = gsum(3);
      STAMP(11);
#pragma unroll 1
      for (u32 spin = 0; (b0 | b1 | b2 | b3) && spin < SPIN_CAP; ++spin) {
        asm volatile("" ::: "memory");
        if (RSLEEP) __builtin_amdgcn_s_sleep(RSLEEP);
        if (b0) gload(0);
        if (b1) gload(1);
        if (b2) gload(2);
        if (b3) gload(3);
        if (b0) b0 = gsum(0);
        if (b1) b1 = gsum(1);
        if (b2) b2 = gsum(2);
        if (b3) b3 = gsum(3);
      }
    }
    __builtin_amdgcn_s_waitcnt(0x0F70);
    STAMP(5);
    {
      float tt[8];
#pragma unroll
      for (int j = 0; j < 8; ++j) tt[j] = (gs[0][j] + gs[1][j]) + (gs[2][j] + gs[3][j]);
      const float zt = (z[0] + z[1]) + (z[2] + z[3]);
      lds_w128(kwa, (u32x4){pk2(tt[0], tt[1]), pk2(tt[2], tt[3]), pk2(tt[4], tt[5]), pk2(tt[6], tt[7])});
      const u32 zz = pk2(zt, zt);
      *(LAS u32x2*)(size_t)kza = (u32x2){zz, zz};
    }
#pragma unroll
    for (int i = 0; i < 2; ++i) dma4(lds0 + L_WB + cw * 8192 + i * 4096, vo, wor, wso + i * 32768);
    BAR();
    STAMP(6);
    {
      SCHED();
      u32 oso[8], ob = lds0 + L_O + cw * OREC, i0 = 0;
      PINS(i0);
#pragma unroll
      for (int i = 1; i < 8; ++i) { oso[i] = WS_O + ((br * 64 + panel * 4 + cw) * 8 + ochunk(i + i0)) * OREC; PINS(oso[i]); }
      PINS(ob);
      WAITV(0);
      BARE();
      if (DELAY3) __builtin_amdgcn_s_sleep(DELAY3);
      STAMP(8);
      auto oissue = [&](int j) { dma_orec(ob + (j % 5) * (4 * OREC), l4, l16, wsr, oso[j]); };
      oissue(1); oissue(2);
      WAITV(5); BARE(); oissue(3);
      WAITV(5); STAMP(15); BARE(); oissue(4); oissue(5);
      WAITV(10); BARE(); oissue(6);
      WAITV(10); BARE(); oissue(7);
      WAITV(10); BARE();
      WAITV(5); BARE();
      WAITV(0); BARE();
    }
    STAMP(9);
    STAMP_OUT();
  } else {
    KLOAD("");
    const int t = wave;
    const u32 l7 = lane & 7, l3 = lane >> 3, lq = lane >> 4;
    const u32 wsw = 16 * (l7 ^ ((lq & 1) << 2));
    u32 vx[4], vw[4];
#pragma unroll
    for (int i = 0; i < 4; ++i) {
      vx[i] = l3 * 1024 + 16 * (l7 ^ (((i & 1) << 2) | lq)) + i * 7168;
      vw[i] = l3 * 3072 + (wsw ^ ((i & 1) << 5)) + i * (8 * 3072 - 1024);
    }
    auto chunk = [&](u32 c) -> u32 {
      const u32 g = grp + idx;
      return c - 2 < 4 ? 2 + ((c - 2) ^ (g & 3)) : c ^ (g & 1);
    };
    const u32 xs0 = (panel * 128 + t * 32) * 1024, ws0 = (t & 1) * (32 * 3072) + (1 + (t >> 1)) * 1024 + grp * 128;
    const u32 ldst = lds0 + t * 4096;
    u32 e0 = ldst, e1 = ldst + 16384, e2 = xs0 + chunk(0) * 128, e3 = ws0 + chunk(0) * (64 * 3072);
    PINS(e0); PINS(e1); PINS(e2); PINS(e3);
#pragma unroll
    for (int i = 0; i < 4; ++i) { PIN(vx[i]); PIN(vw[i]); }
    KWAIT();
    const __amdgpu_buffer_rsrc_t xr = __builtin_amdgcn_make_buffer_rsrc(KPTR(ka, 0), 0, 2048 * 512 * 2, SRD3);
    const __amdgpu_buffer_rsrc_t wr = __builtin_amdgcn_make_buffer_rsrc(KPTR(ka, 2), 0, 512 * 1536 * 2, SRD3);
    STAMP(10);
    dma4(e0, vx, xr, e2);
    dma4(e1, vw, wr, e3);
    {
      const __amdgpu_buffer_rsrc_t pr = __builtin_amdgcn_make_buffer_rsrc(KPTR(ka, 4), 0, 32 * 256 * 2, SRD3);
      dma1(lds0 + L_WPHI + t * 1024, lane * 16, pr, (t < 2 ? grp : gq) * 2048 + (t & 1) * 1024);
    }
    STAMP(11);
    dma4(ldst + SLOT, vx, xr, xs0 + chunk(1) * 128);
    dma4(ldst + SLOT + 16384, vw, wr, ws0 + chunk(1) * (64 * 3072));
    STAMP(1);
    LANE_IDS();
    u32 key = *(const CAS u32*)((const char*)ws + WS_EPOCH + br * 256) + 1u;
    const u32 sw = (r >> 1) & 7;
    u32 xa[4];
#pragma unroll
    for (int s = 0; s < 4; ++s) { xa[s] = lds0 + (32 * t + r) * 128 + 16 * ((2 * s + h) ^ sw); PIN(xa[s]); }
    u32 fpa = lds0 + L_WPHI + 2048 + (4 * h + q) * 32 + (8 * blk + 4 * (p >> 1)) * 2;
    PIN(fpa);
    PINS(key);
    u32x4 xq[32], fa[4];
    u32 wa, wb, kra, o_off, c_off, woa, wob, vto, sto, tho, vo[4], wsa;
#pragma unroll
    for (int c = 0; c < 8; ++c) {
      u32 d0, d1, s0, s1;
      if (c < 6) {
        d0 = ldst + ((c + 2) % 3) * SLOT; d1 = d0 + 16384;
        s0 = xs0 + chunk(c + 2) * 128; s1 = ws0 + chunk(c + 2) * (64 * 3072);
      } else {
        const u32 k0 = 2 * ((2 * (c - 6) + idx) & 3) + (t >> 1), k1 = (k0 + 2) & 7;
        d0 = lds0 + L_WQ1 + ((chunk(k0) + 4) & 7) * 8192 + (t & 1) * 4096; d1 = lds0 + L_WQ1 + ((chunk(k1) + 4) & 7) * 8192 + (t & 1) * 4096;
        s0 = (k0 * 64 + (t & 1) * 32) * 3072 + gq * 128; s1 = (k1 * 64 + (t & 1) * 32) * 3072 + gq * 128;
      }
      PINS(d0); PINS(d1); PINS(s0); PINS(s1);
      WAITV(8);
      BAR();
      if (c < 6) dma4(d0, vx, xr, s0); else dma4(d0, vw, wr, s0);
#pragma unroll
      for (int s = 0; s < 4; ++s) xq[4 * c + s] = lds_r128(xa[s] + (c % 3) * SLOT);
      dma4(d1, vw, wr, s1);
      if (c == 0) {
#pragma unroll
        for (int hd = 0; hd < 4; ++hd) fa[hd] = lds_tr2(fpa + hd * 512, fpa + hd * 512 + 256);
      }
      if (c == 1) {
        lds_w128(lds0 + L_V + (lane & 15) * VF + 1024, (u32x4){0x3F803F80u, 0x3F803F80u, 0x3F803F80u, 0x3F803F80u});
        const u32 sg = (lane & 4) ? 0x80008000u : 0u;
#pragma unroll
        for (int hd = 0; hd < 4; ++hd) { PIN(fa[hd]); fa[hd] ^= (u32x4){sg, sg, sg, sg}; PIN(fa[hd]); }
        wa = lds0 + L_WQ1 + (8 * h + q) * 128 + 16 * ((2 * blk + (p >> 1)) ^ ((((q >> 1) & 1) << 2) | (h << 1))) + 8 * (p & 1);
        wb = wa ^ 64;
        kra = lds0 + L_KV + (16 * h + q) * 48 + (blk ? 32 : 8 * p);
        o_off = WS_O + ((br * 64 + panel * 4 + t) * 8 + gq) * OREC + 256 + lane * 16;
        c_off = WS_O + ((br * 64 + panel * 4 + t) * 8 + gq) * OREC + lane * 4;
        woa = lds0 + L_WO + (8 * h + q) * 128 + 16 * ((2 * blk + (p & 1)) ^ (((q >> 1) & 1) << 2)) + 8 * (p >> 1);
        wob = woa ^ 64;
        vto = r * 128; sto = (idx * 16 + t * 4) * 1024;
        if (BARA) {
#pragma unroll
          for (int i = 0; i < 4; ++i) { vo[i] = l3 * 1024 + wsw + i * 7168; PIN(vo[i]); }
          wsa = ochunk(t) * 65536 + cg * 128; PINS(wsa);
        }
        PIN(wa); PIN(wb); PIN(kra); PIN(o_off); PIN(c_off); PIN(woa); PIN(wob); PIN(vto); PINS(sto);
      }
    }
    TOUCH(tho, vto, wor, sto);
    STAMP(2);
    u32x4 fq[2][2][2];
    {
      f32x16 aq[2], pq[2];
      float mq[2];
      aq[0] = aq[1] = zero;
      u32x4 wf[4];
      auto ldw = [&](int n) {
        const int k = n & 31;
        const u32 o = (n < 32 ? wa : wb) + (k < 16 ? (L_WQ0 - L_WQ1) : 0) + (k & 15) * 2048;
        wf[n & 3] = lds_tr2(o, o + 512);
      };
      if (BARA) {
        BAR();
#pragma unroll
        for (int i = 0; i < 2; ++i) dma4(lds0 + L_WA + t * 8192 + i * 4096, vo, wor, wsa + i * 32768);
        WAITV(9);
      } else WAITV(1);
      BAR();
      SCHED();
      STAMP(12);
      ldw(0); ldw(1); ldw(2);
      SCHED();
#pragma unroll
      for (int n = 0; n < 34; ++n) {
        if (n == B0) { BARE(); SCHED(); }
        ldw(n + 3);
        aq[n >> 5] = mfma(wf[n & 3], xq[n & 31], aq[n >> 5]);
        SGB(0x8, 1); SGB(0x100, 2);
        SCHED();
      }
#pragma unroll
      for (int hh = 0; hh < 2; ++hh) pq[hh] = mfma(fa[hh], pack8(aq[0], hh), zero);
      SCHED();
      STAMP(4);
#pragma unroll
      for (int n = 34; n < 64; ++n) {
        if (n + 3 < 64) ldw(n + 3);
        aq[1] = mfma(wf[n & 3], xq[n & 31], aq[1]);
      }
      mq[0] = negmax16(pq[0]); mq[1] = negmax16(pq[1]);
      f32x2 eq[2][8];
      sm_exp_s(pq[0], pq[1], mq, eq);
      SGB(0x8, 1); SGB(0x100, 2); SGB(0x8, 1); SGB(0x100, 2);
#pragma unroll
      for (int n = 36; n < 41; ++n) { SGB(0x8, 1); SGB(0x100, 2); SGB(0x402, 4); }
#pragma unroll
      for (int n = 41; n < 64; ++n) { SGB(0x8, 1); SGB(0x100, 2); if (n & 1) SGB(0x402, 3); else SGB(0x402, 2); }
#pragma unroll
      for (int i = 0; i < 16; ++i) PIN(eq[i >> 3][i & 7]);
      SCHED();
      STAMP(3);
#pragma unroll
      for (int hh = 0; hh < 2; ++hh) { pq[hh] = mfma(fa[2 + hh], pack8(aq[1], hh), zero); PIN(pq[hh]); }
      SCHED();
      sm_finish(eq, fq[0]);
      SCHED();
      asm volatile("s_waitcnt vmcnt(0)" : "+v"(tho)::"memory");
      mq[0] = negmax16(pq[0]); mq[1] = negmax16(pq[1]);
      PIN(mq[0]); PIN(mq[1]);
      SCHED();
#pragma unroll
      for (int i = 0; i < 4; ++i) PIN(fq[0][i >> 1][i & 1]);
      softmax16x2(pq[0], pq[1], mq, fq[1]);
#pragma unroll
      for (int i = 0; i < 4; ++i) PIN(fq[1][i >> 1][i & 1]);
    }
    STAMP(5);
    BAR();
    STAMP(6);
    u32x4 ob[2][4];
    {
      u32x4 a0[4], a1[4];
#pragma unroll
      for (int hd = 0; hd < 4; ++hd) {
        a0[hd] = lds_tr2(kra + hd * 1536, kra + hd * 1536 + 4 * 48);
        a1[hd] = lds_tr2(kra + hd * 1536 + 8 * 48, kra + hd * 1536 + 12 * 48);
      }
      SCHED();
      f32x16 y[4];
      u32 cs = 0;
      auto num = [&](int hd) {
        y[hd] = mfma(a0[hd], fq[hd >> 1][hd & 1][0], zero);
        y[hd] = mfma(a1[hd], fq[hd >> 1][hd & 1][1], y[hd]);
      };
      auto fin = [&](int hd) {
        const f32x16& yy = y[hd];
        const float rd = __builtin_amdgcn_rcpf(yy[8] + 1e-6f);
        u32 w[4];
#pragma unroll
        for (int e = 0; e < 4; ++e) { float t0 = yy[2 * e] * rd, t1 = yy[2 * e + 1] * rd; if (hd < 2) OPQ(t0); w[e] = pk2(t0, t1); }
        ob[0][hd] = (u32x4){w[0], w[1], w[2], w[3]};
        st128c(ob[0][hd], wsr, o_off + hd * 1024);
        cs = sum4(cs, ob[0][hd]);
      };
      num(0); num(1); PIN(y[0]); PIN(y[1]); SCHED();
      num(2); fin(0); SGB(0x8, 1); SGB(0x402, 6); SGB(0x8, 1); PIN(y[2]); SCHED();
      num(3); fin(1); SGB(0x8, 1); SGB(0x402, 6); SGB(0x8, 1); PIN(y[3]); SCHED();
      fin(2); fin(3);
      st32c(key - cs, wsr, c_off);
    }
    SCHED();
    STAMP(7);
    u32x4 wo[32][2];
    auto ldwo = [&](int n) {
      const u32 o = (n < 16 ? L_WA : L_WB) + (n & 15) * 2048;
      wo[n][0] = lds_tr2(woa + o, woa + o + 512);
      wo[n][1] = lds_tr2(wob + o, wob + o + 512);
    };
#pragma unroll
    for (int n = 0; n < 16; ++n) ldwo(n);
    f32x16 c0 = zero, c1 = zero;
#pragma unroll
    for (int s = 0; s < 4; ++s) { c0 = mfma(wo[s][0], ob[0][s], c0); c1 = mfma(wo[s][1], ob[0][s], c1); }
#pragma unroll
    for (int k = 4; k < 16; ++k) { PIN(wo[k][0]); PIN(wo[k][1]); }
    PIN(c0); PIN(c1);
    WAITV(0);
    BARE();
    SCHED();
#pragma unroll
    for (int n = 16; n < 24; ++n) ldwo(n);
#pragma unroll
    for (int k = 16; k < 24; ++k) { PIN(wo[k][0]); PIN(wo[k][1]); }
    const __amdgpu_buffer_rsrc_t outr = __builtin_amdgcn_make_buffer_rsrc(KPTR(kb, 0), 0, 2048 * 1024 * 2, SRD3);
    u32 oa = lds0 + L_O + t * OREC + 256 + lane * 16;
    u32 orow = ((panel * 128 + t * 32 + l3) * 1024 + br * 512 + cg * 64 + 8 * l7) * 2;
    PIN(oa); PIN(orow);
    u32 oc = oa - 256 - lane * 12, l4 = lane * 4, l16 = lane * 16, oa4 = oa + 4 * (4 * OREC), oc4 = oc + 4 * (4 * OREC);
    u32 sw1 = lds0 + L_WA + t * 4096 + r * 128 + 16 * (h ^ (r & 7)), sr1 = lds0 + L_WA + t * 4096 + l3 * 128 + 16 * (l7 ^ l3);
    u32 sw2 = sw1 ^ 32, sw3 = sw1 ^ 64, sw4 = sw1 ^ 96, or1 = orow + 16384, or2 = orow + 32768, or3 = orow + 49152;
    PIN(oc); PIN(l4); PIN(l16); PIN(sw1); PIN(sr1); PIN(oa4); PIN(oc4); PIN(sw2); PIN(sw3); PIN(sw4); PIN(or1); PIN(or2); PIN(or3);
    STAMP(8);
    u32 nretry = 0;
    u32 chk[2];
    auto oread = [&](int j) {
      const u32 da = j % 5 == 4 ? oa4 : oa + (j % 5) * (4 * OREC), dc = j % 5 == 4 ? oc4 : oc + (j % 5) * (4 * OREC);
#pragma unroll
      for (int s = 0; s < 4; ++s) ob[j & 1][s] = lds_r128(da + s * 1024);
      chk[j & 1] = *(LAS u32*)(size_t)dc;
    };
    auto obad = [&](int j) -> bool {
      const u32 c = sum4(sum4(chk[j & 1], ob[j & 1][0]), ob[j & 1][1]) + sum4(sum4(0u, ob[j & 1][2]), ob[j & 1][3]);
      return __builtin_amdgcn_ballot_w64(c != key) != 0;
    };
#pragma unroll
    for (int j = 1; j < 8; ++j) {
      BARE();
      SCHED();
      oread(j);
      if (j >= 2) {
#pragma unroll
        for (int s = 0; s < 4; ++s) {
          const int k = 4 * (j - 1) + s;
          c0 = mfma(wo[k][0], ob[(j - 1) & 1][s], c0);
          c1 = mfma(wo[k][1], ob[(j - 1) & 1][s], c1);
        }
        if (j >= 3 && j <= 6) { ldwo(24 + 2 * (j - 3)); ldwo(25 + 2 * (j - 3)); }
      }
      const bool bad = obad(j);
      if (j >= 3 && j <= 6) {
        SGB(0x8, 1); SGB(0x100, 3); SGB(0x8, 1); SGB(0x100, 2); SGB(0x8, 1); SGB(0x100, 3); SGB(0x8, 1); SGB(0x100, 3);
        SGB(0x8, 1); SGB(0x2, 4); SGB(0x8, 1); SGB(0x2, 5); SGB(0x8, 1); SGB(0x100, 2); SGB(0x8, 1);
      } else if (j >= 2) {
        SGB(0x8, 1); SGB(0x100, 3); SGB(0x8, 1); SGB(0x100, 2); SGB(0x8, 3); SGB(0x2, 4); SGB(0x8, 1); SGB(0x2, 5); SGB(0x8, 2);
      }
      SCHED();
      if (__builtin_expect(bad, 0)) {
#pragma unroll 1
        for (u32 spin = 0; spin < SPIN_CAP; ++spin) {
          dma_orec(lds0 + L_O + t * OREC + (j % 5) * (4 * OREC), l4, l16, wsr, WS_O + ((br * 64 + panel * 4 + t) * 8 + ochunk(j)) * OREC);
          u32 jj = j + 1, jend = j + AHEAD < 7 ? j + AHEAD : 7;
          PINS(jj); PINS(jend);
#pragma unroll 1
          for (; jj <= jend; ++jj) {
            const u32 sl = (jj >= 5 ? jj - 5 : jj) * (4 * OREC);
            u32 c = *(LAS u32*)(size_t)(oc + sl);
#pragma unroll
            for (int s = 0; s < 4; ++s) c = sum4(c, lds_r128(oa + sl + s * 1024));
            if (__builtin_amdgcn_ballot_w64(c != key) != 0)
              dma_orec(lds0 + L_O + t * OREC + sl, l4, l16, wsr, WS_O + ((br * 64 + panel * 4 + t) * 8 + ochunk(jj)) * OREC);
          }
          WAITV(0);
          oread(j);
          ++nretry;
          if (!obad(j)) break;
        }
      }
    }
#pragma unroll
    for (int s = 0; s < 4; ++s) c0 = mfma(wo[28 + s][0], ob[1][s], c0);
    PIN(c0);
    SCHED();
#pragma unroll
    for (int s = 0; s < 4; ++s) c1 = mfma(wo[28 + s][1], ob[1][s], c1);
    lds_w128(sw1, pack8(c0, 0));
    lds_w128(sw2, pack8(c0, 1));
    SGB(0x8, 2); SGB(0x2, 4); SGB(0x200, 1); SGB(0x8, 1); SGB(0x2, 4); SGB(0x200, 1); SGB(0x8, 1);
    SCHED();
    STAMP(9);
    lds_w128(sw3, pack8(c1, 0));
    lds_w128(sw4, pack8(c1, 1));
    asm volatile("" ::: "memory");
    {
      u32x4 v[4];
#pragma unroll
      for (int i = 0; i < 4; ++i) v[i] = lds_r128(sr1 + i * 1024);
      SCHED();
      st128c(v[0], outr, orow);
      st128c(v[1], outr, or1);
      st128c(v[2], outr, or2);
      st128c(v[3], outr, or3);
    }
    if (t == 0 && panel == 0 && grp == 0 && lane == 0) st32c(key, wsr, WS_EPOCH + br * 256);
    if (PROBE_N == 13) WAITV(0);
    STAMP(13);
    STAMP_OUT();
  }
}
}

extern "C" void kernel_launch(void* const* d_in, const int* in_sizes, int n_in,
                              void* d_out, int out_size, void* d_ws, size_t ws_size,
                              hipStream_t stream) {
  hh::hedgehog<<<256, 512, 0, stream>>>((const u16*)d_in[0], (const u16*)d_in[1], (const u16*)d_in[2], (const u16*)d_in[3],
                                        (const u16*)d_in[4], (const u16*)d_in[5], (const u16*)d_in[6], (const u16*)d_in[7],
                                        (u16*)d_out, (u32*)d_ws);
}
```

```cpp
#include <hip/hip_runtime.h>
#include <stdint.h>

typedef unsigned short u16;
typedef unsigned u32;
typedef short bf16x8 __attribute__((ext_vector_type(8)));
typedef short s16x4 __attribute__((ext_vector_type(4)));
typedef float f32x16 __attribute__((ext_vector_type(16)));
typedef float f32x4 __attribute__((ext_vector_type(4)));
typedef float f32x2 __attribute__((ext_vector_type(2)));
typedef unsigned u32x8 __attribute__((ext_vector_type(8)));
typedef unsigned u32x4 __attribute__((ext_vector_type(4)));
typedef unsigned u32x2 __attribute__((ext_vector_type(2)));
typedef __bf16 bf16x2 __attribute__((ext_vector_type(2)));
#define DI __device__ __forceinline__
#define LAS __attribute__((address_space(3)))
#define CAS __attribute__((address_space(4)))
#define BC(T, x) __builtin_bit_cast(T, x)

namespace hh {
constexpr int AUX_SC1 = 16;
constexpr u32 SRD3 = 0x00020000u;
constexpr u32 WS_EPOCH = 0;
constexpr u32 REC = 1280;
constexpr u32 WS_PART = 4096;
constexpr u32 OREC = 4352;
constexpr u32 WS_O = WS_PART + 2 * 32 * 16 * REC;
constexpr u32 WS_END = WS_O + 2 * 64 * 8 * OREC;
constexpr u32 SLOT = 32768;
constexpr u32 L_WQ0 = 98304;
constexpr u32 L_WQ1 = 65536;
constexpr u32 L_WPHI = 163840 - 4096;
constexpr u32 L_PHI = 0;
constexpr u32 VF = 1040;
constexpr u32 L_V = 131072;
constexpr u32 L_KV = L_V + 16 * VF;
constexpr u32 L_WO = 0;
constexpr u32 L_O = 65536;
constexpr u32 KEY_STEP = 0x9E3779B9u;
constexpr u32 SPIN_CAP = 1u << 16;
constexpr int DELAY1 = 3;
constexpr int DELAY3 = 4;
constexpr u32 CODE_PF = 29184;
constexpr int AHEAD = 1;
constexpr int GQ = 1, GC = 2, OFIRST = 5;

DI u32 pk2(float a, float b) { return BC(u32, __builtin_convertvector((f32x2){a, b}, bf16x2)); }
DI u32x4 pack8(const f32x16& x, int s) {
  return (u32x4){pk2(x[8 * s], x[8 * s + 1]), pk2(x[8 * s + 2], x[8 * s + 3]), pk2(x[8 * s + 4], x[8 * s + 5]), pk2(x[8 * s + 6], x[8 * s + 7])};
}
DI f32x16 mfma(u32x4 a, u32x4 b, f32x16 c) { return __builtin_amdgcn_mfma_f32_32x32x16_bf16(BC(bf16x8, a), BC(bf16x8, b), c, 0, 0, 0); }
DI f32x4 mfma16(u32x4 a, u32x4 b, f32x4 c) { return __builtin_amdgcn_mfma_f32_16x16x32_bf16(BC(bf16x8, a), BC(bf16x8, b), c, 0, 0, 0); }
DI u32x4 lds_r128(u32 a) { return *(LAS u32x4*)(size_t)a; }
DI void lds_w128(u32 a, u32x4 v) { *(LAS u32x4*)(size_t)a = v; }
DI u32x4 lds_tr2(u32 a0, u32 a1) {
  s16x4 lo = __builtin_amdgcn_ds_read_tr16_b64_v4i16((LAS s16x4*)(size_t)a0);
  s16x4 hi = __builtin_amdgcn_ds_read_tr16_b64_v4i16((LAS s16x4*)(size_t)a1);
  return BC(u32x4, __builtin_shufflevector(lo, hi, 0, 1, 2, 3, 4, 5, 6, 7));
}
DI void dma1(u32 lds, u32 voff, __amdgpu_buffer_rsrc_t srd, u32 soff) {
  asm volatile("s_mov_b32 m0, %0\n\ts_nop 0\n\tbuffer_load_dwordx4 %1, %2, %3 offen lds" ::"s"(lds), "v"(voff), "s"(srd), "s"(soff) : "memory");
}
DI void dma4(u32 lds, const u32 (&v)[4], __amdgpu_buffer_rsrc_t srd, u32 soff) {
  asm volatile("s_mov_b32 m0, %0\n\ts_nop 0\n\t"
               "buffer_load_dwordx4 %1, %5, %6 offen lds\n\t"
               "buffer_load_dwordx4 %2, %5, %6 offen offset:1024 lds\n\t"
               "buffer_load_dwordx4 %3, %5, %6 offen offset:2048 lds\n\t"
               "buffer_load_dwordx4 %4, %5, %6 offen offset:3072 lds" ::"s"(lds), "v"(v[0]), "v"(v[1]), "v"(v[2]), "v"(v[3]), "s"(srd), "s"(soff) : "memory");
}
DI void dma_orec(u32 lds, u32 l4, u32 l16, __amdgpu_buffer_rsrc_t srd, u32 soff) {
  asm volatile("s_mov_b32 m0, %0\n\ts_nop 0\n\t"
               "buffer_load_dwordx4 %2, %3, %4 offen offset:256 sc1 lds\n\t"
               "buffer_load_dwordx4 %2, %3, %4 offen offset:1280 sc1 lds\n\t"
               "buffer_load_dwordx4 %2, %3, %4 offen offset:2304 sc1 lds\n\t"
               "buffer_load_dwordx4 %2, %3, %4 offen offset:3328 sc1 lds\n\t"
               "buffer_load_dword %1, %3, %4 offen sc1 lds" ::"s"(lds), "v"(l4), "v"(l16), "s"(srd), "s"(soff) : "memory");
}
#define TOUCH(d, voff, srd, soff) asm volatile("buffer_load_dword %0, %1, %2, %3 offen" : "=v"(d) : "v"(voff), "s"(srd), "s"(soff) : "memory")
DI void st128c(u32x4 v, __amdgpu_buffer_rsrc_t srd, u32 off) { __builtin_amdgcn_raw_buffer_store_b128(v, srd, off, 0, AUX_SC1); }
DI void st32c(u32 v, __amdgpu_buffer_rsrc_t srd, u32 off) { __builtin_amdgcn_raw_buffer_store_b32(v, srd, off, 0, AUX_SC1); }
#define BAR() asm volatile("s_waitcnt lgkmcnt(0)\n\ts_barrier" ::: "memory")
#define BARE() asm volatile("s_barrier" ::: "memory")
#define OPQ(x) asm("" : "+v"(x))
#define PIN(x) asm volatile("" : "+v"(x))
#define PINS(x) asm volatile("" : "+s"(x))
#define WAITV(n) asm volatile("s_waitcnt vmcnt(" #n ")" ::: "memory")
#define SCHED() __builtin_amdgcn_sched_barrier(0)
#define SGB(m, n) __builtin_amdgcn_sched_group_barrier(m, n, 0)
DI u32 sum4(u32 c, u32x4 v) { return (c + v.x + v.y) + v.z + v.w; }
DI u32 pair_sum(u32 s) { auto t = __builtin_amdgcn_permlane32_swap(s, s, false, false); return t[0] + t[1]; }

constexpr float LOG2E = 1.44269504088896340736f;
DI float max3f(float a, float b, float c) { return __builtin_fmaxf(__builtin_fmaxf(a, b), c); }
DI float negmax16(const f32x16& x) {
  const float a = max3f(x[0], x[1], x[2]), b = max3f(x[3], x[4], x[5]), c = max3f(x[6], x[7], x[8]);
  const float d = max3f(x[9], x[10], x[11]), e = max3f(x[12], x[13], x[14]);
  return -max3f(max3f(a, b, x[15]), max3f(c, d, e), a) * LOG2E;
}
DI void sm_exp(const f32x16& p, const f32x16& q, const float (&mc)[2], f32x2 (&e)[2][8]) {
#pragma unroll
  for (int i = 0; i < 8; ++i) {
    const f32x2 t = __builtin_elementwise_fma((f32x2){p[2 * i], p[2 * i + 1]}, (f32x2){LOG2E, LOG2E}, (f32x2){mc[0], mc[0]});
    const f32x2 u = __builtin_elementwise_fma((f32x2){q[2 * i], q[2 * i + 1]}, (f32x2){LOG2E, LOG2E}, (f32x2){mc[1], mc[1]});
    e[0][i] = (f32x2){__builtin_amdgcn_exp2f(t.x), __builtin_amdgcn_exp2f(t.y)};
    e[1][i] = (f32x2){__builtin_amdgcn_exp2f(u.x), __builtin_amdgcn_exp2f(u.y)};
  }
  SGB(0x2, 4); SGB(0x400, 8); SGB(0x2, 4); SGB(0x400, 8); SGB(0x2, 6); SGB(0x400, 8); SGB(0x2, 8); SGB(0x400, 8);
}
DI void sm_exp_s(const f32x16& p, const f32x16& q, const float (&mc)[2], f32x2 (&e)[2][8]) {
#pragma unroll
  for (int i = 0; i < 16; ++i) {
    e[0][i >> 1][i & 1] = __builtin_amdgcn_exp2f(__builtin_fmaf(p[i], LOG2E, mc[0]));
    e[1][i >> 1][i & 1] = __builtin_amdgcn_exp2f(__builtin_fmaf(q[i], LOG2E, mc[1]));
  }
}
DI void sm_finish(const f32x2 (&e)[2][8], u32x4 (&o)[2][2]) {
  float r[2];
#pragma unroll
  for (int u = 0; u < 2; ++u) {
    const f32x2 s = ((e[u][0] + e[u][1]) + (e[u][2] + e[u][3])) + ((e[u][4] + e[u][5]) + (e[u][6] + e[u][7]));
    r[u] = __builtin_amdgcn_rcpf(s.x + s.y);
  }
#pragma unroll
  for (int u = 0; u < 2; ++u) {
    u32 w[8];
#pragma unroll
    for (int i = 0; i < 8; ++i) { const f32x2 t = e[u][i] * (f32x2){r[u], r[u]}; w[i] = pk2(t.x, t.y); }
    o[u][0] = (u32x4){w[0], w[1], w[2], w[3]};
    o[u][1] = (u32x4){w[4], w[5], w[6], w[7]};
  }
}
DI void softmax16x2(const f32x16& p, const f32x16& q, const float (&mc)[2], u32x4 (&o)[2][2]) {
  f32x2 e[2][8];
  sm_exp(p, q, mc, e);
  sm_finish(e, o);
}

__global__ __launch_bounds__(512, 2) void hedgehog(const u16* xA, const u16* wA, const u16* pA, const u16* oA,
                                                   const u16* xB, const u16* wB, const u16* pB, const u16* oB,
                                                   u16* out_, u32* ws_) {
  __shared__ __attribute__((aligned(16))) char smem[163840];
  const u32 lds0 = (u32)(size_t)(LAS char*)smem;
  const int bid = blockIdx.x, xcd = bid & 7, idx = bid >> 3;
  const int br = xcd >> 2;
#define KLOAD(tail)                                                                                       \
  u32x8 ka;                                                                                               \
  u32x4 kb;                                                                                               \
  asm volatile("s_load_dwordx8 %0, %2, %3\n\ts_load_dwordx4 %1, %2, 0x40" tail : "=&s"(ka), "=&s"(kb)     \
               : "s"(__builtin_amdgcn_kernarg_segment_ptr()), "s"(br * 32))
#define KWAIT() asm volatile("s_waitcnt lgkmcnt(0)" : "+s"(ka), "+s"(kb))
#define KPTR(v, i) ((void*)(((unsigned long long)(v)[(i) + 1] << 32) | (v)[i]))
  unsigned long long pcs;
  asm volatile("s_getpc_b64 %0" : "=s"(pcs));
  pcs &= ~255ull;
  const int lane = threadIdx.x & 63;
  const int wave = __builtin_amdgcn_readfirstlane(threadIdx.x >> 6);
  const int panel = (xcd & 3) * 4 + (idx & 3), grp = idx >> 2, gq = (grp + GQ) & 7, cg = (grp + GC) & 7;
  auto ochunk = [&](u32 j) -> u32 {
    const int v = ((int)j + (((int)j - 1 - ((gq - OFIRST) & 7)) >> 31) + OFIRST) & 7, z = ((int)j - 1) >> 31;
    return (u32)__builtin_amdgcn_readfirstlane((v & ~z) | (gq & z));
  };
  f32x16 zero;
#pragma unroll
  for (int i = 0; i < 16; ++i) zero[i] = 0.f;
#define LANE_IDS()                                                                            \
  u32 lane_ = lane;                                                                           \
  PIN(lane_);                                                                                 \
  const u32 r = lane_ & 31, h = lane_ >> 5;                                                   \
  const u32 blk = (lane_ >> 4) & 1, q = (lane_ & 15) >> 2, p = lane_ & 3;                     \
  u32* ws = (u32*)KPTR(kb, 2);                                                                \
  const __amdgpu_buffer_rsrc_t wsr = __builtin_amdgcn_make_buffer_rsrc(ws, 0, WS_END, SRD3);  \
  const __amdgpu_buffer_rsrc_t wor = __builtin_amdgcn_make_buffer_rsrc(KPTR(ka, 6), 0, 512 * 512 * 2, SRD3)

  if (wave >= 4) {
    const int cw = wave - 4;
    const int rh = cw >> 1, hp = cw & 1;
    KLOAD("\n\ts_waitcnt lgkmcnt(0)");
    u32 sp0, sp1, sp2, sp3;
    {
      u32 off = idx * 1024 + cw * 256;
      off = off < CODE_PF ? off : 0;
      asm volatile("s_load_dword %0, %4, %5 offset:0\n\ts_load_dword %1, %4, %5 offset:64\n\ts_load_dword %2, %4, %5 offset:128\n\ts_load_dword %3, %4, %5 offset:192"
                   : "=&s"(sp0), "=&s"(sp1), "=&s"(sp2), "=&s"(sp3) : "s"(pcs), "s"(off));
    }
    LANE_IDS();
    u32 key = *(const CAS u32*)((const char*)ws + WS_EPOCH + br * 256) + KEY_STEP;
    u32 xa[2], xb[2], wa[2], wb2[2];
    {
      const u32 r16 = lane_ & 15, g = lane_ >> 4;
#pragma unroll
      for (int s = 0; s < 2; ++s) {
        xa[s] = lds0 + (64 * rh + r16) * 128 + 16 * ((4 * s + g) ^ (r16 >> 1));
        wa[s] = lds0 + 16384 + (8 * g + q) * 128 + 16 * ((4 * hp + 2 * s + (p >> 1)) ^ ((((q >> 1) & 1) << 2) | (blk << 1))) + 8 * (p & 1);
        xb[s] = xa[s] + 2 * SLOT; wb2[s] = wa[s] + 2 * SLOT;
        PIN(xa[s]); PIN(xb[s]); PIN(wa[s]); PIN(wb2[s]);
      }
    }
    u32 fpa = lds0 + L_WPHI + hp * 1024 + (8 * h + q) * 32 + (8 * blk + 4 * (p >> 1)) * 2;
    PIN(fpa);
    PINS(key);
    f32x4 ak[4][2], av[4][2];
#pragma unroll
    for (int i = 0; i < 8; ++i) ak[i >> 1][i & 1] = av[i >> 1][i & 1] = (f32x4){0.f, 0.f, 0.f, 0.f};
    u32 vra, pra, vwa, pw0, pw1, pub16, pub4;
    u32x4 xf[2][4], wk[2][2], wv[2][2];
    auto ldx = [&](int c, int s) {
      const u32 x = c % 3 == 2 ? xb[s] : xa[s] + (c % 3) * SLOT;
#pragma unroll
      for (int i = 0; i < 4; ++i) xf[s][i] = lds_r128(x + i * 2048);
    };
    auto ldw = [&](int c, int s, int m) {
#pragma unroll
      for (int n = 0; n < 2; ++n) {
        const u32 w = (c % 3 == 2 ? wb2[n] : wa[n] + (c % 3) * SLOT) + s * 4096 + m * 8192;
        (m ? wv : wk)[s][n] = lds_tr2(w, w + 512);
      }
    };
    auto mk = [&](int s) {
#pragma unroll
      for (int i = 0; i < 8; ++i) ak[i >> 1][i & 1] = mfma16(wk[s][i & 1], xf[s][i >> 1], ak[i >> 1][i & 1]);
    };
    auto mv = [&](int s) {
#pragma unroll
      for (int i = 0; i < 8; ++i) av[i >> 1][i & 1] = mfma16(xf[s][i >> 1], wv[s][i & 1], av[i >> 1][i & 1]);
    };
    auto mix = [&](int n) {
#pragma unroll
      for (int i = 0; i < 8; ++i) {
        SGB(0x8, 1);
        if (n == 12) { if (i < 4) SGB(0x100, 2); else SGB(0x100, 1); }
        else if (n == 8) SGB(0x100, 1);
        else if (n == 4) { if (i < 4) SGB(0x100, 1); }
        else if (n < 0) SGB(0x6, 2);
      }
      SCHED();
    };
    BAR();
    asm volatile("" ::"s"(sp0), "s"(sp1), "s"(sp2), "s"(sp3));
    ldw(0, 0, 0); ldx(0, 0); ldw(0, 0, 1);
    u32x4 fa[2];
#pragma unroll
    for (int hh = 0; hh < 2; ++hh) fa[hh] = lds_tr2(fpa + hh * 512, fpa + hh * 512 + 128);
    SCHED();
#pragma unroll
    for (int c = 0; c < 7; ++c) {
      ldw(c, 1, 0); ldx(c, 1); mk(0); mix(8);
      ldw(c, 1, 1); mv(0); mix(4);
      u32 ln = lane;
      PIN(ln);
      const u32 r_ = ln & 31, h_ = ln >> 5, q_ = (ln & 15) >> 2;
      if (c == 1) {
        const u32 sg = (ln & 4) ? 0x80008000u : 0u;
#pragma unroll
        for (int hh = 0; hh < 2; ++hh) { OPQ(fa[hh]); fa[hh] ^= (u32x4){sg, sg, sg, sg}; }
      }
      if (c == 2) vra = lds0 + L_V + (cw >> 1) * (2 * VF) + (r_ < 16 ? (ln + 16 * (cw & 1)) * 16 : 1024);
      if (c == 3) pra = lds0 + L_PHI + cw * 8192 + (4 * h_ + q_) * 64 + 16 * ((((ln >> 3) & 2) + ((ln >> 1) & 1)) ^ ((2 * h_ + (q_ >> 1)) & 3)) + 8 * (ln & 1);
      if (c == 4) {
        const u32 rec = WS_PART + ((br * 32 + grp * 4 + cw) * 16 + panel) * REC;
        pub16 = rec + ln * 16; pub4 = rec + 1024 + ln * 4;
      }
      if (c == 5) { pw0 = lds0 + L_PHI + (2 * hp) * 8192 + (64 * rh + r_) * 64 + 16 * ((2 * h_) ^ ((r_ >> 1) & 3)); pw1 = pw0 ^ 16; }
      if (c == 6) vwa = lds0 + L_V + (8 * rh + 2 * hp) * VF + ((ln & 16) * 2 + (ln & 15)) * 16 + h_ * 8;
      mk(1); mix(c >= 1 ? -1 : 0);
      if (c == 1) { PIN(fa[0]); PIN(fa[1]); }
      if (c == 2) PIN(vra);
      if (c == 3) PIN(pra);
      if (c == 4) { PIN(pub16); PIN(pub4); }
      if (c == 5) { PIN(pw0); PIN(pw1); }
      if (c == 6) PIN(vwa);
      BAR();
      ldw(c + 1, 0, 0); ldx(c + 1, 0); ldw(c + 1, 0, 1);
      mv(1); mix(12);
    }
    ldw(7, 1, 0); ldx(7, 1); ldw(7, 1, 1); mk(0); mix(12);
    mk(1); mix(0);
    __builtin_amdgcn_s_setprio(2);
    f32x16 pk[2][2];
    {
      u32x4 kf[2][2];
      auto kpack = [&](int i) {
#pragma unroll
        for (int hh = 0; hh < 2; ++hh) {
          const f32x4 &t0 = ak[2 * i][hh], &t1 = ak[2 * i + 1][hh];
          const auto lo = __builtin_amdgcn_permlane16_swap(pk2(t0[0], t0[1]), pk2(t1[0], t1[1]), false, false);
          const auto hi = __builtin_amdgcn_permlane16_swap(pk2(t0[2], t0[3]), pk2(t1[2], t1[3]), false, false);
          kf[i][hh] = (u32x4){lo[0], hi[0], lo[1], hi[1]};
        }
      };
      mv(0); kpack(0);
      SGB(0x8, 2);
#pragma unroll
      for (int i = 0; i < 6; ++i) { SGB(0x2, 2); SGB(0x8, 1); }
      SCHED();
      pk[0][0] = mfma(fa[0], kf[0][0], zero);
      pk[0][1] = mfma(fa[1], kf[0][1], zero);
      kpack(1);
      SGB(0x8, 1); SGB(0x2, 6); SGB(0x8, 1); SGB(0x2, 6);
      PIN(pk[0][0]); PIN(pk[0][1]); PIN(kf[1][0]); PIN(kf[1][1]);
      SCHED();
      mv(1);
      pk[1][0] = mfma(fa[0], kf[1][0], zero);
      pk[1][1] = mfma(fa[1], kf[1][1], zero);
    }
    float m0[2], m1[2];
    m0[0] = negmax16(pk[0][0]); m0[1] = negmax16(pk[0][1]);
    SGB(0x8, 2);
#pragma unroll
    for (int i = 0; i < 6; ++i) { SGB(0x2, 2); SGB(0x8, 1); }
    SGB(0x8, 1); SGB(0x2, 6); SGB(0x8, 1);
#pragma unroll
    for (int i = 0; i < 8; ++i) PIN(av[i >> 1][i & 1]);
    PIN(pk[1][0]); PIN(pk[1][1]); PIN(m0[0]); PIN(m0[1]);
    SCHED();
#pragma unroll
    for (int i = 0; i < 8; ++i) {
      const f32x4& t = av[i >> 1][i & 1];
      *(LAS u32x2*)(size_t)(vwa + (4 * (i >> 2) + ((i >> 1) & 1)) * VF + (i & 1) * 256) = (u32x2){pk2(t[0], t[1]), pk2(t[2], t[3])};
    }
    SCHED();
    {
      u32x4 o[2][2];
      softmax16x2(pk[0][0], pk[0][1], m0, o);
      lds_w128(pw0, o[0][0]); lds_w128(pw1, o[0][1]);
      lds_w128(pw0 + 8192, o[1][0]); lds_w128(pw1 + 8192, o[1][1]);
      SCHED();
      m1[0] = negmax16(pk[1][0]); m1[1] = negmax16(pk[1][1]);
      PIN(m1[0]); PIN(m1[1]);
      SCHED();
      softmax16x2(pk[1][0], pk[1][1], m1, o);
      lds_w128(pw0 + 2048, o[0][0]); lds_w128(pw1 + 2048, o[0][1]);
      lds_w128(pw0 + 2048 + 8192, o[1][0]); lds_w128(pw1 + 2048 + 8192, o[1][1]);
      SGB(0x402, 26); SGB(0x200, 1); SGB(0x402, 8); SGB(0x200, 1); SGB(0x402, 8); SGB(0x200, 1); SGB(0x402, 8); SGB(0x200, 1);
      SCHED();
      BARE();
      BAR();
      SCHED();
    }
    f32x16 kv = zero;
    {
      u32x4 a[8], b[8];
#pragma unroll
      for (int u = 0; u < 8; ++u) {
        a[u] = lds_r128(vra + ((u >> 1) * 4 + (u & 1)) * VF);
        b[u] = lds_tr2(pra + 16 * u * 64, pra + (16 * u + 8) * 64);
      }
#pragma unroll
      for (int u = 0; u < 8; ++u) kv = mfma(a[u], b[u], kv);
      SGB(0x100, 12); SGB(0x8, 1); SGB(0x100, 6); SGB(0x8, 1); SGB(0x100, 6); SGB(0x8, 6);
    }
    {
      const u32x4 w = pack8(kv, 0);
      st128c(w, wsr, pub16);
      const u32 zb = __float_as_uint(kv[8]);
      const u32 chk = key - pair_sum(sum4(h ? 0u : zb, w));
      st32c(h ? chk : zb, wsr, pub4);
    }
    SCHED();
    u32 l16, l4, kwa, kza, gso[4], vo[4], wso;
    u32x4 sel;
    {
      u32 ln = lane;
      PIN(ln);
#pragma unroll
      for (int i = 0; i < 4; ++i) vo[i] = (ln >> 3) * 1024 + 16 * ((ln & 7) ^ (((ln >> 4) & 1) << 2)) + i * 7168;
#pragma unroll
      for (int i = 0; i < 2; ++i) dma4(lds0 + L_WO + cw * 8192 + i * 4096, vo, wor, ochunk(cw) * 65536 + i * 32768 + cg * 128);
      wso = ochunk(4 + cw) * 65536 + cg * 128;
      const u32 r_ = ln & 31, h_ = ln >> 5;
      l16 = ln * 16; l4 = ln * 4;
      kwa = lds0 + L_KV + cw * 1536 + r_ * 48 + h_ * 16; kza = lds0 + L_KV + cw * 1536 + r_ * 48 + 32 + h_ * 8;
      const bool hit = r_ < 16 && ((r_ >> 2) & 1) == h_;
      const u32 j = (r_ & 3) + 4 * (r_ >> 3);
#pragma unroll
      for (int e = 0; e < 4; ++e) sel[e] = (hit && (j >> 1) == e) ? ((j & 1) ? 0x3F800000u : 0x00003F80u) : 0u;
#pragma unroll
      for (int i = 0; i < 4; ++i) { gso[i] = WS_PART + ((br * 32 + gq * 4 + cw) * 16 + ((i + idx) & 3) * 4) * REC; PINS(gso[i]); }
      PIN(l16); PIN(l4); PIN(kwa); PIN(kza); PIN(sel); PINS(wso);
    }
    __builtin_amdgcn_s_sleep(DELAY1);
    f32x16 gs[4];
    float z[4];
    {
      u32x4 g[16];
      u32 gz[16];
      auto gload = [&](int gi) {
#pragma unroll
        for (int i = 0; i < 4; ++i) {
          g[4 * gi + i] = __builtin_amdgcn_raw_buffer_load_b128(wsr, l16 + i * REC, gso[gi], AUX_SC1);
          gz[4 * gi + i] = __builtin_amdgcn_raw_buffer_load_b32(wsr, l4 + (1024 + i * REC - 1024 * (i == 3)), gso[gi] + 1024 * (i == 3), AUX_SC1);
          SCHED();
        }
      };
      auto gsum = [&](int gi) -> bool {
        u32 c = 0;
        PIN(gz[4 * gi + 3]); SCHED();
#pragma unroll
        for (int i = 0; i < 4; ++i) {
          c = sum4(c + gz[4 * gi + i], g[4 * gi + i]);
          gs[gi] = mfma(sel, g[4 * gi + i], i == 0 ? zero : gs[gi]);
          z[gi] = i == 0 ? __uint_as_float(gz[4 * gi]) : z[gi] + __uint_as_float(gz[4 * gi + i]);
        }
        const bool bad = __builtin_amdgcn_ballot_w64(pair_sum(c) != 4u * key) != 0;
        SGB(0x8, 1); SGB(0x2, 5); SGB(0x8, 1); SGB(0x2, 5); SGB(0x8, 1); SGB(0x2, 4); SGB(0x8, 1);
        PIN(z[gi]);
        SCHED();
        return bad;
      };
      gload(0); gload(1); gload(2); gload(3);
      bool b0 = gsum(0), b1 = gsum(1), b2 = gsum(2), b3 = gsum(3);
#pragma unroll 1
      for (u32 spin = 0; (b0 | b1 | b2 | b3) && spin < SPIN_CAP; ++spin) {
        asm volatile("" ::: "memory");
        if (b0) gload(0);
        if (b1) gload(1);
        if (b2) gload(2);
        if (b3) gload(3);
        if (b0) b0 = gsum(0);
        if (b1) b1 = gsum(1);
        if (b2) b2 = gsum(2);
        if (b3) b3 = gsum(3);
      }
    }
    __builtin_amdgcn_s_waitcnt(0x0F70);
    {
      float tt[8];
#pragma unroll
      for (int j = 0; j < 8; ++j) tt[j] = (gs[0][j] + gs[1][j]) + (gs[2][j] + gs[3][j]);
      const float zt = (z[0] + z[1]) + (z[2] + z[3]);
      lds_w128(kwa, (u32x4){pk2(tt[0], tt[1]), pk2(tt[2], tt[3]), pk2(tt[4], tt[5]), pk2(tt[6], tt[7])});
      const u32 zz = pk2(zt, zt);
      *(LAS u32x2*)(size_t)kza = (u32x2){zz, zz};
    }
#pragma unroll
    for (int i = 0; i < 2; ++i) dma4(lds0 + L_WO + 32768 + cw * 8192 + i * 4096, vo, wor, wso + i * 32768);
    BAR();
    {
      SCHED();
      u32 oso[8], ob = lds0 + L_O + cw * OREC, i0 = 0;
      PINS(i0);
#pragma unroll
      for (int i = 1; i < 8; ++i) { oso[i] = WS_O + ((br * 64 + panel * 4 + cw) * 8 + ochunk(i + i0)) * OREC; PINS(oso[i]); }
      PINS(ob);
      WAITV(0);
      BARE();
      __builtin_amdgcn_s_sleep(DELAY3);
      auto oissue = [&](int j) { dma_orec(ob + (j % 5) * (4 * OREC), l4, l16, wsr, oso[j]); };
      oissue(1); oissue(2);
      WAITV(5); BARE(); oissue(3);
      WAITV(5); BARE(); oissue(4); oissue(5);
      WAITV(10); BARE(); oissue(6);
      WAITV(10); BARE(); oissue(7);
      WAITV(10); BARE();
      WAITV(5); BARE();
      WAITV(0); BARE();
    }
  } else {
    KLOAD("");
    const int t = wave;
    const u32 l7 = lane & 7, l3 = lane >> 3, lq = lane >> 4;
    const u32 wsw = 16 * (l7 ^ ((lq & 1) << 2));
    u32 vx[4], vw[4];
#pragma unroll
    for (int i = 0; i < 4; ++i) {
      vx[i] = l3 * 1024 + 16 * (l7 ^ (((i & 1) << 2) | lq)) + i * 7168;
      vw[i] = l3 * 3072 + (wsw ^ ((i & 1) << 5)) + i * (8 * 3072 - 1024);
    }
    auto chunk = [&](u32 c) -> u32 {
      const u32 g = grp + idx;
      return c - 2 < 4 ? 2 + ((c - 2) ^ (g & 3)) : c ^ (g & 1);
    };
    const u32 xs0 = (panel * 128 + t * 32) * 1024, ws0 = (t & 1) * (32 * 3072) + (1 + (t >> 1)) * 1024 + grp * 128;
    const u32 ldst = lds0 + t * 4096;
    u32 e0 = ldst, e1 = ldst + 16384, e2 = xs0 + chunk(0) * 128, e3 = ws0 + chunk(0) * (64 * 3072);
    PINS(e0); PINS(e1); PINS(e2); PINS(e3);
#pragma unroll
    for (int i = 0; i < 4; ++i) { PIN(vx[i]); PIN(vw[i]); }
    KWAIT();
    const __amdgpu_buffer_rsrc_t xr = __builtin_amdgcn_make_buffer_rsrc(KPTR(ka, 0), 0, 2048 * 512 * 2, SRD3);
    const __amdgpu_buffer_rsrc_t wr = __builtin_amdgcn_make_buffer_rsrc(KPTR(ka, 2), 0, 512 * 1536 * 2, SRD3);
    dma4(e0, vx, xr, e2);
    dma4(e1, vw, wr, e3);
    {
      const __amdgpu_buffer_rsrc_t pr = __builtin_amdgcn_make_buffer_rsrc(KPTR(ka, 4), 0, 32 * 256 * 2, SRD3);
      dma1(lds0 + L_WPHI + t * 1024, lane * 16, pr, (t < 2 ? grp : gq) * 2048 + (t & 1) * 1024);
    }
    dma4(ldst + SLOT, vx, xr, xs0 + chunk(1) * 128);
    dma4(ldst + SLOT + 16384, vw, wr, ws0 + chunk(1) * (64 * 3072));
    LANE_IDS();
    u32 key = *(const CAS u32*)((const char*)ws + WS_EPOCH + br * 256) + KEY_STEP;
    const u32 sw = (r >> 1) & 7;
    u32 xa[4];
#pragma unroll
    for (int s = 0; s < 4; ++s) { xa[s] = lds0 + (32 * t + r) * 128 + 16 * ((2 * s + h) ^ sw); PIN(xa[s]); }
    u32 fpa = lds0 + L_WPHI + 2048 + (4 * h + q) * 32 + (8 * blk + 4 * (p >> 1)) * 2;
    PIN(fpa);
    PINS(key);
    u32x4 xq[32], fa[4];
    u32 wa, wb, kra, o_off, c_off, woa, wob, vto, sto, tho;
#pragma unroll
    for (int c = 0; c < 8; ++c) {
      u32 d0, d1, s0, s1;
      if (c < 6) {
        d0 = ldst + ((c + 2) % 3) * SLOT; d1 = d0 + 16384;
        s0 = xs0 + chunk(c + 2) * 128; s1 = ws0 + chunk(c + 2) * (64 * 3072);
      } else {
        const u32 k0 = 2 * ((2 * (c - 6) + idx) & 3) + (t >> 1), k1 = (k0 + 2) & 7;
        d0 = lds0 + L_WQ1 + ((chunk(k0) + 4) & 7) * 8192 + (t & 1) * 4096; d1 = lds0 + L_WQ1 + ((chunk(k1) + 4) & 7) * 8192 + (t & 1) * 4096;
        s0 = (k0 * 64 + (t & 1) * 32) * 3072 + gq * 128; s1 = (k1 * 64 + (t & 1) * 32) * 3072 + gq * 128;
      }
      PINS(d0); PINS(d1); PINS(s0); PINS(s1);
      WAITV(8);
      BAR();
      if (c < 6) dma4(d0, vx, xr, s0); else dma4(d0, vw, wr, s0);
#pragma unroll
      for (int s = 0; s < 4; ++s) xq[4 * c + s] = lds_r128(xa[s] + (c % 3) * SLOT);
      dma4(d1, vw, wr, s1);
      if (c == 0) {
#pragma unroll
        for (int hd = 0; hd < 4; ++hd) fa[hd] = lds_tr2(fpa + hd * 512, fpa + hd * 512 + 256);
      }
      if (c == 1) {
        lds_w128(lds0 + L_V + (lane & 15) * VF + 1024, (u32x4){0x3F803F80u, 0x3F803F80u, 0x3F803F80u, 0x3F803F80u});
        const u32 sg = (lane & 4) ? 0x80008000u : 0u;
#pragma unroll
        for (int hd = 0; hd < 4; ++hd) { PIN(fa[hd]); fa[hd] ^= (u32x4){sg, sg, sg, sg}; PIN(fa[hd]); }
        wa = lds0 + L_WQ1 + (8 * h + q) * 128 + 16 * ((2 * blk + (p >> 1)) ^ ((((q >> 1) & 1) << 2) | (h << 1))) + 8 * (p & 1);
        wb = wa ^ 64;
        kra = lds0 + L_KV + (16 * h + q) * 48 + (blk ? 32 : 8 * p);
        o_off = WS_O + ((br * 64 + panel * 4 + t) * 8 + gq) * OREC + 256 + lane * 16;
        c_off = WS_O + ((br * 64 + panel * 4 + t) * 8 + gq) * OREC + lane * 4;
        woa = lds0 + L_WO + (8 * h + q) * 128 + 16 * ((2 * blk + (p & 1)) ^ (((q >> 1) & 1) << 2)) + 8 * (p >> 1);
        wob = woa ^ 64;
        vto = r * 128; sto = (idx * 16 + t * 4) * 1024;
        PIN(wa); PIN(wb); PIN(kra); PIN(o_off); PIN(c_off); PIN(woa); PIN(wob); PIN(vto); PINS(sto);
      }
    }
    TOUCH(tho, vto, wor, sto);
    u32x4 fq[2][2][2];
    {
      f32x16 aq[2], pq[2];
      float mq[2];
      aq[0] = aq[1] = zero;
      u32x4 wf[4];
      auto ldw = [&](int n) {
        const int k = n & 31;
        const u32 o = (n < 32 ? wa : wb) + (k < 16 ? (L_WQ0 - L_WQ1) : 0) + (k & 15) * 2048;
        wf[n & 3] = lds_tr2(o, o + 512);
      };
      WAITV(1);
      BAR();
      SCHED();
      ldw(0); ldw(1); ldw(2);
      SCHED();
      BARE();
      SCHED();
#pragma unroll
      for (int n = 0; n < 34; ++n) {
        ldw(n + 3);
        aq[n >> 5] = mfma(wf[n & 3], xq[n & 31], aq[n >> 5]);
        SGB(0x8, 1); SGB(0x100, 2);
        SCHED();
      }
#pragma unroll
      for (int hh = 0; hh < 2; ++hh) pq[hh] = mfma(fa[hh], pack8(aq[0], hh), zero);
      SCHED();
#pragma unroll
      for (int n = 34; n < 64; ++n) {
        if (n + 3 < 64) ldw(n + 3);
        aq[1] = mfma(wf[n & 3], xq[n & 31], aq[1]);
      }
      mq[0] = negmax16(pq[0]); mq[1] = negmax16(pq[1]);
      f32x2 eq[2][8];
      sm_exp_s(pq[0], pq[1], mq, eq);
      SGB(0x8, 1); SGB(0x100, 2); SGB(0x8, 1); SGB(0x100, 2);
#pragma unroll
      for (int n = 36; n < 41; ++n) { SGB(0x8, 1); SGB(0x100, 2); SGB(0x402, 4); }
#pragma unroll
      for (int n = 41; n < 64; ++n) { SGB(0x8, 1); SGB(0x100, 2); if (n & 1) SGB(0x402, 3); else SGB(0x402, 2); }
#pragma unroll
      for (int i = 0; i < 16; ++i) PIN(eq[i >> 3][i & 7]);
      SCHED();
#pragma unroll
      for (int hh = 0; hh < 2; ++hh) { pq[hh] = mfma(fa[2 + hh], pack8(aq[1], hh), zero); PIN(pq[hh]); }
      SCHED();
      sm_finish(eq, fq[0]);
      SCHED();
      asm volatile("s_waitcnt vmcnt(0)" : "+v"(tho)::"memory");
      mq[0] = negmax16(pq[0]); mq[1] = negmax16(pq[1]);
      PIN(mq[0]); PIN(mq[1]);
      SCHED();
#pragma unroll
      for (int i = 0; i < 4; ++i) PIN(fq[0][i >> 1][i & 1]);
      softmax16x2(pq[0], pq[1], mq, fq[1]);
#pragma unroll
      for (int i = 0; i < 4; ++i) PIN(fq[1][i >> 1][i & 1]);
    }
    BAR();
    u32x4 ob[2][4];
    {
      u32x4 a0[4], a1[4];
#pragma unroll
      for (int hd = 0; hd < 4; ++hd) {
        a0[hd] = lds_tr2(kra + hd * 1536, kra + hd * 1536 + 4 * 48);
        a1[hd] = lds_tr2(kra + hd * 1536 + 8 * 48, kra + hd * 1536 + 12 * 48);
      }
      SCHED();
      f32x16 y[4];
      u32 cs = 0;
      auto num = [&](int hd) {
        y[hd] = mfma(a0[hd], fq[hd >> 1][hd & 1][0], zero);
        y[hd] = mfma(a1[hd], fq[hd >> 1][hd & 1][1], y[hd]);
      };
      auto fin = [&](int hd) {
        const f32x16& yy = y[hd];
        const float rd = __builtin_amdgcn_rcpf(yy[8] + 1e-6f);
        u32 w[4];
#pragma unroll
        for (int e = 0; e < 4; ++e) { float t0 = yy[2 * e] * rd, t1 = yy[2 * e + 1] * rd; if (hd < 2) OPQ(t0); w[e] = pk2(t0, t1); }
        ob[0][hd] = (u32x4){w[0], w[1], w[2], w[3]};
        st128c(ob[0][hd], wsr, o_off + hd * 1024);
        cs = sum4(cs, ob[0][hd]);
      };
      num(0); num(1); PIN(y[0]); PIN(y[1]); SCHED();
      num(2); fin(0); SGB(0x8, 1); SGB(0x402, 6); SGB(0x8, 1); PIN(y[2]); SCHED();
      num(3); fin(1); SGB(0x8, 1); SGB(0x402, 6); SGB(0x8, 1); PIN(y[3]); SCHED();
      fin(2); fin(3);
      st32c(key - cs, wsr, c_off);
    }
    SCHED();
    u32x4 wo[32][2];
    auto ldwo = [&](int n) {
      wo[n][0] = lds_tr2(woa + n * 2048, woa + n * 2048 + 512);
      wo[n][1] = lds_tr2(wob + n * 2048, wob + n * 2048 + 512);
    };
#pragma unroll
    for (int n = 0; n < 16; ++n) ldwo(n);
    f32x16 c0 = zero, c1 = zero;
#pragma unroll
    for (int s = 0; s < 4; ++s) { c0 = mfma(wo[s][0], ob[0][s], c0); c1 = mfma(wo[s][1], ob[0][s], c1); }
#pragma unroll
    for (int k = 4; k < 16; ++k) { PIN(wo[k][0]); PIN(wo[k][1]); }
    PIN(c0); PIN(c1);
    WAITV(0);
    BARE();
    SCHED();
#pragma unroll
    for (int n = 16; n < 24; ++n) ldwo(n);
#pragma unroll
    for (int k = 16; k < 24; ++k) { PIN(wo[k][0]); PIN(wo[k][1]); }
    const __amdgpu_buffer_rsrc_t outr = __builtin_amdgcn_make_buffer_rsrc(KPTR(kb, 0), 0, 2048 * 1024 * 2, SRD3);
    u32 oa = lds0 + L_O + t * OREC + 256 + lane * 16;
    u32 orow = ((panel * 128 + t * 32 + l3) * 1024 + br * 512 + cg * 64 + 8 * l7) * 2;
    PIN(oa); PIN(orow);
    u32 oc = oa - 256 - lane * 12, l4 = lane * 4, l16 = lane * 16, oa4 = oa + 4 * (4 * OREC), oc4 = oc + 4 * (4 * OREC);
    u32 sw1 = lds0 + t * 4096 + r * 128 + 16 * (h ^ (r & 7)), sr1 = lds0 + t * 4096 + l3 * 128 + 16 * (l7 ^ l3);
    u32 sw2 = sw1 ^ 32, sw3 = sw1 ^ 64, sw4 = sw1 ^ 96, or1 = orow + 16384, or2 = orow + 32768, or3 = orow + 49152;
    PIN(oc); PIN(l4); PIN(l16); PIN(sw1); PIN(sr1); PIN(oa4); PIN(oc4); PIN(sw2); PIN(sw3); PIN(sw4); PIN(or1); PIN(or2); PIN(or3);
    u32 chk[2];
    auto oread = [&](int j) {
      const u32 da = j % 5 == 4 ? oa4 : oa + (j % 5) * (4 * OREC), dc = j % 5 == 4 ? oc4 : oc + (j % 5) * (4 * OREC);
#pragma unroll
      for (int s = 0; s < 4; ++s) ob[j & 1][s] = lds_r128(da + s * 1024);
      chk[j & 1] = *(LAS u32*)(size_t)dc;
    };
    auto obad = [&](int j) -> bool {
      const u32 c = sum4(sum4(chk[j & 1], ob[j & 1][0]), ob[j & 1][1]) + sum4(sum4(0u, ob[j & 1][2]), ob[j & 1][3]);
      return __builtin_amdgcn_ballot_w64(c != key) != 0;
    };
#pragma unroll
    for (int j = 1; j < 8; ++j) {
      BARE();
      SCHED();
      oread(j);
      if (j >= 2) {
#pragma unroll
        for (int s = 0; s < 4; ++s) {
          const int k = 4 * (j - 1) + s;
          c0 = mfma(wo[k][0], ob[(j - 1) & 1][s], c0);
          c1 = mfma(wo[k][1], ob[(j - 1) & 1][s], c1);
        }
        if (j >= 3 && j <= 6) { ldwo(24 + 2 * (j - 3)); ldwo(25 + 2 * (j - 3)); }
      }
      const bool bad = obad(j);
      if (j >= 3 && j <= 6) {
        SGB(0x8, 1); SGB(0x100, 3); SGB(0x8, 1); SGB(0x100, 2); SGB(0x8, 1); SGB(0x100, 3); SGB(0x8, 1); SGB(0x100, 3);
        SGB(0x8, 1); SGB(0x2, 4); SGB(0x8, 1); SGB(0x2, 5); SGB(0x8, 1); SGB(0x100, 2); SGB(0x8, 1);
      } else if (j >= 2) {
        SGB(0x8, 1); SGB(0x100, 3); SGB(0x8, 1); SGB(0x100, 2); SGB(0x8, 3); SGB(0x2, 4); SGB(0x8, 1); SGB(0x2, 5); SGB(0x8, 2);
      }
      SCHED();
      if (__builtin_expect(bad, 0)) {
#pragma unroll 1
        for (u32 spin = 0; spin < SPIN_CAP; ++spin) {
          dma_orec(lds0 + L_O + t * OREC + (j % 5) * (4 * OREC), l4, l16, wsr, WS_O + ((br * 64 + panel * 4 + t) * 8 + ochunk(j)) * OREC);
          u32 jj = j + 1, jend = j + AHEAD < 7 ? j + AHEAD : 7;
          PINS(jj); PINS(jend);
#pragma unroll 1
          for (; jj <= jend; ++jj) {
            const u32 sl = (jj >= 5 ? jj - 5 : jj) * (4 * OREC);
            u32 c = *(LAS u32*)(size_t)(oc + sl);
#pragma unroll
            for (int s = 0; s < 4; ++s) c = sum4(c, lds_r128(oa + sl + s * 1024));
            if (__builtin_amdgcn_ballot_w64(c != key) != 0)
              dma_orec(lds0 + L_O + t * OREC + sl, l4, l16, wsr, WS_O + ((br * 64 + panel * 4 + t) * 8 + ochunk(jj)) * OREC);
          }
          WAITV(0);
          oread(j);
          if (!obad(j)) break;
        }
      }
    }
#pragma unroll
    for (int s = 0; s < 4; ++s) c0 = mfma(wo[28 + s][0], ob[1][s], c0);
    PIN(c0);
    SCHED();
#pragma unroll
    for (int s = 0; s < 4; ++s) c1 = mfma(wo[28 + s][1], ob[1][s], c1);
    lds_w128(sw1, pack8(c0, 0));
    lds_w128(sw2, pack8(c0, 1));
    SGB(0x8, 2); SGB(0x2, 4); SGB(0x200, 1); SGB(0x8, 1); SGB(0x2, 4); SGB(0x200, 1); SGB(0x8, 1);
    SCHED();
    lds_w128(sw3, pack8(c1, 0));
    lds_w128(sw4, pack8(c1, 1));
    asm volatile("" ::: "memory");
    {
      u32x4 v[4];
#pragma unroll
      for (int i = 0; i < 4; ++i) v[i] = lds_r128(sr1 + i * 1024);
      SCHED();
      st128c(v[0], outr, orow);
      st128c(v[1], outr, or1);
      st128c(v[2], outr, or2);
      st128c(v[3], outr, or3);
    }
    if (t == 0 && panel == 0 && grp == 0 && lane == 0) st32c(key, wsr, WS_EPOCH + br * 256);
  }
}
}

extern "C" void kernel_launch(void* const* d_in, const int* in_sizes, int n_in,
                              void* d_out, int out_size, void* d_ws, size_t ws_size,
                              hipStream_t stream) {
  hh::hedgehog<<<256, 512, 0, stream>>>((const u16*)d_in[0], (const u16*)d_in[1], (const u16*)d_in[2], (const u16*)d_in[3],
                                        (const u16*)d_in[4], (const u16*)d_in[5], (const u16*)d_in[6], (const u16*)d_in[7],
                                        (u16*)d_out, (u32*)d_ws);
}
```
